```python
import torch

def _softmax(x, axis=-1):
    return torch.softmax(x, dim=axis)

def _gelu(x):
    return torch.nn.functional.gelu(x, approximate='tanh')

def _silu(x):
    return torch.nn.functional.silu(x)

def _sigmoid(x):
    return torch.sigmoid(x)

def _layernorm(x, axis=-1, eps=1e-05):
    m = x.mean(dim=axis, keepdim=True)
    v = x.var(dim=axis, keepdim=True, unbiased=False)
    return (x - m) / torch.sqrt(v + eps)

def _scatter_add(tgt, idx, src, axis=0):
    return tgt.clone().index_add_(axis, idx, src)

def _segment_sum(src, seg, n):
    out = torch.zeros((n,) + src.shape[1:], dtype=src.dtype)
    return out.index_add_(0, seg, src)

def _pad(x, pw):
    flat = tuple((v for pair in reversed(pw) for v in pair))
    return torch.nn.functional.pad(x, flat)
kernel_name = 'adaLN_cond__mha_block_cached__geglu_ffn__prompt'

def setup_inputs():

    _w = lambda a: (lambda t: t.to(torch.bfloat16) if t.is_floating_point() else t)(torch.as_tensor(a))

    def _A_setup():
        B, Sq, D = (1, 2048, 1024)
        H, Sk, Dh = (32, 2048, 128)
        return {'x': _w((lambda *s: torch.randn(*s, dtype=torch.bfloat16) * 0.05)(B, Sq, D)), 'k_cache': _w((lambda *s: torch.randn(*s, dtype=torch.bfloat16) * 3.2)(B, H, Sk, Dh)), 'v_cache': _w((lambda *s: torch.randn(*s, dtype=torch.bfloat16) * 0.4)(B, H, Sk, Dh)), 'wq': _w((lambda *s: torch.randn(*s, dtype=torch.bfloat16) * 3.2)(D, H * Dh) * D ** (-0.5)), 'wo': _w((lambda *s: torch.randn(*s, dtype=torch.bfloat16) * 0.4)(H * Dh, D) * (H * Dh) ** (-0.5))}
    _A_in = _A_setup()

    def _B_setup():
        B, S, D, F = (1, 2048, 1024, 3584)
        return {'x': _w((lambda *s: torch.randn(*s, dtype=torch.bfloat16) * 0.05)(B, S, D)), 'w_gate': _w((lambda *s: torch.randn(*s, dtype=torch.bfloat16) * 0.05)(D, F)), 'w_up': _w((lambda *s: torch.randn(*s, dtype=torch.bfloat16) * 0.05)(D, F)), 'w_down': _w((lambda *s: torch.randn(*s, dtype=torch.bfloat16) * 0.05)(F, D))}
    _B_in = _B_setup()
    return {'A_x': _A_in['x'], 'A_k_cache': _A_in['k_cache'], 'A_v_cache': _A_in['v_cache'], 'A_wq': _A_in['wq'], 'A_wo': _A_in['wo'], 'B_x': _B_in['x'], 'B_w_gate': _B_in['w_gate'], 'B_w_up': _B_in['w_up'], 'B_w_down': _B_in['w_down'], 'c': _w((lambda *s: torch.randn(*s, dtype=torch.bfloat16) * 0.1)(1, 1024) * 8.0), 'w_mod': _w((lambda *s: torch.randn(*s, dtype=torch.bfloat16) * 0.1)(1024, 6 * 1024) * 1024 ** (-0.5) * 8.0)}

def reference(A_x, A_k_cache, A_v_cache, A_wq, A_wo, B_x, B_w_gate, B_w_up, B_w_down, c, w_mod):

    def _A_ref(x, k_cache, v_cache, wq, wo):
        B, Sq, D = x.shape
        H, Dh = (32, 128)
        q = (lambda x, *d: x.permute(*d))((x @ wq).reshape(B, Sq, H, Dh), 0, 2, 1, 3)
        scores = torch.einsum('bhqd,bhkd->bhqk', q, k_cache) * Dh ** (-0.5)
        p = _softmax(scores, axis=-1)
        o = torch.einsum('bhqk,bhkd->bhqd', p, v_cache)
        return (lambda x, *d: x.permute(*d))(o, 0, 2, 1, 3).reshape(B, Sq, H * Dh) @ wo

    def _B_ref(x, w_gate, w_up, w_down):
        return _gelu(x @ w_gate) * (x @ w_up) @ w_down
    x = A_x
    mod = (_silu(c) @ w_mod).reshape(c.shape[0], 6, 1, -1)
    sa, ca, ga, sb, cb, gb = (mod[:, 0].clone(), mod[:, 1].clone(), mod[:, 2].clone(), mod[:, 3].clone(), mod[:, 4].clone(), mod[:, 5].clone())
    h = _layernorm(x, axis=-1) * (1 + ca) + sa
    x = x + ga * _A_ref(h, A_k_cache, A_v_cache, A_wq, A_wo)
    h = _layernorm(x, axis=-1) * (1 + cb) + sb
    x = x + gb * _B_ref(h, B_w_gate, B_w_up, B_w_down)
    return x
```

```cpp
#include <hip/hip_runtime.h>
#include <hip/hip_bf16.h>
typedef unsigned short u16;
__device__ inline float bf2f(u16 b){ return __uint_as_float(((unsigned)b)<<16); }
__device__ inline u16 f2bf(float f){ unsigned u=__float_as_uint(f); u += 0x7FFFu + ((u>>16)&1u); return (u16)(u>>16); }

__global__ void k_mod(const u16* c, const u16* w, float* mod){
  int j = blockIdx.x*blockDim.x+threadIdx.x;
  float acc=0.f;
  for(int k=0;k<1024;k++){ float cv=bf2f(c[k]); float s=cv/(1.f+__expf(-cv)); s=bf2f(f2bf(s)); acc+= s*bf2f(w[(size_t)k*6144+j]); }
  mod[j]=bf2f(f2bf(acc));
}
template<typename T> __device__ inline float ld(const T* p);
template<> __device__ inline float ld<u16>(const u16* p){return bf2f(*p);}
template<> __device__ inline float ld<float>(const float* p){return *p;}
template<typename T>
__global__ void k_ln(const T* x, const float* shift, const float* scale, u16* h){
  int row=blockIdx.x; int t=threadIdx.x;
  __shared__ float red[256];
  float v[4]; float s=0;
  for(int i=0;i<4;i++){ v[i]=ld<T>(x+(size_t)row*1024+t+256*i); s+=v[i]; }
  red[t]=s; __syncthreads();
  for(int o=128;o>0;o>>=1){ if(t<o) red[t]+=red[t+o]; __syncthreads(); }
  float mean=red[0]/1024.f; __syncthreads();
  s=0; for(int i=0;i<4;i++){ float d=v[i]-mean; s+=d*d; }
  red[t]=s; __syncthreads();
  for(int o=128;o>0;o>>=1){ if(t<o) red[t]+=red[t+o]; __syncthreads(); }
  float rstd=rsqrtf(red[0]/1024.f+1e-5f);
  for(int i=0;i<4;i++){ int c=t+256*i; h[(size_t)row*1024+c]=f2bf((v[i]-mean)*rstd*(1.f+scale[c])+shift[c]); }
}
template<int MODE>
__global__ void k_gemm(const u16* A, const u16* B, int M,int N,int K, void* out, const void* res, const float* g){
  __shared__ float As[32][33], Bs[32][33];
  int tx=threadIdx.x, ty=threadIdx.y; int row=blockIdx.y*32+ty, col=blockIdx.x*32+tx;
  float acc=0;
  for(int k0=0;k0<K;k0+=32){
    As[ty][tx]=bf2f(A[(size_t)row*K+k0+tx]); Bs[ty][tx]=bf2f(B[(size_t)(k0+ty)*N+col]);
    __syncthreads();
    for(int k=0;k<32;k++) acc+=As[ty][k]*Bs[k][tx];
    __syncthreads();
  }
  size_t o=(size_t)row*N+col;
  if(MODE==0) ((u16*)out)[o]=f2bf(acc);
  else if(MODE==1) ((float*)out)[o]=bf2f(((const u16*)res)[o])+g[col]*acc;
  else ((u16*)out)[o]=f2bf(((const float*)res)[o]+g[col]*acc);
}
__global__ void k_attn(const u16* q, const u16* K, const u16* V, u16* o){
  int h=blockIdx.y, r=blockIdx.x, t=threadIdx.x;
  __shared__ float qs[128]; __shared__ float p[2048]; __shared__ float red[256];
  if(t<128) qs[t]=bf2f(q[(size_t)r*4096+h*128+t]);
  __syncthreads();
  float mx=-1e30f;
  for(int k=t;k<2048;k+=256){ const u16* kp=K+((size_t)h*2048+k)*128; float s=0; for(int d=0;d<128;d++) s+=qs[d]*bf2f(kp[d]); s*=0.08838834764f; p[k]=s; mx=fmaxf(mx,s);}
  red[t]=mx; __syncthreads();
  for(int of=128;of>0;of>>=1){ if(t<of) red[t]=fmaxf(red[t],red[t+of]); __syncthreads(); }
  mx=red[0]; __syncthreads();
  float sum=0; for(int k=t;k<2048;k+=256){ float e=__expf(p[k]-mx); p[k]=e; sum+=e; }
  red[t]=sum; __syncthreads();
  for(int of=128;of>0;of>>=1){ if(t<of) red[t]+=red[t+of]; __syncthreads(); }
  float l=red[0];
  if(t<128){ float acc=0; for(int k=0;k<2048;k++) acc+=p[k]*bf2f(V[((size_t)h*2048+k)*128+t]); o[(size_t)r*4096+h*128+t]=f2bf(acc/l); }
}
__global__ void k_geglu(const u16* g, const u16* u, u16* a, int n){
  int i=blockIdx.x*blockDim.x+threadIdx.x; if(i>=n) return;
  float x=bf2f(g[i]); float t=0.5f*x*(1.f+tanhf(0.7978845608f*(x+0.044715f*x*x*x))); a[i]=f2bf(t*bf2f(u[i]));
}
extern "C" void kernel_launch(void* const* d_in, const int* in_sizes, int n_in, void* d_out, int out_size, void* d_ws, size_t ws_size, hipStream_t stream) {
  const u16* x=(const u16*)d_in[0]; const u16* kc=(const u16*)d_in[1]; const u16* vc=(const u16*)d_in[2];
  const u16* wq=(const u16*)d_in[3]; const u16* wo=(const u16*)d_in[4];
  const u16* wg=(const u16*)d_in[6]; const u16* wu=(const u16*)d_in[7]; const u16* wd=(const u16*)d_in[8];
  const u16* c=(const u16*)d_in[9]; const u16* wm=(const u16*)d_in[10];
  char* ws=(char*)d_ws;
  float* mod=(float*)ws; ws+=32768;
  u16* h=(u16*)ws; ws+=2048*1024*2;
  u16* q=(u16*)ws; ws+=2048*4096*2;
  u16* o=(u16*)ws; ws+=2048*4096*2;
  float* x1=(float*)ws; ws+=2048*1024*4;
  u16* g=(u16*)ws; ws+=2048*3584*2;
  u16* u=(u16*)ws; ws+=2048*3584*2;
  u16* a=(u16*)ws; ws+=2048*3584*2;
  k_mod<<<24,256,0,stream>>>(c,wm,mod);
  k_ln<u16><<<2048,256,0,stream>>>(x,mod,mod+1024,h);
  k_gemm<0><<<dim3(4096/32,2048/32),dim3(32,32),0,stream>>>(h,wq,2048,4096,1024,q,nullptr,nullptr);
  k_attn<<<dim3(2048,32),256,0,stream>>>(q,kc,vc,o);
  k_gemm<1><<<dim3(1024/32,2048/32),dim3(32,32),0,stream>>>(o,wo,2048,1024,4096,x1,x,mod+2048);
  k_ln<float><<<2048,256,0,stream>>>(x1,mod+3072,mod+4096,h);
  k_gemm<0><<<dim3(3584/32,2048/32),dim3(32,32),0,stream>>>(h,wg,2048,3584,1024,g,nullptr,nullptr);
  k_gemm<0><<<dim3(3584/32,2048/32),dim3(32,32),0,stream>>>(h,wu,2048,3584,1024,u,nullptr,nullptr);
  k_geglu<<<(2048*3584+255)/256,256,0,stream>>>(g,u,a,2048*3584);
  k_gemm<2><<<dim3(1024/32,2048/32),dim3(32,32),0,stream>>>(a,wd,2048,1024,3584,d_out,x1,mod+5120);
}
```

```cpp
#define TLBW 65536u
#define FSH 4
#define PSL 1
#include <hip/hip_runtime.h>
#include <hip/hip_bf16.h>
typedef unsigned short u16;
typedef unsigned u32x4 __attribute__((ext_vector_type(4)));
typedef float f32x4 __attribute__((ext_vector_type(4)));
typedef float f32x2 __attribute__((ext_vector_type(2)));
typedef __bf16 bf16x2_t __attribute__((ext_vector_type(2)));
__device__ __forceinline__ float bf2f(u16 b){ return __uint_as_float(((unsigned)b)<<16); }
__device__ __forceinline__ float bflo(unsigned w){ return __uint_as_float(w<<16); }
__device__ __forceinline__ float bfhi(unsigned w){ return __uint_as_float(w&0xffff0000u); }
__device__ __forceinline__ unsigned pk2(float lo, float hi){ f32x2 v={lo,hi}; bf16x2_t b=__builtin_convertvector(v,bf16x2_t); return __builtin_bit_cast(unsigned,b); }
__device__ __forceinline__ u16 f2bf(float f){ return (u16)(pk2(f,0.f)&0xffff); }
__device__ __forceinline__ size_t tix(int row, int k, int K){ return ((size_t)((row>>5)*(K>>3) + (k>>3))*32 + (row&31))*8 + (k&7); }

typedef __attribute__((address_space(1))) unsigned gu32;
#define FLG(base, i) ((base) + ((size_t)(i) << FSH))
__device__ __forceinline__ int lane_now(){ int l; asm volatile("v_mbcnt_lo_u32_b32 %0, -1, 0\n\tv_mbcnt_hi_u32_b32 %0, -1, %0" : "=v"(l)); return l; }
__device__ __forceinline__ void panel_publish(unsigned* flag, const int t, const unsigned tag){
  asm volatile("s_waitcnt vmcnt(0)" ::: "memory");
  __syncthreads();
  if(t==0) __hip_atomic_store((gu32*)flag, tag, __ATOMIC_RELAXED, __HIP_MEMORY_SCOPE_AGENT);
}
__device__ __forceinline__ float xor8(float v){ return __builtin_bit_cast(float, __builtin_amdgcn_update_dpp(0, __builtin_bit_cast(int,v), 0x128, 0xf, 0xf, true)); }
__device__ __forceinline__ float xor16(float v, int t){ auto r=__builtin_amdgcn_permlane16_swap(__float_as_uint(v), __float_as_uint(v), false, false); return __uint_as_float(((t>>4)&1)? r[0] : r[1]); }
__device__ __forceinline__ float xor32(float v, int t){ auto r=__builtin_amdgcn_permlane32_swap(__float_as_uint(v), __float_as_uint(v), false, false); return __uint_as_float(((t>>5)&1)? r[0] : r[1]); }
__device__ __forceinline__ void row_stats(const float (&f)[4][8], float (*red)[8][2], float& mean, float& rstd, const int t){
  const int w=t>>6, l=t&63, r=l&7;
  float s=0;
#pragma unroll
  for(int i=0;i<4;i++)
#pragma unroll
    for(int j=0;j<8;j++) s+=f[i][j];
  float m=s*(1.f/32.f), q=0;
#pragma unroll
  for(int i=0;i<4;i++)
#pragma unroll
    for(int j=0;j<8;j++){ const float d=f[i][j]-m; q+=d*d; }
  { float m2=xor8(m), q2=xor8(q), d=m-m2;   q=q+q2+16.f*d*d; m=0.5f*(m+m2); }
  { float m2=xor16(m,t), q2=xor16(q,t), d=m-m2; q=q+q2+32.f*d*d; m=0.5f*(m+m2); }
  { float m2=xor32(m,t), q2=xor32(q,t), d=m-m2; q=q+q2+64.f*d*d; m=0.5f*(m+m2); }
  if(l<8){ red[w][l][0]=m; red[w][l][1]=q; }
  __syncthreads();
  const float a=red[0][r][0], b=red[1][r][0], c=red[2][r][0], d=red[3][r][0];
  mean=0.25f*((a+b)+(c+d));
  const float da=a-mean, db=b-mean, dc=c-mean, dd=d-mean;
  const float M2=(red[0][r][1]+red[1][r][1])+(red[2][r][1]+red[3][r][1])+256.f*((da*da+db*db)+(dc*dc+dd*dd));
  rstd=rsqrtf(M2*(1.f/1024.f)+1e-5f);
}
__device__ __forceinline__ void row_stats1(const float (&f)[4][8], float (*red)[8][2], float& mean, float& rstd, const int t){
  const int w=t>>6, l=t&63, r=l&7;
  f32x2 s2={0.f,0.f}, q2={0.f,0.f};
#pragma unroll
  for(int i=0;i<4;i++)
#pragma unroll
    for(int j=0;j<4;j++){ const f32x2 v={f[i][2*j],f[i][2*j+1]}; s2+=v; q2+=v*v; }
  float s=s2[0]+s2[1], q=q2[0]+q2[1];
  s+=xor8(s); q+=xor8(q);
  s+=xor16(s,t); q+=xor16(q,t);
  s+=xor32(s,t); q+=xor32(q,t);
  if(l<8) *(f32x2*)&red[w][l][0]=f32x2{s,q};
  __syncthreads();
  const f32x2 a=*(const f32x2*)&red[0][r][0], b=*(const f32x2*)&red[1][r][0], c=*(const f32x2*)&red[2][r][0], d=*(const f32x2*)&red[3][r][0];
  const f32x2 z=(a+b)+(c+d);
  mean=z[0]*(1.f/1024.f);
  const float var=fmaxf(z[1]*(1.f/1024.f)-mean*mean, 0.f);
  rstd=rsqrtf(var+1e-5f);
}
template<bool NT> __device__ __forceinline__ void touch_line(const void* base, unsigned off){
  if(NT) asm volatile("s_mov_b32 m0, 0x27000\n\ts_nop 0\n\tglobal_load_lds_dword %0, %1 nt" :: "v"(off), "s"(base) : "memory");
  else   asm volatile("s_mov_b32 m0, 0x27000\n\ts_nop 0\n\tglobal_load_lds_dword %0, %1" :: "v"(off), "s"(base) : "memory");
}
__device__ __forceinline__ void touch_line_sc1(const void* base, unsigned off){
  asm volatile("s_mov_b32 m0, 0x27000\n\ts_nop 0\n\tglobal_load_lds_dword %0, %1 sc1" :: "v"(off), "s"(base) : "memory");
}
__device__ __forceinline__ float silu_f(float x){ return x*__builtin_amdgcn_rcpf(1.f+__expf(-x)); }
template<int NR> __device__ __forceinline__ void mod_load(u32x4 (&wv)[NR], u32x4 (&cv)[NR/8], const u16* __restrict__ c, const u16* __restrict__ w, int row0, int col0, const int ch, const int r){
#pragma unroll
  for(int i=0;i<NR/8;i++) cv[i]=*(const u32x4*)(c+row0+r*NR+i*8);
#pragma unroll
  for(int i=0;i<NR;i++) wv[i]=__builtin_nontemporal_load((const u32x4*)(w+(size_t)(row0+r*NR+i)*6144+col0+ch*8));
}
template<int NR> __device__ __forceinline__ void mod_fma(const u32x4 (&wv)[NR], const u32x4 (&cv)[NR/8], float* __restrict__ redrow, const int ch){
  float a[8];
#pragma unroll
  for(int i=0;i<8;i++) a[i]=0.f;
#pragma unroll
  for(int i=0;i<NR;i++){
    const unsigned cw=cv[i>>3][(i>>1)&3];
    const float s=silu_f((i&1)? bfhi(cw) : bflo(cw));
#pragma unroll
    for(int j=0;j<4;j++){ a[2*j]+=s*bflo(wv[i][j]); a[2*j+1]+=s*bfhi(wv[i][j]); }
  }
  *(f32x4*)(redrow+ch*8)=f32x4{a[0],a[1],a[2],a[3]}; *(f32x4*)(redrow+ch*8+4)=f32x4{a[4],a[5],a[6],a[7]};
}
template<int NROW> __device__ __forceinline__ void mod_store(float* __restrict__ dst, float (*red)[68], const int t){
  if(t<64){ float s=0;
#pragma unroll
    for(int i=0;i<NROW;i++) s+=red[i][t];
    __hip_atomic_store((__attribute__((address_space(1))) float*)(dst+t), s, __ATOMIC_RELAXED, __HIP_MEMORY_SCOPE_AGENT); }
}
__device__ __forceinline__ void one_prologue(const u16* __restrict__ x, const u16* __restrict__ c, const u16* __restrict__ wm, float* __restrict__ mpart, unsigned* __restrict__ fl,
                                             u16* __restrict__ h, const void* wq, const void* kc, const void* vc, const void* code, unsigned codebytes, const void* tlb2, const void* tlb3, char* smem, const int t, const int b, const unsigned wid, const unsigned tag){
  float (*redm)[68]=(float (*)[68])(smem+16384);
  float* ms=(float*)smem; float (*red)[8][2]=(float (*)[8][2])(smem+8192);
  const int w=t>>6, l=t&63, r=l&7, cg=l>>3;
  const int row=b*8+r;
  u32x4 xv[4];
  const bool crit=b<128;
  const bool latew = !crit && (wid==1 || wid==2);
  u32x4 wb[32], cb[4];
  if(crit){
    u32x4 wa[8], ca[1];
    mod_load<8>(wa, ca, c, wm, (b>>5)*256, (b&31)*64, t&7, t>>3);
#pragma unroll
    for(int i=0;i<4;i++) xv[i]=*(const u32x4*)(x+(size_t)row*1024+(w*32+i*8+cg)*8);
    mod_fma<8>(wa, ca, redm[t>>3], t&7);
    __syncthreads();
    mod_store<32>(mpart+(b>>5)*2048+(b&31)*64, redm, t);
    if(t<64){ asm volatile("s_waitcnt vmcnt(0)" ::: "memory"); if(t==0) __hip_atomic_store((gu32*)FLG(fl, 1024+b), tag, __ATOMIC_RELAXED, __HIP_MEMORY_SCOPE_AGENT); }
  } else {
#pragma unroll
    for(int i=0;i<4;i++) xv[i]=*(const u32x4*)(x+(size_t)row*1024+(w*32+i*8+cg)*8);
    if(latew) mod_load<32>(wb, cb, c, wm, ((b-128)>>6)*512+(wid-1)*256, 2048+((b-128)&63)*64, l&7, l>>3);
  }
  float f[4][8];
#pragma unroll
  for(int i=0;i<4;i++)
#pragma unroll
    for(int j=0;j<4;j++){ f[i][2*j]=bflo(xv[i][j]); f[i][2*j+1]=bfhi(xv[i][j]); }
  float mean, rstd; row_stats(f, red, mean, rstd, t);
  if(wid==3){
    const unsigned xg=b&7, j=b>>3;
#pragma unroll
    for(int i=0;i<4;i++){ const unsigned n=j*256+i*64+l; touch_line<false>(wq, (n>>3)*8192u+xg*1024u+(n&7)*128u); }
    { const unsigned line=j*32+l; if(l<32 && line*128u<codebytes) touch_line<false>(code, line*128u); }
#pragma unroll
    for(int i=0;i<2;i++){ const unsigned u=j*128+i*64, n=(u&1023)+l; touch_line<false>(((u&1023)<512)? kc : vc, (xg*4+(u>>10))*524288u+(n&511)*128u); }
    { const unsigned n=j*64+l;
      if(n*TLBW < (4u<<20)) touch_line_sc1(tlb3, n*TLBW); }
  }
  if(latew) mod_fma<32>(wb, cb, redm[(wid-1)*8+(l>>3)], l&7);
  if(wid==0){
    const int fb=1024;
    unsigned spins=0;
    while(true){
      const unsigned v0=__hip_atomic_load((gu32*)FLG(fl, fb+l), __ATOMIC_RELAXED, __HIP_MEMORY_SCOPE_AGENT);
      const unsigned v1=__hip_atomic_load((gu32*)FLG(fl, fb+64+l), __ATOMIC_RELAXED, __HIP_MEMORY_SCOPE_AGENT);
      if(__all(v0==tag && v1==tag) || ++spins>(1u<<22)) break;
      __builtin_amdgcn_s_sleep(PSL);
    }
    const __amdgpu_buffer_rsrc_t rm=__builtin_amdgcn_make_buffer_rsrc((void*)mpart,(short)0,65536,0x00020000);
    f32x4 p[8][4];
#pragma unroll
    for(int g=0;g<8;g++)
#pragma unroll
      for(int k=0;k<4;k++) p[g][k]=__builtin_bit_cast(f32x4,__builtin_amdgcn_raw_buffer_load_b128(rm,(k*2048+g*256+l*4)*4,0,16));
#pragma unroll
    for(int g=0;g<8;g++) *(f32x4*)(ms+g*256+l*4)=(p[g][0]+p[g][1])+(p[g][2]+p[g][3]);
  }
  __syncthreads();
  const float mul=0.12751743082f;
  const __amdgpu_buffer_rsrc_t rs=__builtin_amdgcn_make_buffer_rsrc((void*)h,(short)0,2048*1024*2,0x00020000);
#pragma unroll
  for(int i=0;i<4;i++){
    const int k0=(w*32+i*8+cg)*8;
    const f32x4 sh0=*(const f32x4*)(ms+k0), sh1=*(const f32x4*)(ms+k0+4), sc0=*(const f32x4*)(ms+1024+k0), sc1=*(const f32x4*)(ms+1024+k0+4);
    float o[8];
#pragma unroll
    for(int j=0;j<4;j++){ o[j]=((f[i][j]-mean)*rstd*(1.f+sc0[j])+sh0[j])*mul; o[4+j]=((f[i][4+j]-mean)*rstd*(1.f+sc1[j])+sh1[j])*mul; }
    u32x4 ov={pk2(o[0],o[1]),pk2(o[2],o[3]),pk2(o[4],o[5]),pk2(o[6],o[7])};
    __builtin_amdgcn_raw_buffer_store_b128(ov, rs, (int)(tix(row,k0,1024)*2), 0, 16);
  }
  if(!crit) mod_store<16>(mpart+8192+((b-128)>>6)*4096+((b-128)&63)*64, redm, t);
  panel_publish(FLG(fl, 2048+b), t, tag);
}
__device__ __forceinline__ void wait_flags(const unsigned* p0, int i0, int n, const int t, const unsigned tag){
  if(t<64){
    const unsigned* q=FLG(p0, i0+(t<n?t:0));
    unsigned spins=0;
    while(true){
      const unsigned v=__hip_atomic_load((gu32*)q, __ATOMIC_RELAXED, __HIP_MEMORY_SCOPE_AGENT);
      if(__all(v==tag) || ++spins>(1u<<22)) break;
      __builtin_amdgcn_s_sleep(PSL);
    }
  }
  __syncthreads();
}
#define NPART 2
struct Ln2Regs { u32x4 v[4], p[NPART][4]; f32x4 prm[2][3]; };
__device__ __forceinline__ void ln2_load_a(Ln2Regs& R, const u16* __restrict__ x, const float* __restrict__ mpart, const int t, const int b){
  const int w=t>>6, l=t&63, r=l&7, cg=l>>3;
  const int row=b*8+r;
#pragma unroll
  for(int i=0;i<4;i++) R.v[i]=*(const u32x4*)(x+(size_t)row*1024+(w*32+i*8+cg)*8);
#pragma unroll
  for(int q=0;q<2;q++)
#pragma unroll
    for(int k=0;k<3;k++){
      const __amdgpu_buffer_rsrc_t rm=__builtin_amdgcn_make_buffer_rsrc((void*)mpart,(short)0,65536,0x00020000);
      R.prm[q][k]=__builtin_bit_cast(f32x4,__builtin_amdgcn_raw_buffer_load_b128(rm,(8192+q*4096+k*1024+t*4)*4,0,16));
    }
}
__device__ __forceinline__ void xch_clear(void* __restrict__ xch, const int t, const int b){
  const int x=b&7, j=b>>3, tile=(4*(x&3)+(j&3))*8+(j>>2);
  const __amdgpu_buffer_rsrc_t rc=__builtin_amdgcn_make_buffer_rsrc(xch,(short)0,8<<20,0x00020000);
  const u32x4 ones={~0u,~0u,~0u,~0u};
#pragma unroll
  for(int i=0;i<4;i++) __builtin_amdgcn_raw_buffer_store_b128(ones, rc, (tile*2+(x>>2))*16384+i*4096+t*16, 0, 16);
}
__device__ __forceinline__ void ln2_stage(Ln2Regs& R, char* smem, const int t){
  float* prm=(float*)(smem+256);
#pragma unroll
  for(int k=0;k<3;k++) *(f32x4*)(prm+k*1024+t*4)=R.prm[0][k]+R.prm[1][k];
}
template<bool MEGA> __device__ __forceinline__ void ln2_load_b(Ln2Regs& R, const u16* __restrict__ parts, const int t, const int b){
  const int w=t>>6, l=t&63, r=l&7, cg=l>>3;
  const int row=b*8+r;
  const __amdgpu_buffer_rsrc_t rp=__builtin_amdgcn_make_buffer_rsrc((void*)parts,(short)0,NPART*2048*1024*2,0x00020000);
#pragma unroll
  for(int i=0;i<4;i++){
    const int k0=(w*32+i*8+cg)*8;
#pragma unroll
    for(int q=0;q<NPART;q++) R.p[q][i]=__builtin_bit_cast(u32x4,__builtin_amdgcn_raw_buffer_load_b128(rp,(int)((q*2048*1024+tix(row,k0,1024))*2),0,MEGA?16:0));
  }
}
template<bool MEGA> __device__ __forceinline__ void ln2_phase(Ln2Regs& R, u16* __restrict__ x1, u16* __restrict__ h, unsigned* __restrict__ cnt0, const int cnti, char* smem, const int t, const int b, const unsigned tag){
  float (*red)[8][2]=(float (*)[8][2])smem;
  float* prm=(float*)(smem+256);
  const int w=t>>6, l=t&63, r=l&7, cg=l>>3;
  const int row=b*8+r;
  float f[4][8];
  if(!MEGA){ ln2_stage(R, smem, t); __syncthreads(); }
  const __amdgpu_buffer_rsrc_t rx=__builtin_amdgcn_make_buffer_rsrc((void*)x1,(short)0,2048*1024*2,0x00020000);
  __builtin_amdgcn_sched_barrier(0);
  f32x4 ga[4][2], sh[4][2], sc[4][2];
#pragma unroll
  for(int i=0;i<4;i++){
    const int k0=(w*32+i*8+cg)*8;
#pragma unroll
    for(int u=0;u<2;u++){ ga[i][u]=*(const f32x4*)(prm+k0+4*u); sh[i][u]=*(const f32x4*)(prm+1024+k0+4*u); sc[i][u]=*(const f32x4*)(prm+2048+k0+4*u)+1.f; }
  }
#pragma unroll
  for(int i=0;i<4;i++){
    const int k0=(w*32+i*8+cg)*8;
    u32x4 xo;
#pragma unroll
    for(int j=0;j<4;j++){
      float lo=bflo(R.p[0][i][j])+bflo(R.p[1][i][j]);
      float hi=bfhi(R.p[0][i][j])+bfhi(R.p[1][i][j]);
      const float gl=ga[i][j>>1][(2*j)&3], gh=ga[i][j>>1][(2*j+1)&3];
      xo[j]=pk2(bflo(R.v[i][j])+gl*lo, bfhi(R.v[i][j])+gh*hi);
      f[i][2*j]=bflo(xo[j]); f[i][2*j+1]=bfhi(xo[j]);
    }
    __builtin_amdgcn_raw_buffer_store_b128(xo, rx, (int)(((size_t)row*1024+k0)*2), 0, MEGA?16:0);
  }
  float mean, rstd; row_stats1(f, red, mean, rstd, t);
  const __amdgpu_buffer_rsrc_t rs=__builtin_amdgcn_make_buffer_rsrc((void*)h,(short)0,2048*1024*2,0x00020000);
#pragma unroll
  for(int i=0;i<4;i++){
    const int k0=(w*32+i*8+cg)*8;
    float o[8];
#pragma unroll
    for(int j=0;j<4;j++){ o[j]=(f[i][j]-mean)*rstd*sc[i][0][j]+sh[i][0][j]; o[4+j]=(f[i][4+j]-mean)*rstd*sc[i][1][j]+sh[i][1][j]; }
    u32x4 ov={pk2(o[0],o[1]),pk2(o[2],o[3]),pk2(o[4],o[5]),pk2(o[6],o[7])};
    __builtin_amdgcn_raw_buffer_store_b128(ov, rs, (int)(tix(row,k0,1024)*2), 0, 16);
  }
  panel_publish(FLG(cnt0, cnti+b), t, tag);
}

__device__ __forceinline__ void attn_slow(const u16* __restrict__ H, const u16* __restrict__ WQ, const u16* __restrict__ K, const u16* __restrict__ V, u16* __restrict__ O, int head, int row){
  __builtin_amdgcn_fence(__ATOMIC_ACQUIRE, "agent");
  asm volatile("s_waitcnt vmcnt(0)" ::: "memory");
  float q[128];
#pragma unroll
  for(int d=0;d<128;d++) q[d]=0.f;
#pragma unroll 1
  for(int k=0;k<1024;k++){
    const float h=bf2f(H[tix(row,k,1024)]);
    const unsigned* wr=(const unsigned*)(WQ+(size_t)k*4096+head*128);
#pragma unroll
    for(int d=0;d<64;d++){ const unsigned w=wr[d]; q[2*d]+=h*bflo(w); q[2*d+1]+=h*bfhi(w); }
  }
#pragma unroll
  for(int d=0;d<128;d++) q[d]=bf2f(f2bf(q[d]));
  const u16* Kh=K+(size_t)head*2048*128; const u16* Vh=V+(size_t)head*2048*128;
#pragma unroll 1
  for(int dc=0;dc<4;dc++){
    float acc[32], m=-INFINITY, l=0.f;
#pragma unroll
    for(int j=0;j<32;j++) acc[j]=0.f;
#pragma unroll 1
    for(int key=0;key<2048;key++){
      const unsigned* kr=(const unsigned*)(Kh+(size_t)key*128);
      float s=0.f;
#pragma unroll
      for(int d=0;d<64;d++){ const unsigned w=kr[d]; s+=q[2*d]*bflo(w); s+=q[2*d+1]*bfhi(w); }
      const float mn=fmaxf(m,s);
      const float f=(m==mn)?1.f:exp2f(m-mn), p=(s==mn)?1.f:exp2f(s-mn);
      const unsigned* vr=(const unsigned*)(Vh+(size_t)key*128+dc*32);
      l=l*f+p; m=mn;
#pragma unroll
      for(int j=0;j<16;j++){ const unsigned w=vr[j]; acc[2*j]=acc[2*j]*f+p*bflo(w); acc[2*j+1]=acc[2*j+1]*f+p*bfhi(w); }
    }
    const float il=1.f/l;
    const __amdgpu_buffer_rsrc_t ro=__builtin_amdgcn_make_buffer_rsrc((void*)O,(short)0,2048*4096*2,0x00020000);
#pragma unroll
    for(int c=0;c<4;c++){
      u32x4 ov={pk2(acc[8*c]*il,acc[8*c+1]*il),pk2(acc[8*c+2]*il,acc[8*c+3]*il),pk2(acc[8*c+4]*il,acc[8*c+5]*il),pk2(acc[8*c+6]*il,acc[8*c+7]*il)};
      __builtin_amdgcn_raw_buffer_store_b128(ov, ro, (int)(tix(row,head*128+dc*32+c*8,4096)*2), 0, 16);
    }
  }
}
extern "C" __global__ __launch_bounds__(256, 1) void k_mega(void* H_, void* WQ_, void* K_, void* V_, void* O_, void* WO_, void* WG_, void* WU_, void* WD_, void* FL_, void* X_, void* MPART_, void* PART_, void* H2_, void* ACT_, void* X1_, void* XCH_, void* OUT_, void* C_, void* WM_) {
  __shared__ __attribute__((aligned(1024))) char smem[163840];
  unsigned ldsb = (unsigned)(size_t)smem;
  unsigned wid = __builtin_amdgcn_readfirstlane(threadIdx.x >> 6);
  unsigned bid = blockIdx.x;
  const void* karg = (const void*)__builtin_amdgcn_kernarg_segment_ptr();
  typedef const __attribute__((address_space(4))) unsigned long long* kap;
  kap ka = (kap)__builtin_amdgcn_kernarg_segment_ptr();
  unsigned long long pc; unsigned codebytes;
  asm volatile("s_getpc_b64 s[20:21]\n\ts_add_u32 s20, s20, k_mega@rel32@lo+4\n\ts_addc_u32 s21, s21, k_mega@rel32@hi+12\n\ts_mov_b64 %0, s[20:21]\n\ts_mov_b32 %1, MEGA_END-k_mega" : "=s"(pc), "=s"(codebytes) :: "s20", "s21", "scc");
  touch_line<false>((const void*)pc, threadIdx.x*128u);
  const unsigned tag = *(const __attribute__((address_space(4))) unsigned*)(ka[9] + (12288u << FSH)) + 1u;
  one_prologue(((const u16*)(__attribute__((address_space(1))) const u16*)ka[10]), ((const u16*)(__attribute__((address_space(1))) const u16*)ka[18]), ((const u16*)(__attribute__((address_space(1))) const u16*)ka[19]), ((float*)(__attribute__((address_space(1))) float*)ka[11]), ((unsigned*)(__attribute__((address_space(1))) unsigned*)ka[9]), ((u16*)(__attribute__((address_space(1))) u16*)ka[0]),
               (const void*)ka[1], (const void*)ka[2], (const void*)ka[3], (const void*)pc, codebytes, (const void*)ka[12], (const void*)ka[17], smem, threadIdx.x, bid, wid, tag);
  unsigned flag;
  asm volatile(
    "s_load_dwordx2 s[36:37], %1, 0\n\t"
    "s_load_dwordx2 s[38:39], %1, 8\n\t"
    "s_load_dwordx2 s[40:41], %1, 16\n\t"
    "s_load_dwordx2 s[42:43], %1, 24\n\t"
    "s_load_dwordx2 s[44:45], %1, 32\n\t"
    "s_load_dwordx2 s[46:47], %1, 40\n\t"
    "s_load_dwordx2 s[48:49], %1, 48\n\t"
    "s_load_dwordx2 s[50:51], %1, 56\n\t"
    "s_load_dwordx2 s[52:53], %1, 64\n\t"
    "s_load_dwordx2 s[54:55], %1, 72\n\t"
    "s_mov_b32 s34, %2\n\t"
    "s_mov_b32 s35, %3\n\t"
    "s_mov_b32 s33, %4\n\t"
    "s_waitcnt lgkmcnt(0)\n\t"
    "v_mbcnt_lo_u32_b32 v251, -1, 0\n\t"
    "v_mbcnt_hi_u32_b32 v251, -1, v251\n\t"
    "s_and_b32 s88, s35, 7\n\t"
    "s_lshr_b32 s89, s35, 3\n\t"
    "s_lshl_b32 s88, s88, 2\n\t"
    "s_and_b32 s98, s89, 3\n\t"
    "s_add_u32 s79, s88, s98\n\t"
    "s_lshr_b32 s80, s89, 2\n\t"
    "s_lshl_b32 s88, s80, 3\n\t"
    "s_lshl_b32 s89, s34, 1\n\t"
    "s_add_u32 s91, s88, s89\n\t"
    "s_sub_u32 s60, s38, 4096\n\t"
    "s_subb_u32 s61, s39, 0\n\t"
    "s_and_b32 s61, s61, 0xffff\n\t"
    "s_mov_b32 s62, 8392704\n\t"
    "s_mov_b32 s63, 0x20000\n\t"
    "s_lshl_b32 s73, s79, 8\n\t"
    "s_mul_i32 s88, s34, 131072\n\t"
    "s_add_u32 s73, s73, s88\n\t"
    "s_lshl_b32 s88, s34, 12\n\t"
    "s_add_u32 s76, s33, s88\n\t"
    "s_add_u32 s77, s76, 0\n\t"
    "s_add_u32 s78, s76, 65536\n\t"
    "s_add_u32 s76, s76, 98304\n\t"
    "v_mov_b32 v243, 4096\n\t"
    "v_bfe_u32 v253, v251, 0, 2\n\t"
    "v_lshl_add_u32 v243, v253, 4, v243\n\t"
    "v_bfe_u32 v253, v251, 2, 2\n\t"
    "v_lshl_add_u32 v243, v253, 13, v243\n\t"
    "v_bfe_u32 v253, v251, 4, 1\n\t"
    "v_lshl_add_u32 v243, v253, 16, v243\n\t"
    "v_bfe_u32 v253, v251, 5, 1\n\t"
    "v_lshl_add_u32 v243, v253, 6, v243\n\t"
    "v_add_u32 v244, 4294966400, v243\n\t"
    "v_add_u32 v245, 30720, v243\n\t"
    "v_add_u32 v246, 29824, v243\n\t"
    "s_mov_b32 s56, s36\n\t"
    "s_and_b32 s57, s37, 0xffff\n\t"
    "s_mov_b32 s58, 4194304\n\t"
    "s_mov_b32 s59, 0x20000\n\t"
    "s_lshl_b32 s72, s91, 16\n\t"
    "v_lshlrev_b32 v241, 4, v251\n\t"
    "v_add_u32 v242, 65536, v241\n\t"
    "s_add_u32 m0, s76, 0\n\t"
    "s_nop 0\n\t"
    "buffer_load_dwordx4 v243, s[60:63], s73 offen offset:0 nt lds\n\t"
    "buffer_load_dwordx4 v244, s[60:63], s73 offen offset:1024 nt lds\n\t"
    "buffer_load_dwordx4 v245, s[60:63], s73 offen offset:2048 nt lds\n\t"
    "buffer_load_dwordx4 v246, s[60:63], s73 offen offset:3072 nt lds\n\t"
    "s_add_u32 s73, s73, 524288\n\t"
    "s_add_u32 m0, s76, 16384\n\t"
    "s_nop 0\n\t"
    "buffer_load_dwordx4 v243, s[60:63], s73 offen offset:0 nt lds\n\t"
    "buffer_load_dwordx4 v244, s[60:63], s73 offen offset:1024 nt lds\n\t"
    "buffer_load_dwordx4 v245, s[60:63], s73 offen offset:2048 nt lds\n\t"
    "buffer_load_dwordx4 v246, s[60:63], s73 offen offset:3072 nt lds\n\t"
    "s_add_u32 s73, s73, 524288\n\t"
    "s_add_u32 m0, s76, 32768\n\t"
    "s_nop 0\n\t"
    "buffer_load_dwordx4 v243, s[60:63], s73 offen offset:0 nt lds\n\t"
    "buffer_load_dwordx4 v244, s[60:63], s73 offen offset:1024 nt lds\n\t"
    "buffer_load_dwordx4 v245, s[60:63], s73 offen offset:2048 nt lds\n\t"
    "buffer_load_dwordx4 v246, s[60:63], s73 offen offset:3072 nt lds\n\t"
    "s_add_u32 s73, s73, 524288\n\t"
    "s_lshl_b32 s86, s80, 5\n\t"
    "s_cmp_lg_u32 s34, 0\n\t"
    "s_cbranch_scc1 LPW_m_attn\n\t"
    "v_mbcnt_lo_u32_b32 v249, -1, 0\n\t"
    "v_mbcnt_hi_u32_b32 v249, -1, v249\n\t"
    "s_lshl_b32 s88, s86, 2\n\t"
    "s_add_u32 s88, s88, 8192\n\t"
    "v_mov_b32 v248, v249\n\t"
    "v_min_u32 v248, 31, v248\n\t"
    "v_lshlrev_b32 v248, 2, v248\n\t"
    "v_add_u32 v250, s88, v248\n\t"
    "s_mov_b64 exec, -1\n\t"
    "v_lshlrev_b32 v250, 4, v250\n\t"
    "s_mov_b32 s89, 0x400000\n\t"
    "LPP_m_attn:\n\t"
    "global_load_dword v249, v250, s[54:55] sc1\n\t"
    "s_waitcnt vmcnt(0)\n\t"
    "v_cmp_ne_u32 vcc, %5, v249\n\t"
    "s_sub_u32 s89, s89, 1\n\t"
    "s_cmp_eq_u32 s89, 0\n\t"
    "s_cbranch_scc1 LPD_m_attn\n\t"
    "s_cmp_eq_u64 vcc, 0\n\t"
    "s_cbranch_scc1 LPD_m_attn\n\t"
    "s_sleep 1\n\t"
    "s_branch LPP_m_attn\n\t"
    "LPD_m_attn:\n\t"
    "LPW_m_attn:\n\t"
    "s_barrier\n\t"
    "buffer_load_dwordx4 a[0:3], v241, s[56:59], s72 offen offset:0 sc1\n\t"
    "buffer_load_dwordx4 a[4:7], v242, s[56:59], s72 offen offset:0 sc1\n\t"
    "buffer_load_dwordx4 a[8:11], v241, s[56:59], s72 offen offset:1024 sc1\n\t"
    "buffer_load_dwordx4 a[12:15], v242, s[56:59], s72 offen offset:1024 sc1\n\t"
    "buffer_load_dwordx4 a[16:19], v241, s[56:59], s72 offen offset:2048 sc1\n\t"
    "buffer_load_dwordx4 a[20:23], v242, s[56:59], s72 offen offset:2048 sc1\n\t"
    "buffer_load_dwordx4 a[24:27], v241, s[56:59], s72 offen offset:3072 sc1\n\t"
    "buffer_load_dwordx4 a[28:31], v242, s[56:59], s72 offen offset:3072 sc1\n\t"
    "s_add_u32 s72, s72, 4096\n\t"
    "s_mov_b32 s64, s40\n\t"
    "s_and_b32 s65, s41, 0xffff\n\t"
    "s_mov_b32 s66, 16777216\n\t"
    "s_mov_b32 s67, 0x20000\n\t"
    "s_sub_u32 s68, s42, 1024\n\t"
    "s_subb_u32 s69, s43, 0\n\t"
    "s_and_b32 s69, s69, 0xffff\n\t"
    "s_mov_b32 s70, 16778240\n\t"
    "s_mov_b32 s71, 0x20000\n\t"
    "s_lshl_b32 s74, s79, 19\n\t"
    "s_lshl_b32 s88, s34, 12\n\t"
    "s_add_u32 s74, s74, s88\n\t"
    "s_mov_b32 s75, s74\n\t"
    "v_mov_b32 v247, 0\n\t"
    "v_bfe_u32 v253, v251, 0, 1\n\t"
    "v_lshl_add_u32 v247, v253, 4, v247\n\t"
    "v_bfe_u32 v253, v251, 1, 1\n\t"
    "v_lshl_add_u32 v247, v253, 3, v247\n\t"
    "v_bfe_u32 v253, v251, 2, 2\n\t"
    "v_lshl_add_u32 v247, v253, 6, v247\n\t"
    "v_bfe_u32 v253, v251, 4, 1\n\t"
    "v_lshl_add_u32 v247, v253, 5, v247\n\t"
    "v_bfe_u32 v253, v251, 5, 1\n\t"
    "v_lshl_add_u32 v247, v253, 8, v247\n\t"
    "v_add_u32 v247, s33, v247\n\t"
    "v_add_u32 v247, 98304, v247\n\t"
    "v_mov_b32 v232, 0\n\t"
    "v_bfe_u32 v253, v251, 0, 2\n\t"
    "v_lshl_add_u32 v232, v253, 3, v232\n\t"
    "v_bfe_u32 v253, v251, 2, 2\n\t"
    "v_lshl_add_u32 v232, v253, 6, v232\n\t"
    "v_bfe_u32 v253, v251, 4, 1\n\t"
    "v_lshl_add_u32 v232, v253, 5, v232\n\t"
    "v_bfe_u32 v253, v251, 5, 1\n\t"
    "v_lshl_add_u32 v232, v253, 8, v232\n\t"
    "v_add_u32 v232, s33, v232\n\t"
    "v_add_u32 v232, 65536, v232\n\t"
    "v_mov_b32 v224, 0\n\t"
    "v_bfe_u32 v253, v251, 0, 4\n\t"
    "s_mov_b32 s98, 272\n\t"
    "v_mad_i32_i24 v224, v253, s98, v224\n\t"
    "v_bfe_u32 v253, v251, 4, 1\n\t"
    "v_lshl_add_u32 v224, v253, 12, v224\n\t"
    "v_bfe_u32 v253, v251, 5, 1\n\t"
    "v_lshl_add_u32 v224, v253, 4, v224\n\t"
    "v_bfe_u32 v253, v251, 0, 1\n\t"
    "v_bfe_u32 v254, v251, 5, 1\n\t"
    "v_and_b32 v253, v253, v254\n\t"
    "s_mov_b32 s98, 4294967264\n\t"
    "v_mad_i32_i24 v224, v253, s98, v224\n\t"
    "v_xor_b32 v225, 32, v224\n\t"
    "v_xor_b32 v226, 64, v224\n\t"
    "v_xor_b32 v227, 96, v224\n\t"
    "v_xor_b32 v228, 128, v224\n\t"
    "v_xor_b32 v229, 160, v224\n\t"
    "v_xor_b32 v230, 192, v224\n\t"
    "v_xor_b32 v231, 224, v224\n\t"
    "v_add_u32 v224, s33, v224\n\t"
    "v_add_u32 v225, s33, v225\n\t"
    "v_add_u32 v226, s33, v226\n\t"
    "v_add_u32 v227, s33, v227\n\t"
    "v_add_u32 v228, s33, v228\n\t"
    "v_add_u32 v229, s33, v229\n\t"
    "v_add_u32 v230, s33, v230\n\t"
    "v_add_u32 v231, s33, v231\n\t"
    "v_mov_b32 v233, 0\n\t"
    "v_bfe_u32 v253, v251, 0, 4\n\t"
    "v_lshl_add_u32 v233, v253, 4, v233\n\t"
    "v_bfe_u32 v253, v251, 4, 2\n\t"
    "s_mov_b32 s98, 272\n\t"
    "v_mad_i32_i24 v233, v253, s98, v233\n\t"
    "v_bfe_u32 v253, v251, 0, 1\n\t"
    "v_bfe_u32 v254, v251, 4, 1\n\t"
    "v_and_b32 v253, v253, v254\n\t"
    "s_mov_b32 s98, 4294967264\n\t"
    "v_mad_i32_i24 v233, v253, s98, v233\n\t"
    "v_bfe_u32 v253, v251, 1, 1\n\t"
    "v_bfe_u32 v254, v251, 5, 1\n\t"
    "v_and_b32 v253, v253, v254\n\t"
    "s_mov_b32 s98, 4294967232\n\t"
    "v_mad_i32_i24 v233, v253, s98, v233\n\t"
    "v_xor_b32 v234, 64, v233\n\t"
    "v_xor_b32 v235, 128, v233\n\t"
    "v_xor_b32 v236, 192, v233\n\t"
    "v_mov_b32 v237, 1024\n\t"
    "v_bfe_u32 v253, v251, 0, 2\n\t"
    "v_lshl_add_u32 v237, v253, 4, v237\n\t"
    "v_bfe_u32 v253, v251, 2, 3\n\t"
    "v_lshl_add_u32 v237, v253, 8, v237\n\t"
    "v_bfe_u32 v253, v251, 5, 1\n\t"
    "v_lshl_add_u32 v237, v253, 6, v237\n\t"
    "v_add_u32 v238, 4294966400, v237\n\t"
    "s_waitcnt vmcnt(16)\n\t"
    "s_barrier\n\t"
    "ds_read_b64_tr_b16 a[128:129], v247 offset:0\n\t"
    "ds_read_b64_tr_b16 a[130:131], v247 offset:2048\n\t"
    "ds_read_b64_tr_b16 a[132:133], v247 offset:512\n\t"
    "ds_read_b64_tr_b16 a[134:135], v247 offset:2560\n\t"
    "ds_read_b64_tr_b16 a[136:137], v247 offset:1024\n\t"
    "ds_read_b64_tr_b16 a[138:139], v247 offset:3072\n\t"
    "ds_read_b64_tr_b16 a[140:141], v247 offset:1536\n\t"
    "ds_read_b64_tr_b16 a[142:143], v247 offset:3584\n\t"
    "ds_read_b64_tr_b16 a[144:145], v247 offset:4096\n\t"
    "ds_read_b64_tr_b16 a[146:147], v247 offset:6144\n\t"
    "ds_read_b64_tr_b16 a[148:149], v247 offset:4608\n\t"
    "ds_read_b64_tr_b16 a[150:151], v247 offset:6656\n\t"
    "ds_read_b64_tr_b16 a[152:153], v247 offset:5120\n\t"
    "ds_read_b64_tr_b16 a[154:155], v247 offset:7168\n\t"
    "ds_read_b64_tr_b16 a[156:157], v247 offset:5632\n\t"
    "ds_read_b64_tr_b16 a[158:159], v247 offset:7680\n\t"
    "ds_read_b64_tr_b16 a[160:161], v247 offset:8192\n\t"
    "ds_read_b64_tr_b16 a[162:163], v247 offset:10240\n\t"
    "ds_read_b64_tr_b16 a[164:165], v247 offset:8704\n\t"
    "ds_read_b64_tr_b16 a[166:167], v247 offset:10752\n\t"
    "ds_read_b64_tr_b16 a[168:169], v247 offset:9216\n\t"
    "ds_read_b64_tr_b16 a[170:171], v247 offset:11264\n\t"
    "ds_read_b64_tr_b16 a[172:173], v247 offset:9728\n\t"
    "ds_read_b64_tr_b16 a[174:175], v247 offset:11776\n\t"
    "ds_read_b64_tr_b16 a[176:177], v247 offset:12288\n\t"
    "ds_read_b64_tr_b16 a[178:179], v247 offset:14336\n\t"
    "ds_read_b64_tr_b16 a[180:181], v247 offset:12800\n\t"
    "ds_read_b64_tr_b16 a[182:183], v247 offset:14848\n\t"
    "ds_read_b64_tr_b16 a[184:185], v247 offset:13312\n\t"
    "ds_read_b64_tr_b16 a[186:187], v247 offset:15360\n\t"
    "ds_read_b64_tr_b16 a[188:189], v247 offset:13824\n\t"
    "ds_read_b64_tr_b16 a[190:191], v247 offset:15872\n\t"
    "s_waitcnt vmcnt(7) lgkmcnt(15)\n\t"
    "v_mfma_f32_32x32x16_bf16 v[0:15], a[128:131], a[0:3], 0\n\t"
    "s_barrier\n\t"
    "buffer_load_dwordx4 a[32:35], v241, s[56:59], s72 offen offset:0 sc1\n\t"
    "s_waitcnt vmcnt(7)\n\t"
    "v_mfma_f32_32x32x16_bf16 v[16:31], a[128:131], a[4:7], 0\n\t"
    "buffer_load_dwordx4 a[36:39], v242, s[56:59], s72 offen offset:0 sc1\n\t"
    "v_mfma_f32_32x32x16_bf16 v[32:47], a[132:135], a[0:3], 0\n\t"
    "ds_read_b64_tr_b16 a[128:129], v247 offset:16384\n\t"
    "buffer_load_dwordx4 a[40:43], v241, s[56:59], s72 offen offset:1024 sc1\n\t"
    "v_mfma_f32_32x32x16_bf16 v[48:63], a[132:135], a[4:7], 0\n\t"
    "ds_read_b64_tr_b16 a[130:131], v247 offset:18432\n\t"
    "buffer_load_dwordx4 a[44:47], v242, s[56:59], s72 offen offset:1024 sc1\n\t"
    "v_mfma_f32_32x32x16_bf16 v[64:79], a[136:139], a[0:3], 0\n\t"
    "ds_read_b64_tr_b16 a[132:133], v247 offset:16896\n\t"
    "v_mfma_f32_32x32x16_bf16 v[80:95], a[136:139], a[4:7], 0\n\t"
    "ds_read_b64_tr_b16 a[134:135], v247 offset:18944\n\t"
    "buffer_load_dwordx4 a[48:51], v241, s[56:59], s72 offen offset:2048 sc1\n\t"
    "v_mfma_f32_32x32x16_bf16 v[96:111], a[140:143], a[0:3], 0\n\t"
    "ds_read_b64_tr_b16 a[136:137], v247 offset:17408\n\t"
    "buffer_load_dwordx4 a[52:55], v242, s[56:59], s72 offen offset:2048 sc1\n\t"
    "v_mfma_f32_32x32x16_bf16 v[112:127], a[140:143], a[4:7], 0\n\t"
    "ds_read_b64_tr_b16 a[138:139], v247 offset:19456\n\t"
    "buffer_load_dwordx4 a[56:59], v241, s[56:59], s72 offen offset:3072 sc1\n\t"
    "s_waitcnt vmcnt(11)\n\t"
    "v_mfma_f32_32x32x16_bf16 v[0:15], a[144:147], a[8:11], v[0:15]\n\t"
    "ds_read_b64_tr_b16 a[140:141], v247 offset:17920\n\t"
    "buffer_load_dwordx4 a[60:63], v242, s[56:59], s72 offen offset:3072 sc1\n\t"
    "s_add_u32 s72, s72, 4096\n\t"
    "v_mfma_f32_32x32x16_bf16 v[16:31], a[144:147], a[12:15], v[16:31]\n\t"
    "ds_read_b64_tr_b16 a[142:143], v247 offset:19968\n\t"
    "v_mfma_f32_32x32x16_bf16 v[32:47], a[148:151], a[8:11], v[32:47]\n\t"
    "ds_read_b64_tr_b16 a[144:145], v247 offset:20480\n\t"
    "buffer_load_dwordx4 a[192:195], v241, s[56:59], s72 offen offset:0 sc1\n\t"
    "v_mfma_f32_32x32x16_bf16 v[48:63], a[148:151], a[12:15], v[48:63]\n\t"
    "ds_read_b64_tr_b16 a[146:147], v247 offset:22528\n\t"
    "buffer_load_dwordx4 a[196:199], v242, s[56:59], s72 offen offset:0 sc1\n\t"
    "v_mfma_f32_32x32x16_bf16 v[64:79], a[152:155], a[8:11], v[64:79]\n\t"
    "ds_read_b64_tr_b16 a[148:149], v247 offset:20992\n\t"
    "buffer_load_dwordx4 a[200:203], v241, s[56:59], s72 offen offset:1024 sc1\n\t"
    "v_mfma_f32_32x32x16_bf16 v[80:95], a[152:155], a[12:15], v[80:95]\n\t"
    "ds_read_b64_tr_b16 a[150:151], v247 offset:23040\n\t"
    "buffer_load_dwordx4 a[204:207], v242, s[56:59], s72 offen offset:1024 sc1\n\t"
    "v_mfma_f32_32x32x16_bf16 v[96:111], a[156:159], a[8:11], v[96:111]\n\t"
    "ds_read_b64_tr_b16 a[152:153], v247 offset:21504\n\t"
    "v_mfma_f32_32x32x16_bf16 v[112:127], a[156:159], a[12:15], v[112:127]\n\t"
    "ds_read_b64_tr_b16 a[154:155], v247 offset:23552\n\t"
    "buffer_load_dwordx4 a[208:211], v241, s[56:59], s72 offen offset:2048 sc1\n\t"
    "s_waitcnt vmcnt(15) lgkmcnt(15)\n\t"
    "v_mfma_f32_32x32x16_bf16 v[0:15], a[160:163], a[16:19], v[0:15]\n\t"
    "ds_read_b64_tr_b16 a[156:157], v247 offset:22016\n\t"
    "buffer_load_dwordx4 a[212:215], v242, s[56:59], s72 offen offset:2048 sc1\n\t"
    "v_mfma_f32_32x32x16_bf16 v[16:31], a[160:163], a[20:23], v[16:31]\n\t"
    "ds_read_b64_tr_b16 a[158:159], v247 offset:24064\n\t"
    "buffer_load_dwordx4 a[216:219], v241, s[56:59], s72 offen offset:3072 sc1\n\t"
    "v_mfma_f32_32x32x16_bf16 v[32:47], a[164:167], a[16:19], v[32:47]\n\t"
    "ds_read_b64_tr_b16 a[160:161], v247 offset:24576\n\t"
    "buffer_load_dwordx4 a[220:223], v242, s[56:59], s72 offen offset:3072 sc1\n\t"
    "s_add_u32 s72, s72, 4096\n\t"
    "v_mfma_f32_32x32x16_bf16 v[48:63], a[164:167], a[20:23], v[48:63]\n\t"
    "ds_read_b64_tr_b16 a[162:163], v247 offset:26624\n\t"
    "v_mfma_f32_32x32x16_bf16 v[64:79], a[168:171], a[16:19], v[64:79]\n\t"
    "ds_read_b64_tr_b16 a[164:165], v247 offset:25088\n\t"
    "s_add_u32 m0, s76, 49152\n\t"
    "s_nop 0\n\t"
    "buffer_load_dwordx4 v243, s[60:63], s73 offen offset:0 nt lds\n\t"
    "v_mfma_f32_32x32x16_bf16 v[80:95], a[168:171], a[20:23], v[80:95]\n\t"
    "ds_read_b64_tr_b16 a[166:167], v247 offset:27136\n\t"
    "buffer_load_dwordx4 v244, s[60:63], s73 offen offset:1024 nt lds\n\t"
    "v_mfma_f32_32x32x16_bf16 v[96:111], a[172:175], a[16:19], v[96:111]\n\t"
    "ds_read_b64_tr_b16 a[168:169], v247 offset:25600\n\t"
    "buffer_load_dwordx4 v245, s[60:63], s73 offen offset:2048 nt lds\n\t"
    "v_mfma_f32_32x32x16_bf16 v[112:127], a[172:175], a[20:23], v[112:127]\n\t"
    "ds_read_b64_tr_b16 a[170:171], v247 offset:27648\n\t"
    "buffer_load_dwordx4 v246, s[60:63], s73 offen offset:3072 nt lds\n\t"
    "s_add_u32 s73, s73, 524288\n\t"
    "s_waitcnt vmcnt(20) lgkmcnt(15)\n\t"
    "v_mfma_f32_32x32x16_bf16 v[0:15], a[176:179], a[24:27], v[0:15]\n\t"
    "ds_read_b64_tr_b16 a[172:173], v247 offset:26112\n\t"
    "buffer_load_dwordx4 a[224:227], v241, s[56:59], s72 offen offset:0 sc1\n\t"
    "v_mfma_f32_32x32x16_bf16 v[16:31], a[176:179], a[28:31], v[16:31]\n\t"
    "ds_read_b64_tr_b16 a[174:175], v247 offset:28160\n\t"
    "buffer_load_dwordx4 a[228:231], v242, s[56:59], s72 offen offset:0 sc1\n\t"
    "v_mfma_f32_32x32x16_bf16 v[32:47], a[180:183], a[24:27], v[32:47]\n\t"
    "ds_read_b64_tr_b16 a[176:177], v247 offset:28672\n\t"
    "buffer_load_dwordx4 a[232:235], v241, s[56:59], s72 offen offset:1024 sc1\n\t"
    "v_mfma_f32_32x32x16_bf16 v[48:63], a[180:183], a[28:31], v[48:63]\n\t"
    "ds_read_b64_tr_b16 a[178:179], v247 offset:30720\n\t"
    "buffer_load_dwordx4 a[236:239], v242, s[56:59], s72 offen offset:1024 sc1\n\t"
    "v_mfma_f32_32x32x16_bf16 v[64:79], a[184:187], a[24:27], v[64:79]\n\t"
    "ds_read_b64_tr_b16 a[180:181], v247 offset:29184\n\t"
    "buffer_load_dwordx4 a[240:243], v241, s[56:59], s72 offen offset:2048 sc1\n\t"
    "v_mfma_f32_32x32x16_bf16 v[80:95], a[184:187], a[28:31], v[80:95]\n\t"
    "ds_read_b64_tr_b16 a[182:183], v247 offset:31232\n\t"
    "buffer_load_dwordx4 a[244:247], v242, s[56:59], s72 offen offset:2048 sc1\n\t"
    "v_mfma_f32_32x32x16_bf16 v[96:111], a[188:191], a[24:27], v[96:111]\n\t"
    "ds_read_b64_tr_b16 a[184:185], v247 offset:29696\n\t"
    "buffer_load_dwordx4 a[248:251], v241, s[56:59], s72 offen offset:3072 sc1\n\t"
    "v_mfma_f32_32x32x16_bf16 v[112:127], a[188:191], a[28:31], v[112:127]\n\t"
    "ds_read_b64_tr_b16 a[186:187], v247 offset:31744\n\t"
    "buffer_load_dwordx4 a[252:255], v242, s[56:59], s72 offen offset:3072 sc1\n\t"
    "s_add_u32 s72, s72, 4096\n\t"
    "s_waitcnt vmcnt(26) lgkmcnt(15)\n\t"
    "v_mfma_f32_32x32x16_bf16 v[0:15], a[128:131], a[32:35], v[0:15]\n\t"
    "s_barrier\n\t"
    "ds_read_b64_tr_b16 a[188:189], v247 offset:30208\n\t"
    "v_mfma_f32_32x32x16_bf16 v[16:31], a[128:131], a[36:39], v[16:31]\n\t"
    "ds_read_b64_tr_b16 a[190:191], v247 offset:32256\n\t"
    "s_add_u32 m0, s76, 0\n\t"
    "s_nop 0\n\t"
    "buffer_load_dwordx4 v243, s[60:63], s73 offen offset:0 nt lds\n\t"
    "v_mfma_f32_32x32x16_bf16 v[32:47], a[132:135], a[32:35], v[32:47]\n\t"
    "ds_read_b64_tr_b16 a[128:129], v247 offset:32768\n\t"
    "v_mfma_f32_32x32x16_bf16 v[48:63], a[132:135], a[36:39], v[48:63]\n\t"
    "ds_read_b64_tr_b16 a[130:131], v247 offset:34816\n\t"
    "buffer_load_dwordx4 v244, s[60:63], s73 offen offset:1024 nt lds\n\t"
    "v_mfma_f32_32x32x16_bf16 v[64:79], a[136:139], a[32:35], v[64:79]\n\t"
    "ds_read_b64_tr_b16 a[132:133], v247 offset:33280\n\t"
    "v_mfma_f32_32x32x16_bf16 v[80:95], a[136:139], a[36:39], v[80:95]\n\t"
    "ds_read_b64_tr_b16 a[134:135], v247 offset:35328\n\t"
    "v_mfma_f32_32x32x16_bf16 v[96:111], a[140:143], a[32:35], v[96:111]\n\t"
    "ds_read_b64_tr_b16 a[136:137], v247 offset:33792\n\t"
    "buffer_load_dwordx4 v245, s[60:63], s73 offen offset:2048 nt lds\n\t"
    "v_mfma_f32_32x32x16_bf16 v[112:127], a[140:143], a[36:39], v[112:127]\n\t"
    "ds_read_b64_tr_b16 a[138:139], v247 offset:35840\n\t"
    "s_waitcnt vmcnt(27) lgkmcnt(15)\n\t"
    "v_mfma_f32_32x32x16_bf16 v[0:15], a[144:147], a[40:43], v[0:15]\n\t"
    "ds_read_b64_tr_b16 a[140:141], v247 offset:34304\n\t"
    "buffer_load_dwordx4 v246, s[60:63], s73 offen offset:3072 nt lds\n\t"
    "s_add_u32 s73, s73, 524288\n\t"
    "v_mfma_f32_32x32x16_bf16 v[16:31], a[144:147], a[44:47], v[16:31]\n\t"
    "ds_read_b64_tr_b16 a[142:143], v247 offset:36352\n\t"
    "v_mfma_f32_32x32x16_bf16 v[32:47], a[148:151], a[40:43], v[32:47]\n\t"
    "ds_read_b64_tr_b16 a[144:145], v247 offset:36864\n\t"
    "v_mfma_f32_32x32x16_bf16 v[48:63], a[148:151], a[44:47], v[48:63]\n\t"
    "ds_read_b64_tr_b16 a[146:147], v247 offset:38912\n\t"
    "buffer_load_dwordx4 a[0:3], v241, s[56:59], s72 offen offset:0 sc1\n\t"
    "v_mfma_f32_32x32x16_bf16 v[64:79], a[152:155], a[40:43], v[64:79]\n\t"
    "ds_read_b64_tr_b16 a[148:149], v247 offset:37376\n\t"
    "v_mfma_f32_32x32x16_bf16 v[80:95], a[152:155], a[44:47], v[80:95]\n\t"
    "ds_read_b64_tr_b16 a[150:151], v247 offset:39424\n\t"
    "buffer_load_dwordx4 a[4:7], v242, s[56:59], s72 offen offset:0 sc1\n\t"
    "v_mfma_f32_32x32x16_bf16 v[96:111], a[156:159], a[40:43], v[96:111]\n\t"
    "ds_read_b64_tr_b16 a[152:153], v247 offset:37888\n\t"
    "v_mfma_f32_32x32x16_bf16 v[112:127], a[156:159], a[44:47], v[112:127]\n\t"
    "ds_read_b64_tr_b16 a[154:155], v247 offset:39936\n\t"
    "s_waitcnt vmcnt(28) lgkmcnt(15)\n\t"
    "v_mfma_f32_32x32x16_bf16 v[0:15], a[160:163], a[48:51], v[0:15]\n\t"
    "ds_read_b64_tr_b16 a[156:157], v247 offset:38400\n\t"
    "buffer_load_dwordx4 a[8:11], v241, s[56:59], s72 offen offset:1024 sc1\n\t"
    "v_mfma_f32_32x32x16_bf16 v[16:31], a[160:163], a[52:55], v[16:31]\n\t"
    "ds_read_b64_tr_b16 a[158:159], v247 offset:40448\n\t"
    "v_mfma_f32_32x32x16_bf16 v[32:47], a[164:167], a[48:51], v[32:47]\n\t"
    "ds_read_b64_tr_b16 a[160:161], v247 offset:40960\n\t"
    "buffer_load_dwordx4 a[12:15], v242, s[56:59], s72 offen offset:1024 sc1\n\t"
    "v_mfma_f32_32x32x16_bf16 v[48:63], a[164:167], a[52:55], v[48:63]\n\t"
    "ds_read_b64_tr_b16 a[162:163], v247 offset:43008\n\t"
    "v_mfma_f32_32x32x16_bf16 v[64:79], a[168:171], a[48:51], v[64:79]\n\t"
    "ds_read_b64_tr_b16 a[164:165], v247 offset:41472\n\t"
    "v_mfma_f32_32x32x16_bf16 v[80:95], a[168:171], a[52:55], v[80:95]\n\t"
    "ds_read_b64_tr_b16 a[166:167], v247 offset:43520\n\t"
    "buffer_load_dwordx4 a[16:19], v241, s[56:59], s72 offen offset:2048 sc1\n\t"
    "v_mfma_f32_32x32x16_bf16 v[96:111], a[172:175], a[48:51], v[96:111]\n\t"
    "ds_read_b64_tr_b16 a[168:169], v247 offset:41984\n\t"
    "v_mfma_f32_32x32x16_bf16 v[112:127], a[172:175], a[52:55], v[112:127]\n\t"
    "ds_read_b64_tr_b16 a[170:171], v247 offset:44032\n\t"
    "buffer_load_dwordx4 a[20:23], v242, s[56:59], s72 offen offset:2048 sc1\n\t"
    "s_waitcnt vmcnt(30) lgkmcnt(15)\n\t"
    "v_mfma_f32_32x32x16_bf16 v[0:15], a[176:179], a[56:59], v[0:15]\n\t"
    "ds_read_b64_tr_b16 a[172:173], v247 offset:42496\n\t"
    "v_mfma_f32_32x32x16_bf16 v[16:31], a[176:179], a[60:63], v[16:31]\n\t"
    "ds_read_b64_tr_b16 a[174:175], v247 offset:44544\n\t"
    "v_mfma_f32_32x32x16_bf16 v[32:47], a[180:183], a[56:59], v[32:47]\n\t"
    "ds_read_b64_tr_b16 a[176:177], v247 offset:45056\n\t"
    "buffer_load_dwordx4 a[24:27], v241, s[56:59], s72 offen offset:3072 sc1\n\t"
    "v_mfma_f32_32x32x16_bf16 v[48:63], a[180:183], a[60:63], v[48:63]\n\t"
    "ds_read_b64_tr_b16 a[178:179], v247 offset:47104\n\t"
    "v_mfma_f32_32x32x16_bf16 v[64:79], a[184:187], a[56:59], v[64:79]\n\t"
    "ds_read_b64_tr_b16 a[180:181], v247 offset:45568\n\t"
    "buffer_load_dwordx4 a[28:31], v242, s[56:59], s72 offen offset:3072 sc1\n\t"
    "s_add_u32 s72, s72, 4096\n\t"
    "v_mfma_f32_32x32x16_bf16 v[80:95], a[184:187], a[60:63], v[80:95]\n\t"
    "ds_read_b64_tr_b16 a[182:183], v247 offset:47616\n\t"
    "v_mfma_f32_32x32x16_bf16 v[96:111], a[188:191], a[56:59], v[96:111]\n\t"
    "ds_read_b64_tr_b16 a[184:185], v247 offset:46080\n\t"
    "v_mfma_f32_32x32x16_bf16 v[112:127], a[188:191], a[60:63], v[112:127]\n\t"
    "ds_read_b64_tr_b16 a[186:187], v247 offset:48128\n\t"
    "s_waitcnt vmcnt(30) lgkmcnt(15)\n\t"
    "v_mfma_f32_32x32x16_bf16 v[0:15], a[128:131], a[192:195], v[0:15]\n\t"
    "s_waitcnt vmcnt(20)\n\t"
    "s_barrier\n\t"
    "ds_read_b64_tr_b16 a[188:189], v247 offset:46592\n\t"
    "v_mfma_f32_32x32x16_bf16 v[16:31], a[128:131], a[196:199], v[16:31]\n\t"
    "ds_read_b64_tr_b16 a[190:191], v247 offset:48640\n\t"
    "s_add_u32 m0, s76, 16384\n\t"
    "s_nop 0\n\t"
    "buffer_load_dwordx4 v243, s[60:63], s73 offen offset:0 nt lds\n\t"
    "v_mfma_f32_32x32x16_bf16 v[32:47], a[132:135], a[192:195], v[32:47]\n\t"
    "ds_read_b64_tr_b16 a[128:129], v247 offset:49152\n\t"
    "v_mfma_f32_32x32x16_bf16 v[48:63], a[132:135], a[196:199], v[48:63]\n\t"
    "ds_read_b64_tr_b16 a[130:131], v247 offset:51200\n\t"
    "buffer_load_dwordx4 v244, s[60:63], s73 offen offset:1024 nt lds\n\t"
    "v_mfma_f32_32x32x16_bf16 v[64:79], a[136:139], a[192:195], v[64:79]\n\t"
    "ds_read_b64_tr_b16 a[132:133], v247 offset:49664\n\t"
    "v_mfma_f32_32x32x16_bf16 v[80:95], a[136:139], a[196:199], v[80:95]\n\t"
    "ds_read_b64_tr_b16 a[134:135], v247 offset:51712\n\t"
    "v_mfma_f32_32x32x16_bf16 v[96:111], a[140:143], a[192:195], v[96:111]\n\t"
    "ds_read_b64_tr_b16 a[136:137], v247 offset:50176\n\t"
    "buffer_load_dwordx4 v245, s[60:63], s73 offen offset:2048 nt lds\n\t"
    "v_mfma_f32_32x32x16_bf16 v[112:127], a[140:143], a[196:199], v[112:127]\n\t"
    "ds_read_b64_tr_b16 a[138:139], v247 offset:52224\n\t"
    "s_waitcnt lgkmcnt(15)\n\t"
    "v_mfma_f32_32x32x16_bf16 v[0:15], a[144:147], a[200:203], v[0:15]\n\t"
    "ds_read_b64_tr_b16 a[140:141], v247 offset:50688\n\t"
    "buffer_load_dwordx4 v246, s[60:63], s73 offen offset:3072 nt lds\n\t"
    "s_add_u32 s73, s73, 524288\n\t"
    "v_mfma_f32_32x32x16_bf16 v[16:31], a[144:147], a[204:207], v[16:31]\n\t"
    "ds_read_b64_tr_b16 a[142:143], v247 offset:52736\n\t"
    "v_mfma_f32_32x32x16_bf16 v[32:47], a[148:151], a[200:203], v[32:47]\n\t"
    "ds_read_b64_tr_b16 a[144:145], v247 offset:53248\n\t"
    "v_mfma_f32_32x32x16_bf16 v[48:63], a[148:151], a[204:207], v[48:63]\n\t"
    "ds_read_b64_tr_b16 a[146:147], v247 offset:55296\n\t"
    "buffer_load_dwordx4 a[32:35], v241, s[56:59], s72 offen offset:0 sc1\n\t"
    "v_mfma_f32_32x32x16_bf16 v[64:79], a[152:155], a[200:203], v[64:79]\n\t"
    "ds_read_b64_tr_b16 a[148:149], v247 offset:53760\n\t"
    "v_mfma_f32_32x32x16_bf16 v[80:95], a[152:155], a[204:207], v[80:95]\n\t"
    "ds_read_b64_tr_b16 a[150:151], v247 offset:55808\n\t"
    "buffer_load_dwordx4 a[36:39], v242, s[56:59], s72 offen offset:0 sc1\n\t"
    "v_mfma_f32_32x32x16_bf16 v[96:111], a[156:159], a[200:203], v[96:111]\n\t"
    "ds_read_b64_tr_b16 a[152:153], v247 offset:54272\n\t"
    "v_mfma_f32_32x32x16_bf16 v[112:127], a[156:159], a[204:207], v[112:127]\n\t"
    "ds_read_b64_tr_b16 a[154:155], v247 offset:56320\n\t"
    "s_waitcnt lgkmcnt(15)\n\t"
    "v_mfma_f32_32x32x16_bf16 v[0:15], a[160:163], a[208:211], v[0:15]\n\t"
    "ds_read_b64_tr_b16 a[156:157], v247 offset:54784\n\t"
    "buffer_load_dwordx4 a[40:43], v241, s[56:59], s72 offen offset:1024 sc1\n\t"
    "v_mfma_f32_32x32x16_bf16 v[16:31], a[160:163], a[212:215], v[16:31]\n\t"
    "ds_read_b64_tr_b16 a[158:159], v247 offset:56832\n\t"
    "v_mfma_f32_32x32x16_bf16 v[32:47], a[164:167], a[208:211], v[32:47]\n\t"
    "ds_read_b64_tr_b16 a[160:161], v247 offset:57344\n\t"
    "buffer_load_dwordx4 a[44:47], v242, s[56:59], s72 offen offset:1024 sc1\n\t"
    "v_mfma_f32_32x32x16_bf16 v[48:63], a[164:167], a[212:215], v[48:63]\n\t"
    "ds_read_b64_tr_b16 a[162:163], v247 offset:59392\n\t"
    "v_mfma_f32_32x32x16_bf16 v[64:79], a[168:171], a[208:211], v[64:79]\n\t"
    "ds_read_b64_tr_b16 a[164:165], v247 offset:57856\n\t"
    "v_mfma_f32_32x32x16_bf16 v[80:95], a[168:171], a[212:215], v[80:95]\n\t"
    "ds_read_b64_tr_b16 a[166:167], v247 offset:59904\n\t"
    "buffer_load_dwordx4 a[48:51], v241, s[56:59], s72 offen offset:2048 sc1\n\t"
    "v_mfma_f32_32x32x16_bf16 v[96:111], a[172:175], a[208:211], v[96:111]\n\t"
    "ds_read_b64_tr_b16 a[168:169], v247 offset:58368\n\t"
    "v_mfma_f32_32x32x16_bf16 v[112:127], a[172:175], a[212:215], v[112:127]\n\t"
    "ds_read_b64_tr_b16 a[170:171], v247 offset:60416\n\t"
    "buffer_load_dwordx4 a[52:55], v242, s[56:59], s72 offen offset:2048 sc1\n\t"
    "s_waitcnt lgkmcnt(15)\n\t"
    "v_mfma_f32_32x32x16_bf16 v[0:15], a[176:179], a[216:219], v[0:15]\n\t"
    "ds_read_b64_tr_b16 a[172:173], v247 offset:58880\n\t"
    "v_mfma_f32_32x32x16_bf16 v[16:31], a[176:179], a[220:223], v[16:31]\n\t"
    "ds_read_b64_tr_b16 a[174:175], v247 offset:60928\n\t"
    "v_mfma_f32_32x32x16_bf16 v[32:47], a[180:183], a[216:219], v[32:47]\n\t"
    "ds_read_b64_tr_b16 a[176:177], v247 offset:61440\n\t"
    "buffer_load_dwordx4 a[56:59], v241, s[56:59], s72 offen offset:3072 sc1\n\t"
    "v_mfma_f32_32x32x16_bf16 v[48:63], a[180:183], a[220:223], v[48:63]\n\t"
    "ds_read_b64_tr_b16 a[178:179], v247 offset:63488\n\t"
    "v_mfma_f32_32x32x16_bf16 v[64:79], a[184:187], a[216:219], v[64:79]\n\t"
    "ds_read_b64_tr_b16 a[180:181], v247 offset:61952\n\t"
    "buffer_load_dwordx4 a[60:63], v242, s[56:59], s72 offen offset:3072 sc1\n\t"
    "s_add_u32 s72, s72, 4096\n\t"
    "v_mfma_f32_32x32x16_bf16 v[80:95], a[184:187], a[220:223], v[80:95]\n\t"
    "ds_read_b64_tr_b16 a[182:183], v247 offset:64000\n\t"
    "v_mfma_f32_32x32x16_bf16 v[96:111], a[188:191], a[216:219], v[96:111]\n\t"
    "ds_read_b64_tr_b16 a[184:185], v247 offset:62464\n\t"
    "v_mfma_f32_32x32x16_bf16 v[112:127], a[188:191], a[220:223], v[112:127]\n\t"
    "ds_read_b64_tr_b16 a[186:187], v247 offset:64512\n\t"
    "s_waitcnt vmcnt(30) lgkmcnt(15)\n\t"
    "v_mfma_f32_32x32x16_bf16 v[0:15], a[128:131], a[224:227], v[0:15]\n\t"
    "s_waitcnt vmcnt(20)\n\t"
    "s_barrier\n\t"
    "ds_read_b64_tr_b16 a[188:189], v247 offset:62976\n\t"
    "v_mfma_f32_32x32x16_bf16 v[16:31], a[128:131], a[228:231], v[16:31]\n\t"
    "ds_read_b64_tr_b16 a[190:191], v247 offset:65024\n\t"
    "s_add_u32 m0, s76, 32768\n\t"
    "s_nop 0\n\t"
    "buffer_load_dwordx4 v243, s[60:63], s73 offen offset:0 nt lds\n\t"
    "v_mfma_f32_32x32x16_bf16 v[32:47], a[132:135], a[224:227], v[32:47]\n\t"
    "ds_read_b64_tr_b16 a[128:129], v247 offset:0\n\t"
    "v_mfma_f32_32x32x16_bf16 v[48:63], a[132:135], a[228:231], v[48:63]\n\t"
    "ds_read_b64_tr_b16 a[130:131], v247 offset:2048\n\t"
    "buffer_load_dwordx4 v244, s[60:63], s73 offen offset:1024 nt lds\n\t"
    "v_mfma_f32_32x32x16_bf16 v[64:79], a[136:139], a[224:227], v[64:79]\n\t"
    "ds_read_b64_tr_b16 a[132:133], v247 offset:512\n\t"
    "v_mfma_f32_32x32x16_bf16 v[80:95], a[136:139], a[228:231], v[80:95]\n\t"
    "ds_read_b64_tr_b16 a[134:135], v247 offset:2560\n\t"
    "v_mfma_f32_32x32x16_bf16 v[96:111], a[140:143], a[224:227], v[96:111]\n\t"
    "ds_read_b64_tr_b16 a[136:137], v247 offset:1024\n\t"
    "buffer_load_dwordx4 v245, s[60:63], s73 offen offset:2048 nt lds\n\t"
    "v_mfma_f32_32x32x16_bf16 v[112:127], a[140:143], a[228:231], v[112:127]\n\t"
    "ds_read_b64_tr_b16 a[138:139], v247 offset:3072\n\t"
    "s_waitcnt lgkmcnt(15)\n\t"
    "v_mfma_f32_32x32x16_bf16 v[0:15], a[144:147], a[232:235], v[0:15]\n\t"
    "ds_read_b64_tr_b16 a[140:141], v247 offset:1536\n\t"
    "buffer_load_dwordx4 v246, s[60:63], s73 offen offset:3072 nt lds\n\t"
    "s_add_u32 s73, s73, 524288\n\t"
    "v_mfma_f32_32x32x16_bf16 v[16:31], a[144:147], a[236:239], v[16:31]\n\t"
    "ds_read_b64_tr_b16 a[142:143], v247 offset:3584\n\t"
    "v_mfma_f32_32x32x16_bf16 v[32:47], a[148:151], a[232:235], v[32:47]\n\t"
    "ds_read_b64_tr_b16 a[144:145], v247 offset:4096\n\t"
    "v_mfma_f32_32x32x16_bf16 v[48:63], a[148:151], a[236:239], v[48:63]\n\t"
    "ds_read_b64_tr_b16 a[146:147], v247 offset:6144\n\t"
    "buffer_load_dwordx4 a[192:195], v241, s[56:59], s72 offen offset:0 sc1\n\t"
    "v_mfma_f32_32x32x16_bf16 v[64:79], a[152:155], a[232:235], v[64:79]\n\t"
    "ds_read_b64_tr_b16 a[148:149], v247 offset:4608\n\t"
    "v_mfma_f32_32x32x16_bf16 v[80:95], a[152:155], a[236:239], v[80:95]\n\t"
    "ds_read_b64_tr_b16 a[150:151], v247 offset:6656\n\t"
    "buffer_load_dwordx4 a[196:199], v242, s[56:59], s72 offen offset:0 sc1\n\t"
    "v_mfma_f32_32x32x16_bf16 v[96:111], a[156:159], a[232:235], v[96:111]\n\t"
    "ds_read_b64_tr_b16 a[152:153], v247 offset:5120\n\t"
    "v_mfma_f32_32x32x16_bf16 v[112:127], a[156:159], a[236:239], v[112:127]\n\t"
    "ds_read_b64_tr_b16 a[154:155], v247 offset:7168\n\t"
    "s_waitcnt lgkmcnt(15)\n\t"
    "v_mfma_f32_32x32x16_bf16 v[0:15], a[160:163], a[240:243], v[0:15]\n\t"
    "ds_read_b64_tr_b16 a[156:157], v247 offset:5632\n\t"
    "buffer_load_dwordx4 a[200:203], v241, s[56:59], s72 offen offset:1024 sc1\n\t"
    "v_mfma_f32_32x32x16_bf16 v[16:31], a[160:163], a[244:247], v[16:31]\n\t"
    "ds_read_b64_tr_b16 a[158:159], v247 offset:7680\n\t"
    "v_mfma_f32_32x32x16_bf16 v[32:47], a[164:167], a[240:243], v[32:47]\n\t"
    "ds_read_b64_tr_b16 a[160:161], v247 offset:8192\n\t"
    "buffer_load_dwordx4 a[204:207], v242, s[56:59], s72 offen offset:1024 sc1\n\t"
    "v_mfma_f32_32x32x16_bf16 v[48:63], a[164:167], a[244:247], v[48:63]\n\t"
    "ds_read_b64_tr_b16 a[162:163], v247 offset:10240\n\t"
    "v_mfma_f32_32x32x16_bf16 v[64:79], a[168:171], a[240:243], v[64:79]\n\t"
    "ds_read_b64_tr_b16 a[164:165], v247 offset:8704\n\t"
    "v_mfma_f32_32x32x16_bf16 v[80:95], a[168:171], a[244:247], v[80:95]\n\t"
    "ds_read_b64_tr_b16 a[166:167], v247 offset:10752\n\t"
    "buffer_load_dwordx4 a[208:211], v241, s[56:59], s72 offen offset:2048 sc1\n\t"
    "v_mfma_f32_32x32x16_bf16 v[96:111], a[172:175], a[240:243], v[96:111]\n\t"
    "ds_read_b64_tr_b16 a[168:169], v247 offset:9216\n\t"
    "v_mfma_f32_32x32x16_bf16 v[112:127], a[172:175], a[244:247], v[112:127]\n\t"
    "ds_read_b64_tr_b16 a[170:171], v247 offset:11264\n\t"
    "buffer_load_dwordx4 a[212:215], v242, s[56:59], s72 offen offset:2048 sc1\n\t"
    "s_waitcnt lgkmcnt(15)\n\t"
    "v_mfma_f32_32x32x16_bf16 v[0:15], a[176:179], a[248:251], v[0:15]\n\t"
    "ds_read_b64_tr_b16 a[172:173], v247 offset:9728\n\t"
    "v_mfma_f32_32x32x16_bf16 v[16:31], a[176:179], a[252:255], v[16:31]\n\t"
    "ds_read_b64_tr_b16 a[174:175], v247 offset:11776\n\t"
    "v_mfma_f32_32x32x16_bf16 v[32:47], a[180:183], a[248:251], v[32:47]\n\t"
    "ds_read_b64_tr_b16 a[176:177], v247 offset:12288\n\t"
    "buffer_load_dwordx4 a[216:219], v241, s[56:59], s72 offen offset:3072 sc1\n\t"
    "v_mfma_f32_32x32x16_bf16 v[48:63], a[180:183], a[252:255], v[48:63]\n\t"
    "ds_read_b64_tr_b16 a[178:179], v247 offset:14336\n\t"
    "v_mfma_f32_32x32x16_bf16 v[64:79], a[184:187], a[248:251], v[64:79]\n\t"
    "ds_read_b64_tr_b16 a[180:181], v247 offset:12800\n\t"
    "buffer_load_dwordx4 a[220:223], v242, s[56:59], s72 offen offset:3072 sc1\n\t"
    "s_add_u32 s72, s72, 4096\n\t"
    "v_mfma_f32_32x32x16_bf16 v[80:95], a[184:187], a[252:255], v[80:95]\n\t"
    "ds_read_b64_tr_b16 a[182:183], v247 offset:14848\n\t"
    "v_mfma_f32_32x32x16_bf16 v[96:111], a[188:191], a[248:251], v[96:111]\n\t"
    "ds_read_b64_tr_b16 a[184:185], v247 offset:13312\n\t"
    "v_mfma_f32_32x32x16_bf16 v[112:127], a[188:191], a[252:255], v[112:127]\n\t"
    "ds_read_b64_tr_b16 a[186:187], v247 offset:15360\n\t"
    "s_waitcnt vmcnt(30) lgkmcnt(15)\n\t"
    "v_mfma_f32_32x32x16_bf16 v[0:15], a[128:131], a[0:3], v[0:15]\n\t"
    "s_waitcnt vmcnt(20)\n\t"
    "s_barrier\n\t"
    "ds_read_b64_tr_b16 a[188:189], v247 offset:13824\n\t"
    "v_mfma_f32_32x32x16_bf16 v[16:31], a[128:131], a[4:7], v[16:31]\n\t"
    "ds_read_b64_tr_b16 a[190:191], v247 offset:15872\n\t"
    "s_add_u32 m0, s76, 49152\n\t"
    "s_nop 0\n\t"
    "buffer_load_dwordx4 v243, s[60:63], s73 offen offset:0 nt lds\n\t"
    "v_mfma_f32_32x32x16_bf16 v[32:47], a[132:135], a[0:3], v[32:47]\n\t"
    "ds_read_b64_tr_b16 a[128:129], v247 offset:16384\n\t"
    "v_mfma_f32_32x32x16_bf16 v[48:63], a[132:135], a[4:7], v[48:63]\n\t"
    "ds_read_b64_tr_b16 a[130:131], v247 offset:18432\n\t"
    "buffer_load_dwordx4 v244, s[60:63], s73 offen offset:1024 nt lds\n\t"
    "v_mfma_f32_32x32x16_bf16 v[64:79], a[136:139], a[0:3], v[64:79]\n\t"
    "ds_read_b64_tr_b16 a[132:133], v247 offset:16896\n\t"
    "v_mfma_f32_32x32x16_bf16 v[80:95], a[136:139], a[4:7], v[80:95]\n\t"
    "ds_read_b64_tr_b16 a[134:135], v247 offset:18944\n\t"
    "v_mfma_f32_32x32x16_bf16 v[96:111], a[140:143], a[0:3], v[96:111]\n\t"
    "ds_read_b64_tr_b16 a[136:137], v247 offset:17408\n\t"
    "buffer_load_dwordx4 v245, s[60:63], s73 offen offset:2048 nt lds\n\t"
    "v_mfma_f32_32x32x16_bf16 v[112:127], a[140:143], a[4:7], v[112:127]\n\t"
    "ds_read_b64_tr_b16 a[138:139], v247 offset:19456\n\t"
    "s_waitcnt lgkmcnt(15)\n\t"
    "v_mfma_f32_32x32x16_bf16 v[0:15], a[144:147], a[8:11], v[0:15]\n\t"
    "ds_read_b64_tr_b16 a[140:141], v247 offset:17920\n\t"
    "buffer_load_dwordx4 v246, s[60:63], s73 offen offset:3072 nt lds\n\t"
    "s_add_u32 s73, s73, 524288\n\t"
    "v_mfma_f32_32x32x16_bf16 v[16:31], a[144:147], a[12:15], v[16:31]\n\t"
    "ds_read_b64_tr_b16 a[142:143], v247 offset:19968\n\t"
    "v_mfma_f32_32x32x16_bf16 v[32:47], a[148:151], a[8:11], v[32:47]\n\t"
    "ds_read_b64_tr_b16 a[144:145], v247 offset:20480\n\t"
    "v_mfma_f32_32x32x16_bf16 v[48:63], a[148:151], a[12:15], v[48:63]\n\t"
    "ds_read_b64_tr_b16 a[146:147], v247 offset:22528\n\t"
    "buffer_load_dwordx4 a[224:227], v241, s[56:59], s72 offen offset:0 sc1\n\t"
    "v_mfma_f32_32x32x16_bf16 v[64:79], a[152:155], a[8:11], v[64:79]\n\t"
    "ds_read_b64_tr_b16 a[148:149], v247 offset:20992\n\t"
    "v_mfma_f32_32x32x16_bf16 v[80:95], a[152:155], a[12:15], v[80:95]\n\t"
    "ds_read_b64_tr_b16 a[150:151], v247 offset:23040\n\t"
    "buffer_load_dwordx4 a[228:231], v242, s[56:59], s72 offen offset:0 sc1\n\t"
    "v_mfma_f32_32x32x16_bf16 v[96:111], a[156:159], a[8:11], v[96:111]\n\t"
    "ds_read_b64_tr_b16 a[152:153], v247 offset:21504\n\t"
    "v_mfma_f32_32x32x16_bf16 v[112:127], a[156:159], a[12:15], v[112:127]\n\t"
    "ds_read_b64_tr_b16 a[154:155], v247 offset:23552\n\t"
    "s_waitcnt lgkmcnt(15)\n\t"
    "v_mfma_f32_32x32x16_bf16 v[0:15], a[160:163], a[16:19], v[0:15]\n\t"
    "ds_read_b64_tr_b16 a[156:157], v247 offset:22016\n\t"
    "buffer_load_dwordx4 a[232:235], v241, s[56:59], s72 offen offset:1024 sc1\n\t"
    "v_mfma_f32_32x32x16_bf16 v[16:31], a[160:163], a[20:23], v[16:31]\n\t"
    "ds_read_b64_tr_b16 a[158:159], v247 offset:24064\n\t"
    "v_mfma_f32_32x32x16_bf16 v[32:47], a[164:167], a[16:19], v[32:47]\n\t"
    "ds_read_b64_tr_b16 a[160:161], v247 offset:24576\n\t"
    "buffer_load_dwordx4 a[236:239], v242, s[56:59], s72 offen offset:1024 sc1\n\t"
    "v_mfma_f32_32x32x16_bf16 v[48:63], a[164:167], a[20:23], v[48:63]\n\t"
    "ds_read_b64_tr_b16 a[162:163], v247 offset:26624\n\t"
    "v_mfma_f32_32x32x16_bf16 v[64:79], a[168:171], a[16:19], v[64:79]\n\t"
    "ds_read_b64_tr_b16 a[164:165], v247 offset:25088\n\t"
    "v_mfma_f32_32x32x16_bf16 v[80:95], a[168:171], a[20:23], v[80:95]\n\t"
    "ds_read_b64_tr_b16 a[166:167], v247 offset:27136\n\t"
    "buffer_load_dwordx4 a[240:243], v241, s[56:59], s72 offen offset:2048 sc1\n\t"
    "v_mfma_f32_32x32x16_bf16 v[96:111], a[172:175], a[16:19], v[96:111]\n\t"
    "ds_read_b64_tr_b16 a[168:169], v247 offset:25600\n\t"
    "v_mfma_f32_32x32x16_bf16 v[112:127], a[172:175], a[20:23], v[112:127]\n\t"
    "ds_read_b64_tr_b16 a[170:171], v247 offset:27648\n\t"
    "buffer_load_dwordx4 a[244:247], v242, s[56:59], s72 offen offset:2048 sc1\n\t"
    "s_waitcnt lgkmcnt(15)\n\t"
    "v_mfma_f32_32x32x16_bf16 v[0:15], a[176:179], a[24:27], v[0:15]\n\t"
    "ds_read_b64_tr_b16 a[172:173], v247 offset:26112\n\t"
    "v_mfma_f32_32x32x16_bf16 v[16:31], a[176:179], a[28:31], v[16:31]\n\t"
    "ds_read_b64_tr_b16 a[174:175], v247 offset:28160\n\t"
    "v_mfma_f32_32x32x16_bf16 v[32:47], a[180:183], a[24:27], v[32:47]\n\t"
    "ds_read_b64_tr_b16 a[176:177], v247 offset:28672\n\t"
    "buffer_load_dwordx4 a[248:251], v241, s[56:59], s72 offen offset:3072 sc1\n\t"
    "v_mfma_f32_32x32x16_bf16 v[48:63], a[180:183], a[28:31], v[48:63]\n\t"
    "ds_read_b64_tr_b16 a[178:179], v247 offset:30720\n\t"
    "v_mfma_f32_32x32x16_bf16 v[64:79], a[184:187], a[24:27], v[64:79]\n\t"
    "ds_read_b64_tr_b16 a[180:181], v247 offset:29184\n\t"
    "buffer_load_dwordx4 a[252:255], v242, s[56:59], s72 offen offset:3072 sc1\n\t"
    "s_add_u32 s72, s72, 4096\n\t"
    "v_mfma_f32_32x32x16_bf16 v[80:95], a[184:187], a[28:31], v[80:95]\n\t"
    "ds_read_b64_tr_b16 a[182:183], v247 offset:31232\n\t"
    "v_mfma_f32_32x32x16_bf16 v[96:111], a[188:191], a[24:27], v[96:111]\n\t"
    "ds_read_b64_tr_b16 a[184:185], v247 offset:29696\n\t"
    "v_mfma_f32_32x32x16_bf16 v[112:127], a[188:191], a[28:31], v[112:127]\n\t"
    "ds_read_b64_tr_b16 a[186:187], v247 offset:31744\n\t"
    "s_waitcnt vmcnt(30) lgkmcnt(15)\n\t"
    "s_mov_b32 s90, 2\n\t"
    ".p2align 6\n\t"
    "Lm_attn_0:\n\t"
    "v_mfma_f32_32x32x16_bf16 v[0:15], a[128:131], a[32:35], v[0:15]\n\t"
    "s_waitcnt vmcnt(20)\n\t"
    "s_barrier\n\t"
    "ds_read_b64_tr_b16 a[188:189], v247 offset:30208\n\t"
    "v_mfma_f32_32x32x16_bf16 v[16:31], a[128:131], a[36:39], v[16:31]\n\t"
    "ds_read_b64_tr_b16 a[190:191], v247 offset:32256\n\t"
    "s_add_u32 m0, s76, 0\n\t"
    "s_nop 0\n\t"
    "buffer_load_dwordx4 v243, s[60:63], s73 offen offset:0 nt lds\n\t"
    "v_mfma_f32_32x32x16_bf16 v[32:47], a[132:135], a[32:35], v[32:47]\n\t"
    "ds_read_b64_tr_b16 a[128:129], v247 offset:32768\n\t"
    "v_mfma_f32_32x32x16_bf16 v[48:63], a[132:135], a[36:39], v[48:63]\n\t"
    "ds_read_b64_tr_b16 a[130:131], v247 offset:34816\n\t"
    "buffer_load_dwordx4 v244, s[60:63], s73 offen offset:1024 nt lds\n\t"
    "v_mfma_f32_32x32x16_bf16 v[64:79], a[136:139], a[32:35], v[64:79]\n\t"
    "ds_read_b64_tr_b16 a[132:133], v247 offset:33280\n\t"
    "v_mfma_f32_32x32x16_bf16 v[80:95], a[136:139], a[36:39], v[80:95]\n\t"
    "ds_read_b64_tr_b16 a[134:135], v247 offset:35328\n\t"
    "v_mfma_f32_32x32x16_bf16 v[96:111], a[140:143], a[32:35], v[96:111]\n\t"
    "ds_read_b64_tr_b16 a[136:137], v247 offset:33792\n\t"
    "buffer_load_dwordx4 v245, s[60:63], s73 offen offset:2048 nt lds\n\t"
    "v_mfma_f32_32x32x16_bf16 v[112:127], a[140:143], a[36:39], v[112:127]\n\t"
    "ds_read_b64_tr_b16 a[138:139], v247 offset:35840\n\t"
    "s_waitcnt lgkmcnt(15)\n\t"
    "v_mfma_f32_32x32x16_bf16 v[0:15], a[144:147], a[40:43], v[0:15]\n\t"
    "ds_read_b64_tr_b16 a[140:141], v247 offset:34304\n\t"
    "buffer_load_dwordx4 v246, s[60:63], s73 offen offset:3072 nt lds\n\t"
    "s_add_u32 s73, s73, 524288\n\t"
    "v_mfma_f32_32x32x16_bf16 v[16:31], a[144:147], a[44:47], v[16:31]\n\t"
    "ds_read_b64_tr_b16 a[142:143], v247 offset:36352\n\t"
    "v_mfma_f32_32x32x16_bf16 v[32:47], a[148:151], a[40:43], v[32:47]\n\t"
    "ds_read_b64_tr_b16 a[144:145], v247 offset:36864\n\t"
    "v_mfma_f32_32x32x16_bf16 v[48:63], a[148:151], a[44:47], v[48:63]\n\t"
    "ds_read_b64_tr_b16 a[146:147], v247 offset:38912\n\t"
    "buffer_load_dwordx4 a[0:3], v241, s[56:59], s72 offen offset:0 sc1\n\t"
    "v_mfma_f32_32x32x16_bf16 v[64:79], a[152:155], a[40:43], v[64:79]\n\t"
    "ds_read_b64_tr_b16 a[148:149], v247 offset:37376\n\t"
    "v_mfma_f32_32x32x16_bf16 v[80:95], a[152:155], a[44:47], v[80:95]\n\t"
    "ds_read_b64_tr_b16 a[150:151], v247 offset:39424\n\t"
    "buffer_load_dwordx4 a[4:7], v242, s[56:59], s72 offen offset:0 sc1\n\t"
    "v_mfma_f32_32x32x16_bf16 v[96:111], a[156:159], a[40:43], v[96:111]\n\t"
    "ds_read_b64_tr_b16 a[152:153], v247 offset:37888\n\t"
    "v_mfma_f32_32x32x16_bf16 v[112:127], a[156:159], a[44:47], v[112:127]\n\t"
    "ds_read_b64_tr_b16 a[154:155], v247 offset:39936\n\t"
    "s_waitcnt lgkmcnt(15)\n\t"
    "v_mfma_f32_32x32x16_bf16 v[0:15], a[160:163], a[48:51], v[0:15]\n\t"
    "ds_read_b64_tr_b16 a[156:157], v247 offset:38400\n\t"
    "buffer_load_dwordx4 a[8:11], v241, s[56:59], s72 offen offset:1024 sc1\n\t"
    "v_mfma_f32_32x32x16_bf16 v[16:31], a[160:163], a[52:55], v[16:31]\n\t"
    "ds_read_b64_tr_b16 a[158:159], v247 offset:40448\n\t"
    "v_mfma_f32_32x32x16_bf16 v[32:47], a[164:167], a[48:51], v[32:47]\n\t"
    "ds_read_b64_tr_b16 a[160:161], v247 offset:40960\n\t"
    "buffer_load_dwordx4 a[12:15], v242, s[56:59], s72 offen offset:1024 sc1\n\t"
    "v_mfma_f32_32x32x16_bf16 v[48:63], a[164:167], a[52:55], v[48:63]\n\t"
    "ds_read_b64_tr_b16 a[162:163], v247 offset:43008\n\t"
    "v_mfma_f32_32x32x16_bf16 v[64:79], a[168:171], a[48:51], v[64:79]\n\t"
    "ds_read_b64_tr_b16 a[164:165], v247 offset:41472\n\t"
    "v_mfma_f32_32x32x16_bf16 v[80:95], a[168:171], a[52:55], v[80:95]\n\t"
    "ds_read_b64_tr_b16 a[166:167], v247 offset:43520\n\t"
    "buffer_load_dwordx4 a[16:19], v241, s[56:59], s72 offen offset:2048 sc1\n\t"
    "v_mfma_f32_32x32x16_bf16 v[96:111], a[172:175], a[48:51], v[96:111]\n\t"
    "ds_read_b64_tr_b16 a[168:169], v247 offset:41984\n\t"
    "v_mfma_f32_32x32x16_bf16 v[112:127], a[172:175], a[52:55], v[112:127]\n\t"
    "ds_read_b64_tr_b16 a[170:171], v247 offset:44032\n\t"
    "buffer_load_dwordx4 a[20:23], v242, s[56:59], s72 offen offset:2048 sc1\n\t"
    "s_waitcnt lgkmcnt(15)\n\t"
    "v_mfma_f32_32x32x16_bf16 v[0:15], a[176:179], a[56:59], v[0:15]\n\t"
    "ds_read_b64_tr_b16 a[172:173], v247 offset:42496\n\t"
    "v_mfma_f32_32x32x16_bf16 v[16:31], a[176:179], a[60:63], v[16:31]\n\t"
    "ds_read_b64_tr_b16 a[174:175], v247 offset:44544\n\t"
    "v_mfma_f32_32x32x16_bf16 v[32:47], a[180:183], a[56:59], v[32:47]\n\t"
    "ds_read_b64_tr_b16 a[176:177], v247 offset:45056\n\t"
    "buffer_load_dwordx4 a[24:27], v241, s[56:59], s72 offen offset:3072 sc1\n\t"
    "v_mfma_f32_32x32x16_bf16 v[48:63], a[180:183], a[60:63], v[48:63]\n\t"
    "ds_read_b64_tr_b16 a[178:179], v247 offset:47104\n\t"
    "v_mfma_f32_32x32x16_bf16 v[64:79], a[184:187], a[56:59], v[64:79]\n\t"
    "ds_read_b64_tr_b16 a[180:181], v247 offset:45568\n\t"
    "buffer_load_dwordx4 a[28:31], v242, s[56:59], s72 offen offset:3072 sc1\n\t"
    "s_add_u32 s72, s72, 4096\n\t"
    "v_mfma_f32_32x32x16_bf16 v[80:95], a[184:187], a[60:63], v[80:95]\n\t"
    "ds_read_b64_tr_b16 a[182:183], v247 offset:47616\n\t"
    "v_mfma_f32_32x32x16_bf16 v[96:111], a[188:191], a[56:59], v[96:111]\n\t"
    "ds_read_b64_tr_b16 a[184:185], v247 offset:46080\n\t"
    "v_mfma_f32_32x32x16_bf16 v[112:127], a[188:191], a[60:63], v[112:127]\n\t"
    "ds_read_b64_tr_b16 a[186:187], v247 offset:48128\n\t"
    "s_waitcnt vmcnt(30) lgkmcnt(15)\n\t"
    "v_mfma_f32_32x32x16_bf16 v[0:15], a[128:131], a[192:195], v[0:15]\n\t"
    "s_waitcnt vmcnt(20)\n\t"
    "s_barrier\n\t"
    "ds_read_b64_tr_b16 a[188:189], v247 offset:46592\n\t"
    "v_mfma_f32_32x32x16_bf16 v[16:31], a[128:131], a[196:199], v[16:31]\n\t"
    "ds_read_b64_tr_b16 a[190:191], v247 offset:48640\n\t"
    "s_add_u32 m0, s76, 16384\n\t"
    "s_nop 0\n\t"
    "buffer_load_dwordx4 v243, s[60:63], s73 offen offset:0 nt lds\n\t"
    "v_mfma_f32_32x32x16_bf16 v[32:47], a[132:135], a[192:195], v[32:47]\n\t"
    "ds_read_b64_tr_b16 a[128:129], v247 offset:49152\n\t"
    "v_mfma_f32_32x32x16_bf16 v[48:63], a[132:135], a[196:199], v[48:63]\n\t"
    "ds_read_b64_tr_b16 a[130:131], v247 offset:51200\n\t"
    "buffer_load_dwordx4 v244, s[60:63], s73 offen offset:1024 nt lds\n\t"
    "v_mfma_f32_32x32x16_bf16 v[64:79], a[136:139], a[192:195], v[64:79]\n\t"
    "ds_read_b64_tr_b16 a[132:133], v247 offset:49664\n\t"
    "v_mfma_f32_32x32x16_bf16 v[80:95], a[136:139], a[196:199], v[80:95]\n\t"
    "ds_read_b64_tr_b16 a[134:135], v247 offset:51712\n\t"
    "v_mfma_f32_32x32x16_bf16 v[96:111], a[140:143], a[192:195], v[96:111]\n\t"
    "ds_read_b64_tr_b16 a[136:137], v247 offset:50176\n\t"
    "buffer_load_dwordx4 v245, s[60:63], s73 offen offset:2048 nt lds\n\t"
    "v_mfma_f32_32x32x16_bf16 v[112:127], a[140:143], a[196:199], v[112:127]\n\t"
    "ds_read_b64_tr_b16 a[138:139], v247 offset:52224\n\t"
    "s_waitcnt lgkmcnt(15)\n\t"
    "v_mfma_f32_32x32x16_bf16 v[0:15], a[144:147], a[200:203], v[0:15]\n\t"
    "ds_read_b64_tr_b16 a[140:141], v247 offset:50688\n\t"
    "buffer_load_dwordx4 v246, s[60:63], s73 offen offset:3072 nt lds\n\t"
    "s_add_u32 s73, s73, 524288\n\t"
    "v_mfma_f32_32x32x16_bf16 v[16:31], a[144:147], a[204:207], v[16:31]\n\t"
    "ds_read_b64_tr_b16 a[142:143], v247 offset:52736\n\t"
    "v_mfma_f32_32x32x16_bf16 v[32:47], a[148:151], a[200:203], v[32:47]\n\t"
    "ds_read_b64_tr_b16 a[144:145], v247 offset:53248\n\t"
    "v_mfma_f32_32x32x16_bf16 v[48:63], a[148:151], a[204:207], v[48:63]\n\t"
    "ds_read_b64_tr_b16 a[146:147], v247 offset:55296\n\t"
    "buffer_load_dwordx4 a[32:35], v241, s[56:59], s72 offen offset:0 sc1\n\t"
    "v_mfma_f32_32x32x16_bf16 v[64:79], a[152:155], a[200:203], v[64:79]\n\t"
    "ds_read_b64_tr_b16 a[148:149], v247 offset:53760\n\t"
    "v_mfma_f32_32x32x16_bf16 v[80:95], a[152:155], a[204:207], v[80:95]\n\t"
    "ds_read_b64_tr_b16 a[150:151], v247 offset:55808\n\t"
    "buffer_load_dwordx4 a[36:39], v242, s[56:59], s72 offen offset:0 sc1\n\t"
    "v_mfma_f32_32x32x16_bf16 v[96:111], a[156:159], a[200:203], v[96:111]\n\t"
    "ds_read_b64_tr_b16 a[152:153], v247 offset:54272\n\t"
    "v_mfma_f32_32x32x16_bf16 v[112:127], a[156:159], a[204:207], v[112:127]\n\t"
    "ds_read_b64_tr_b16 a[154:155], v247 offset:56320\n\t"
    "s_waitcnt lgkmcnt(15)\n\t"
    "v_mfma_f32_32x32x16_bf16 v[0:15], a[160:163], a[208:211], v[0:15]\n\t"
    "ds_read_b64_tr_b16 a[156:157], v247 offset:54784\n\t"
    "buffer_load_dwordx4 a[40:43], v241, s[56:59], s72 offen offset:1024 sc1\n\t"
    "v_mfma_f32_32x32x16_bf16 v[16:31], a[160:163], a[212:215], v[16:31]\n\t"
    "ds_read_b64_tr_b16 a[158:159], v247 offset:56832\n\t"
    "v_mfma_f32_32x32x16_bf16 v[32:47], a[164:167], a[208:211], v[32:47]\n\t"
    "ds_read_b64_tr_b16 a[160:161], v247 offset:57344\n\t"
    "buffer_load_dwordx4 a[44:47], v242, s[56:59], s72 offen offset:1024 sc1\n\t"
    "v_mfma_f32_32x32x16_bf16 v[48:63], a[164:167], a[212:215], v[48:63]\n\t"
    "ds_read_b64_tr_b16 a[162:163], v247 offset:59392\n\t"
    "v_mfma_f32_32x32x16_bf16 v[64:79], a[168:171], a[208:211], v[64:79]\n\t"
    "ds_read_b64_tr_b16 a[164:165], v247 offset:57856\n\t"
    "v_mfma_f32_32x32x16_bf16 v[80:95], a[168:171], a[212:215], v[80:95]\n\t"
    "ds_read_b64_tr_b16 a[166:167], v247 offset:59904\n\t"
    "buffer_load_dwordx4 a[48:51], v241, s[56:59], s72 offen offset:2048 sc1\n\t"
    "v_mfma_f32_32x32x16_bf16 v[96:111], a[172:175], a[208:211], v[96:111]\n\t"
    "ds_read_b64_tr_b16 a[168:169], v247 offset:58368\n\t"
    "v_mfma_f32_32x32x16_bf16 v[112:127], a[172:175], a[212:215], v[112:127]\n\t"
    "ds_read_b64_tr_b16 a[170:171], v247 offset:60416\n\t"
    "buffer_load_dwordx4 a[52:55], v242, s[56:59], s72 offen offset:2048 sc1\n\t"
    "s_waitcnt lgkmcnt(15)\n\t"
    "v_mfma_f32_32x32x16_bf16 v[0:15], a[176:179], a[216:219], v[0:15]\n\t"
    "ds_read_b64_tr_b16 a[172:173], v247 offset:58880\n\t"
    "v_mfma_f32_32x32x16_bf16 v[16:31], a[176:179], a[220:223], v[16:31]\n\t"
    "ds_read_b64_tr_b16 a[174:175], v247 offset:60928\n\t"
    "v_mfma_f32_32x32x16_bf16 v[32:47], a[180:183], a[216:219], v[32:47]\n\t"
    "ds_read_b64_tr_b16 a[176:177], v247 offset:61440\n\t"
    "buffer_load_dwordx4 a[56:59], v241, s[56:59], s72 offen offset:3072 sc1\n\t"
    "v_mfma_f32_32x32x16_bf16 v[48:63], a[180:183], a[220:223], v[48:63]\n\t"
    "ds_read_b64_tr_b16 a[178:179], v247 offset:63488\n\t"
    "v_mfma_f32_32x32x16_bf16 v[64:79], a[184:187], a[216:219], v[64:79]\n\t"
    "ds_read_b64_tr_b16 a[180:181], v247 offset:61952\n\t"
    "buffer_load_dwordx4 a[60:63], v242, s[56:59], s72 offen offset:3072 sc1\n\t"
    "s_add_u32 s72, s72, 4096\n\t"
    "v_mfma_f32_32x32x16_bf16 v[80:95], a[184:187], a[220:223], v[80:95]\n\t"
    "ds_read_b64_tr_b16 a[182:183], v247 offset:64000\n\t"
    "v_mfma_f32_32x32x16_bf16 v[96:111], a[188:191], a[216:219], v[96:111]\n\t"
    "ds_read_b64_tr_b16 a[184:185], v247 offset:62464\n\t"
    "v_mfma_f32_32x32x16_bf16 v[112:127], a[188:191], a[220:223], v[112:127]\n\t"
    "ds_read_b64_tr_b16 a[186:187], v247 offset:64512\n\t"
    "s_waitcnt vmcnt(30) lgkmcnt(15)\n\t"
    "v_mfma_f32_32x32x16_bf16 v[0:15], a[128:131], a[224:227], v[0:15]\n\t"
    "s_waitcnt vmcnt(20)\n\t"
    "s_barrier\n\t"
    "ds_read_b64_tr_b16 a[188:189], v247 offset:62976\n\t"
    "v_mfma_f32_32x32x16_bf16 v[16:31], a[128:131], a[228:231], v[16:31]\n\t"
    "ds_read_b64_tr_b16 a[190:191], v247 offset:65024\n\t"
    "s_add_u32 m0, s76, 32768\n\t"
    "s_nop 0\n\t"
    "buffer_load_dwordx4 v243, s[60:63], s73 offen offset:0 nt lds\n\t"
    "v_mfma_f32_32x32x16_bf16 v[32:47], a[132:135], a[224:227], v[32:47]\n\t"
    "ds_read_b64_tr_b16 a[128:129], v247 offset:0\n\t"
    "v_mfma_f32_32x32x16_bf16 v[48:63], a[132:135], a[228:231], v[48:63]\n\t"
    "ds_read_b64_tr_b16 a[130:131], v247 offset:2048\n\t"
    "buffer_load_dwordx4 v244, s[60:63], s73 offen offset:1024 nt lds\n\t"
    "v_mfma_f32_32x32x16_bf16 v[64:79], a[136:139], a[224:227], v[64:79]\n\t"
    "ds_read_b64_tr_b16 a[132:133], v247 offset:512\n\t"
    "v_mfma_f32_32x32x16_bf16 v[80:95], a[136:139], a[228:231], v[80:95]\n\t"
    "ds_read_b64_tr_b16 a[134:135], v247 offset:2560\n\t"
    "v_mfma_f32_32x32x16_bf16 v[96:111], a[140:143], a[224:227], v[96:111]\n\t"
    "ds_read_b64_tr_b16 a[136:137], v247 offset:1024\n\t"
    "buffer_load_dwordx4 v245, s[60:63], s73 offen offset:2048 nt lds\n\t"
    "v_mfma_f32_32x32x16_bf16 v[112:127], a[140:143], a[228:231], v[112:127]\n\t"
    "ds_read_b64_tr_b16 a[138:139], v247 offset:3072\n\t"
    "s_waitcnt lgkmcnt(15)\n\t"
    "v_mfma_f32_32x32x16_bf16 v[0:15], a[144:147], a[232:235], v[0:15]\n\t"
    "ds_read_b64_tr_b16 a[140:141], v247 offset:1536\n\t"
    "buffer_load_dwordx4 v246, s[60:63], s73 offen offset:3072 nt lds\n\t"
    "s_add_u32 s73, s73, 524288\n\t"
    "v_mfma_f32_32x32x16_bf16 v[16:31], a[144:147], a[236:239], v[16:31]\n\t"
    "ds_read_b64_tr_b16 a[142:143], v247 offset:3584\n\t"
    "v_mfma_f32_32x32x16_bf16 v[32:47], a[148:151], a[232:235], v[32:47]\n\t"
    "ds_read_b64_tr_b16 a[144:145], v247 offset:4096\n\t"
    "v_mfma_f32_32x32x16_bf16 v[48:63], a[148:151], a[236:239], v[48:63]\n\t"
    "ds_read_b64_tr_b16 a[146:147], v247 offset:6144\n\t"
    "buffer_load_dwordx4 a[192:195], v241, s[56:59], s72 offen offset:0 sc1\n\t"
    "v_mfma_f32_32x32x16_bf16 v[64:79], a[152:155], a[232:235], v[64:79]\n\t"
    "ds_read_b64_tr_b16 a[148:149], v247 offset:4608\n\t"
    "v_mfma_f32_32x32x16_bf16 v[80:95], a[152:155], a[236:239], v[80:95]\n\t"
    "ds_read_b64_tr_b16 a[150:151], v247 offset:6656\n\t"
    "buffer_load_dwordx4 a[196:199], v242, s[56:59], s72 offen offset:0 sc1\n\t"
    "v_mfma_f32_32x32x16_bf16 v[96:111], a[156:159], a[232:235], v[96:111]\n\t"
    "ds_read_b64_tr_b16 a[152:153], v247 offset:5120\n\t"
    "v_mfma_f32_32x32x16_bf16 v[112:127], a[156:159], a[236:239], v[112:127]\n\t"
    "ds_read_b64_tr_b16 a[154:155], v247 offset:7168\n\t"
    "s_waitcnt lgkmcnt(15)\n\t"
    "v_mfma_f32_32x32x16_bf16 v[0:15], a[160:163], a[240:243], v[0:15]\n\t"
    "ds_read_b64_tr_b16 a[156:157], v247 offset:5632\n\t"
    "buffer_load_dwordx4 a[200:203], v241, s[56:59], s72 offen offset:1024 sc1\n\t"
    "v_mfma_f32_32x32x16_bf16 v[16:31], a[160:163], a[244:247], v[16:31]\n\t"
    "ds_read_b64_tr_b16 a[158:159], v247 offset:7680\n\t"
    "v_mfma_f32_32x32x16_bf16 v[32:47], a[164:167], a[240:243], v[32:47]\n\t"
    "ds_read_b64_tr_b16 a[160:161], v247 offset:8192\n\t"
    "buffer_load_dwordx4 a[204:207], v242, s[56:59], s72 offen offset:1024 sc1\n\t"
    "v_mfma_f32_32x32x16_bf16 v[48:63], a[164:167], a[244:247], v[48:63]\n\t"
    "ds_read_b64_tr_b16 a[162:163], v247 offset:10240\n\t"
    "v_mfma_f32_32x32x16_bf16 v[64:79], a[168:171], a[240:243], v[64:79]\n\t"
    "ds_read_b64_tr_b16 a[164:165], v247 offset:8704\n\t"
    "v_mfma_f32_32x32x16_bf16 v[80:95], a[168:171], a[244:247], v[80:95]\n\t"
    "ds_read_b64_tr_b16 a[166:167], v247 offset:10752\n\t"
    "buffer_load_dwordx4 a[208:211], v241, s[56:59], s72 offen offset:2048 sc1\n\t"
    "v_mfma_f32_32x32x16_bf16 v[96:111], a[172:175], a[240:243], v[96:111]\n\t"
    "ds_read_b64_tr_b16 a[168:169], v247 offset:9216\n\t"
    "v_mfma_f32_32x32x16_bf16 v[112:127], a[172:175], a[244:247], v[112:127]\n\t"
    "ds_read_b64_tr_b16 a[170:171], v247 offset:11264\n\t"
    "buffer_load_dwordx4 a[212:215], v242, s[56:59], s72 offen offset:2048 sc1\n\t"
    "s_waitcnt lgkmcnt(15)\n\t"
    "v_mfma_f32_32x32x16_bf16 v[0:15], a[176:179], a[248:251], v[0:15]\n\t"
    "ds_read_b64_tr_b16 a[172:173], v247 offset:9728\n\t"
    "v_mfma_f32_32x32x16_bf16 v[16:31], a[176:179], a[252:255], v[16:31]\n\t"
    "ds_read_b64_tr_b16 a[174:175], v247 offset:11776\n\t"
    "v_mfma_f32_32x32x16_bf16 v[32:47], a[180:183], a[248:251], v[32:47]\n\t"
    "ds_read_b64_tr_b16 a[176:177], v247 offset:12288\n\t"
    "buffer_load_dwordx4 a[216:219], v241, s[56:59], s72 offen offset:3072 sc1\n\t"
    "v_mfma_f32_32x32x16_bf16 v[48:63], a[180:183], a[252:255], v[48:63]\n\t"
    "ds_read_b64_tr_b16 a[178:179], v247 offset:14336\n\t"
    "v_mfma_f32_32x32x16_bf16 v[64:79], a[184:187], a[248:251], v[64:79]\n\t"
    "ds_read_b64_tr_b16 a[180:181], v247 offset:12800\n\t"
    "buffer_load_dwordx4 a[220:223], v242, s[56:59], s72 offen offset:3072 sc1\n\t"
    "s_add_u32 s72, s72, 4096\n\t"
    "v_mfma_f32_32x32x16_bf16 v[80:95], a[184:187], a[252:255], v[80:95]\n\t"
    "ds_read_b64_tr_b16 a[182:183], v247 offset:14848\n\t"
    "v_mfma_f32_32x32x16_bf16 v[96:111], a[188:191], a[248:251], v[96:111]\n\t"
    "ds_read_b64_tr_b16 a[184:185], v247 offset:13312\n\t"
    "v_mfma_f32_32x32x16_bf16 v[112:127], a[188:191], a[252:255], v[112:127]\n\t"
    "ds_read_b64_tr_b16 a[186:187], v247 offset:15360\n\t"
    "s_waitcnt vmcnt(30) lgkmcnt(15)\n\t"
    "v_mfma_f32_32x32x16_bf16 v[0:15], a[128:131], a[0:3], v[0:15]\n\t"
    "s_waitcnt vmcnt(20)\n\t"
    "s_barrier\n\t"
    "ds_read_b64_tr_b16 a[188:189], v247 offset:13824\n\t"
    "v_mfma_f32_32x32x16_bf16 v[16:31], a[128:131], a[4:7], v[16:31]\n\t"
    "ds_read_b64_tr_b16 a[190:191], v247 offset:15872\n\t"
    "s_add_u32 m0, s76, 49152\n\t"
    "s_nop 0\n\t"
    "buffer_load_dwordx4 v243, s[60:63], s73 offen offset:0 nt lds\n\t"
    "v_mfma_f32_32x32x16_bf16 v[32:47], a[132:135], a[0:3], v[32:47]\n\t"
    "ds_read_b64_tr_b16 a[128:129], v247 offset:16384\n\t"
    "v_mfma_f32_32x32x16_bf16 v[48:63], a[132:135], a[4:7], v[48:63]\n\t"
    "ds_read_b64_tr_b16 a[130:131], v247 offset:18432\n\t"
    "buffer_load_dwordx4 v244, s[60:63], s73 offen offset:1024 nt lds\n\t"
    "v_mfma_f32_32x32x16_bf16 v[64:79], a[136:139], a[0:3], v[64:79]\n\t"
    "ds_read_b64_tr_b16 a[132:133], v247 offset:16896\n\t"
    "v_mfma_f32_32x32x16_bf16 v[80:95], a[136:139], a[4:7], v[80:95]\n\t"
    "ds_read_b64_tr_b16 a[134:135], v247 offset:18944\n\t"
    "v_mfma_f32_32x32x16_bf16 v[96:111], a[140:143], a[0:3], v[96:111]\n\t"
    "ds_read_b64_tr_b16 a[136:137], v247 offset:17408\n\t"
    "buffer_load_dwordx4 v245, s[60:63], s73 offen offset:2048 nt lds\n\t"
    "v_mfma_f32_32x32x16_bf16 v[112:127], a[140:143], a[4:7], v[112:127]\n\t"
    "ds_read_b64_tr_b16 a[138:139], v247 offset:19456\n\t"
    "s_waitcnt lgkmcnt(15)\n\t"
    "v_mfma_f32_32x32x16_bf16 v[0:15], a[144:147], a[8:11], v[0:15]\n\t"
    "ds_read_b64_tr_b16 a[140:141], v247 offset:17920\n\t"
    "buffer_load_dwordx4 v246, s[60:63], s73 offen offset:3072 nt lds\n\t"
    "s_add_u32 s73, s73, 524288\n\t"
    "v_mfma_f32_32x32x16_bf16 v[16:31], a[144:147], a[12:15], v[16:31]\n\t"
    "ds_read_b64_tr_b16 a[142:143], v247 offset:19968\n\t"
    "v_mfma_f32_32x32x16_bf16 v[32:47], a[148:151], a[8:11], v[32:47]\n\t"
    "ds_read_b64_tr_b16 a[144:145], v247 offset:20480\n\t"
    "v_mfma_f32_32x32x16_bf16 v[48:63], a[148:151], a[12:15], v[48:63]\n\t"
    "ds_read_b64_tr_b16 a[146:147], v247 offset:22528\n\t"
    "buffer_load_dwordx4 a[224:227], v241, s[56:59], s72 offen offset:0 sc1\n\t"
    "v_mfma_f32_32x32x16_bf16 v[64:79], a[152:155], a[8:11], v[64:79]\n\t"
    "ds_read_b64_tr_b16 a[148:149], v247 offset:20992\n\t"
    "v_mfma_f32_32x32x16_bf16 v[80:95], a[152:155], a[12:15], v[80:95]\n\t"
    "ds_read_b64_tr_b16 a[150:151], v247 offset:23040\n\t"
    "buffer_load_dwordx4 a[228:231], v242, s[56:59], s72 offen offset:0 sc1\n\t"
    "v_mfma_f32_32x32x16_bf16 v[96:111], a[156:159], a[8:11], v[96:111]\n\t"
    "ds_read_b64_tr_b16 a[152:153], v247 offset:21504\n\t"
    "v_mfma_f32_32x32x16_bf16 v[112:127], a[156:159], a[12:15], v[112:127]\n\t"
    "ds_read_b64_tr_b16 a[154:155], v247 offset:23552\n\t"
    "s_waitcnt lgkmcnt(15)\n\t"
    "v_mfma_f32_32x32x16_bf16 v[0:15], a[160:163], a[16:19], v[0:15]\n\t"
    "ds_read_b64_tr_b16 a[156:157], v247 offset:22016\n\t"
    "buffer_load_dwordx4 a[232:235], v241, s[56:59], s72 offen offset:1024 sc1\n\t"
    "v_mfma_f32_32x32x16_bf16 v[16:31], a[160:163], a[20:23], v[16:31]\n\t"
    "ds_read_b64_tr_b16 a[158:159], v247 offset:24064\n\t"
    "v_mfma_f32_32x32x16_bf16 v[32:47], a[164:167], a[16:19], v[32:47]\n\t"
    "ds_read_b64_tr_b16 a[160:161], v247 offset:24576\n\t"
    "buffer_load_dwordx4 a[236:239], v242, s[56:59], s72 offen offset:1024 sc1\n\t"
    "v_mfma_f32_32x32x16_bf16 v[48:63], a[164:167], a[20:23], v[48:63]\n\t"
    "ds_read_b64_tr_b16 a[162:163], v247 offset:26624\n\t"
    "v_mfma_f32_32x32x16_bf16 v[64:79], a[168:171], a[16:19], v[64:79]\n\t"
    "ds_read_b64_tr_b16 a[164:165], v247 offset:25088\n\t"
    "v_mfma_f32_32x32x16_bf16 v[80:95], a[168:171], a[20:23], v[80:95]\n\t"
    "ds_read_b64_tr_b16 a[166:167], v247 offset:27136\n\t"
    "buffer_load_dwordx4 a[240:243], v241, s[56:59], s72 offen offset:2048 sc1\n\t"
    "v_mfma_f32_32x32x16_bf16 v[96:111], a[172:175], a[16:19], v[96:111]\n\t"
    "ds_read_b64_tr_b16 a[168:169], v247 offset:25600\n\t"
    "v_mfma_f32_32x32x16_bf16 v[112:127], a[172:175], a[20:23], v[112:127]\n\t"
    "ds_read_b64_tr_b16 a[170:171], v247 offset:27648\n\t"
    "buffer_load_dwordx4 a[244:247], v242, s[56:59], s72 offen offset:2048 sc1\n\t"
    "s_waitcnt lgkmcnt(15)\n\t"
    "v_mfma_f32_32x32x16_bf16 v[0:15], a[176:179], a[24:27], v[0:15]\n\t"
    "ds_read_b64_tr_b16 a[172:173], v247 offset:26112\n\t"
    "v_mfma_f32_32x32x16_bf16 v[16:31], a[176:179], a[28:31], v[16:31]\n\t"
    "ds_read_b64_tr_b16 a[174:175], v247 offset:28160\n\t"
    "v_mfma_f32_32x32x16_bf16 v[32:47], a[180:183], a[24:27], v[32:47]\n\t"
    "ds_read_b64_tr_b16 a[176:177], v247 offset:28672\n\t"
    "buffer_load_dwordx4 a[248:251], v241, s[56:59], s72 offen offset:3072 sc1\n\t"
    "v_mfma_f32_32x32x16_bf16 v[48:63], a[180:183], a[28:31], v[48:63]\n\t"
    "ds_read_b64_tr_b16 a[178:179], v247 offset:30720\n\t"
    "v_mfma_f32_32x32x16_bf16 v[64:79], a[184:187], a[24:27], v[64:79]\n\t"
    "ds_read_b64_tr_b16 a[180:181], v247 offset:29184\n\t"
    "buffer_load_dwordx4 a[252:255], v242, s[56:59], s72 offen offset:3072 sc1\n\t"
    "s_add_u32 s72, s72, 4096\n\t"
    "v_mfma_f32_32x32x16_bf16 v[80:95], a[184:187], a[28:31], v[80:95]\n\t"
    "ds_read_b64_tr_b16 a[182:183], v247 offset:31232\n\t"
    "v_mfma_f32_32x32x16_bf16 v[96:111], a[188:191], a[24:27], v[96:111]\n\t"
    "ds_read_b64_tr_b16 a[184:185], v247 offset:29696\n\t"
    "v_mfma_f32_32x32x16_bf16 v[112:127], a[188:191], a[28:31], v[112:127]\n\t"
    "ds_read_b64_tr_b16 a[186:187], v247 offset:31744\n\t"
    "s_waitcnt vmcnt(30) lgkmcnt(15)\n\t"
    "s_sub_u32 s90, s90, 1\n\t"
    "s_cmp_lg_u32 s90, 0\n\t"
    "s_cbranch_scc1 Lm_attn_0\n\t"
    "v_mfma_f32_32x32x16_bf16 v[0:15], a[128:131], a[32:35], v[0:15]\n\t"
    "s_waitcnt vmcnt(20)\n\t"
    "s_barrier\n\t"
    "ds_read_b64_tr_b16 a[188:189], v247 offset:30208\n\t"
    "v_mfma_f32_32x32x16_bf16 v[16:31], a[128:131], a[36:39], v[16:31]\n\t"
    "ds_read_b64_tr_b16 a[190:191], v247 offset:32256\n\t"
    "s_add_u32 m0, s77, 0\n\t"
    "s_nop 0\n\t"
    "buffer_load_dwordx4 v233, s[64:67], s74 offen offset:0 nt lds\n\t"
    "v_mfma_f32_32x32x16_bf16 v[32:47], a[132:135], a[32:35], v[32:47]\n\t"
    "ds_read_b64_tr_b16 a[128:129], v247 offset:32768\n\t"
    "v_mfma_f32_32x32x16_bf16 v[48:63], a[132:135], a[36:39], v[48:63]\n\t"
    "ds_read_b64_tr_b16 a[130:131], v247 offset:34816\n\t"
    "v_mfma_f32_32x32x16_bf16 v[64:79], a[136:139], a[32:35], v[64:79]\n\t"
    "ds_read_b64_tr_b16 a[132:133], v247 offset:33280\n\t"
    "buffer_load_dwordx4 v234, s[64:67], s74 offen offset:1024 nt lds\n\t"
    "v_mfma_f32_32x32x16_bf16 v[80:95], a[136:139], a[36:39], v[80:95]\n\t"
    "ds_read_b64_tr_b16 a[134:135], v247 offset:35328\n\t"
    "v_mfma_f32_32x32x16_bf16 v[96:111], a[140:143], a[32:35], v[96:111]\n\t"
    "ds_read_b64_tr_b16 a[136:137], v247 offset:33792\n\t"
    "v_mfma_f32_32x32x16_bf16 v[112:127], a[140:143], a[36:39], v[112:127]\n\t"
    "ds_read_b64_tr_b16 a[138:139], v247 offset:35840\n\t"
    "buffer_load_dwordx4 v235, s[64:67], s74 offen offset:2048 nt lds\n\t"
    "s_waitcnt lgkmcnt(15)\n\t"
    "v_mfma_f32_32x32x16_bf16 v[0:15], a[144:147], a[40:43], v[0:15]\n\t"
    "ds_read_b64_tr_b16 a[140:141], v247 offset:34304\n\t"
    "v_mfma_f32_32x32x16_bf16 v[16:31], a[144:147], a[44:47], v[16:31]\n\t"
    "ds_read_b64_tr_b16 a[142:143], v247 offset:36352\n\t"
    "v_mfma_f32_32x32x16_bf16 v[32:47], a[148:151], a[40:43], v[32:47]\n\t"
    "ds_read_b64_tr_b16 a[144:145], v247 offset:36864\n\t"
    "buffer_load_dwordx4 v236, s[64:67], s74 offen offset:3072 nt lds\n\t"
    "s_add_u32 s74, s74, 16384\n\t"
    "v_mfma_f32_32x32x16_bf16 v[48:63], a[148:151], a[44:47], v[48:63]\n\t"
    "ds_read_b64_tr_b16 a[146:147], v247 offset:38912\n\t"
    "v_mfma_f32_32x32x16_bf16 v[64:79], a[152:155], a[40:43], v[64:79]\n\t"
    "ds_read_b64_tr_b16 a[148:149], v247 offset:37376\n\t"
    "v_mfma_f32_32x32x16_bf16 v[80:95], a[152:155], a[44:47], v[80:95]\n\t"
    "ds_read_b64_tr_b16 a[150:151], v247 offset:39424\n\t"
    "v_mfma_f32_32x32x16_bf16 v[96:111], a[156:159], a[40:43], v[96:111]\n\t"
    "ds_read_b64_tr_b16 a[152:153], v247 offset:37888\n\t"
    "s_add_u32 m0, s78, 0\n\t"
    "s_nop 0\n\t"
    "buffer_load_dwordx4 v237, s[68:71], s75 offen offset:0 nt lds\n\t"
    "v_mfma_f32_32x32x16_bf16 v[112:127], a[156:159], a[44:47], v[112:127]\n\t"
    "ds_read_b64_tr_b16 a[154:155], v247 offset:39936\n\t"
    "s_waitcnt lgkmcnt(15)\n\t"
    "v_mfma_f32_32x32x16_bf16 v[0:15], a[160:163], a[48:51], v[0:15]\n\t"
    "ds_read_b64_tr_b16 a[156:157], v247 offset:38400\n\t"
    "v_mfma_f32_32x32x16_bf16 v[16:31], a[160:163], a[52:55], v[16:31]\n\t"
    "ds_read_b64_tr_b16 a[158:159], v247 offset:40448\n\t"
    "buffer_load_dwordx4 v238, s[68:71], s75 offen offset:1024 nt lds\n\t"
    "v_mfma_f32_32x32x16_bf16 v[32:47], a[164:167], a[48:51], v[32:47]\n\t"
    "ds_read_b64_tr_b16 a[160:161], v247 offset:40960\n\t"
    "v_mfma_f32_32x32x16_bf16 v[48:63], a[164:167], a[52:55], v[48:63]\n\t"
    "ds_read_b64_tr_b16 a[162:163], v247 offset:43008\n\t"
    "v_mfma_f32_32x32x16_bf16 v[64:79], a[168:171], a[48:51], v[64:79]\n\t"
    "ds_read_b64_tr_b16 a[164:165], v247 offset:41472\n\t"
    "buffer_load_dwordx4 v237, s[68:71], s75 offen offset:2048 nt lds\n\t"
    "v_mfma_f32_32x32x16_bf16 v[80:95], a[168:171], a[52:55], v[80:95]\n\t"
    "ds_read_b64_tr_b16 a[166:167], v247 offset:43520\n\t"
    "v_mfma_f32_32x32x16_bf16 v[96:111], a[172:175], a[48:51], v[96:111]\n\t"
    "ds_read_b64_tr_b16 a[168:169], v247 offset:41984\n\t"
    "v_mfma_f32_32x32x16_bf16 v[112:127], a[172:175], a[52:55], v[112:127]\n\t"
    "ds_read_b64_tr_b16 a[170:171], v247 offset:44032\n\t"
    "s_waitcnt lgkmcnt(15)\n\t"
    "v_mfma_f32_32x32x16_bf16 v[0:15], a[176:179], a[56:59], v[0:15]\n\t"
    "ds_read_b64_tr_b16 a[172:173], v247 offset:42496\n\t"
    "buffer_load_dwordx4 v238, s[68:71], s75 offen offset:3072 nt lds\n\t"
    "s_add_u32 s75, s75, 16384\n\t"
    "v_mfma_f32_32x32x16_bf16 v[16:31], a[176:179], a[60:63], v[16:31]\n\t"
    "ds_read_b64_tr_b16 a[174:175], v247 offset:44544\n\t"
    "v_mfma_f32_32x32x16_bf16 v[32:47], a[180:183], a[56:59], v[32:47]\n\t"
    "ds_read_b64_tr_b16 a[176:177], v247 offset:45056\n\t"
    "v_mfma_f32_32x32x16_bf16 v[48:63], a[180:183], a[60:63], v[48:63]\n\t"
    "ds_read_b64_tr_b16 a[178:179], v247 offset:47104\n\t"
    "s_add_u32 m0, s77, 16384\n\t"
    "s_nop 0\n\t"
    "buffer_load_dwordx4 v233, s[64:67], s74 offen offset:0 nt lds\n\t"
    "v_mfma_f32_32x32x16_bf16 v[64:79], a[184:187], a[56:59], v[64:79]\n\t"
    "ds_read_b64_tr_b16 a[180:181], v247 offset:45568\n\t"
    "v_mfma_f32_32x32x16_bf16 v[80:95], a[184:187], a[60:63], v[80:95]\n\t"
    "ds_read_b64_tr_b16 a[182:183], v247 offset:47616\n\t"
    "v_mfma_f32_32x32x16_bf16 v[96:111], a[188:191], a[56:59], v[96:111]\n\t"
    "ds_read_b64_tr_b16 a[184:185], v247 offset:46080\n\t"
    "buffer_load_dwordx4 v234, s[64:67], s74 offen offset:1024 nt lds\n\t"
    "v_mfma_f32_32x32x16_bf16 v[112:127], a[188:191], a[60:63], v[112:127]\n\t"
    "ds_read_b64_tr_b16 a[186:187], v247 offset:48128\n\t"
    "s_waitcnt vmcnt(28) lgkmcnt(15)\n\t"
    "v_mfma_f32_32x32x16_bf16 v[0:15], a[128:131], a[192:195], v[0:15]\n\t"
    "s_waitcnt vmcnt(18)\n\t"
    "s_barrier\n\t"
    "ds_read_b64_tr_b16 a[188:189], v247 offset:46592\n\t"
    "v_mfma_f32_32x32x16_bf16 v[16:31], a[128:131], a[196:199], v[16:31]\n\t"
    "ds_read_b64_tr_b16 a[190:191], v247 offset:48640\n\t"
    "buffer_load_dwordx4 v235, s[64:67], s74 offen offset:2048 nt lds\n\t"
    "v_mfma_f32_32x32x16_bf16 v[32:47], a[132:135], a[192:195], v[32:47]\n\t"
    "ds_read_b64_tr_b16 a[128:129], v247 offset:49152\n\t"
    "v_mfma_f32_32x32x16_bf16 v[48:63], a[132:135], a[196:199], v[48:63]\n\t"
    "ds_read_b64_tr_b16 a[130:131], v247 offset:51200\n\t"
    "v_mfma_f32_32x32x16_bf16 v[64:79], a[136:139], a[192:195], v[64:79]\n\t"
    "ds_read_b64_tr_b16 a[132:133], v247 offset:49664\n\t"
    "v_mfma_f32_32x32x16_bf16 v[80:95], a[136:139], a[196:199], v[80:95]\n\t"
    "ds_read_b64_tr_b16 a[134:135], v247 offset:51712\n\t"
    "buffer_load_dwordx4 v236, s[64:67], s74 offen offset:3072 nt lds\n\t"
    "s_add_u32 s74, s74, 16384\n\t"
    "v_mfma_f32_32x32x16_bf16 v[96:111], a[140:143], a[192:195], v[96:111]\n\t"
    "ds_read_b64_tr_b16 a[136:137], v247 offset:50176\n\t"
    "v_mfma_f32_32x32x16_bf16 v[112:127], a[140:143], a[196:199], v[112:127]\n\t"
    "ds_read_b64_tr_b16 a[138:139], v247 offset:52224\n\t"
    "s_waitcnt lgkmcnt(15)\n\t"
    "v_mfma_f32_32x32x16_bf16 v[0:15], a[144:147], a[200:203], v[0:15]\n\t"
    "ds_read_b64_tr_b16 a[140:141], v247 offset:50688\n\t"
    "s_add_u32 m0, s77, 32768\n\t"
    "s_nop 0\n\t"
    "buffer_load_dwordx4 v233, s[64:67], s74 offen offset:0 nt lds\n\t"
    "v_mfma_f32_32x32x16_bf16 v[16:31], a[144:147], a[204:207], v[16:31]\n\t"
    "ds_read_b64_tr_b16 a[142:143], v247 offset:52736\n\t"
    "v_mfma_f32_32x32x16_bf16 v[32:47], a[148:151], a[200:203], v[32:47]\n\t"
    "ds_read_b64_tr_b16 a[144:145], v247 offset:53248\n\t"
    "v_mfma_f32_32x32x16_bf16 v[48:63], a[148:151], a[204:207], v[48:63]\n\t"
    "ds_read_b64_tr_b16 a[146:147], v247 offset:55296\n\t"
    "buffer_load_dwordx4 v234, s[64:67], s74 offen offset:1024 nt lds\n\t"
    "v_mfma_f32_32x32x16_bf16 v[64:79], a[152:155], a[200:203], v[64:79]\n\t"
    "ds_read_b64_tr_b16 a[148:149], v247 offset:53760\n\t"
    "v_mfma_f32_32x32x16_bf16 v[80:95], a[152:155], a[204:207], v[80:95]\n\t"
    "ds_read_b64_tr_b16 a[150:151], v247 offset:55808\n\t"
    "v_mfma_f32_32x32x16_bf16 v[96:111], a[156:159], a[200:203], v[96:111]\n\t"
    "ds_read_b64_tr_b16 a[152:153], v247 offset:54272\n\t"
    "v_mfma_f32_32x32x16_bf16 v[112:127], a[156:159], a[204:207], v[112:127]\n\t"
    "ds_read_b64_tr_b16 a[154:155], v247 offset:56320\n\t"
    "buffer_load_dwordx4 v235, s[64:67], s74 offen offset:2048 nt lds\n\t"
    "s_waitcnt lgkmcnt(15)\n\t"
    "v_mfma_f32_32x32x16_bf16 v[0:15], a[160:163], a[208:211], v[0:15]\n\t"
    "ds_read_b64_tr_b16 a[156:157], v247 offset:54784\n\t"
    "v_mfma_f32_32x32x16_bf16 v[16:31], a[160:163], a[212:215], v[16:31]\n\t"
    "ds_read_b64_tr_b16 a[158:159], v247 offset:56832\n\t"
    "v_mfma_f32_32x32x16_bf16 v[32:47], a[164:167], a[208:211], v[32:47]\n\t"
    "ds_read_b64_tr_b16 a[160:161], v247 offset:57344\n\t"
    "buffer_load_dwordx4 v236, s[64:67], s74 offen offset:3072 nt lds\n\t"
    "s_add_u32 s74, s74, 16384\n\t"
    "v_mfma_f32_32x32x16_bf16 v[48:63], a[164:167], a[212:215], v[48:63]\n\t"
    "ds_read_b64_tr_b16 a[162:163], v247 offset:59392\n\t"
    "v_mfma_f32_32x32x16_bf16 v[64:79], a[168:171], a[208:211], v[64:79]\n\t"
    "ds_read_b64_tr_b16 a[164:165], v247 offset:57856\n\t"
    "v_mfma_f32_32x32x16_bf16 v[80:95], a[168:171], a[212:215], v[80:95]\n\t"
    "ds_read_b64_tr_b16 a[166:167], v247 offset:59904\n\t"
    "s_add_u32 m0, s78, 16384\n\t"
    "s_nop 0\n\t"
    "buffer_load_dwordx4 v237, s[68:71], s75 offen offset:0 nt lds\n\t"
    "v_mfma_f32_32x32x16_bf16 v[96:111], a[172:175], a[208:211], v[96:111]\n\t"
    "ds_read_b64_tr_b16 a[168:169], v247 offset:58368\n\t"
    "v_mfma_f32_32x32x16_bf16 v[112:127], a[172:175], a[212:215], v[112:127]\n\t"
    "ds_read_b64_tr_b16 a[170:171], v247 offset:60416\n\t"
    "s_waitcnt lgkmcnt(15)\n\t"
    "v_mfma_f32_32x32x16_bf16 v[0:15], a[176:179], a[216:219], v[0:15]\n\t"
    "ds_read_b64_tr_b16 a[172:173], v247 offset:58880\n\t"
    "buffer_load_dwordx4 v238, s[68:71], s75 offen offset:1024 nt lds\n\t"
    "v_mfma_f32_32x32x16_bf16 v[16:31], a[176:179], a[220:223], v[16:31]\n\t"
    "ds_read_b64_tr_b16 a[174:175], v247 offset:60928\n\t"
    "v_mfma_f32_32x32x16_bf16 v[32:47], a[180:183], a[216:219], v[32:47]\n\t"
    "ds_read_b64_tr_b16 a[176:177], v247 offset:61440\n\t"
    "v_mfma_f32_32x32x16_bf16 v[48:63], a[180:183], a[220:223], v[48:63]\n\t"
    "ds_read_b64_tr_b16 a[178:179], v247 offset:63488\n\t"
    "v_mfma_f32_32x32x16_bf16 v[64:79], a[184:187], a[216:219], v[64:79]\n\t"
    "ds_read_b64_tr_b16 a[180:181], v247 offset:61952\n\t"
    "buffer_load_dwordx4 v237, s[68:71], s75 offen offset:2048 nt lds\n\t"
    "v_mfma_f32_32x32x16_bf16 v[80:95], a[184:187], a[220:223], v[80:95]\n\t"
    "ds_read_b64_tr_b16 a[182:183], v247 offset:64000\n\t"
    "v_mfma_f32_32x32x16_bf16 v[96:111], a[188:191], a[216:219], v[96:111]\n\t"
    "ds_read_b64_tr_b16 a[184:185], v247 offset:62464\n\t"
    "v_mfma_f32_32x32x16_bf16 v[112:127], a[188:191], a[220:223], v[112:127]\n\t"
    "ds_read_b64_tr_b16 a[186:187], v247 offset:64512\n\t"
    "buffer_load_dwordx4 v238, s[68:71], s75 offen offset:3072 nt lds\n\t"
    "s_add_u32 s75, s75, 16384\n\t"
    "s_waitcnt vmcnt(20) lgkmcnt(4)\n\t"
    "v_mfma_f32_32x32x16_bf16 v[0:15], a[128:131], a[224:227], v[0:15]\n\t"
    "s_waitcnt vmcnt(16)\n\t"
    "s_barrier\n\t"
    "v_mov_b32 v239, 0\n\t"
    "v_mov_b32 v240, 0\n\t"
    "s_lshr_b32 s88, s35, 3\n\t"
    "s_lshl_b32 s88, s88, 2\n\t"
    "s_add_u32 s88, s88, s34\n\t"
    "s_lshl_b32 s88, s88, 3\n\t"
    "s_and_b32 s89, s35, 7\n\t"
    "s_add_u32 s88, s88, s89\n\t"
    "s_lshl_b32 s88, s88, 13\n\t"
    "v_lshlrev_b32 v243, 7, v251\n\t"
    "v_add_u32 v243, s88, v243\n\t"
    "v_min_u32 v244, 7339904, v243\n\t"
    "s_lshr_b32 s88, s35, 3\n\t"
    "s_lshl_b32 s88, s88, 2\n\t"
    "s_add_u32 s88, s88, s34\n\t"
    "s_lshl_b32 s88, s88, 6\n\t"
    "v_add_u32 v246, s88, v251\n\t"
    "v_min_u32 v246, 703, v246\n\t"
    "v_lshlrev_b32 v246, 16, v246\n\t"
    "s_sub_u32 s36, s44, 4194304\n\t"
    "s_subb_u32 s37, s45, 0\n\t"
    "s_add_u32 s38, s44, 56623104\n\t"
    "s_addc_u32 s39, s45, 0\n\t"
    "s_mov_b32 s40, 0\n\t"
    "ds_read_b64_tr_b16 a[188:189], v247 offset:62976\n\t"
    "v_mfma_f32_32x32x16_bf16 v[16:31], a[128:131], a[228:231], v[16:31]\n\t"
    "ds_read_b64_tr_b16 a[190:191], v247 offset:65024\n\t"
    "v_mfma_f32_32x32x16_bf16 v[0:15], a[144:147], a[232:235], v[0:15]\n\t"
    "ds_read_b128 a[64:67], v224 offset:0\n\t"
    "s_add_u32 m0, s77, 49152\n\t"
    "s_nop 0\n\t"
    "buffer_load_dwordx4 v233, s[64:67], s74 offen offset:0 nt lds\n\t"
    "v_mfma_f32_32x32x16_bf16 v[16:31], a[144:147], a[236:239], v[16:31]\n\t"
    "ds_read_b128 a[68:71], v225 offset:0\n\t"
    "v_mfma_f32_32x32x16_bf16 v[0:15], a[160:163], a[240:243], v[0:15]\n\t"
    "ds_read_b128 a[72:75], v226 offset:0\n\t"
    "v_mfma_f32_32x32x16_bf16 v[16:31], a[160:163], a[244:247], v[16:31]\n\t"
    "ds_read_b128 a[76:79], v227 offset:0\n\t"
    "v_mfma_f32_32x32x16_bf16 v[0:15], a[176:179], a[248:251], v[0:15]\n\t"
    "ds_read_b128 a[80:83], v228 offset:0\n\t"
    "buffer_load_dwordx4 v234, s[64:67], s74 offen offset:1024 nt lds\n\t"
    "v_mfma_f32_32x32x16_bf16 v[16:31], a[176:179], a[252:255], v[16:31]\n\t"
    "ds_read_b128 a[84:87], v229 offset:0\n\t"
    "s_waitcnt lgkmcnt(10)\n\t"
    "v_mfma_f32_32x32x16_bf16 v[32:47], a[132:135], a[224:227], v[32:47]\n\t"
    "ds_read_b128 a[88:91], v230 offset:0\n\t"
    "v_mfma_f32_32x32x16_bf16 v[48:63], a[132:135], a[228:231], v[48:63]\n\t"
    "ds_read_b128 a[92:95], v231 offset:0\n\t"
    "buffer_load_dwordx4 v235, s[64:67], s74 offen offset:2048 nt lds\n\t"
    "s_nop 1\n\t"
    "v_cvt_pk_bf16_f32 v248, v0, v1\n\t"
    "v_accvgpr_write_b32 a0, v248\n\t"
    "v_cvt_pk_bf16_f32 v249, v2, v3\n\t"
    "v_accvgpr_write_b32 a1, v249\n\t"
    "v_cvt_pk_bf16_f32 v248, v4, v5\n\t"
    "v_mfma_f32_32x32x16_bf16 v[32:47], a[148:151], a[232:235], v[32:47]\n\t"
    "v_accvgpr_write_b32 a2, v248\n\t"
    "v_cvt_pk_bf16_f32 v249, v6, v7\n\t"
    "v_accvgpr_write_b32 a3, v249\n\t"
    "v_cvt_pk_bf16_f32 v248, v16, v17\n\t"
    "v_accvgpr_write_b32 a32, v248\n\t"
    "v_mfma_f32_32x32x16_bf16 v[48:63], a[148:151], a[236:239], v[48:63]\n\t"
    "v_cvt_pk_bf16_f32 v249, v18, v19\n\t"
    "v_accvgpr_write_b32 a33, v249\n\t"
    "v_cvt_pk_bf16_f32 v248, v20, v21\n\t"
    "v_accvgpr_write_b32 a34, v248\n\t"
    "v_cvt_pk_bf16_f32 v249, v22, v23\n\t"
    "v_mfma_f32_32x32x16_bf16 v[32:47], a[164:167], a[240:243], v[32:47]\n\t"
    "buffer_load_dwordx4 v236, s[64:67], s74 offen offset:3072 nt lds\n\t"
    "s_add_u32 s74, s74, 16384\n\t"
    "v_accvgpr_write_b32 a35, v249\n\t"
    "v_cvt_pk_bf16_f32 v248, v8, v9\n\t"
    "v_accvgpr_write_b32 a4, v248\n\t"
    "v_cvt_pk_bf16_f32 v249, v10, v11\n\t"
    "v_accvgpr_write_b32 a5, v249\n\t"
    "v_mfma_f32_32x32x16_bf16 v[48:63], a[164:167], a[244:247], v[48:63]\n\t"
    "v_cvt_pk_bf16_f32 v248, v12, v13\n\t"
    "v_accvgpr_write_b32 a6, v248\n\t"
    "v_cvt_pk_bf16_f32 v249, v14, v15\n\t"
    "v_accvgpr_write_b32 a7, v249\n\t"
    "v_cvt_pk_bf16_f32 v248, v24, v25\n\t"
    "v_mfma_f32_32x32x16_bf16 v[32:47], a[180:183], a[248:251], v[32:47]\n\t"
    "v_accvgpr_write_b32 a36, v248\n\t"
    "v_cvt_pk_bf16_f32 v249, v26, v27\n\t"
    "v_accvgpr_write_b32 a37, v249\n\t"
    "v_cvt_pk_bf16_f32 v248, v28, v29\n\t"
    "v_accvgpr_write_b32 a38, v248\n\t"
    "v_mfma_f32_32x32x16_bf16 v[48:63], a[180:183], a[252:255], v[48:63]\n\t"
    "s_add_u32 m0, s78, 32768\n\t"
    "s_nop 0\n\t"
    "buffer_load_dwordx4 v237, s[68:71], s75 offen offset:0 nt lds\n\t"
    "v_cvt_pk_bf16_f32 v249, v30, v31\n\t"
    "v_accvgpr_write_b32 a39, v249\n\t"
    "s_waitcnt lgkmcnt(10)\n\t"
    "v_mfma_f32_32x32x16_bf16 v[64:79], a[136:139], a[224:227], v[64:79]\n\t"
    "v_mfma_f32_32x32x16_bf16 v[80:95], a[136:139], a[228:231], v[80:95]\n\t"
    "v_cvt_pk_bf16_f32 v248, v32, v33\n\t"
    "v_accvgpr_write_b32 a8, v248\n\t"
    "v_cvt_pk_bf16_f32 v249, v34, v35\n\t"
    "v_accvgpr_write_b32 a9, v249\n\t"
    "v_cvt_pk_bf16_f32 v248, v36, v37\n\t"
    "v_mfma_f32_32x32x16_bf16 v[64:79], a[152:155], a[232:235], v[64:79]\n\t"
    "v_accvgpr_write_b32 a10, v248\n\t"
    "v_cvt_pk_bf16_f32 v249, v38, v39\n\t"
    "v_accvgpr_write_b32 a11, v249\n\t"
    "v_cvt_pk_bf16_f32 v248, v48, v49\n\t"
    "v_accvgpr_write_b32 a40, v248\n\t"
    "v_mfma_f32_32x32x16_bf16 v[80:95], a[152:155], a[236:239], v[80:95]\n\t"
    "buffer_load_dwordx4 v238, s[68:71], s75 offen offset:1024 nt lds\n\t"
    "v_cvt_pk_bf16_f32 v249, v50, v51\n\t"
    "v_accvgpr_write_b32 a41, v249\n\t"
    "v_cvt_pk_bf16_f32 v248, v52, v53\n\t"
    "v_accvgpr_write_b32 a42, v248\n\t"
    "v_cvt_pk_bf16_f32 v249, v54, v55\n\t"
    "v_mfma_f32_32x32x16_bf16 v[64:79], a[168:171], a[240:243], v[64:79]\n\t"
    "v_accvgpr_write_b32 a43, v249\n\t"
    "v_cvt_pk_bf16_f32 v248, v40, v41\n\t"
    "v_accvgpr_write_b32 a12, v248\n\t"
    "v_cvt_pk_bf16_f32 v249, v42, v43\n\t"
    "v_accvgpr_write_b32 a13, v249\n\t"
    "v_mfma_f32_32x32x16_bf16 v[80:95], a[168:171], a[244:247], v[80:95]\n\t"
    "v_cvt_pk_bf16_f32 v248, v44, v45\n\t"
    "v_accvgpr_write_b32 a14, v248\n\t"
    "v_cvt_pk_bf16_f32 v249, v46, v47\n\t"
    "v_accvgpr_write_b32 a15, v249\n\t"
    "v_cvt_pk_bf16_f32 v248, v56, v57\n\t"
    "v_mfma_f32_32x32x16_bf16 v[64:79], a[184:187], a[248:251], v[64:79]\n\t"
    "buffer_load_dwordx4 v237, s[68:71], s75 offen offset:2048 nt lds\n\t"
    "v_accvgpr_write_b32 a44, v248\n\t"
    "v_cvt_pk_bf16_f32 v249, v58, v59\n\t"
    "v_accvgpr_write_b32 a45, v249\n\t"
    "v_cvt_pk_bf16_f32 v248, v60, v61\n\t"
    "v_accvgpr_write_b32 a46, v248\n\t"
    "v_mfma_f32_32x32x16_bf16 v[80:95], a[184:187], a[252:255], v[80:95]\n\t"
    "v_cvt_pk_bf16_f32 v249, v62, v63\n\t"
    "v_accvgpr_write_b32 a47, v249\n\t"
    "s_waitcnt lgkmcnt(8)\n\t"
    "v_mfma_f32_32x32x16_bf16 v[96:111], a[140:143], a[224:227], v[96:111]\n\t"
    "v_mfma_f32_32x32x16_bf16 v[112:127], a[140:143], a[228:231], v[112:127]\n\t"
    "buffer_load_dwordx4 v238, s[68:71], s75 offen offset:3072 nt lds\n\t"
    "s_add_u32 s75, s75, 16384\n\t"
    "v_cvt_pk_bf16_f32 v248, v64, v65\n\t"
    "v_accvgpr_write_b32 a16, v248\n\t"
    "v_cvt_pk_bf16_f32 v249, v66, v67\n\t"
    "v_accvgpr_write_b32 a17, v249\n\t"
    "v_cvt_pk_bf16_f32 v248, v68, v69\n\t"
    "v_mfma_f32_32x32x16_bf16 v[96:111], a[156:159], a[232:235], v[96:111]\n\t"
    "v_accvgpr_write_b32 a18, v248\n\t"
    "v_cvt_pk_bf16_f32 v249, v70, v71\n\t"
    "v_accvgpr_write_b32 a19, v249\n\t"
    "v_cvt_pk_bf16_f32 v248, v80, v81\n\t"
    "v_accvgpr_write_b32 a48, v248\n\t"
    "v_mfma_f32_32x32x16_bf16 v[112:127], a[156:159], a[236:239], v[112:127]\n\t"
    "v_cvt_pk_bf16_f32 v249, v82, v83\n\t"
    "v_accvgpr_write_b32 a49, v249\n\t"
    "v_cvt_pk_bf16_f32 v248, v84, v85\n\t"
    "v_accvgpr_write_b32 a50, v248\n\t"
    "v_cvt_pk_bf16_f32 v249, v86, v87\n\t"
    "v_mfma_f32_32x32x16_bf16 v[96:111], a[172:175], a[240:243], v[96:111]\n\t"
    "v_accvgpr_write_b32 a51, v249\n\t"
    "v_cvt_pk_bf16_f32 v248, v72, v73\n\t"
    "v_accvgpr_write_b32 a20, v248\n\t"
    "v_cvt_pk_bf16_f32 v249, v74, v75\n\t"
    "v_accvgpr_write_b32 a21, v249\n\t"
    "v_mfma_f32_32x32x16_bf16 v[112:127], a[172:175], a[244:247], v[112:127]\n\t"
    "v_cvt_pk_bf16_f32 v248, v76, v77\n\t"
    "v_accvgpr_write_b32 a22, v248\n\t"
    "v_cvt_pk_bf16_f32 v249, v78, v79\n\t"
    "v_accvgpr_write_b32 a23, v249\n\t"
    "v_cvt_pk_bf16_f32 v248, v88, v89\n\t"
    "v_mfma_f32_32x32x16_bf16 v[96:111], a[188:191], a[248:251], v[96:111]\n\t"
    "v_accvgpr_write_b32 a52, v248\n\t"
    "v_cvt_pk_bf16_f32 v249, v90, v91\n\t"
    "v_accvgpr_write_b32 a53, v249\n\t"
    "v_cvt_pk_bf16_f32 v248, v92, v93\n\t"
    "v_accvgpr_write_b32 a54, v248\n\t"
    "v_mfma_f32_32x32x16_bf16 v[112:127], a[188:191], a[252:255], v[112:127]\n\t"
    "v_cvt_pk_bf16_f32 v249, v94, v95\n\t"
    "v_accvgpr_write_b32 a55, v249\n\t"
    "s_waitcnt lgkmcnt(0)\n\t"
    "v_mfma_f32_32x32x16_bf16 v[128:143], a[64:67], a[0:3], 0\n\t"
    "s_waitcnt vmcnt(16)\n\t"
    "s_barrier\n\t"
    "v_mfma_f32_32x32x16_bf16 v[144:159], a[64:67], a[32:35], 0\n\t"
    "v_cvt_pk_bf16_f32 v248, v96, v97\n\t"
    "v_accvgpr_write_b32 a24, v248\n\t"
    "v_cvt_pk_bf16_f32 v249, v98, v99\n\t"
    "v_mfma_f32_32x32x16_bf16 v[128:143], a[68:71], a[4:7], v[128:143]\n\t"
    "v_accvgpr_write_b32 a25, v249\n\t"
    "v_cvt_pk_bf16_f32 v248, v100, v101\n\t"
    "v_accvgpr_write_b32 a26, v248\n\t"
    "v_mfma_f32_32x32x16_bf16 v[144:159], a[68:71], a[36:39], v[144:159]\n\t"
    "ds_read_b128 a[64:67], v224 offset:8192\n\t"
    "v_cvt_pk_bf16_f32 v249, v102, v103\n\t"
    "v_accvgpr_write_b32 a27, v249\n\t"
    "v_cvt_pk_bf16_f32 v248, v112, v113\n\t"
    "v_mfma_f32_32x32x16_bf16 v[128:143], a[72:75], a[8:11], v[128:143]\n\t"
    "v_accvgpr_write_b32 a56, v248\n\t"
    "v_cvt_pk_bf16_f32 v249, v114, v115\n\t"
    "v_accvgpr_write_b32 a57, v249\n\t"
    "v_mfma_f32_32x32x16_bf16 v[144:159], a[72:75], a[40:43], v[144:159]\n\t"
    "ds_read_b128 a[68:71], v225 offset:8192\n\t"
    "v_cvt_pk_bf16_f32 v248, v116, v117\n\t"
    "v_accvgpr_write_b32 a58, v248\n\t"
    "v_cvt_pk_bf16_f32 v249, v118, v119\n\t"
    "v_mfma_f32_32x32x16_bf16 v[128:143], a[76:79], a[12:15], v[128:143]\n\t"
    "v_accvgpr_write_b32 a59, v249\n\t"
    "v_cvt_pk_bf16_f32 v248, v104, v105\n\t"
    "v_accvgpr_write_b32 a28, v248\n\t"
    "v_mfma_f32_32x32x16_bf16 v[144:159], a[76:79], a[44:47], v[144:159]\n\t"
    "ds_read_b128 a[72:75], v226 offset:8192\n\t"
    "v_cvt_pk_bf16_f32 v249, v106, v107\n\t"
    "v_accvgpr_write_b32 a29, v249\n\t"
    "v_cvt_pk_bf16_f32 v248, v108, v109\n\t"
    "v_mfma_f32_32x32x16_bf16 v[128:143], a[80:83], a[16:19], v[128:143]\n\t"
    "v_accvgpr_write_b32 a30, v248\n\t"
    "v_cvt_pk_bf16_f32 v249, v110, v111\n\t"
    "v_accvgpr_write_b32 a31, v249\n\t"
    "v_mfma_f32_32x32x16_bf16 v[144:159], a[80:83], a[48:51], v[144:159]\n\t"
    "ds_read_b128 a[76:79], v227 offset:8192\n\t"
    "v_cvt_pk_bf16_f32 v248, v120, v121\n\t"
    "v_accvgpr_write_b32 a60, v248\n\t"
    "v_cvt_pk_bf16_f32 v249, v122, v123\n\t"
    "v_mfma_f32_32x32x16_bf16 v[128:143], a[84:87], a[20:23], v[128:143]\n\t"
    "v_accvgpr_write_b32 a61, v249\n\t"
    "v_cvt_pk_bf16_f32 v248, v124, v125\n\t"
    "v_accvgpr_write_b32 a62, v248\n\t"
    "v_mfma_f32_32x32x16_bf16 v[144:159], a[84:87], a[52:55], v[144:159]\n\t"
    "ds_read_b128 a[80:83], v228 offset:8192\n\t"
    "v_cvt_pk_bf16_f32 v249, v126, v127\n\t"
    "v_accvgpr_write_b32 a63, v249\n\t"
    "v_mfma_f32_32x32x16_bf16 v[128:143], a[88:91], a[24:27], v[128:143]\n\t"
    "v_mfma_f32_32x32x16_bf16 v[144:159], a[88:91], a[56:59], v[144:159]\n\t"
    "ds_read_b128 a[84:87], v229 offset:8192\n\t"
    "v_mfma_f32_32x32x16_bf16 v[128:143], a[92:95], a[28:31], v[128:143]\n\t"
    "v_mfma_f32_32x32x16_bf16 v[144:159], a[92:95], a[60:63], v[144:159]\n\t"
    "ds_read_b128 a[88:91], v230 offset:8192\n\t"
    "s_nop 9\n\t"
    "v_max3_f32 v248, v128, v129, v130\n\t"
    "v_max3_f32 v248, v248, v131, v132\n\t"
    "v_max3_f32 v248, v248, v133, v134\n\t"
    "v_max3_f32 v248, v248, v135, v136\n\t"
    "v_max3_f32 v248, v248, v137, v138\n\t"
    "v_max3_f32 v248, v248, v139, v140\n\t"
    "v_max3_f32 v248, v248, v141, v142\n\t"
    "v_max_f32 v248, v248, v143\n\t"
    "v_max3_f32 v249, v144, v145, v146\n\t"
    "v_max3_f32 v249, v249, v147, v148\n\t"
    "v_max3_f32 v249, v249, v149, v150\n\t"
    "v_max3_f32 v249, v249, v151, v152\n\t"
    "v_max3_f32 v249, v249, v153, v154\n\t"
    "v_max3_f32 v249, v249, v155, v156\n\t"
    "v_max3_f32 v249, v249, v157, v158\n\t"
    "v_max_f32 v249, v249, v159\n\t"
    "v_mov_b32 v250, v248\n\t"
    "s_nop 1\n\t"
    "v_permlane32_swap_b32 v248, v250\n\t"
    "v_max_f32 v248, v248, v250\n\t"
    "v_add_f32 v248, 0x42d20000, v248\n\t"
    "v_sub_f32 v192, 0, v248\n\t"
    "v_mov_b32 v193, v192\n\t"
    "v_mov_b32 v194, v192\n\t"
    "v_mov_b32 v195, v192\n\t"
    "v_mov_b32 v196, v192\n\t"
    "v_mov_b32 v197, v192\n\t"
    "v_mov_b32 v198, v192\n\t"
    "v_mov_b32 v199, v192\n\t"
    "v_mov_b32 v200, v192\n\t"
    "v_mov_b32 v201, v192\n\t"
    "v_mov_b32 v202, v192\n\t"
    "v_mov_b32 v203, v192\n\t"
    "v_mov_b32 v204, v192\n\t"
    "v_mov_b32 v205, v192\n\t"
    "v_mov_b32 v206, v192\n\t"
    "v_mov_b32 v207, v192\n\t"
    "v_add_f32 v128, v128, v192\n\t"
    "v_add_f32 v129, v129, v192\n\t"
    "v_add_f32 v130, v130, v192\n\t"
    "v_add_f32 v131, v131, v192\n\t"
    "v_add_f32 v132, v132, v192\n\t"
    "v_add_f32 v133, v133, v192\n\t"
    "v_add_f32 v134, v134, v192\n\t"
    "v_add_f32 v135, v135, v192\n\t"
    "v_add_f32 v136, v136, v192\n\t"
    "v_add_f32 v137, v137, v192\n\t"
    "v_add_f32 v138, v138, v192\n\t"
    "v_add_f32 v139, v139, v192\n\t"
    "v_add_f32 v140, v140, v192\n\t"
    "v_add_f32 v141, v141, v192\n\t"
    "v_add_f32 v142, v142, v192\n\t"
    "v_add_f32 v143, v143, v192\n\t"
    "v_mov_b32 v250, v249\n\t"
    "s_nop 1\n\t"
    "v_permlane32_swap_b32 v249, v250\n\t"
    "v_max_f32 v249, v249, v250\n\t"
    "v_add_f32 v249, 0x42d20000, v249\n\t"
    "v_sub_f32 v208, 0, v249\n\t"
    "v_mov_b32 v209, v208\n\t"
    "v_mov_b32 v210, v208\n\t"
    "v_mov_b32 v211, v208\n\t"
    "v_mov_b32 v212, v208\n\t"
    "v_mov_b32 v213, v208\n\t"
    "v_mov_b32 v214, v208\n\t"
    "v_mov_b32 v215, v208\n\t"
    "v_mov_b32 v216, v208\n\t"
    "v_mov_b32 v217, v208\n\t"
    "v_mov_b32 v218, v208\n\t"
    "v_mov_b32 v219, v208\n\t"
    "v_mov_b32 v220, v208\n\t"
    "v_mov_b32 v221, v208\n\t"
    "v_mov_b32 v222, v208\n\t"
    "v_mov_b32 v223, v208\n\t"
    "v_add_f32 v144, v144, v208\n\t"
    "v_add_f32 v145, v145, v208\n\t"
    "v_add_f32 v146, v146, v208\n\t"
    "v_add_f32 v147, v147, v208\n\t"
    "v_add_f32 v148, v148, v208\n\t"
    "v_add_f32 v149, v149, v208\n\t"
    "v_add_f32 v150, v150, v208\n\t"
    "v_add_f32 v151, v151, v208\n\t"
    "v_add_f32 v152, v152, v208\n\t"
    "v_add_f32 v153, v153, v208\n\t"
    "v_add_f32 v154, v154, v208\n\t"
    "v_add_f32 v155, v155, v208\n\t"
    "v_add_f32 v156, v156, v208\n\t"
    "v_add_f32 v157, v157, v208\n\t"
    "v_add_f32 v158, v158, v208\n\t"
    "v_add_f32 v159, v159, v208\n\t"
    "ds_read_b128 a[92:95], v231 offset:8192\n\t"
    "ds_read_b64_tr_b16 a[96:97], v232 offset:0\n\t"
    "ds_read_b64_tr_b16 a[98:99], v232 offset:2048\n\t"
    "v_exp_f32 v128, v128\n\t"
    "ds_read_b64_tr_b16 a[100:101], v232 offset:512\n\t"
    "v_exp_f32 v129, v129\n\t"
    "v_add_f32 v239, v239, v128\n\t"
    "ds_read_b64_tr_b16 a[102:103], v232 offset:2560\n\t"
    "v_exp_f32 v130, v130\n\t"
    "v_add_f32 v239, v239, v129\n\t"
    "ds_read_b64_tr_b16 a[104:105], v232 offset:1024\n\t"
    "v_exp_f32 v131, v131\n\t"
    "v_add_f32 v239, v239, v130\n\t"
    "v_cvt_pk_bf16_f32 v128, v128, v129\n\t"
    "ds_read_b64_tr_b16 a[106:107], v232 offset:3072\n\t"
    "v_exp_f32 v132, v132\n\t"
    "v_add_f32 v239, v239, v131\n\t"
    "ds_read_b64_tr_b16 a[108:109], v232 offset:1536\n\t"
    "v_exp_f32 v133, v133\n\t"
    "v_add_f32 v239, v239, v132\n\t"
    "v_cvt_pk_bf16_f32 v129, v130, v131\n\t"
    "ds_read_b64_tr_b16 a[110:111], v232 offset:3584\n\t"
    "v_exp_f32 v134, v134\n\t"
    "v_add_f32 v239, v239, v133\n\t"
    "ds_read_b64_tr_b16 a[112:113], v232 offset:4096\n\t"
    "v_exp_f32 v135, v135\n\t"
    "v_add_f32 v239, v239, v134\n\t"
    "v_cvt_pk_bf16_f32 v130, v132, v133\n\t"
    "ds_read_b64_tr_b16 a[114:115], v232 offset:6144\n\t"
    "v_exp_f32 v144, v144\n\t"
    "v_add_f32 v239, v239, v135\n\t"
    "ds_read_b64_tr_b16 a[116:117], v232 offset:4608\n\t"
    "v_exp_f32 v145, v145\n\t"
    "v_add_f32 v240, v240, v144\n\t"
    "v_cvt_pk_bf16_f32 v131, v134, v135\n\t"
    "ds_read_b64_tr_b16 a[118:119], v232 offset:6656\n\t"
    "v_exp_f32 v146, v146\n\t"
    "v_add_f32 v240, v240, v145\n\t"
    "ds_read_b64_tr_b16 a[120:121], v232 offset:5120\n\t"
    "v_exp_f32 v147, v147\n\t"
    "v_add_f32 v240, v240, v146\n\t"
    "v_cvt_pk_bf16_f32 v144, v144, v145\n\t"
    "ds_read_b64_tr_b16 a[122:123], v232 offset:7168\n\t"
    "v_exp_f32 v148, v148\n\t"
    "v_add_f32 v240, v240, v147\n\t"
    "s_waitcnt lgkmcnt(14)\n\t"
    "v_mfma_f32_32x32x16_bf16 v[160:175], a[64:67], a[0:3], v[192:207]\n\t"
    "ds_read_b64_tr_b16 a[124:125], v232 offset:5632\n\t"
    "v_exp_f32 v149, v149\n\t"
    "v_add_f32 v240, v240, v148\n\t"
    "v_cvt_pk_bf16_f32 v145, v146, v147\n\t"
    "v_mfma_f32_32x32x16_bf16 v[176:191], a[64:67], a[32:35], v[208:223]\n\t"
    "ds_read_b64_tr_b16 a[126:127], v232 offset:7680\n\t"
    "v_exp_f32 v150, v150\n\t"
    "v_add_f32 v240, v240, v149\n\t"
    "v_mfma_f32_32x32x16_bf16 v[160:175], a[68:71], a[4:7], v[160:175]\n\t"
    "v_exp_f32 v151, v151\n\t"
    "v_add_f32 v240, v240, v150\n\t"
    "v_cvt_pk_bf16_f32 v146, v148, v149\n\t"
    "v_mfma_f32_32x32x16_bf16 v[176:191], a[68:71], a[36:39], v[176:191]\n\t"
    "ds_read_b128 a[64:67], v224 offset:16384\n\t"
    "v_exp_f32 v136, v136\n\t"
    "v_add_f32 v240, v240, v151\n\t"
    "v_mfma_f32_32x32x16_bf16 v[160:175], a[72:75], a[8:11], v[160:175]\n\t"
    "v_exp_f32 v137, v137\n\t"
    "v_add_f32 v239, v239, v136\n\t"
    "v_cvt_pk_bf16_f32 v147, v150, v151\n\t"
    "v_mfma_f32_32x32x16_bf16 v[176:191], a[72:75], a[40:43], v[176:191]\n\t"
    "ds_read_b128 a[68:71], v225 offset:16384\n\t"
    "v_exp_f32 v138, v138\n\t"
    "v_add_f32 v239, v239, v137\n\t"
    "v_mfma_f32_32x32x16_bf16 v[160:175], a[76:79], a[12:15], v[160:175]\n\t"
    "v_exp_f32 v139, v139\n\t"
    "v_add_f32 v239, v239, v138\n\t"
    "v_cvt_pk_bf16_f32 v136, v136, v137\n\t"
    "v_mfma_f32_32x32x16_bf16 v[176:191], a[76:79], a[44:47], v[176:191]\n\t"
    "ds_read_b128 a[72:75], v226 offset:16384\n\t"
    "v_exp_f32 v140, v140\n\t"
    "v_add_f32 v239, v239, v139\n\t"
    "v_mfma_f32_32x32x16_bf16 v[160:175], a[80:83], a[16:19], v[160:175]\n\t"
    "v_exp_f32 v141, v141\n\t"
    "v_add_f32 v239, v239, v140\n\t"
    "v_cvt_pk_bf16_f32 v137, v138, v139\n\t"
    "v_mfma_f32_32x32x16_bf16 v[176:191], a[80:83], a[48:51], v[176:191]\n\t"
    "ds_read_b128 a[76:79], v227 offset:16384\n\t"
    "v_exp_f32 v142, v142\n\t"
    "v_add_f32 v239, v239, v141\n\t"
    "v_mfma_f32_32x32x16_bf16 v[160:175], a[84:87], a[20:23], v[160:175]\n\t"
    "v_exp_f32 v143, v143\n\t"
    "v_add_f32 v239, v239, v142\n\t"
    "v_cvt_pk_bf16_f32 v138, v140, v141\n\t"
    "v_mfma_f32_32x32x16_bf16 v[176:191], a[84:87], a[52:55], v[176:191]\n\t"
    "ds_read_b128 a[80:83], v228 offset:16384\n\t"
    "v_exp_f32 v152, v152\n\t"
    "v_add_f32 v239, v239, v143\n\t"
    "v_mfma_f32_32x32x16_bf16 v[160:175], a[88:91], a[24:27], v[160:175]\n\t"
    "v_exp_f32 v153, v153\n\t"
    "v_add_f32 v240, v240, v152\n\t"
    "v_cvt_pk_bf16_f32 v139, v142, v143\n\t"
    "v_mfma_f32_32x32x16_bf16 v[176:191], a[88:91], a[56:59], v[176:191]\n\t"
    "ds_read_b128 a[84:87], v229 offset:16384\n\t"
    "v_exp_f32 v154, v154\n\t"
    "v_add_f32 v240, v240, v153\n\t"
    "v_mfma_f32_32x32x16_bf16 v[160:175], a[92:95], a[28:31], v[160:175]\n\t"
    "v_exp_f32 v155, v155\n\t"
    "v_add_f32 v240, v240, v154\n\t"
    "v_cvt_pk_bf16_f32 v152, v152, v153\n\t"
    "v_mfma_f32_32x32x16_bf16 v[176:191], a[92:95], a[60:63], v[176:191]\n\t"
    "ds_read_b128 a[88:91], v230 offset:16384\n\t"
    "v_exp_f32 v156, v156\n\t"
    "v_add_f32 v240, v240, v155\n\t"
    "s_waitcnt lgkmcnt(7)\n\t"
    "v_mfma_f32_32x32x16_bf16 v[0:15], a[96:99], v[128:131], 0\n\t"
    "v_exp_f32 v157, v157\n\t"
    "v_add_f32 v240, v240, v156\n\t"
    "v_cvt_pk_bf16_f32 v153, v154, v155\n\t"
    "v_mfma_f32_32x32x16_bf16 v[16:31], a[96:99], v[144:147], 0\n\t"
    "ds_read_b128 a[92:95], v231 offset:16384\n\t"
    "v_exp_f32 v158, v158\n\t"
    "v_add_f32 v240, v240, v157\n\t"
    "v_mfma_f32_32x32x16_bf16 v[32:47], a[100:103], v[128:131], 0\n\t"
    "ds_read_b64_tr_b16 a[96:97], v232 offset:8192\n\t"
    "v_exp_f32 v159, v159\n\t"
    "v_add_f32 v240, v240, v158\n\t"
    "v_cvt_pk_bf16_f32 v154, v156, v157\n\t"
    "v_mfma_f32_32x32x16_bf16 v[48:63], a[100:103], v[144:147], 0\n\t"
    "ds_read_b64_tr_b16 a[98:99], v232 offset:10240\n\t"
    "v_exp_f32 v160, v160\n\t"
    "v_add_f32 v240, v240, v159\n\t"
    "v_mfma_f32_32x32x16_bf16 v[64:79], a[104:107], v[128:131], 0\n\t"
    "ds_read_b64_tr_b16 a[100:101], v232 offset:8704\n\t"
    "v_exp_f32 v161, v161\n\t"
    "v_add_f32 v239, v239, v160\n\t"
    "v_cvt_pk_bf16_f32 v155, v158, v159\n\t"
    "v_mfma_f32_32x32x16_bf16 v[80:95], a[104:107], v[144:147], 0\n\t"
    "ds_read_b64_tr_b16 a[102:103], v232 offset:10752\n\t"
    "v_exp_f32 v162, v162\n\t"
    "v_add_f32 v239, v239, v161\n\t"
    "v_mfma_f32_32x32x16_bf16 v[96:111], a[108:111], v[128:131], 0\n\t"
    "ds_read_b64_tr_b16 a[104:105], v232 offset:9216\n\t"
    "v_exp_f32 v163, v163\n\t"
    "v_add_f32 v239, v239, v162\n\t"
    "v_cvt_pk_bf16_f32 v160, v160, v161\n\t"
    "v_mfma_f32_32x32x16_bf16 v[112:127], a[108:111], v[144:147], 0\n\t"
    "ds_read_b64_tr_b16 a[106:107], v232 offset:11264\n\t"
    "v_exp_f32 v164, v164\n\t"
    "v_add_f32 v239, v239, v163\n\t"
    "v_mfma_f32_32x32x16_bf16 v[0:15], a[112:115], v[136:139], v[0:15]\n\t"
    "ds_read_b64_tr_b16 a[108:109], v232 offset:9728\n\t"
    "v_exp_f32 v165, v165\n\t"
    "v_add_f32 v239, v239, v164\n\t"
    "v_cvt_pk_bf16_f32 v161, v162, v163\n\t"
    "v_mfma_f32_32x32x16_bf16 v[16:31], a[112:115], v[152:155], v[16:31]\n\t"
    "ds_read_b64_tr_b16 a[110:111], v232 offset:11776\n\t"
    "v_exp_f32 v166, v166\n\t"
    "v_add_f32 v239, v239, v165\n\t"
    "v_mfma_f32_32x32x16_bf16 v[32:47], a[116:119], v[136:139], v[32:47]\n\t"
    "ds_read_b64_tr_b16 a[112:113], v232 offset:12288\n\t"
    "v_exp_f32 v167, v167\n\t"
    "v_add_f32 v239, v239, v166\n\t"
    "v_cvt_pk_bf16_f32 v162, v164, v165\n\t"
    "v_mfma_f32_32x32x16_bf16 v[48:63], a[116:119], v[152:155], v[48:63]\n\t"
    "ds_read_b64_tr_b16 a[114:115], v232 offset:14336\n\t"
    "v_exp_f32 v176, v176\n\t"
    "v_add_f32 v239, v239, v167\n\t"
    "v_mfma_f32_32x32x16_bf16 v[64:79], a[120:123], v[136:139], v[64:79]\n\t"
    "ds_read_b64_tr_b16 a[116:117], v232 offset:12800\n\t"
    "v_exp_f32 v177, v177\n\t"
    "v_add_f32 v240, v240, v176\n\t"
    "v_cvt_pk_bf16_f32 v163, v166, v167\n\t"
    "v_mfma_f32_32x32x16_bf16 v[80:95], a[120:123], v[152:155], v[80:95]\n\t"
    "ds_read_b64_tr_b16 a[118:119], v232 offset:14848\n\t"
    "v_exp_f32 v178, v178\n\t"
    "v_add_f32 v240, v240, v177\n\t"
    "v_mfma_f32_32x32x16_bf16 v[96:111], a[124:127], v[136:139], v[96:111]\n\t"
    "ds_read_b64_tr_b16 a[120:121], v232 offset:13312\n\t"
    "v_exp_f32 v179, v179\n\t"
    "v_add_f32 v240, v240, v178\n\t"
    "v_cvt_pk_bf16_f32 v176, v176, v177\n\t"
    "v_mfma_f32_32x32x16_bf16 v[112:127], a[124:127], v[152:155], v[112:127]\n\t"
    "ds_read_b64_tr_b16 a[122:123], v232 offset:15360\n\t"
    "v_exp_f32 v180, v180\n\t"
    "v_add_f32 v240, v240, v179\n\t"
    "s_mov_b32 s90, 7\n\t"
    ".p2align 6\n\t"
    "Lm_attn_1:\n\t"
    "s_waitcnt lgkmcnt(14)\n\t"
    "v_mfma_f32_32x32x16_bf16 v[128:143], a[64:67], a[0:3], v[192:207]\n\t"
    "s_waitcnt vmcnt(8)\n\t"
    "s_barrier\n\t"
    "ds_read_b64_tr_b16 a[124:125], v232 offset:13824\n\t"
    "v_exp_f32 v181, v181\n\t"
    "v_add_f32 v240, v240, v180\n\t"
    "v_cvt_pk_bf16_f32 v177, v178, v179\n\t"
    "v_mfma_f32_32x32x16_bf16 v[144:159], a[64:67], a[32:35], v[208:223]\n\t"
    "ds_read_b64_tr_b16 a[126:127], v232 offset:15872\n\t"
    "v_exp_f32 v182, v182\n\t"
    "v_add_f32 v240, v240, v181\n\t"
    "v_mfma_f32_32x32x16_bf16 v[128:143], a[68:71], a[4:7], v[128:143]\n\t"
    "s_add_u32 m0, s77, 0\n\t"
    "s_nop 0\n\t"
    "buffer_load_dwordx4 v233, s[64:67], s74 offen offset:0 nt lds\n\t"
    "v_exp_f32 v183, v183\n\t"
    "v_add_f32 v240, v240, v182\n\t"
    "v_cvt_pk_bf16_f32 v178, v180, v181\n\t"
    "v_mfma_f32_32x32x16_bf16 v[144:159], a[68:71], a[36:39], v[144:159]\n\t"
    "ds_read_b128 a[64:67], v224 offset:24576\n\t"
    "v_exp_f32 v168, v168\n\t"
    "v_add_f32 v240, v240, v183\n\t"
    "v_mfma_f32_32x32x16_bf16 v[128:143], a[72:75], a[8:11], v[128:143]\n\t"
    "buffer_load_dwordx4 v234, s[64:67], s74 offen offset:1024 nt lds\n\t"
    "v_exp_f32 v169, v169\n\t"
    "v_add_f32 v239, v239, v168\n\t"
    "v_cvt_pk_bf16_f32 v179, v182, v183\n\t"
    "v_mfma_f32_32x32x16_bf16 v[144:159], a[72:75], a[40:43], v[144:159]\n\t"
    "ds_read_b128 a[68:71], v225 offset:24576\n\t"
    "v_exp_f32 v170, v170\n\t"
    "v_add_f32 v239, v239, v169\n\t"
    "v_mfma_f32_32x32x16_bf16 v[128:143], a[76:79], a[12:15], v[128:143]\n\t"
    "buffer_load_dwordx4 v235, s[64:67], s74 offen offset:2048 nt lds\n\t"
    "v_exp_f32 v171, v171\n\t"
    "v_add_f32 v239, v239, v170\n\t"
    "v_cvt_pk_bf16_f32 v168, v168, v169\n\t"
    "v_mfma_f32_32x32x16_bf16 v[144:159], a[76:79], a[44:47], v[144:159]\n\t"
    "ds_read_b128 a[72:75], v226 offset:24576\n\t"
    "v_exp_f32 v172, v172\n\t"
    "v_add_f32 v239, v239, v171\n\t"
    "v_mfma_f32_32x32x16_bf16 v[128:143], a[80:83], a[16:19], v[128:143]\n\t"
    "buffer_load_dwordx4 v236, s[64:67], s74 offen offset:3072 nt lds\n\t"
    "s_add_u32 s74, s74, 16384\n\t"
    "v_exp_f32 v173, v173\n\t"
    "v_add_f32 v239, v239, v172\n\t"
    "v_cvt_pk_bf16_f32 v169, v170, v171\n\t"
    "v_mfma_f32_32x32x16_bf16 v[144:159], a[80:83], a[48:51], v[144:159]\n\t"
    "ds_read_b128 a[76:79], v227 offset:24576\n\t"
    "v_exp_f32 v174, v174\n\t"
    "v_add_f32 v239, v239, v173\n\t"
    "v_mfma_f32_32x32x16_bf16 v[128:143], a[84:87], a[20:23], v[128:143]\n\t"
    "s_add_u32 m0, s78, 49152\n\t"
    "s_nop 0\n\t"
    "buffer_load_dwordx4 v237, s[68:71], s75 offen offset:0 nt lds\n\t"
    "v_exp_f32 v175, v175\n\t"
    "v_add_f32 v239, v239, v174\n\t"
    "v_cvt_pk_bf16_f32 v170, v172, v173\n\t"
    "v_mfma_f32_32x32x16_bf16 v[144:159], a[84:87], a[52:55], v[144:159]\n\t"
    "ds_read_b128 a[80:83], v228 offset:24576\n\t"
    "v_exp_f32 v184, v184\n\t"
    "v_add_f32 v239, v239, v175\n\t"
    "v_mfma_f32_32x32x16_bf16 v[128:143], a[88:91], a[24:27], v[128:143]\n\t"
    "buffer_load_dwordx4 v238, s[68:71], s75 offen offset:1024 nt lds\n\t"
    "v_exp_f32 v185, v185\n\t"
    "v_add_f32 v240, v240, v184\n\t"
    "v_cvt_pk_bf16_f32 v171, v174, v175\n\t"
    "v_mfma_f32_32x32x16_bf16 v[144:159], a[88:91], a[56:59], v[144:159]\n\t"
    "ds_read_b128 a[84:87], v229 offset:24576\n\t"
    "v_exp_f32 v186, v186\n\t"
    "v_add_f32 v240, v240, v185\n\t"
    "v_mfma_f32_32x32x16_bf16 v[128:143], a[92:95], a[28:31], v[128:143]\n\t"
    "buffer_load_dwordx4 v237, s[68:71], s75 offen offset:2048 nt lds\n\t"
    "v_exp_f32 v187, v187\n\t"
    "v_add_f32 v240, v240, v186\n\t"
    "v_cvt_pk_bf16_f32 v184, v184, v185\n\t"
    "v_mfma_f32_32x32x16_bf16 v[144:159], a[92:95], a[60:63], v[144:159]\n\t"
    "ds_read_b128 a[88:91], v230 offset:24576\n\t"
    "v_exp_f32 v188, v188\n\t"
    "v_add_f32 v240, v240, v187\n\t"
    "s_waitcnt lgkmcnt(7)\n\t"
    "v_mfma_f32_32x32x16_bf16 v[0:15], a[96:99], v[160:163], v[0:15]\n\t"
    "buffer_load_dwordx4 v238, s[68:71], s75 offen offset:3072 nt lds\n\t"
    "s_add_u32 s75, s75, 16384\n\t"
    "v_exp_f32 v189, v189\n\t"
    "v_add_f32 v240, v240, v188\n\t"
    "v_cvt_pk_bf16_f32 v185, v186, v187\n\t"
    "v_mfma_f32_32x32x16_bf16 v[16:31], a[96:99], v[176:179], v[16:31]\n\t"
    "ds_read_b128 a[92:95], v231 offset:24576\n\t"
    "v_exp_f32 v190, v190\n\t"
    "v_add_f32 v240, v240, v189\n\t"
    "v_mfma_f32_32x32x16_bf16 v[32:47], a[100:103], v[160:163], v[32:47]\n\t"
    "ds_read_b64_tr_b16 a[96:97], v232 offset:16384\n\t"
    "v_exp_f32 v191, v191\n\t"
    "v_add_f32 v240, v240, v190\n\t"
    "v_cvt_pk_bf16_f32 v186, v188, v189\n\t"
    "v_mfma_f32_32x32x16_bf16 v[48:63], a[100:103], v[176:179], v[48:63]\n\t"
    "ds_read_b64_tr_b16 a[98:99], v232 offset:18432\n\t"
    "v_exp_f32 v128, v128\n\t"
    "v_add_f32 v240, v240, v191\n\t"
    "v_mfma_f32_32x32x16_bf16 v[64:79], a[104:107], v[160:163], v[64:79]\n\t"
    "ds_read_b64_tr_b16 a[100:101], v232 offset:16896\n\t"
    "global_load_dword v245, v243, s[46:47] sc1\n\t"
    "s_add_u32 s40, s40, 1\n\ts_cmp_ge_u32 s40, 3\n\ts_cselect_b64 vcc, -1, 0\n\t"
    "v_mov_b32 v243, v244\n\t"
    "v_cndmask_b32 v244, v244, v246, vcc\n\t"
    "s_mov_b64 s[46:47], s[48:49]\n\t"
    "s_mov_b64 s[48:49], s[50:51]\n\t"
    "s_mov_b64 s[50:51], s[52:53]\n\t"
    "s_mov_b64 s[52:53], s[36:37]\n\t"
    "s_mov_b64 s[36:37], s[38:39]\n\t"
    "v_exp_f32 v129, v129\n\t"
    "v_add_f32 v239, v239, v128\n\t"
    "v_cvt_pk_bf16_f32 v187, v190, v191\n\t"
    "v_mfma_f32_32x32x16_bf16 v[80:95], a[104:107], v[176:179], v[80:95]\n\t"
    "ds_read_b64_tr_b16 a[102:103], v232 offset:18944\n\t"
    "v_exp_f32 v130, v130\n\t"
    "v_add_f32 v239, v239, v129\n\t"
    "v_mfma_f32_32x32x16_bf16 v[96:111], a[108:111], v[160:163], v[96:111]\n\t"
    "ds_read_b64_tr_b16 a[104:105], v232 offset:17408\n\t"
    "v_exp_f32 v131, v131\n\t"
    "v_add_f32 v239, v239, v130\n\t"
    "v_cvt_pk_bf16_f32 v128, v128, v129\n\t"
    "v_mfma_f32_32x32x16_bf16 v[112:127], a[108:111], v[176:179], v[112:127]\n\t"
    "ds_read_b64_tr_b16 a[106:107], v232 offset:19456\n\t"
    "v_exp_f32 v132, v132\n\t"
    "v_add_f32 v239, v239, v131\n\t"
    "v_mfma_f32_32x32x16_bf16 v[0:15], a[112:115], v[168:171], v[0:15]\n\t"
    "ds_read_b64_tr_b16 a[108:109], v232 offset:17920\n\t"
    "v_exp_f32 v133, v133\n\t"
    "v_add_f32 v239, v239, v132\n\t"
    "v_cvt_pk_bf16_f32 v129, v130, v131\n\t"
    "v_mfma_f32_32x32x16_bf16 v[16:31], a[112:115], v[184:187], v[16:31]\n\t"
    "ds_read_b64_tr_b16 a[110:111], v232 offset:19968\n\t"
    "v_exp_f32 v134, v134\n\t"
    "v_add_f32 v239, v239, v133\n\t"
    "v_mfma_f32_32x32x16_bf16 v[32:47], a[116:119], v[168:171], v[32:47]\n\t"
    "ds_read_b64_tr_b16 a[112:113], v232 offset:20480\n\t"
    "v_exp_f32 v135, v135\n\t"
    "v_add_f32 v239, v239, v134\n\t"
    "v_cvt_pk_bf16_f32 v130, v132, v133\n\t"
    "v_mfma_f32_32x32x16_bf16 v[48:63], a[116:119], v[184:187], v[48:63]\n\t"
    "ds_read_b64_tr_b16 a[114:115], v232 offset:22528\n\t"
    "v_exp_f32 v144, v144\n\t"
    "v_add_f32 v239, v239, v135\n\t"
    "v_mfma_f32_32x32x16_bf16 v[64:79], a[120:123], v[168:171], v[64:79]\n\t"
    "ds_read_b64_tr_b16 a[116:117], v232 offset:20992\n\t"
    "v_exp_f32 v145, v145\n\t"
    "v_add_f32 v240, v240, v144\n\t"
    "v_cvt_pk_bf16_f32 v131, v134, v135\n\t"
    "v_mfma_f32_32x32x16_bf16 v[80:95], a[120:123], v[184:187], v[80:95]\n\t"
    "ds_read_b64_tr_b16 a[118:119], v232 offset:23040\n\t"
    "v_exp_f32 v146, v146\n\t"
    "v_add_f32 v240, v240, v145\n\t"
    "v_mfma_f32_32x32x16_bf16 v[96:111], a[124:127], v[168:171], v[96:111]\n\t"
    "ds_read_b64_tr_b16 a[120:121], v232 offset:21504\n\t"
    "v_exp_f32 v147, v147\n\t"
    "v_add_f32 v240, v240, v146\n\t"
    "v_cvt_pk_bf16_f32 v144, v144, v145\n\t"
    "v_mfma_f32_32x32x16_bf16 v[112:127], a[124:127], v[184:187], v[112:127]\n\t"
    "ds_read_b64_tr_b16 a[122:123], v232 offset:23552\n\t"
    "v_exp_f32 v148, v148\n\t"
    "v_add_f32 v240, v240, v147\n\t"
    "s_waitcnt lgkmcnt(14)\n\t"
    "v_mfma_f32_32x32x16_bf16 v[160:175], a[64:67], a[0:3], v[192:207]\n\t"
    "ds_read_b64_tr_b16 a[124:125], v232 offset:22016\n\t"
    "v_exp_f32 v149, v149\n\t"
    "v_add_f32 v240, v240, v148\n\t"
    "v_cvt_pk_bf16_f32 v145, v146, v147\n\t"
    "v_mfma_f32_32x32x16_bf16 v[176:191], a[64:67], a[32:35], v[208:223]\n\t"
    "ds_read_b64_tr_b16 a[126:127], v232 offset:24064\n\t"
    "v_exp_f32 v150, v150\n\t"
    "v_add_f32 v240, v240, v149\n\t"
    "v_mfma_f32_32x32x16_bf16 v[160:175], a[68:71], a[4:7], v[160:175]\n\t"
    "v_exp_f32 v151, v151\n\t"
    "v_add_f32 v240, v240, v150\n\t"
    "v_cvt_pk_bf16_f32 v146, v148, v149\n\t"
    "v_mfma_f32_32x32x16_bf16 v[176:191], a[68:71], a[36:39], v[176:191]\n\t"
    "ds_read_b128 a[64:67], v224 offset:32768\n\t"
    "v_exp_f32 v136, v136\n\t"
    "v_add_f32 v240, v240, v151\n\t"
    "v_mfma_f32_32x32x16_bf16 v[160:175], a[72:75], a[8:11], v[160:175]\n\t"
    "v_exp_f32 v137, v137\n\t"
    "v_add_f32 v239, v239, v136\n\t"
    "v_cvt_pk_bf16_f32 v147, v150, v151\n\t"
    "v_mfma_f32_32x32x16_bf16 v[176:191], a[72:75], a[40:43], v[176:191]\n\t"
    "ds_read_b128 a[68:71], v225 offset:32768\n\t"
    "v_exp_f32 v138, v138\n\t"
    "v_add_f32 v239, v239, v137\n\t"
    "v_mfma_f32_32x32x16_bf16 v[160:175], a[76:79], a[12:15], v[160:175]\n\t"
    "v_exp_f32 v139, v139\n\t"
    "v_add_f32 v239, v239, v138\n\t"
    "v_cvt_pk_bf16_f32 v136, v136, v137\n\t"
    "v_mfma_f32_32x32x16_bf16 v[176:191], a[76:79], a[44:47], v[176:191]\n\t"
    "ds_read_b128 a[72:75], v226 offset:32768\n\t"
    "v_exp_f32 v140, v140\n\t"
    "v_add_f32 v239, v239, v139\n\t"
    "v_mfma_f32_32x32x16_bf16 v[160:175], a[80:83], a[16:19], v[160:175]\n\t"
    "v_exp_f32 v141, v141\n\t"
    "v_add_f32 v239, v239, v140\n\t"
    "v_cvt_pk_bf16_f32 v137, v138, v139\n\t"
    "v_mfma_f32_32x32x16_bf16 v[176:191], a[80:83], a[48:51], v[176:191]\n\t"
    "ds_read_b128 a[76:79], v227 offset:32768\n\t"
    "v_exp_f32 v142, v142\n\t"
    "v_add_f32 v239, v239, v141\n\t"
    "v_mfma_f32_32x32x16_bf16 v[160:175], a[84:87], a[20:23], v[160:175]\n\t"
    "v_exp_f32 v143, v143\n\t"
    "v_add_f32 v239, v239, v142\n\t"
    "v_cvt_pk_bf16_f32 v138, v140, v141\n\t"
    "v_mfma_f32_32x32x16_bf16 v[176:191], a[84:87], a[52:55], v[176:191]\n\t"
    "ds_read_b128 a[80:83], v228 offset:32768\n\t"
    "v_exp_f32 v152, v152\n\t"
    "v_add_f32 v239, v239, v143\n\t"
    "v_mfma_f32_32x32x16_bf16 v[160:175], a[88:91], a[24:27], v[160:175]\n\t"
    "v_exp_f32 v153, v153\n\t"
    "v_add_f32 v240, v240, v152\n\t"
    "v_cvt_pk_bf16_f32 v139, v142, v143\n\t"
    "v_mfma_f32_32x32x16_bf16 v[176:191], a[88:91], a[56:59], v[176:191]\n\t"
    "ds_read_b128 a[84:87], v229 offset:32768\n\t"
    "v_exp_f32 v154, v154\n\t"
    "v_add_f32 v240, v240, v153\n\t"
    "v_mfma_f32_32x32x16_bf16 v[160:175], a[92:95], a[28:31], v[160:175]\n\t"
    "v_exp_f32 v155, v155\n\t"
    "v_add_f32 v240, v240, v154\n\t"
    "v_cvt_pk_bf16_f32 v152, v152, v153\n\t"
    "v_mfma_f32_32x32x16_bf16 v[176:191], a[92:95], a[60:63], v[176:191]\n\t"
    "ds_read_b128 a[88:91], v230 offset:32768\n\t"
    "v_exp_f32 v156, v156\n\t"
    "v_add_f32 v240, v240, v155\n\t"
    "s_waitcnt lgkmcnt(7)\n\t"
    "v_mfma_f32_32x32x16_bf16 v[0:15], a[96:99], v[128:131], v[0:15]\n\t"
    "v_exp_f32 v157, v157\n\t"
    "v_add_f32 v240, v240, v156\n\t"
    "v_cvt_pk_bf16_f32 v153, v154, v155\n\t"
    "v_mfma_f32_32x32x16_bf16 v[16:31], a[96:99], v[144:147], v[16:31]\n\t"
    "ds_read_b128 a[92:95], v231 offset:32768\n\t"
    "v_exp_f32 v158, v158\n\t"
    "v_add_f32 v240, v240, v157\n\t"
    "v_mfma_f32_32x32x16_bf16 v[32:47], a[100:103], v[128:131], v[32:47]\n\t"
    "ds_read_b64_tr_b16 a[96:97], v232 offset:24576\n\t"
    "v_exp_f32 v159, v159\n\t"
    "v_add_f32 v240, v240, v158\n\t"
    "v_cvt_pk_bf16_f32 v154, v156, v157\n\t"
    "v_mfma_f32_32x32x16_bf16 v[48:63], a[100:103], v[144:147], v[48:63]\n\t"
    "ds_read_b64_tr_b16 a[98:99], v232 offset:26624\n\t"
    "v_exp_f32 v160, v160\n\t"
    "v_add_f32 v240, v240, v159\n\t"
    "v_mfma_f32_32x32x16_bf16 v[64:79], a[104:107], v[128:131], v[64:79]\n\t"
    "ds_read_b64_tr_b16 a[100:101], v232 offset:25088\n\t"
    "v_exp_f32 v161, v161\n\t"
    "v_add_f32 v239, v239, v160\n\t"
    "v_cvt_pk_bf16_f32 v155, v158, v159\n\t"
    "v_mfma_f32_32x32x16_bf16 v[80:95], a[104:107], v[144:147], v[80:95]\n\t"
    "ds_read_b64_tr_b16 a[102:103], v232 offset:27136\n\t"
    "v_exp_f32 v162, v162\n\t"
    "v_add_f32 v239, v239, v161\n\t"
    "v_mfma_f32_32x32x16_bf16 v[96:111], a[108:111], v[128:131], v[96:111]\n\t"
    "ds_read_b64_tr_b16 a[104:105], v232 offset:25600\n\t"
    "v_exp_f32 v163, v163\n\t"
    "v_add_f32 v239, v239, v162\n\t"
    "v_cvt_pk_bf16_f32 v160, v160, v161\n\t"
    "v_mfma_f32_32x32x16_bf16 v[112:127], a[108:111], v[144:147], v[112:127]\n\t"
    "ds_read_b64_tr_b16 a[106:107], v232 offset:27648\n\t"
    "v_exp_f32 v164, v164\n\t"
    "v_add_f32 v239, v239, v163\n\t"
    "v_mfma_f32_32x32x16_bf16 v[0:15], a[112:115], v[136:139], v[0:15]\n\t"
    "ds_read_b64_tr_b16 a[108:109], v232 offset:26112\n\t"
    "v_exp_f32 v165, v165\n\t"
    "v_add_f32 v239, v239, v164\n\t"
    "v_cvt_pk_bf16_f32 v161, v162, v163\n\t"
    "v_mfma_f32_32x32x16_bf16 v[16:31], a[112:115], v[152:155], v[16:31]\n\t"
    "ds_read_b64_tr_b16 a[110:111], v232 offset:28160\n\t"
    "v_exp_f32 v166, v166\n\t"
    "v_add_f32 v239, v239, v165\n\t"
    "v_mfma_f32_32x32x16_bf16 v[32:47], a[116:119], v[136:139], v[32:47]\n\t"
    "ds_read_b64_tr_b16 a[112:113], v232 offset:28672\n\t"
    "v_exp_f32 v167, v167\n\t"
    "v_add_f32 v239, v239, v166\n\t"
    "v_cvt_pk_bf16_f32 v162, v164, v165\n\t"
    "v_mfma_f32_32x32x16_bf16 v[48:63], a[116:119], v[152:155], v[48:63]\n\t"
    "ds_read_b64_tr_b16 a[114:115], v232 offset:30720\n\t"
    "v_exp_f32 v176, v176\n\t"
    "v_add_f32 v239, v239, v167\n\t"
    "v_mfma_f32_32x32x16_bf16 v[64:79], a[120:123], v[136:139], v[64:79]\n\t"
    "ds_read_b64_tr_b16 a[116:117], v232 offset:29184\n\t"
    "v_exp_f32 v177, v177\n\t"
    "v_add_f32 v240, v240, v176\n\t"
    "v_cvt_pk_bf16_f32 v163, v166, v167\n\t"
    "v_mfma_f32_32x32x16_bf16 v[80:95], a[120:123], v[152:155], v[80:95]\n\t"
    "ds_read_b64_tr_b16 a[118:119], v232 offset:31232\n\t"
    "v_exp_f32 v178, v178\n\t"
    "v_add_f32 v240, v240, v177\n\t"
    "v_mfma_f32_32x32x16_bf16 v[96:111], a[124:127], v[136:139], v[96:111]\n\t"
    "ds_read_b64_tr_b16 a[120:121], v232 offset:29696\n\t"
    "v_exp_f32 v179, v179\n\t"
    "v_add_f32 v240, v240, v178\n\t"
    "v_cvt_pk_bf16_f32 v176, v176, v177\n\t"
    "v_mfma_f32_32x32x16_bf16 v[112:127], a[124:127], v[152:155], v[112:127]\n\t"
    "ds_read_b64_tr_b16 a[122:123], v232 offset:31744\n\t"
    "v_exp_f32 v180, v180\n\t"
    "v_add_f32 v240, v240, v179\n\t"
    "s_waitcnt lgkmcnt(14)\n\t"
    "v_mfma_f32_32x32x16_bf16 v[128:143], a[64:67], a[0:3], v[192:207]\n\t"
    "s_waitcnt vmcnt(9)\n\t"
    "s_barrier\n\t"
    "ds_read_b64_tr_b16 a[124:125], v232 offset:30208\n\t"
    "v_exp_f32 v181, v181\n\t"
    "v_add_f32 v240, v240, v180\n\t"
    "v_cvt_pk_bf16_f32 v177, v178, v179\n\t"
    "v_mfma_f32_32x32x16_bf16 v[144:159], a[64:67], a[32:35], v[208:223]\n\t"
    "ds_read_b64_tr_b16 a[126:127], v232 offset:32256\n\t"
    "v_exp_f32 v182, v182\n\t"
    "v_add_f32 v240, v240, v181\n\t"
    "v_mfma_f32_32x32x16_bf16 v[128:143], a[68:71], a[4:7], v[128:143]\n\t"
    "s_add_u32 m0, s77, 16384\n\t"
    "s_nop 0\n\t"
    "buffer_load_dwordx4 v233, s[64:67], s74 offen offset:0 nt lds\n\t"
    "v_exp_f32 v183, v183\n\t"
    "v_add_f32 v240, v240, v182\n\t"
    "v_cvt_pk_bf16_f32 v178, v180, v181\n\t"
    "v_mfma_f32_32x32x16_bf16 v[144:159], a[68:71], a[36:39], v[144:159]\n\t"
    "ds_read_b128 a[64:67], v224 offset:40960\n\t"
    "v_exp_f32 v168, v168\n\t"
    "v_add_f32 v240, v240, v183\n\t"
    "v_mfma_f32_32x32x16_bf16 v[128:143], a[72:75], a[8:11], v[128:143]\n\t"
    "buffer_load_dwordx4 v234, s[64:67], s74 offen offset:1024 nt lds\n\t"
    "v_exp_f32 v169, v169\n\t"
    "v_add_f32 v239, v239, v168\n\t"
    "v_cvt_pk_bf16_f32 v179, v182, v183\n\t"
    "v_mfma_f32_32x32x16_bf16 v[144:159], a[72:75], a[40:43], v[144:159]\n\t"
    "ds_read_b128 a[68:71], v225 offset:40960\n\t"
    "v_exp_f32 v170, v170\n\t"
    "v_add_f32 v239, v239, v169\n\t"
    "v_mfma_f32_32x32x16_bf16 v[128:143], a[76:79], a[12:15], v[128:143]\n\t"
    "buffer_load_dwordx4 v235, s[64:67], s74 offen offset:2048 nt lds\n\t"
    "v_exp_f32 v171, v171\n\t"
    "v_add_f32 v239, v239, v170\n\t"
    "v_cvt_pk_bf16_f32 v168, v168, v169\n\t"
    "v_mfma_f32_32x32x16_bf16 v[144:159], a[76:79], a[44:47], v[144:159]\n\t"
    "ds_read_b128 a[72:75], v226 offset:40960\n\t"
    "v_exp_f32 v172, v172\n\t"
    "v_add_f32 v239, v239, v171\n\t"
    "v_mfma_f32_32x32x16_bf16 v[128:143], a[80:83], a[16:19], v[128:143]\n\t"
    "buffer_load_dwordx4 v236, s[64:67], s74 offen offset:3072 nt lds\n\t"
    "s_add_u32 s74, s74, 16384\n\t"
    "v_exp_f32 v173, v173\n\t"
    "v_add_f32 v239, v239, v172\n\t"
    "v_cvt_pk_bf16_f32 v169, v170, v171\n\t"
    "v_mfma_f32_32x32x16_bf16 v[144:159], a[80:83], a[48:51], v[144:159]\n\t"
    "ds_read_b128 a[76:79], v227 offset:40960\n\t"
    "v_exp_f32 v174, v174\n\t"
    "v_add_f32 v239, v239, v173\n\t"
    "v_mfma_f32_32x32x16_bf16 v[128:143], a[84:87], a[20:23], v[128:143]\n\t"
    "s_add_u32 m0, s78, 0\n\t"
    "s_nop 0\n\t"
    "buffer_load_dwordx4 v237, s[68:71], s75 offen offset:0 nt lds\n\t"
    "v_exp_f32 v175, v175\n\t"
    "v_add_f32 v239, v239, v174\n\t"
    "v_cvt_pk_bf16_f32 v170, v172, v173\n\t"
    "v_mfma_f32_32x32x16_bf16 v[144:159], a[84:87], a[52:55], v[144:159]\n\t"
    "ds_read_b128 a[80:83], v228 offset:40960\n\t"
    "v_exp_f32 v184, v184\n\t"
    "v_add_f32 v239, v239, v175\n\t"
    "v_mfma_f32_32x32x16_bf16 v[128:143], a[88:91], a[24:27], v[128:143]\n\t"
    "buffer_load_dwordx4 v238, s[68:71], s75 offen offset:1024 nt lds\n\t"
    "v_exp_f32 v185, v185\n\t"
    "v_add_f32 v240, v240, v184\n\t"
    "v_cvt_pk_bf16_f32 v171, v174, v175\n\t"
    "v_mfma_f32_32x32x16_bf16 v[144:159], a[88:91], a[56:59], v[144:159]\n\t"
    "ds_read_b128 a[84:87], v229 offset:40960\n\t"
    "v_exp_f32 v186, v186\n\t"
    "v_add_f32 v240, v240, v185\n\t"
    "v_mfma_f32_32x32x16_bf16 v[128:143], a[92:95], a[28:31], v[128:143]\n\t"
    "buffer_load_dwordx4 v237, s[68:71], s75 offen offset:2048 nt lds\n\t"
    "v_exp_f32 v187, v187\n\t"
    "v_add_f32 v240, v240, v186\n\t"
    "v_cvt_pk_bf16_f32 v184, v184, v185\n\t"
    "v_mfma_f32_32x32x16_bf16 v[144:159], a[92:95], a[60:63], v[144:159]\n\t"
    "ds_read_b128 a[88:91], v230 offset:40960\n\t"
    "v_exp_f32 v188, v188\n\t"
    "v_add_f32 v240, v240, v187\n\t"
    "s_waitcnt lgkmcnt(7)\n\t"
    "v_mfma_f32_32x32x16_bf16 v[0:15], a[96:99], v[160:163], v[0:15]\n\t"
    "buffer_load_dwordx4 v238, s[68:71], s75 offen offset:3072 nt lds\n\t"
    "s_add_u32 s75, s75, 16384\n\t"
    "v_exp_f32 v189, v189\n\t"
    "v_add_f32 v240, v240, v188\n\t"
    "v_cvt_pk_bf16_f32 v185, v186, v187\n\t"
    "v_mfma_f32_32x32x16_bf16 v[16:31], a[96:99], v[176:179], v[16:31]\n\t"
    "ds_read_b128 a[92:95], v231 offset:40960\n\t"
    "v_exp_f32 v190, v190\n\t"
    "v_add_f32 v240, v240, v189\n\t"
    "v_mfma_f32_32x32x16_bf16 v[32:47], a[100:103], v[160:163], v[32:47]\n\t"
    "ds_read_b64_tr_b16 a[96:97], v232 offset:32768\n\t"
    "v_exp_f32 v191, v191\n\t"
    "v_add_f32 v240, v240, v190\n\t"
    "v_cvt_pk_bf16_f32 v186, v188, v189\n\t"
    "v_mfma_f32_32x32x16_bf16 v[48:63], a[100:103], v[176:179], v[48:63]\n\t"
    "ds_read_b64_tr_b16 a[98:99], v232 offset:34816\n\t"
    "v_exp_f32 v128, v128\n\t"
    "v_add_f32 v240, v240, v191\n\t"
    "v_mfma_f32_32x32x16_bf16 v[64:79], a[104:107], v[160:163], v[64:79]\n\t"
    "ds_read_b64_tr_b16 a[100:101], v232 offset:33280\n\t"
    "v_exp_f32 v129, v129\n\t"
    "v_add_f32 v239, v239, v128\n\t"
    "v_cvt_pk_bf16_f32 v187, v190, v191\n\t"
    "v_mfma_f32_32x32x16_bf16 v[80:95], a[104:107], v[176:179], v[80:95]\n\t"
    "ds_read_b64_tr_b16 a[102:103], v232 offset:35328\n\t"
    "v_exp_f32 v130, v130\n\t"
    "v_add_f32 v239, v239, v129\n\t"
    "v_mfma_f32_32x32x16_bf16 v[96:111], a[108:111], v[160:163], v[96:111]\n\t"
    "ds_read_b64_tr_b16 a[104:105], v232 offset:33792\n\t"
    "v_exp_f32 v131, v131\n\t"
    "v_add_f32 v239, v239, v130\n\t"
    "v_cvt_pk_bf16_f32 v128, v128, v129\n\t"
    "v_mfma_f32_32x32x16_bf16 v[112:127], a[108:111], v[176:179], v[112:127]\n\t"
    "ds_read_b64_tr_b16 a[106:107], v232 offset:35840\n\t"
    "v_exp_f32 v132, v132\n\t"
    "v_add_f32 v239, v239, v131\n\t"
    "v_mfma_f32_32x32x16_bf16 v[0:15], a[112:115], v[168:171], v[0:15]\n\t"
    "ds_read_b64_tr_b16 a[108:109], v232 offset:34304\n\t"
    "v_exp_f32 v133, v133\n\t"
    "v_add_f32 v239, v239, v132\n\t"
    "v_cvt_pk_bf16_f32 v129, v130, v131\n\t"
    "v_mfma_f32_32x32x16_bf16 v[16:31], a[112:115], v[184:187], v[16:31]\n\t"
    "ds_read_b64_tr_b16 a[110:111], v232 offset:36352\n\t"
    "v_exp_f32 v134, v134\n\t"
    "v_add_f32 v239, v239, v133\n\t"
    "v_mfma_f32_32x32x16_bf16 v[32:47], a[116:119], v[168:171], v[32:47]\n\t"
    "ds_read_b64_tr_b16 a[112:113], v232 offset:36864\n\t"
    "v_exp_f32 v135, v135\n\t"
    "v_add_f32 v239, v239, v134\n\t"
    "v_cvt_pk_bf16_f32 v130, v132, v133\n\t"
    "v_mfma_f32_32x32x16_bf16 v[48:63], a[116:119], v[184:187], v[48:63]\n\t"
    "ds_read_b64_tr_b16 a[114:115], v232 offset:38912\n\t"
    "v_exp_f32 v144, v144\n\t"
    "v_add_f32 v239, v239, v135\n\t"
    "v_mfma_f32_32x32x16_bf16 v[64:79], a[120:123], v[168:171], v[64:79]\n\t"
    "ds_read_b64_tr_b16 a[116:117], v232 offset:37376\n\t"
    "v_exp_f32 v145, v145\n\t"
    "v_add_f32 v240, v240, v144\n\t"
    "v_cvt_pk_bf16_f32 v131, v134, v135\n\t"
    "v_mfma_f32_32x32x16_bf16 v[80:95], a[120:123], v[184:187], v[80:95]\n\t"
    "ds_read_b64_tr_b16 a[118:119], v232 offset:39424\n\t"
    "v_exp_f32 v146, v146\n\t"
    "v_add_f32 v240, v240, v145\n\t"
    "v_mfma_f32_32x32x16_bf16 v[96:111], a[124:127], v[168:171], v[96:111]\n\t"
    "ds_read_b64_tr_b16 a[120:121], v232 offset:37888\n\t"
    "v_exp_f32 v147, v147\n\t"
    "v_add_f32 v240, v240, v146\n\t"
    "v_cvt_pk_bf16_f32 v144, v144, v145\n\t"
    "v_mfma_f32_32x32x16_bf16 v[112:127], a[124:127], v[184:187], v[112:127]\n\t"
    "ds_read_b64_tr_b16 a[122:123], v232 offset:39936\n\t"
    "v_exp_f32 v148, v148\n\t"
    "v_add_f32 v240, v240, v147\n\t"
    "s_waitcnt lgkmcnt(14)\n\t"
    "v_mfma_f32_32x32x16_bf16 v[160:175], a[64:67], a[0:3], v[192:207]\n\t"
    "ds_read_b64_tr_b16 a[124:125], v232 offset:38400\n\t"
    "v_exp_f32 v149, v149\n\t"
    "v_add_f32 v240, v240, v148\n\t"
    "v_cvt_pk_bf16_f32 v145, v146, v147\n\t"
    "v_mfma_f32_32x32x16_bf16 v[176:191], a[64:67], a[32:35], v[208:223]\n\t"
    "ds_read_b64_tr_b16 a[126:127], v232 offset:40448\n\t"
    "v_exp_f32 v150, v150\n\t"
    "v_add_f32 v240, v240, v149\n\t"
    "v_mfma_f32_32x32x16_bf16 v[160:175], a[68:71], a[4:7], v[160:175]\n\t"
    "v_exp_f32 v151, v151\n\t"
    "v_add_f32 v240, v240, v150\n\t"
    "v_cvt_pk_bf16_f32 v146, v148, v149\n\t"
    "v_mfma_f32_32x32x16_bf16 v[176:191], a[68:71], a[36:39], v[176:191]\n\t"
    "ds_read_b128 a[64:67], v224 offset:49152\n\t"
    "v_exp_f32 v136, v136\n\t"
    "v_add_f32 v240, v240, v151\n\t"
    "v_mfma_f32_32x32x16_bf16 v[160:175], a[72:75], a[8:11], v[160:175]\n\t"
    "v_exp_f32 v137, v137\n\t"
    "v_add_f32 v239, v239, v136\n\t"
    "v_cvt_pk_bf16_f32 v147, v150, v151\n\t"
    "v_mfma_f32_32x32x16_bf16 v[176:191], a[72:75], a[40:43], v[176:191]\n\t"
    "ds_read_b128 a[68:71], v225 offset:49152\n\t"
    "v_exp_f32 v138, v138\n\t"
    "v_add_f32 v239, v239, v137\n\t"
    "v_mfma_f32_32x32x16_bf16 v[160:175], a[76:79], a[12:15], v[160:175]\n\t"
    "v_exp_f32 v139, v139\n\t"
    "v_add_f32 v239, v239, v138\n\t"
    "v_cvt_pk_bf16_f32 v136, v136, v137\n\t"
    "v_mfma_f32_32x32x16_bf16 v[176:191], a[76:79], a[44:47], v[176:191]\n\t"
    "ds_read_b128 a[72:75], v226 offset:49152\n\t"
    "v_exp_f32 v140, v140\n\t"
    "v_add_f32 v239, v239, v139\n\t"
    "v_mfma_f32_32x32x16_bf16 v[160:175], a[80:83], a[16:19], v[160:175]\n\t"
    "v_exp_f32 v141, v141\n\t"
    "v_add_f32 v239, v239, v140\n\t"
    "v_cvt_pk_bf16_f32 v137, v138, v139\n\t"
    "v_mfma_f32_32x32x16_bf16 v[176:191], a[80:83], a[48:51], v[176:191]\n\t"
    "ds_read_b128 a[76:79], v227 offset:49152\n\t"
    "v_exp_f32 v142, v142\n\t"
    "v_add_f32 v239, v239, v141\n\t"
    "v_mfma_f32_32x32x16_bf16 v[160:175], a[84:87], a[20:23], v[160:175]\n\t"
    "v_exp_f32 v143, v143\n\t"
    "v_add_f32 v239, v239, v142\n\t"
    "v_cvt_pk_bf16_f32 v138, v140, v141\n\t"
    "v_mfma_f32_32x32x16_bf16 v[176:191], a[84:87], a[52:55], v[176:191]\n\t"
    "ds_read_b128 a[80:83], v228 offset:49152\n\t"
    "v_exp_f32 v152, v152\n\t"
    "v_add_f32 v239, v239, v143\n\t"
    "v_mfma_f32_32x32x16_bf16 v[160:175], a[88:91], a[24:27], v[160:175]\n\t"
    "v_exp_f32 v153, v153\n\t"
    "v_add_f32 v240, v240, v152\n\t"
    "v_cvt_pk_bf16_f32 v139, v142, v143\n\t"
    "v_mfma_f32_32x32x16_bf16 v[176:191], a[88:91], a[56:59], v[176:191]\n\t"
    "ds_read_b128 a[84:87], v229 offset:49152\n\t"
    "v_exp_f32 v154, v154\n\t"
    "v_add_f32 v240, v240, v153\n\t"
    "v_mfma_f32_32x32x16_bf16 v[160:175], a[92:95], a[28:31], v[160:175]\n\t"
    "v_exp_f32 v155, v155\n\t"
    "v_add_f32 v240, v240, v154\n\t"
    "v_cvt_pk_bf16_f32 v152, v152, v153\n\t"
    "v_mfma_f32_32x32x16_bf16 v[176:191], a[92:95], a[60:63], v[176:191]\n\t"
    "ds_read_b128 a[88:91], v230 offset:49152\n\t"
    "v_exp_f32 v156, v156\n\t"
    "v_add_f32 v240, v240, v155\n\t"
    "s_waitcnt lgkmcnt(7)\n\t"
    "v_mfma_f32_32x32x16_bf16 v[0:15], a[96:99], v[128:131], v[0:15]\n\t"
    "v_exp_f32 v157, v157\n\t"
    "v_add_f32 v240, v240, v156\n\t"
    "v_cvt_pk_bf16_f32 v153, v154, v155\n\t"
    "v_mfma_f32_32x32x16_bf16 v[16:31], a[96:99], v[144:147], v[16:31]\n\t"
    "ds_read_b128 a[92:95], v231 offset:49152\n\t"
    "v_exp_f32 v158, v158\n\t"
    "v_add_f32 v240, v240, v157\n\t"
    "v_mfma_f32_32x32x16_bf16 v[32:47], a[100:103], v[128:131], v[32:47]\n\t"
    "ds_read_b64_tr_b16 a[96:97], v232 offset:40960\n\t"
    "v_exp_f32 v159, v159\n\t"
    "v_add_f32 v240, v240, v158\n\t"
    "v_cvt_pk_bf16_f32 v154, v156, v157\n\t"
    "v_mfma_f32_32x32x16_bf16 v[48:63], a[100:103], v[144:147], v[48:63]\n\t"
    "ds_read_b64_tr_b16 a[98:99], v232 offset:43008\n\t"
    "v_exp_f32 v160, v160\n\t"
    "v_add_f32 v240, v240, v159\n\t"
    "v_mfma_f32_32x32x16_bf16 v[64:79], a[104:107], v[128:131], v[64:79]\n\t"
    "ds_read_b64_tr_b16 a[100:101], v232 offset:41472\n\t"
    "v_exp_f32 v161, v161\n\t"
    "v_add_f32 v239, v239, v160\n\t"
    "v_cvt_pk_bf16_f32 v155, v158, v159\n\t"
    "v_mfma_f32_32x32x16_bf16 v[80:95], a[104:107], v[144:147], v[80:95]\n\t"
    "ds_read_b64_tr_b16 a[102:103], v232 offset:43520\n\t"
    "v_exp_f32 v162, v162\n\t"
    "v_add_f32 v239, v239, v161\n\t"
    "v_mfma_f32_32x32x16_bf16 v[96:111], a[108:111], v[128:131], v[96:111]\n\t"
    "ds_read_b64_tr_b16 a[104:105], v232 offset:41984\n\t"
    "v_exp_f32 v163, v163\n\t"
    "v_add_f32 v239, v239, v162\n\t"
    "v_cvt_pk_bf16_f32 v160, v160, v161\n\t"
    "v_mfma_f32_32x32x16_bf16 v[112:127], a[108:111], v[144:147], v[112:127]\n\t"
    "ds_read_b64_tr_b16 a[106:107], v232 offset:44032\n\t"
    "v_exp_f32 v164, v164\n\t"
    "v_add_f32 v239, v239, v163\n\t"
    "v_mfma_f32_32x32x16_bf16 v[0:15], a[112:115], v[136:139], v[0:15]\n\t"
    "ds_read_b64_tr_b16 a[108:109], v232 offset:42496\n\t"
    "v_exp_f32 v165, v165\n\t"
    "v_add_f32 v239, v239, v164\n\t"
    "v_cvt_pk_bf16_f32 v161, v162, v163\n\t"
    "v_mfma_f32_32x32x16_bf16 v[16:31], a[112:115], v[152:155], v[16:31]\n\t"
    "ds_read_b64_tr_b16 a[110:111], v232 offset:44544\n\t"
    "v_exp_f32 v166, v166\n\t"
    "v_add_f32 v239, v239, v165\n\t"
    "v_mfma_f32_32x32x16_bf16 v[32:47], a[116:119], v[136:139], v[32:47]\n\t"
    "ds_read_b64_tr_b16 a[112:113], v232 offset:45056\n\t"
    "v_exp_f32 v167, v167\n\t"
    "v_add_f32 v239, v239, v166\n\t"
    "v_cvt_pk_bf16_f32 v162, v164, v165\n\t"
    "v_mfma_f32_32x32x16_bf16 v[48:63], a[116:119], v[152:155], v[48:63]\n\t"
    "ds_read_b64_tr_b16 a[114:115], v232 offset:47104\n\t"
    "v_exp_f32 v176, v176\n\t"
    "v_add_f32 v239, v239, v167\n\t"
    "v_mfma_f32_32x32x16_bf16 v[64:79], a[120:123], v[136:139], v[64:79]\n\t"
    "ds_read_b64_tr_b16 a[116:117], v232 offset:45568\n\t"
    "v_exp_f32 v177, v177\n\t"
    "v_add_f32 v240, v240, v176\n\t"
    "v_cvt_pk_bf16_f32 v163, v166, v167\n\t"
    "v_mfma_f32_32x32x16_bf16 v[80:95], a[120:123], v[152:155], v[80:95]\n\t"
    "ds_read_b64_tr_b16 a[118:119], v232 offset:47616\n\t"
    "v_exp_f32 v178, v178\n\t"
    "v_add_f32 v240, v240, v177\n\t"
    "v_mfma_f32_32x32x16_bf16 v[96:111], a[124:127], v[136:139], v[96:111]\n\t"
    "ds_read_b64_tr_b16 a[120:121], v232 offset:46080\n\t"
    "v_exp_f32 v179, v179\n\t"
    "v_add_f32 v240, v240, v178\n\t"
    "v_cvt_pk_bf16_f32 v176, v176, v177\n\t"
    "v_mfma_f32_32x32x16_bf16 v[112:127], a[124:127], v[152:155], v[112:127]\n\t"
    "ds_read_b64_tr_b16 a[122:123], v232 offset:48128\n\t"
    "v_exp_f32 v180, v180\n\t"
    "v_add_f32 v240, v240, v179\n\t"
    "s_waitcnt lgkmcnt(14)\n\t"
    "v_mfma_f32_32x32x16_bf16 v[128:143], a[64:67], a[0:3], v[192:207]\n\t"
    "s_waitcnt vmcnt(9)\n\t"
    "s_barrier\n\t"
    "ds_read_b64_tr_b16 a[124:125], v232 offset:46592\n\t"
    "v_exp_f32 v181, v181\n\t"
    "v_add_f32 v240, v240, v180\n\t"
    "v_cvt_pk_bf16_f32 v177, v178, v179\n\t"
    "v_mfma_f32_32x32x16_bf16 v[144:159], a[64:67], a[32:35], v[208:223]\n\t"
    "ds_read_b64_tr_b16 a[126:127], v232 offset:48640\n\t"
    "v_exp_f32 v182, v182\n\t"
    "v_add_f32 v240, v240, v181\n\t"
    "v_mfma_f32_32x32x16_bf16 v[128:143], a[68:71], a[4:7], v[128:143]\n\t"
    "s_add_u32 m0, s77, 32768\n\t"
    "s_nop 0\n\t"
    "buffer_load_dwordx4 v233, s[64:67], s74 offen offset:0 nt lds\n\t"
    "v_exp_f32 v183, v183\n\t"
    "v_add_f32 v240, v240, v182\n\t"
    "v_cvt_pk_bf16_f32 v178, v180, v181\n\t"
    "v_mfma_f32_32x32x16_bf16 v[144:159], a[68:71], a[36:39], v[144:159]\n\t"
    "ds_read_b128 a[64:67], v224 offset:57344\n\t"
    "v_exp_f32 v168, v168\n\t"
    "v_add_f32 v240, v240, v183\n\t"
    "v_mfma_f32_32x32x16_bf16 v[128:143], a[72:75], a[8:11], v[128:143]\n\t"
    "buffer_load_dwordx4 v234, s[64:67], s74 offen offset:1024 nt lds\n\t"
    "v_exp_f32 v169, v169\n\t"
    "v_add_f32 v239, v239, v168\n\t"
    "v_cvt_pk_bf16_f32 v179, v182, v183\n\t"
    "v_mfma_f32_32x32x16_bf16 v[144:159], a[72:75], a[40:43], v[144:159]\n\t"
    "ds_read_b128 a[68:71], v225 offset:57344\n\t"
    "v_exp_f32 v170, v170\n\t"
    "v_add_f32 v239, v239, v169\n\t"
    "v_mfma_f32_32x32x16_bf16 v[128:143], a[76:79], a[12:15], v[128:143]\n\t"
    "buffer_load_dwordx4 v235, s[64:67], s74 offen offset:2048 nt lds\n\t"
    "v_exp_f32 v171, v171\n\t"
    "v_add_f32 v239, v239, v170\n\t"
    "v_cvt_pk_bf16_f32 v168, v168, v169\n\t"
    "v_mfma_f32_32x32x16_bf16 v[144:159], a[76:79], a[44:47], v[144:159]\n\t"
    "ds_read_b128 a[72:75], v226 offset:57344\n\t"
    "v_exp_f32 v172, v172\n\t"
    "v_add_f32 v239, v239, v171\n\t"
    "v_mfma_f32_32x32x16_bf16 v[128:143], a[80:83], a[16:19], v[128:143]\n\t"
    "buffer_load_dwordx4 v236, s[64:67], s74 offen offset:3072 nt lds\n\t"
    "s_add_u32 s74, s74, 16384\n\t"
    "v_exp_f32 v173, v173\n\t"
    "v_add_f32 v239, v239, v172\n\t"
    "v_cvt_pk_bf16_f32 v169, v170, v171\n\t"
    "v_mfma_f32_32x32x16_bf16 v[144:159], a[80:83], a[48:51], v[144:159]\n\t"
    "ds_read_b128 a[76:79], v227 offset:57344\n\t"
    "v_exp_f32 v174, v174\n\t"
    "v_add_f32 v239, v239, v173\n\t"
    "v_mfma_f32_32x32x16_bf16 v[128:143], a[84:87], a[20:23], v[128:143]\n\t"
    "s_add_u32 m0, s78, 16384\n\t"
    "s_nop 0\n\t"
    "buffer_load_dwordx4 v237, s[68:71], s75 offen offset:0 nt lds\n\t"
    "v_exp_f32 v175, v175\n\t"
    "v_add_f32 v239, v239, v174\n\t"
    "v_cvt_pk_bf16_f32 v170, v172, v173\n\t"
    "v_mfma_f32_32x32x16_bf16 v[144:159], a[84:87], a[52:55], v[144:159]\n\t"
    "ds_read_b128 a[80:83], v228 offset:57344\n\t"
    "v_exp_f32 v184, v184\n\t"
    "v_add_f32 v239, v239, v175\n\t"
    "v_mfma_f32_32x32x16_bf16 v[128:143], a[88:91], a[24:27], v[128:143]\n\t"
    "buffer_load_dwordx4 v238, s[68:71], s75 offen offset:1024 nt lds\n\t"
    "v_exp_f32 v185, v185\n\t"
    "v_add_f32 v240, v240, v184\n\t"
    "v_cvt_pk_bf16_f32 v171, v174, v175\n\t"
    "v_mfma_f32_32x32x16_bf16 v[144:159], a[88:91], a[56:59], v[144:159]\n\t"
    "ds_read_b128 a[84:87], v229 offset:57344\n\t"
    "v_exp_f32 v186, v186\n\t"
    "v_add_f32 v240, v240, v185\n\t"
    "v_mfma_f32_32x32x16_bf16 v[128:143], a[92:95], a[28:31], v[128:143]\n\t"
    "buffer_load_dwordx4 v237, s[68:71], s75 offen offset:2048 nt lds\n\t"
    "v_exp_f32 v187, v187\n\t"
    "v_add_f32 v240, v240, v186\n\t"
    "v_cvt_pk_bf16_f32 v184, v184, v185\n\t"
    "v_mfma_f32_32x32x16_bf16 v[144:159], a[92:95], a[60:63], v[144:159]\n\t"
    "ds_read_b128 a[88:91], v230 offset:57344\n\t"
    "v_exp_f32 v188, v188\n\t"
    "v_add_f32 v240, v240, v187\n\t"
    "s_waitcnt lgkmcnt(7)\n\t"
    "v_mfma_f32_32x32x16_bf16 v[0:15], a[96:99], v[160:163], v[0:15]\n\t"
    "buffer_load_dwordx4 v238, s[68:71], s75 offen offset:3072 nt lds\n\t"
    "s_add_u32 s75, s75, 16384\n\t"
    "v_exp_f32 v189, v189\n\t"
    "v_add_f32 v240, v240, v188\n\t"
    "v_cvt_pk_bf16_f32 v185, v186, v187\n\t"
    "v_mfma_f32_32x32x16_bf16 v[16:31], a[96:99], v[176:179], v[16:31]\n\t"
    "ds_read_b128 a[92:95], v231 offset:57344\n\t"
    "v_exp_f32 v190, v190\n\t"
    "v_add_f32 v240, v240, v189\n\t"
    "v_mfma_f32_32x32x16_bf16 v[32:47], a[100:103], v[160:163], v[32:47]\n\t"
    "ds_read_b64_tr_b16 a[96:97], v232 offset:49152\n\t"
    "v_exp_f32 v191, v191\n\t"
    "v_add_f32 v240, v240, v190\n\t"
    "v_cvt_pk_bf16_f32 v186, v188, v189\n\t"
    "v_mfma_f32_32x32x16_bf16 v[48:63], a[100:103], v[176:179], v[48:63]\n\t"
    "ds_read_b64_tr_b16 a[98:99], v232 offset:51200\n\t"
    "v_exp_f32 v128, v128\n\t"
    "v_add_f32 v240, v240, v191\n\t"
    "v_mfma_f32_32x32x16_bf16 v[64:79], a[104:107], v[160:163], v[64:79]\n\t"
    "ds_read_b64_tr_b16 a[100:101], v232 offset:49664\n\t"
    "v_exp_f32 v129, v129\n\t"
    "v_add_f32 v239, v239, v128\n\t"
    "v_cvt_pk_bf16_f32 v187, v190, v191\n\t"
    "v_mfma_f32_32x32x16_bf16 v[80:95], a[104:107], v[176:179], v[80:95]\n\t"
    "ds_read_b64_tr_b16 a[102:103], v232 offset:51712\n\t"
    "v_exp_f32 v130, v130\n\t"
    "v_add_f32 v239, v239, v129\n\t"
    "v_mfma_f32_32x32x16_bf16 v[96:111], a[108:111], v[160:163], v[96:111]\n\t"
    "ds_read_b64_tr_b16 a[104:105], v232 offset:50176\n\t"
    "v_exp_f32 v131, v131\n\t"
    "v_add_f32 v239, v239, v130\n\t"
    "v_cvt_pk_bf16_f32 v128, v128, v129\n\t"
    "v_mfma_f32_32x32x16_bf16 v[112:127], a[108:111], v[176:179], v[112:127]\n\t"
    "ds_read_b64_tr_b16 a[106:107], v232 offset:52224\n\t"
    "v_exp_f32 v132, v132\n\t"
    "v_add_f32 v239, v239, v131\n\t"
    "v_mfma_f32_32x32x16_bf16 v[0:15], a[112:115], v[168:171], v[0:15]\n\t"
    "ds_read_b64_tr_b16 a[108:109], v232 offset:50688\n\t"
    "v_exp_f32 v133, v133\n\t"
    "v_add_f32 v239, v239, v132\n\t"
    "v_cvt_pk_bf16_f32 v129, v130, v131\n\t"
    "v_mfma_f32_32x32x16_bf16 v[16:31], a[112:115], v[184:187], v[16:31]\n\t"
    "ds_read_b64_tr_b16 a[110:111], v232 offset:52736\n\t"
    "v_exp_f32 v134, v134\n\t"
    "v_add_f32 v239, v239, v133\n\t"
    "v_mfma_f32_32x32x16_bf16 v[32:47], a[116:119], v[168:171], v[32:47]\n\t"
    "ds_read_b64_tr_b16 a[112:113], v232 offset:53248\n\t"
    "v_exp_f32 v135, v135\n\t"
    "v_add_f32 v239, v239, v134\n\t"
    "v_cvt_pk_bf16_f32 v130, v132, v133\n\t"
    "v_mfma_f32_32x32x16_bf16 v[48:63], a[116:119], v[184:187], v[48:63]\n\t"
    "ds_read_b64_tr_b16 a[114:115], v232 offset:55296\n\t"
    "v_exp_f32 v144, v144\n\t"
    "v_add_f32 v239, v239, v135\n\t"
    "v_mfma_f32_32x32x16_bf16 v[64:79], a[120:123], v[168:171], v[64:79]\n\t"
    "ds_read_b64_tr_b16 a[116:117], v232 offset:53760\n\t"
    "v_exp_f32 v145, v145\n\t"
    "v_add_f32 v240, v240, v144\n\t"
    "v_cvt_pk_bf16_f32 v131, v134, v135\n\t"
    "v_mfma_f32_32x32x16_bf16 v[80:95], a[120:123], v[184:187], v[80:95]\n\t"
    "ds_read_b64_tr_b16 a[118:119], v232 offset:55808\n\t"
    "v_exp_f32 v146, v146\n\t"
    "v_add_f32 v240, v240, v145\n\t"
    "v_mfma_f32_32x32x16_bf16 v[96:111], a[124:127], v[168:171], v[96:111]\n\t"
    "ds_read_b64_tr_b16 a[120:121], v232 offset:54272\n\t"
    "v_exp_f32 v147, v147\n\t"
    "v_add_f32 v240, v240, v146\n\t"
    "v_cvt_pk_bf16_f32 v144, v144, v145\n\t"
    "v_mfma_f32_32x32x16_bf16 v[112:127], a[124:127], v[184:187], v[112:127]\n\t"
    "ds_read_b64_tr_b16 a[122:123], v232 offset:56320\n\t"
    "v_exp_f32 v148, v148\n\t"
    "v_add_f32 v240, v240, v147\n\t"
    "s_waitcnt lgkmcnt(14)\n\t"
    "v_mfma_f32_32x32x16_bf16 v[160:175], a[64:67], a[0:3], v[192:207]\n\t"
    "ds_read_b64_tr_b16 a[124:125], v232 offset:54784\n\t"
    "v_exp_f32 v149, v149\n\t"
    "v_add_f32 v240, v240, v148\n\t"
    "v_cvt_pk_bf16_f32 v145, v146, v147\n\t"
    "v_mfma_f32_32x32x16_bf16 v[176:191], a[64:67], a[32:35], v[208:223]\n\t"
    "ds_read_b64_tr_b16 a[126:127], v232 offset:56832\n\t"
    "v_exp_f32 v150, v150\n\t"
    "v_add_f32 v240, v240, v149\n\t"
    "v_mfma_f32_32x32x16_bf16 v[160:175], a[68:71], a[4:7], v[160:175]\n\t"
    "v_exp_f32 v151, v151\n\t"
    "v_add_f32 v240, v240, v150\n\t"
    "v_cvt_pk_bf16_f32 v146, v148, v149\n\t"
    "v_mfma_f32_32x32x16_bf16 v[176:191], a[68:71], a[36:39], v[176:191]\n\t"
    "ds_read_b128 a[64:67], v224 offset:0\n\t"
    "v_exp_f32 v136, v136\n\t"
    "v_add_f32 v240, v240, v151\n\t"
    "v_mfma_f32_32x32x16_bf16 v[160:175], a[72:75], a[8:11], v[160:175]\n\t"
    "v_exp_f32 v137, v137\n\t"
    "v_add_f32 v239, v239, v136\n\t"
    "v_cvt_pk_bf16_f32 v147, v150, v151\n\t"
    "v_mfma_f32_32x32x16_bf16 v[176:191], a[72:75], a[40:43], v[176:191]\n\t"
    "ds_read_b128 a[68:71], v225 offset:0\n\t"
    "v_exp_f32 v138, v138\n\t"
    "v_add_f32 v239, v239, v137\n\t"
    "v_mfma_f32_32x32x16_bf16 v[160:175], a[76:79], a[12:15], v[160:175]\n\t"
    "v_exp_f32 v139, v139\n\t"
    "v_add_f32 v239, v239, v138\n\t"
    "v_cvt_pk_bf16_f32 v136, v136, v137\n\t"
    "v_mfma_f32_32x32x16_bf16 v[176:191], a[76:79], a[44:47], v[176:191]\n\t"
    "ds_read_b128 a[72:75], v226 offset:0\n\t"
    "v_exp_f32 v140, v140\n\t"
    "v_add_f32 v239, v239, v139\n\t"
    "v_mfma_f32_32x32x16_bf16 v[160:175], a[80:83], a[16:19], v[160:175]\n\t"
    "v_exp_f32 v141, v141\n\t"
    "v_add_f32 v239, v239, v140\n\t"
    "v_cvt_pk_bf16_f32 v137, v138, v139\n\t"
    "v_mfma_f32_32x32x16_bf16 v[176:191], a[80:83], a[48:51], v[176:191]\n\t"
    "ds_read_b128 a[76:79], v227 offset:0\n\t"
    "v_exp_f32 v142, v142\n\t"
    "v_add_f32 v239, v239, v141\n\t"
    "v_mfma_f32_32x32x16_bf16 v[160:175], a[84:87], a[20:23], v[160:175]\n\t"
    "v_exp_f32 v143, v143\n\t"
    "v_add_f32 v239, v239, v142\n\t"
    "v_cvt_pk_bf16_f32 v138, v140, v141\n\t"
    "v_mfma_f32_32x32x16_bf16 v[176:191], a[84:87], a[52:55], v[176:191]\n\t"
    "ds_read_b128 a[80:83], v228 offset:0\n\t"
    "v_exp_f32 v152, v152\n\t"
    "v_add_f32 v239, v239, v143\n\t"
    "v_mfma_f32_32x32x16_bf16 v[160:175], a[88:91], a[24:27], v[160:175]\n\t"
    "v_exp_f32 v153, v153\n\t"
    "v_add_f32 v240, v240, v152\n\t"
    "v_cvt_pk_bf16_f32 v139, v142, v143\n\t"
    "v_mfma_f32_32x32x16_bf16 v[176:191], a[88:91], a[56:59], v[176:191]\n\t"
    "ds_read_b128 a[84:87], v229 offset:0\n\t"
    "v_exp_f32 v154, v154\n\t"
    "v_add_f32 v240, v240, v153\n\t"
    "v_mfma_f32_32x32x16_bf16 v[160:175], a[92:95], a[28:31], v[160:175]\n\t"
    "v_exp_f32 v155, v155\n\t"
    "v_add_f32 v240, v240, v154\n\t"
    "v_cvt_pk_bf16_f32 v152, v152, v153\n\t"
    "v_mfma_f32_32x32x16_bf16 v[176:191], a[92:95], a[60:63], v[176:191]\n\t"
    "ds_read_b128 a[88:91], v230 offset:0\n\t"
    "v_exp_f32 v156, v156\n\t"
    "v_add_f32 v240, v240, v155\n\t"
    "s_waitcnt lgkmcnt(7)\n\t"
    "v_mfma_f32_32x32x16_bf16 v[0:15], a[96:99], v[128:131], v[0:15]\n\t"
    "v_exp_f32 v157, v157\n\t"
    "v_add_f32 v240, v240, v156\n\t"
    "v_cvt_pk_bf16_f32 v153, v154, v155\n\t"
    "v_mfma_f32_32x32x16_bf16 v[16:31], a[96:99], v[144:147], v[16:31]\n\t"
    "ds_read_b128 a[92:95], v231 offset:0\n\t"
    "v_exp_f32 v158, v158\n\t"
    "v_add_f32 v240, v240, v157\n\t"
    "v_mfma_f32_32x32x16_bf16 v[32:47], a[100:103], v[128:131], v[32:47]\n\t"
    "ds_read_b64_tr_b16 a[96:97], v232 offset:57344\n\t"
    "v_exp_f32 v159, v159\n\t"
    "v_add_f32 v240, v240, v158\n\t"
    "v_cvt_pk_bf16_f32 v154, v156, v157\n\t"
    "v_mfma_f32_32x32x16_bf16 v[48:63], a[100:103], v[144:147], v[48:63]\n\t"
    "ds_read_b64_tr_b16 a[98:99], v232 offset:59392\n\t"
    "v_exp_f32 v160, v160\n\t"
    "v_add_f32 v240, v240, v159\n\t"
    "v_mfma_f32_32x32x16_bf16 v[64:79], a[104:107], v[128:131], v[64:79]\n\t"
    "ds_read_b64_tr_b16 a[100:101], v232 offset:57856\n\t"
    "v_exp_f32 v161, v161\n\t"
    "v_add_f32 v239, v239, v160\n\t"
    "v_cvt_pk_bf16_f32 v155, v158, v159\n\t"
    "v_mfma_f32_32x32x16_bf16 v[80:95], a[104:107], v[144:147], v[80:95]\n\t"
    "ds_read_b64_tr_b16 a[102:103], v232 offset:59904\n\t"
    "v_exp_f32 v162, v162\n\t"
    "v_add_f32 v239, v239, v161\n\t"
    "v_mfma_f32_32x32x16_bf16 v[96:111], a[108:111], v[128:131], v[96:111]\n\t"
    "ds_read_b64_tr_b16 a[104:105], v232 offset:58368\n\t"
    "v_exp_f32 v163, v163\n\t"
    "v_add_f32 v239, v239, v162\n\t"
    "v_cvt_pk_bf16_f32 v160, v160, v161\n\t"
    "v_mfma_f32_32x32x16_bf16 v[112:127], a[108:111], v[144:147], v[112:127]\n\t"
    "ds_read_b64_tr_b16 a[106:107], v232 offset:60416\n\t"
    "v_exp_f32 v164, v164\n\t"
    "v_add_f32 v239, v239, v163\n\t"
    "v_mfma_f32_32x32x16_bf16 v[0:15], a[112:115], v[136:139], v[0:15]\n\t"
    "ds_read_b64_tr_b16 a[108:109], v232 offset:58880\n\t"
    "v_exp_f32 v165, v165\n\t"
    "v_add_f32 v239, v239, v164\n\t"
    "v_cvt_pk_bf16_f32 v161, v162, v163\n\t"
    "v_mfma_f32_32x32x16_bf16 v[16:31], a[112:115], v[152:155], v[16:31]\n\t"
    "ds_read_b64_tr_b16 a[110:111], v232 offset:60928\n\t"
    "v_exp_f32 v166, v166\n\t"
    "v_add_f32 v239, v239, v165\n\t"
    "v_mfma_f32_32x32x16_bf16 v[32:47], a[116:119], v[136:139], v[32:47]\n\t"
    "ds_read_b64_tr_b16 a[112:113], v232 offset:61440\n\t"
    "v_exp_f32 v167, v167\n\t"
    "v_add_f32 v239, v239, v166\n\t"
    "v_cvt_pk_bf16_f32 v162, v164, v165\n\t"
    "v_mfma_f32_32x32x16_bf16 v[48:63], a[116:119], v[152:155], v[48:63]\n\t"
    "ds_read_b64_tr_b16 a[114:115], v232 offset:63488\n\t"
    "v_exp_f32 v176, v176\n\t"
    "v_add_f32 v239, v239, v167\n\t"
    "v_mfma_f32_32x32x16_bf16 v[64:79], a[120:123], v[136:139], v[64:79]\n\t"
    "ds_read_b64_tr_b16 a[116:117], v232 offset:61952\n\t"
    "v_exp_f32 v177, v177\n\t"
    "v_add_f32 v240, v240, v176\n\t"
    "v_cvt_pk_bf16_f32 v163, v166, v167\n\t"
    "v_mfma_f32_32x32x16_bf16 v[80:95], a[120:123], v[152:155], v[80:95]\n\t"
    "ds_read_b64_tr_b16 a[118:119], v232 offset:64000\n\t"
    "v_exp_f32 v178, v178\n\t"
    "v_add_f32 v240, v240, v177\n\t"
    "v_mfma_f32_32x32x16_bf16 v[96:111], a[124:127], v[136:139], v[96:111]\n\t"
    "ds_read_b64_tr_b16 a[120:121], v232 offset:62464\n\t"
    "v_exp_f32 v179, v179\n\t"
    "v_add_f32 v240, v240, v178\n\t"
    "v_cvt_pk_bf16_f32 v176, v176, v177\n\t"
    "v_mfma_f32_32x32x16_bf16 v[112:127], a[124:127], v[152:155], v[112:127]\n\t"
    "ds_read_b64_tr_b16 a[122:123], v232 offset:64512\n\t"
    "v_exp_f32 v180, v180\n\t"
    "v_add_f32 v240, v240, v179\n\t"
    "s_waitcnt lgkmcnt(14)\n\t"
    "v_mfma_f32_32x32x16_bf16 v[128:143], a[64:67], a[0:3], v[192:207]\n\t"
    "s_waitcnt vmcnt(8)\n\t"
    "s_barrier\n\t"
    "ds_read_b64_tr_b16 a[124:125], v232 offset:62976\n\t"
    "v_exp_f32 v181, v181\n\t"
    "v_add_f32 v240, v240, v180\n\t"
    "v_cvt_pk_bf16_f32 v177, v178, v179\n\t"
    "v_mfma_f32_32x32x16_bf16 v[144:159], a[64:67], a[32:35], v[208:223]\n\t"
    "ds_read_b64_tr_b16 a[126:127], v232 offset:65024\n\t"
    "v_exp_f32 v182, v182\n\t"
    "v_add_f32 v240, v240, v181\n\t"
    "v_mfma_f32_32x32x16_bf16 v[128:143], a[68:71], a[4:7], v[128:143]\n\t"
    "s_add_u32 m0, s77, 49152\n\t"
    "s_nop 0\n\t"
    "buffer_load_dwordx4 v233, s[64:67], s74 offen offset:0 nt lds\n\t"
    "v_exp_f32 v183, v183\n\t"
    "v_add_f32 v240, v240, v182\n\t"
    "v_cvt_pk_bf16_f32 v178, v180, v181\n\t"
    "v_mfma_f32_32x32x16_bf16 v[144:159], a[68:71], a[36:39], v[144:159]\n\t"
    "ds_read_b128 a[64:67], v224 offset:8192\n\t"
    "v_exp_f32 v168, v168\n\t"
    "v_add_f32 v240, v240, v183\n\t"
    "v_mfma_f32_32x32x16_bf16 v[128:143], a[72:75], a[8:11], v[128:143]\n\t"
    "buffer_load_dwordx4 v234, s[64:67], s74 offen offset:1024 nt lds\n\t"
    "v_exp_f32 v169, v169\n\t"
    "v_add_f32 v239, v239, v168\n\t"
    "v_cvt_pk_bf16_f32 v179, v182, v183\n\t"
    "v_mfma_f32_32x32x16_bf16 v[144:159], a[72:75], a[40:43], v[144:159]\n\t"
    "ds_read_b128 a[68:71], v225 offset:8192\n\t"
    "v_exp_f32 v170, v170\n\t"
    "v_add_f32 v239, v239, v169\n\t"
    "v_mfma_f32_32x32x16_bf16 v[128:143], a[76:79], a[12:15], v[128:143]\n\t"
    "buffer_load_dwordx4 v235, s[64:67], s74 offen offset:2048 nt lds\n\t"
    "v_exp_f32 v171, v171\n\t"
    "v_add_f32 v239, v239, v170\n\t"
    "v_cvt_pk_bf16_f32 v168, v168, v169\n\t"
    "v_mfma_f32_32x32x16_bf16 v[144:159], a[76:79], a[44:47], v[144:159]\n\t"
    "ds_read_b128 a[72:75], v226 offset:8192\n\t"
    "v_exp_f32 v172, v172\n\t"
    "v_add_f32 v239, v239, v171\n\t"
    "v_mfma_f32_32x32x16_bf16 v[128:143], a[80:83], a[16:19], v[128:143]\n\t"
    "buffer_load_dwordx4 v236, s[64:67], s74 offen offset:3072 nt lds\n\t"
    "s_add_u32 s74, s74, 16384\n\t"
    "v_exp_f32 v173, v173\n\t"
    "v_add_f32 v239, v239, v172\n\t"
    "v_cvt_pk_bf16_f32 v169, v170, v171\n\t"
    "v_mfma_f32_32x32x16_bf16 v[144:159], a[80:83], a[48:51], v[144:159]\n\t"
    "ds_read_b128 a[76:79], v227 offset:8192\n\t"
    "v_exp_f32 v174, v174\n\t"
    "v_add_f32 v239, v239, v173\n\t"
    "v_mfma_f32_32x32x16_bf16 v[128:143], a[84:87], a[20:23], v[128:143]\n\t"
    "s_add_u32 m0, s78, 32768\n\t"
    "s_nop 0\n\t"
    "buffer_load_dwordx4 v237, s[68:71], s75 offen offset:0 nt lds\n\t"
    "v_exp_f32 v175, v175\n\t"
    "v_add_f32 v239, v239, v174\n\t"
    "v_cvt_pk_bf16_f32 v170, v172, v173\n\t"
    "v_mfma_f32_32x32x16_bf16 v[144:159], a[84:87], a[52:55], v[144:159]\n\t"
    "ds_read_b128 a[80:83], v228 offset:8192\n\t"
    "v_exp_f32 v184, v184\n\t"
    "v_add_f32 v239, v239, v175\n\t"
    "v_mfma_f32_32x32x16_bf16 v[128:143], a[88:91], a[24:27], v[128:143]\n\t"
    "buffer_load_dwordx4 v238, s[68:71], s75 offen offset:1024 nt lds\n\t"
    "v_exp_f32 v185, v185\n\t"
    "v_add_f32 v240, v240, v184\n\t"
    "v_cvt_pk_bf16_f32 v171, v174, v175\n\t"
    "v_mfma_f32_32x32x16_bf16 v[144:159], a[88:91], a[56:59], v[144:159]\n\t"
    "ds_read_b128 a[84:87], v229 offset:8192\n\t"
    "v_exp_f32 v186, v186\n\t"
    "v_add_f32 v240, v240, v185\n\t"
    "v_mfma_f32_32x32x16_bf16 v[128:143], a[92:95], a[28:31], v[128:143]\n\t"
    "buffer_load_dwordx4 v237, s[68:71], s75 offen offset:2048 nt lds\n\t"
    "v_exp_f32 v187, v187\n\t"
    "v_add_f32 v240, v240, v186\n\t"
    "v_cvt_pk_bf16_f32 v184, v184, v185\n\t"
    "v_mfma_f32_32x32x16_bf16 v[144:159], a[92:95], a[60:63], v[144:159]\n\t"
    "ds_read_b128 a[88:91], v230 offset:8192\n\t"
    "v_exp_f32 v188, v188\n\t"
    "v_add_f32 v240, v240, v187\n\t"
    "s_waitcnt lgkmcnt(7)\n\t"
    "v_mfma_f32_32x32x16_bf16 v[0:15], a[96:99], v[160:163], v[0:15]\n\t"
    "buffer_load_dwordx4 v238, s[68:71], s75 offen offset:3072 nt lds\n\t"
    "s_add_u32 s75, s75, 16384\n\t"
    "v_exp_f32 v189, v189\n\t"
    "v_add_f32 v240, v240, v188\n\t"
    "v_cvt_pk_bf16_f32 v185, v186, v187\n\t"
    "v_mfma_f32_32x32x16_bf16 v[16:31], a[96:99], v[176:179], v[16:31]\n\t"
    "ds_read_b128 a[92:95], v231 offset:8192\n\t"
    "v_exp_f32 v190, v190\n\t"
    "v_add_f32 v240, v240, v189\n\t"
    "v_mfma_f32_32x32x16_bf16 v[32:47], a[100:103], v[160:163], v[32:47]\n\t"
    "ds_read_b64_tr_b16 a[96:97], v232 offset:0\n\t"
    "v_exp_f32 v191, v191\n\t"
    "v_add_f32 v240, v240, v190\n\t"
    "v_cvt_pk_bf16_f32 v186, v188, v189\n\t"
    "v_mfma_f32_32x32x16_bf16 v[48:63], a[100:103], v[176:179], v[48:63]\n\t"
    "ds_read_b64_tr_b16 a[98:99], v232 offset:2048\n\t"
    "v_exp_f32 v128, v128\n\t"
    "v_add_f32 v240, v240, v191\n\t"
    "v_mfma_f32_32x32x16_bf16 v[64:79], a[104:107], v[160:163], v[64:79]\n\t"
    "ds_read_b64_tr_b16 a[100:101], v232 offset:512\n\t"
    "v_exp_f32 v129, v129\n\t"
    "v_add_f32 v239, v239, v128\n\t"
    "v_cvt_pk_bf16_f32 v187, v190, v191\n\t"
    "v_mfma_f32_32x32x16_bf16 v[80:95], a[104:107], v[176:179], v[80:95]\n\t"
    "ds_read_b64_tr_b16 a[102:103], v232 offset:2560\n\t"
    "v_exp_f32 v130, v130\n\t"
    "v_add_f32 v239, v239, v129\n\t"
    "v_mfma_f32_32x32x16_bf16 v[96:111], a[108:111], v[160:163], v[96:111]\n\t"
    "ds_read_b64_tr_b16 a[104:105], v232 offset:1024\n\t"
    "v_exp_f32 v131, v131\n\t"
    "v_add_f32 v239, v239, v130\n\t"
    "v_cvt_pk_bf16_f32 v128, v128, v129\n\t"
    "v_mfma_f32_32x32x16_bf16 v[112:127], a[108:111], v[176:179], v[112:127]\n\t"
    "ds_read_b64_tr_b16 a[106:107], v232 offset:3072\n\t"
    "v_exp_f32 v132, v132\n\t"
    "v_add_f32 v239, v239, v131\n\t"
    "v_mfma_f32_32x32x16_bf16 v[0:15], a[112:115], v[168:171], v[0:15]\n\t"
    "ds_read_b64_tr_b16 a[108:109], v232 offset:1536\n\t"
    "v_exp_f32 v133, v133\n\t"
    "v_add_f32 v239, v239, v132\n\t"
    "v_cvt_pk_bf16_f32 v129, v130, v131\n\t"
    "v_mfma_f32_32x32x16_bf16 v[16:31], a[112:115], v[184:187], v[16:31]\n\t"
    "ds_read_b64_tr_b16 a[110:111], v232 offset:3584\n\t"
    "v_exp_f32 v134, v134\n\t"
    "v_add_f32 v239, v239, v133\n\t"
    "v_mfma_f32_32x32x16_bf16 v[32:47], a[116:119], v[168:171], v[32:47]\n\t"
    "ds_read_b64_tr_b16 a[112:113], v232 offset:4096\n\t"
    "v_exp_f32 v135, v135\n\t"
    "v_add_f32 v239, v239, v134\n\t"
    "v_cvt_pk_bf16_f32 v130, v132, v133\n\t"
    "v_mfma_f32_32x32x16_bf16 v[48:63], a[116:119], v[184:187], v[48:63]\n\t"
    "ds_read_b64_tr_b16 a[114:115], v232 offset:6144\n\t"
    "v_exp_f32 v144, v144\n\t"
    "v_add_f32 v239, v239, v135\n\t"
    "v_mfma_f32_32x32x16_bf16 v[64:79], a[120:123], v[168:171], v[64:79]\n\t"
    "ds_read_b64_tr_b16 a[116:117], v232 offset:4608\n\t"
    "v_exp_f32 v145, v145\n\t"
    "v_add_f32 v240, v240, v144\n\t"
    "v_cvt_pk_bf16_f32 v131, v134, v135\n\t"
    "v_mfma_f32_32x32x16_bf16 v[80:95], a[120:123], v[184:187], v[80:95]\n\t"
    "ds_read_b64_tr_b16 a[118:119], v232 offset:6656\n\t"
    "v_exp_f32 v146, v146\n\t"
    "v_add_f32 v240, v240, v145\n\t"
    "v_mfma_f32_32x32x16_bf16 v[96:111], a[124:127], v[168:171], v[96:111]\n\t"
    "ds_read_b64_tr_b16 a[120:121], v232 offset:5120\n\t"
    "v_exp_f32 v147, v147\n\t"
    "v_add_f32 v240, v240, v146\n\t"
    "v_cvt_pk_bf16_f32 v144, v144, v145\n\t"
    "v_mfma_f32_32x32x16_bf16 v[112:127], a[124:127], v[184:187], v[112:127]\n\t"
    "ds_read_b64_tr_b16 a[122:123], v232 offset:7168\n\t"
    "v_exp_f32 v148, v148\n\t"
    "v_add_f32 v240, v240, v147\n\t"
    "s_waitcnt lgkmcnt(14)\n\t"
    "v_mfma_f32_32x32x16_bf16 v[160:175], a[64:67], a[0:3], v[192:207]\n\t"
    "ds_read_b64_tr_b16 a[124:125], v232 offset:5632\n\t"
    "v_exp_f32 v149, v149\n\t"
    "v_add_f32 v240, v240, v148\n\t"
    "v_cvt_pk_bf16_f32 v145, v146, v147\n\t"
    "v_mfma_f32_32x32x16_bf16 v[176:191], a[64:67], a[32:35], v[208:223]\n\t"
    "ds_read_b64_tr_b16 a[126:127], v232 offset:7680\n\t"
    "v_exp_f32 v150, v150\n\t"
    "v_add_f32 v240, v240, v149\n\t"
    "v_mfma_f32_32x32x16_bf16 v[160:175], a[68:71], a[4:7], v[160:175]\n\t"
    "v_exp_f32 v151, v151\n\t"
    "v_add_f32 v240, v240, v150\n\t"
    "v_cvt_pk_bf16_f32 v146, v148, v149\n\t"
    "v_mfma_f32_32x32x16_bf16 v[176:191], a[68:71], a[36:39], v[176:191]\n\t"
    "ds_read_b128 a[64:67], v224 offset:16384\n\t"
    "v_exp_f32 v136, v136\n\t"
    "v_add_f32 v240, v240, v151\n\t"
    "v_mfma_f32_32x32x16_bf16 v[160:175], a[72:75], a[8:11], v[160:175]\n\t"
    "v_exp_f32 v137, v137\n\t"
    "v_add_f32 v239, v239, v136\n\t"
    "v_cvt_pk_bf16_f32 v147, v150, v151\n\t"
    "v_mfma_f32_32x32x16_bf16 v[176:191], a[72:75], a[40:43], v[176:191]\n\t"
    "ds_read_b128 a[68:71], v225 offset:16384\n\t"
    "v_exp_f32 v138, v138\n\t"
    "v_add_f32 v239, v239, v137\n\t"
    "v_mfma_f32_32x32x16_bf16 v[160:175], a[76:79], a[12:15], v[160:175]\n\t"
    "v_exp_f32 v139, v139\n\t"
    "v_add_f32 v239, v239, v138\n\t"
    "v_cvt_pk_bf16_f32 v136, v136, v137\n\t"
    "v_mfma_f32_32x32x16_bf16 v[176:191], a[76:79], a[44:47], v[176:191]\n\t"
    "ds_read_b128 a[72:75], v226 offset:16384\n\t"
    "v_exp_f32 v140, v140\n\t"
    "v_add_f32 v239, v239, v139\n\t"
    "v_mfma_f32_32x32x16_bf16 v[160:175], a[80:83], a[16:19], v[160:175]\n\t"
    "v_exp_f32 v141, v141\n\t"
    "v_add_f32 v239, v239, v140\n\t"
    "v_cvt_pk_bf16_f32 v137, v138, v139\n\t"
    "v_mfma_f32_32x32x16_bf16 v[176:191], a[80:83], a[48:51], v[176:191]\n\t"
    "ds_read_b128 a[76:79], v227 offset:16384\n\t"
    "v_exp_f32 v142, v142\n\t"
    "v_add_f32 v239, v239, v141\n\t"
    "v_mfma_f32_32x32x16_bf16 v[160:175], a[84:87], a[20:23], v[160:175]\n\t"
    "v_exp_f32 v143, v143\n\t"
    "v_add_f32 v239, v239, v142\n\t"
    "v_cvt_pk_bf16_f32 v138, v140, v141\n\t"
    "v_mfma_f32_32x32x16_bf16 v[176:191], a[84:87], a[52:55], v[176:191]\n\t"
    "ds_read_b128 a[80:83], v228 offset:16384\n\t"
    "v_exp_f32 v152, v152\n\t"
    "v_add_f32 v239, v239, v143\n\t"
    "v_mfma_f32_32x32x16_bf16 v[160:175], a[88:91], a[24:27], v[160:175]\n\t"
    "v_exp_f32 v153, v153\n\t"
    "v_add_f32 v240, v240, v152\n\t"
    "v_cvt_pk_bf16_f32 v139, v142, v143\n\t"
    "v_mfma_f32_32x32x16_bf16 v[176:191], a[88:91], a[56:59], v[176:191]\n\t"
    "ds_read_b128 a[84:87], v229 offset:16384\n\t"
    "v_exp_f32 v154, v154\n\t"
    "v_add_f32 v240, v240, v153\n\t"
    "v_mfma_f32_32x32x16_bf16 v[160:175], a[92:95], a[28:31], v[160:175]\n\t"
    "v_exp_f32 v155, v155\n\t"
    "v_add_f32 v240, v240, v154\n\t"
    "v_cvt_pk_bf16_f32 v152, v152, v153\n\t"
    "v_mfma_f32_32x32x16_bf16 v[176:191], a[92:95], a[60:63], v[176:191]\n\t"
    "ds_read_b128 a[88:91], v230 offset:16384\n\t"
    "v_exp_f32 v156, v156\n\t"
    "v_add_f32 v240, v240, v155\n\t"
    "s_waitcnt lgkmcnt(7)\n\t"
    "v_mfma_f32_32x32x16_bf16 v[0:15], a[96:99], v[128:131], v[0:15]\n\t"
    "v_exp_f32 v157, v157\n\t"
    "v_add_f32 v240, v240, v156\n\t"
    "v_cvt_pk_bf16_f32 v153, v154, v155\n\t"
    "v_mfma_f32_32x32x16_bf16 v[16:31], a[96:99], v[144:147], v[16:31]\n\t"
    "ds_read_b128 a[92:95], v231 offset:16384\n\t"
    "v_exp_f32 v158, v158\n\t"
    "v_add_f32 v240, v240, v157\n\t"
    "v_mfma_f32_32x32x16_bf16 v[32:47], a[100:103], v[128:131], v[32:47]\n\t"
    "ds_read_b64_tr_b16 a[96:97], v232 offset:8192\n\t"
    "v_exp_f32 v159, v159\n\t"
    "v_add_f32 v240, v240, v158\n\t"
    "v_cvt_pk_bf16_f32 v154, v156, v157\n\t"
    "v_mfma_f32_32x32x16_bf16 v[48:63], a[100:103], v[144:147], v[48:63]\n\t"
    "ds_read_b64_tr_b16 a[98:99], v232 offset:10240\n\t"
    "v_exp_f32 v160, v160\n\t"
    "v_add_f32 v240, v240, v159\n\t"
    "v_mfma_f32_32x32x16_bf16 v[64:79], a[104:107], v[128:131], v[64:79]\n\t"
    "ds_read_b64_tr_b16 a[100:101], v232 offset:8704\n\t"
    "v_exp_f32 v161, v161\n\t"
    "v_add_f32 v239, v239, v160\n\t"
    "v_cvt_pk_bf16_f32 v155, v158, v159\n\t"
    "v_mfma_f32_32x32x16_bf16 v[80:95], a[104:107], v[144:147], v[80:95]\n\t"
    "ds_read_b64_tr_b16 a[102:103], v232 offset:10752\n\t"
    "v_exp_f32 v162, v162\n\t"
    "v_add_f32 v239, v239, v161\n\t"
    "v_mfma_f32_32x32x16_bf16 v[96:111], a[108:111], v[128:131], v[96:111]\n\t"
    "ds_read_b64_tr_b16 a[104:105], v232 offset:9216\n\t"
    "v_exp_f32 v163, v163\n\t"
    "v_add_f32 v239, v239, v162\n\t"
    "v_cvt_pk_bf16_f32 v160, v160, v161\n\t"
    "v_mfma_f32_32x32x16_bf16 v[112:127], a[108:111], v[144:147], v[112:127]\n\t"
    "ds_read_b64_tr_b16 a[106:107], v232 offset:11264\n\t"
    "v_exp_f32 v164, v164\n\t"
    "v_add_f32 v239, v239, v163\n\t"
    "v_mfma_f32_32x32x16_bf16 v[0:15], a[112:115], v[136:139], v[0:15]\n\t"
    "ds_read_b64_tr_b16 a[108:109], v232 offset:9728\n\t"
    "v_exp_f32 v165, v165\n\t"
    "v_add_f32 v239, v239, v164\n\t"
    "v_cvt_pk_bf16_f32 v161, v162, v163\n\t"
    "v_mfma_f32_32x32x16_bf16 v[16:31], a[112:115], v[152:155], v[16:31]\n\t"
    "ds_read_b64_tr_b16 a[110:111], v232 offset:11776\n\t"
    "v_exp_f32 v166, v166\n\t"
    "v_add_f32 v239, v239, v165\n\t"
    "v_mfma_f32_32x32x16_bf16 v[32:47], a[116:119], v[136:139], v[32:47]\n\t"
    "ds_read_b64_tr_b16 a[112:113], v232 offset:12288\n\t"
    "v_exp_f32 v167, v167\n\t"
    "v_add_f32 v239, v239, v166\n\t"
    "v_cvt_pk_bf16_f32 v162, v164, v165\n\t"
    "v_mfma_f32_32x32x16_bf16 v[48:63], a[116:119], v[152:155], v[48:63]\n\t"
    "ds_read_b64_tr_b16 a[114:115], v232 offset:14336\n\t"
    "v_exp_f32 v176, v176\n\t"
    "v_add_f32 v239, v239, v167\n\t"
    "v_mfma_f32_32x32x16_bf16 v[64:79], a[120:123], v[136:139], v[64:79]\n\t"
    "ds_read_b64_tr_b16 a[116:117], v232 offset:12800\n\t"
    "v_exp_f32 v177, v177\n\t"
    "v_add_f32 v240, v240, v176\n\t"
    "v_cvt_pk_bf16_f32 v163, v166, v167\n\t"
    "v_mfma_f32_32x32x16_bf16 v[80:95], a[120:123], v[152:155], v[80:95]\n\t"
    "ds_read_b64_tr_b16 a[118:119], v232 offset:14848\n\t"
    "v_exp_f32 v178, v178\n\t"
    "v_add_f32 v240, v240, v177\n\t"
    "v_mfma_f32_32x32x16_bf16 v[96:111], a[124:127], v[136:139], v[96:111]\n\t"
    "ds_read_b64_tr_b16 a[120:121], v232 offset:13312\n\t"
    "v_exp_f32 v179, v179\n\t"
    "v_add_f32 v240, v240, v178\n\t"
    "v_cvt_pk_bf16_f32 v176, v176, v177\n\t"
    "v_mfma_f32_32x32x16_bf16 v[112:127], a[124:127], v[152:155], v[112:127]\n\t"
    "ds_read_b64_tr_b16 a[122:123], v232 offset:15360\n\t"
    "v_exp_f32 v180, v180\n\t"
    "v_add_f32 v240, v240, v179\n\t"
    "s_sub_u32 s90, s90, 1\n\t"
    "s_cmp_lg_u32 s90, 0\n\t"
    "s_cbranch_scc1 Lm_attn_1\n\t"
    "s_waitcnt lgkmcnt(14)\n\t"
    "v_mfma_f32_32x32x16_bf16 v[128:143], a[64:67], a[0:3], v[192:207]\n\t"
    "s_waitcnt vmcnt(8)\n\t"
    "s_barrier\n\t"
    "ds_read_b64_tr_b16 a[124:125], v232 offset:13824\n\t"
    "v_exp_f32 v181, v181\n\t"
    "v_add_f32 v240, v240, v180\n\t"
    "v_cvt_pk_bf16_f32 v177, v178, v179\n\t"
    "v_mfma_f32_32x32x16_bf16 v[144:159], a[64:67], a[32:35], v[208:223]\n\t"
    "ds_read_b64_tr_b16 a[126:127], v232 offset:15872\n\t"
    "v_exp_f32 v182, v182\n\t"
    "v_add_f32 v240, v240, v181\n\t"
    "v_mfma_f32_32x32x16_bf16 v[128:143], a[68:71], a[4:7], v[128:143]\n\t"
    "v_exp_f32 v183, v183\n\t"
    "v_add_f32 v240, v240, v182\n\t"
    "v_cvt_pk_bf16_f32 v178, v180, v181\n\t"
    "v_mfma_f32_32x32x16_bf16 v[144:159], a[68:71], a[36:39], v[144:159]\n\t"
    "ds_read_b128 a[64:67], v224 offset:24576\n\t"
    "v_exp_f32 v168, v168\n\t"
    "v_add_f32 v240, v240, v183\n\t"
    "v_mfma_f32_32x32x16_bf16 v[128:143], a[72:75], a[8:11], v[128:143]\n\t"
    "v_exp_f32 v169, v169\n\t"
    "v_add_f32 v239, v239, v168\n\t"
    "v_cvt_pk_bf16_f32 v179, v182, v183\n\t"
    "v_mfma_f32_32x32x16_bf16 v[144:159], a[72:75], a[40:43], v[144:159]\n\t"
    "ds_read_b128 a[68:71], v225 offset:24576\n\t"
    "v_exp_f32 v170, v170\n\t"
    "v_add_f32 v239, v239, v169\n\t"
    "v_mfma_f32_32x32x16_bf16 v[128:143], a[76:79], a[12:15], v[128:143]\n\t"
    "v_exp_f32 v171, v171\n\t"
    "v_add_f32 v239, v239, v170\n\t"
    "v_cvt_pk_bf16_f32 v168, v168, v169\n\t"
    "v_mfma_f32_32x32x16_bf16 v[144:159], a[76:79], a[44:47], v[144:159]\n\t"
    "ds_read_b128 a[72:75], v226 offset:24576\n\t"
    "v_exp_f32 v172, v172\n\t"
    "v_add_f32 v239, v239, v171\n\t"
    "v_mfma_f32_32x32x16_bf16 v[128:143], a[80:83], a[16:19], v[128:143]\n\t"
    "v_exp_f32 v173, v173\n\t"
    "v_add_f32 v239, v239, v172\n\t"
    "v_cvt_pk_bf16_f32 v169, v170, v171\n\t"
    "v_mfma_f32_32x32x16_bf16 v[144:159], a[80:83], a[48:51], v[144:159]\n\t"
    "ds_read_b128 a[76:79], v227 offset:24576\n\t"
    "v_exp_f32 v174, v174\n\t"
    "v_add_f32 v239, v239, v173\n\t"
    "v_mfma_f32_32x32x16_bf16 v[128:143], a[84:87], a[20:23], v[128:143]\n\t"
    "s_add_u32 m0, s78, 49152\n\t"
    "s_nop 0\n\t"
    "buffer_load_dwordx4 v237, s[68:71], s75 offen offset:0 nt lds\n\t"
    "v_exp_f32 v175, v175\n\t"
    "v_add_f32 v239, v239, v174\n\t"
    "v_cvt_pk_bf16_f32 v170, v172, v173\n\t"
    "v_mfma_f32_32x32x16_bf16 v[144:159], a[84:87], a[52:55], v[144:159]\n\t"
    "ds_read_b128 a[80:83], v228 offset:24576\n\t"
    "v_exp_f32 v184, v184\n\t"
    "v_add_f32 v239, v239, v175\n\t"
    "v_mfma_f32_32x32x16_bf16 v[128:143], a[88:91], a[24:27], v[128:143]\n\t"
    "buffer_load_dwordx4 v238, s[68:71], s75 offen offset:1024 nt lds\n\t"
    "v_exp_f32 v185, v185\n\t"
    "v_add_f32 v240, v240, v184\n\t"
    "v_cvt_pk_bf16_f32 v171, v174, v175\n\t"
    "v_mfma_f32_32x32x16_bf16 v[144:159], a[88:91], a[56:59], v[144:159]\n\t"
    "ds_read_b128 a[84:87], v229 offset:24576\n\t"
    "v_exp_f32 v186, v186\n\t"
    "v_add_f32 v240, v240, v185\n\t"
    "v_mfma_f32_32x32x16_bf16 v[128:143], a[92:95], a[28:31], v[128:143]\n\t"
    "buffer_load_dwordx4 v237, s[68:71], s75 offen offset:2048 nt lds\n\t"
    "v_exp_f32 v187, v187\n\t"
    "v_add_f32 v240, v240, v186\n\t"
    "v_cvt_pk_bf16_f32 v184, v184, v185\n\t"
    "v_mfma_f32_32x32x16_bf16 v[144:159], a[92:95], a[60:63], v[144:159]\n\t"
    "ds_read_b128 a[88:91], v230 offset:24576\n\t"
    "v_exp_f32 v188, v188\n\t"
    "v_add_f32 v240, v240, v187\n\t"
    "s_waitcnt lgkmcnt(7)\n\t"
    "v_mfma_f32_32x32x16_bf16 v[0:15], a[96:99], v[160:163], v[0:15]\n\t"
    "buffer_load_dwordx4 v238, s[68:71], s75 offen offset:3072 nt lds\n\t"
    "s_add_u32 s75, s75, 16384\n\t"
    "v_exp_f32 v189, v189\n\t"
    "v_add_f32 v240, v240, v188\n\t"
    "v_cvt_pk_bf16_f32 v185, v186, v187\n\t"
    "v_mfma_f32_32x32x16_bf16 v[16:31], a[96:99], v[176:179], v[16:31]\n\t"
    "ds_read_b128 a[92:95], v231 offset:24576\n\t"
    "v_exp_f32 v190, v190\n\t"
    "v_add_f32 v240, v240, v189\n\t"
    "v_mfma_f32_32x32x16_bf16 v[32:47], a[100:103], v[160:163], v[32:47]\n\t"
    "ds_read_b64_tr_b16 a[96:97], v232 offset:16384\n\t"
    "v_exp_f32 v191, v191\n\t"
    "v_add_f32 v240, v240, v190\n\t"
    "v_cvt_pk_bf16_f32 v186, v188, v189\n\t"
    "v_mfma_f32_32x32x16_bf16 v[48:63], a[100:103], v[176:179], v[48:63]\n\t"
    "ds_read_b64_tr_b16 a[98:99], v232 offset:18432\n\t"
    "v_exp_f32 v128, v128\n\t"
    "v_add_f32 v240, v240, v191\n\t"
    "v_mfma_f32_32x32x16_bf16 v[64:79], a[104:107], v[160:163], v[64:79]\n\t"
    "ds_read_b64_tr_b16 a[100:101], v232 offset:16896\n\t"
    "v_exp_f32 v129, v129\n\t"
    "v_add_f32 v239, v239, v128\n\t"
    "v_cvt_pk_bf16_f32 v187, v190, v191\n\t"
    "v_mfma_f32_32x32x16_bf16 v[80:95], a[104:107], v[176:179], v[80:95]\n\t"
    "ds_read_b64_tr_b16 a[102:103], v232 offset:18944\n\t"
    "v_exp_f32 v130, v130\n\t"
    "v_add_f32 v239, v239, v129\n\t"
    "v_mfma_f32_32x32x16_bf16 v[96:111], a[108:111], v[160:163], v[96:111]\n\t"
    "ds_read_b64_tr_b16 a[104:105], v232 offset:17408\n\t"
    "v_exp_f32 v131, v131\n\t"
    "v_add_f32 v239, v239, v130\n\t"
    "v_cvt_pk_bf16_f32 v128, v128, v129\n\t"
    "v_mfma_f32_32x32x16_bf16 v[112:127], a[108:111], v[176:179], v[112:127]\n\t"
    "ds_read_b64_tr_b16 a[106:107], v232 offset:19456\n\t"
    "v_exp_f32 v132, v132\n\t"
    "v_add_f32 v239, v239, v131\n\t"
    "v_mfma_f32_32x32x16_bf16 v[0:15], a[112:115], v[168:171], v[0:15]\n\t"
    "ds_read_b64_tr_b16 a[108:109], v232 offset:17920\n\t"
    "v_exp_f32 v133, v133\n\t"
    "v_add_f32 v239, v239, v132\n\t"
    "v_cvt_pk_bf16_f32 v129, v130, v131\n\t"
    "v_mfma_f32_32x32x16_bf16 v[16:31], a[112:115], v[184:187], v[16:31]\n\t"
    "ds_read_b64_tr_b16 a[110:111], v232 offset:19968\n\t"
    "v_exp_f32 v134, v134\n\t"
    "v_add_f32 v239, v239, v133\n\t"
    "v_mfma_f32_32x32x16_bf16 v[32:47], a[116:119], v[168:171], v[32:47]\n\t"
    "ds_read_b64_tr_b16 a[112:113], v232 offset:20480\n\t"
    "v_exp_f32 v135, v135\n\t"
    "v_add_f32 v239, v239, v134\n\t"
    "v_cvt_pk_bf16_f32 v130, v132, v133\n\t"
    "v_mfma_f32_32x32x16_bf16 v[48:63], a[116:119], v[184:187], v[48:63]\n\t"
    "ds_read_b64_tr_b16 a[114:115], v232 offset:22528\n\t"
    "v_exp_f32 v144, v144\n\t"
    "v_add_f32 v239, v239, v135\n\t"
    "v_mfma_f32_32x32x16_bf16 v[64:79], a[120:123], v[168:171], v[64:79]\n\t"
    "ds_read_b64_tr_b16 a[116:117], v232 offset:20992\n\t"
    "v_exp_f32 v145, v145\n\t"
    "v_add_f32 v240, v240, v144\n\t"
    "v_cvt_pk_bf16_f32 v131, v134, v135\n\t"
    "v_mfma_f32_32x32x16_bf16 v[80:95], a[120:123], v[184:187], v[80:95]\n\t"
    "ds_read_b64_tr_b16 a[118:119], v232 offset:23040\n\t"
    "v_exp_f32 v146, v146\n\t"
    "v_add_f32 v240, v240, v145\n\t"
    "v_mfma_f32_32x32x16_bf16 v[96:111], a[124:127], v[168:171], v[96:111]\n\t"
    "ds_read_b64_tr_b16 a[120:121], v232 offset:21504\n\t"
    "v_exp_f32 v147, v147\n\t"
    "v_add_f32 v240, v240, v146\n\t"
    "v_cvt_pk_bf16_f32 v144, v144, v145\n\t"
    "v_mfma_f32_32x32x16_bf16 v[112:127], a[124:127], v[184:187], v[112:127]\n\t"
    "ds_read_b64_tr_b16 a[122:123], v232 offset:23552\n\t"
    "v_exp_f32 v148, v148\n\t"
    "v_add_f32 v240, v240, v147\n\t"
    "s_waitcnt lgkmcnt(14)\n\t"
    "v_mfma_f32_32x32x16_bf16 v[160:175], a[64:67], a[0:3], v[192:207]\n\t"
    "ds_read_b64_tr_b16 a[124:125], v232 offset:22016\n\t"
    "v_exp_f32 v149, v149\n\t"
    "v_add_f32 v240, v240, v148\n\t"
    "v_cvt_pk_bf16_f32 v145, v146, v147\n\t"
    "v_mfma_f32_32x32x16_bf16 v[176:191], a[64:67], a[32:35], v[208:223]\n\t"
    "ds_read_b64_tr_b16 a[126:127], v232 offset:24064\n\t"
    "v_exp_f32 v150, v150\n\t"
    "v_add_f32 v240, v240, v149\n\t"
    "v_mfma_f32_32x32x16_bf16 v[160:175], a[68:71], a[4:7], v[160:175]\n\t"
    "v_exp_f32 v151, v151\n\t"
    "v_add_f32 v240, v240, v150\n\t"
    "v_cvt_pk_bf16_f32 v146, v148, v149\n\t"
    "v_mfma_f32_32x32x16_bf16 v[176:191], a[68:71], a[36:39], v[176:191]\n\t"
    "ds_read_b128 a[64:67], v224 offset:32768\n\t"
    "v_exp_f32 v136, v136\n\t"
    "v_add_f32 v240, v240, v151\n\t"
    "v_mfma_f32_32x32x16_bf16 v[160:175], a[72:75], a[8:11], v[160:175]\n\t"
    "v_exp_f32 v137, v137\n\t"
    "v_add_f32 v239, v239, v136\n\t"
    "v_cvt_pk_bf16_f32 v147, v150, v151\n\t"
    "v_mfma_f32_32x32x16_bf16 v[176:191], a[72:75], a[40:43], v[176:191]\n\t"
    "ds_read_b128 a[68:71], v225 offset:32768\n\t"
    "v_exp_f32 v138, v138\n\t"
    "v_add_f32 v239, v239, v137\n\t"
    "v_mfma_f32_32x32x16_bf16 v[160:175], a[76:79], a[12:15], v[160:175]\n\t"
    "v_exp_f32 v139, v139\n\t"
    "v_add_f32 v239, v239, v138\n\t"
    "v_cvt_pk_bf16_f32 v136, v136, v137\n\t"
    "v_mfma_f32_32x32x16_bf16 v[176:191], a[76:79], a[44:47], v[176:191]\n\t"
    "ds_read_b128 a[72:75], v226 offset:32768\n\t"
    "v_exp_f32 v140, v140\n\t"
    "v_add_f32 v239, v239, v139\n\t"
    "v_mfma_f32_32x32x16_bf16 v[160:175], a[80:83], a[16:19], v[160:175]\n\t"
    "v_exp_f32 v141, v141\n\t"
    "v_add_f32 v239, v239, v140\n\t"
    "v_cvt_pk_bf16_f32 v137, v138, v139\n\t"
    "v_mfma_f32_32x32x16_bf16 v[176:191], a[80:83], a[48:51], v[176:191]\n\t"
    "ds_read_b128 a[76:79], v227 offset:32768\n\t"
    "v_exp_f32 v142, v142\n\t"
    "v_add_f32 v239, v239, v141\n\t"
    "v_mfma_f32_32x32x16_bf16 v[160:175], a[84:87], a[20:23], v[160:175]\n\t"
    "v_exp_f32 v143, v143\n\t"
    "v_add_f32 v239, v239, v142\n\t"
    "v_cvt_pk_bf16_f32 v138, v140, v141\n\t"
    "v_mfma_f32_32x32x16_bf16 v[176:191], a[84:87], a[52:55], v[176:191]\n\t"
    "ds_read_b128 a[80:83], v228 offset:32768\n\t"
    "v_exp_f32 v152, v152\n\t"
    "v_add_f32 v239, v239, v143\n\t"
    "v_mfma_f32_32x32x16_bf16 v[160:175], a[88:91], a[24:27], v[160:175]\n\t"
    "v_exp_f32 v153, v153\n\t"
    "v_add_f32 v240, v240, v152\n\t"
    "v_cvt_pk_bf16_f32 v139, v142, v143\n\t"
    "v_mfma_f32_32x32x16_bf16 v[176:191], a[88:91], a[56:59], v[176:191]\n\t"
    "ds_read_b128 a[84:87], v229 offset:32768\n\t"
    "v_exp_f32 v154, v154\n\t"
    "v_add_f32 v240, v240, v153\n\t"
    "v_mfma_f32_32x32x16_bf16 v[160:175], a[92:95], a[28:31], v[160:175]\n\t"
    "v_exp_f32 v155, v155\n\t"
    "v_add_f32 v240, v240, v154\n\t"
    "v_cvt_pk_bf16_f32 v152, v152, v153\n\t"
    "v_mfma_f32_32x32x16_bf16 v[176:191], a[92:95], a[60:63], v[176:191]\n\t"
    "ds_read_b128 a[88:91], v230 offset:32768\n\t"
    "v_exp_f32 v156, v156\n\t"
    "v_add_f32 v240, v240, v155\n\t"
    "s_waitcnt lgkmcnt(7)\n\t"
    "v_mfma_f32_32x32x16_bf16 v[0:15], a[96:99], v[128:131], v[0:15]\n\t"
    "v_exp_f32 v157, v157\n\t"
    "v_add_f32 v240, v240, v156\n\t"
    "v_cvt_pk_bf16_f32 v153, v154, v155\n\t"
    "v_mfma_f32_32x32x16_bf16 v[16:31], a[96:99], v[144:147], v[16:31]\n\t"
    "ds_read_b128 a[92:95], v231 offset:32768\n\t"
    "v_exp_f32 v158, v158\n\t"
    "v_add_f32 v240, v240, v157\n\t"
    "v_mfma_f32_32x32x16_bf16 v[32:47], a[100:103], v[128:131], v[32:47]\n\t"
    "ds_read_b64_tr_b16 a[96:97], v232 offset:24576\n\t"
    "v_exp_f32 v159, v159\n\t"
    "v_add_f32 v240, v240, v158\n\t"
    "v_cvt_pk_bf16_f32 v154, v156, v157\n\t"
    "v_mfma_f32_32x32x16_bf16 v[48:63], a[100:103], v[144:147], v[48:63]\n\t"
    "ds_read_b64_tr_b16 a[98:99], v232 offset:26624\n\t"
    "v_exp_f32 v160, v160\n\t"
    "v_add_f32 v240, v240, v159\n\t"
    "v_mfma_f32_32x32x16_bf16 v[64:79], a[104:107], v[128:131], v[64:79]\n\t"
    "ds_read_b64_tr_b16 a[100:101], v232 offset:25088\n\t"
    "v_exp_f32 v161, v161\n\t"
    "v_add_f32 v239, v239, v160\n\t"
    "v_cvt_pk_bf16_f32 v155, v158, v159\n\t"
    "v_mfma_f32_32x32x16_bf16 v[80:95], a[104:107], v[144:147], v[80:95]\n\t"
    "ds_read_b64_tr_b16 a[102:103], v232 offset:27136\n\t"
    "v_exp_f32 v162, v162\n\t"
    "v_add_f32 v239, v239, v161\n\t"
    "v_mfma_f32_32x32x16_bf16 v[96:111], a[108:111], v[128:131], v[96:111]\n\t"
    "ds_read_b64_tr_b16 a[104:105], v232 offset:25600\n\t"
    "v_exp_f32 v163, v163\n\t"
    "v_add_f32 v239, v239, v162\n\t"
    "v_cvt_pk_bf16_f32 v160, v160, v161\n\t"
    "v_mfma_f32_32x32x16_bf16 v[112:127], a[108:111], v[144:147], v[112:127]\n\t"
    "ds_read_b64_tr_b16 a[106:107], v232 offset:27648\n\t"
    "v_exp_f32 v164, v164\n\t"
    "v_add_f32 v239, v239, v163\n\t"
    "v_mfma_f32_32x32x16_bf16 v[0:15], a[112:115], v[136:139], v[0:15]\n\t"
    "ds_read_b64_tr_b16 a[108:109], v232 offset:26112\n\t"
    "v_exp_f32 v165, v165\n\t"
    "v_add_f32 v239, v239, v164\n\t"
    "v_cvt_pk_bf16_f32 v161, v162, v163\n\t"
    "v_mfma_f32_32x32x16_bf16 v[16:31], a[112:115], v[152:155], v[16:31]\n\t"
    "ds_read_b64_tr_b16 a[110:111], v232 offset:28160\n\t"
    "v_exp_f32 v166, v166\n\t"
    "v_add_f32 v239, v239, v165\n\t"
    "v_mfma_f32_32x32x16_bf16 v[32:47], a[116:119], v[136:139], v[32:47]\n\t"
    "ds_read_b64_tr_b16 a[112:113], v232 offset:28672\n\t"
    "v_exp_f32 v167, v167\n\t"
    "v_add_f32 v239, v239, v166\n\t"
    "v_cvt_pk_bf16_f32 v162, v164, v165\n\t"
    "v_mfma_f32_32x32x16_bf16 v[48:63], a[116:119], v[152:155], v[48:63]\n\t"
    "ds_read_b64_tr_b16 a[114:115], v232 offset:30720\n\t"
    "v_exp_f32 v176, v176\n\t"
    "v_add_f32 v239, v239, v167\n\t"
    "v_mfma_f32_32x32x16_bf16 v[64:79], a[120:123], v[136:139], v[64:79]\n\t"
    "ds_read_b64_tr_b16 a[116:117], v232 offset:29184\n\t"
    "v_exp_f32 v177, v177\n\t"
    "v_add_f32 v240, v240, v176\n\t"
    "v_cvt_pk_bf16_f32 v163, v166, v167\n\t"
    "v_mfma_f32_32x32x16_bf16 v[80:95], a[120:123], v[152:155], v[80:95]\n\t"
    "ds_read_b64_tr_b16 a[118:119], v232 offset:31232\n\t"
    "v_exp_f32 v178, v178\n\t"
    "v_add_f32 v240, v240, v177\n\t"
    "v_mfma_f32_32x32x16_bf16 v[96:111], a[124:127], v[136:139], v[96:111]\n\t"
    "ds_read_b64_tr_b16 a[120:121], v232 offset:29696\n\t"
    "v_exp_f32 v179, v179\n\t"
    "v_add_f32 v240, v240, v178\n\t"
    "v_cvt_pk_bf16_f32 v176, v176, v177\n\t"
    "v_mfma_f32_32x32x16_bf16 v[112:127], a[124:127], v[152:155], v[112:127]\n\t"
    "ds_read_b64_tr_b16 a[122:123], v232 offset:31744\n\t"
    "v_exp_f32 v180, v180\n\t"
    "v_add_f32 v240, v240, v179\n\t"
    "s_waitcnt lgkmcnt(14)\n\t"
    "v_mfma_f32_32x32x16_bf16 v[128:143], a[64:67], a[0:3], v[192:207]\n\t"
    "s_waitcnt vmcnt(4)\n\t"
    "s_barrier\n\t"
    "ds_read_b64_tr_b16 a[124:125], v232 offset:30208\n\t"
    "v_exp_f32 v181, v181\n\t"
    "v_add_f32 v240, v240, v180\n\t"
    "v_cvt_pk_bf16_f32 v177, v178, v179\n\t"
    "v_mfma_f32_32x32x16_bf16 v[144:159], a[64:67], a[32:35], v[208:223]\n\t"
    "ds_read_b64_tr_b16 a[126:127], v232 offset:32256\n\t"
    "v_exp_f32 v182, v182\n\t"
    "v_add_f32 v240, v240, v181\n\t"
    "v_mfma_f32_32x32x16_bf16 v[128:143], a[68:71], a[4:7], v[128:143]\n\t"
    "v_exp_f32 v183, v183\n\t"
    "v_add_f32 v240, v240, v182\n\t"
    "v_cvt_pk_bf16_f32 v178, v180, v181\n\t"
    "v_mfma_f32_32x32x16_bf16 v[144:159], a[68:71], a[36:39], v[144:159]\n\t"
    "ds_read_b128 a[64:67], v224 offset:40960\n\t"
    "v_exp_f32 v168, v168\n\t"
    "v_add_f32 v240, v240, v183\n\t"
    "v_mfma_f32_32x32x16_bf16 v[128:143], a[72:75], a[8:11], v[128:143]\n\t"
    "v_exp_f32 v169, v169\n\t"
    "v_add_f32 v239, v239, v168\n\t"
    "v_cvt_pk_bf16_f32 v179, v182, v183\n\t"
    "v_mfma_f32_32x32x16_bf16 v[144:159], a[72:75], a[40:43], v[144:159]\n\t"
    "ds_read_b128 a[68:71], v225 offset:40960\n\t"
    "v_exp_f32 v170, v170\n\t"
    "v_add_f32 v239, v239, v169\n\t"
    "v_mfma_f32_32x32x16_bf16 v[128:143], a[76:79], a[12:15], v[128:143]\n\t"
    "v_exp_f32 v171, v171\n\t"
    "v_add_f32 v239, v239, v170\n\t"
    "v_cvt_pk_bf16_f32 v168, v168, v169\n\t"
    "v_mfma_f32_32x32x16_bf16 v[144:159], a[76:79], a[44:47], v[144:159]\n\t"
    "ds_read_b128 a[72:75], v226 offset:40960\n\t"
    "v_exp_f32 v172, v172\n\t"
    "v_add_f32 v239, v239, v171\n\t"
    "v_mfma_f32_32x32x16_bf16 v[128:143], a[80:83], a[16:19], v[128:143]\n\t"
    "v_exp_f32 v173, v173\n\t"
    "v_add_f32 v239, v239, v172\n\t"
    "v_cvt_pk_bf16_f32 v169, v170, v171\n\t"
    "v_mfma_f32_32x32x16_bf16 v[144:159], a[80:83], a[48:51], v[144:159]\n\t"
    "ds_read_b128 a[76:79], v227 offset:40960\n\t"
    "v_exp_f32 v174, v174\n\t"
    "v_add_f32 v239, v239, v173\n\t"
    "v_mfma_f32_32x32x16_bf16 v[128:143], a[84:87], a[20:23], v[128:143]\n\t"
    "v_exp_f32 v175, v175\n\t"
    "v_add_f32 v239, v239, v174\n\t"
    "v_cvt_pk_bf16_f32 v170, v172, v173\n\t"
    "v_mfma_f32_32x32x16_bf16 v[144:159], a[84:87], a[52:55], v[144:159]\n\t"
    "ds_read_b128 a[80:83], v228 offset:40960\n\t"
    "v_exp_f32 v184, v184\n\t"
    "v_add_f32 v239, v239, v175\n\t"
    "v_mfma_f32_32x32x16_bf16 v[128:143], a[88:91], a[24:27], v[128:143]\n\t"
    "v_exp_f32 v185, v185\n\t"
    "v_add_f32 v240, v240, v184\n\t"
    "v_cvt_pk_bf16_f32 v171, v174, v175\n\t"
    "v_mfma_f32_32x32x16_bf16 v[144:159], a[88:91], a[56:59], v[144:159]\n\t"
    "ds_read_b128 a[84:87], v229 offset:40960\n\t"
    "v_exp_f32 v186, v186\n\t"
    "v_add_f32 v240, v240, v185\n\t"
    "v_mfma_f32_32x32x16_bf16 v[128:143], a[92:95], a[28:31], v[128:143]\n\t"
    "v_exp_f32 v187, v187\n\t"
    "v_add_f32 v240, v240, v186\n\t"
    "v_cvt_pk_bf16_f32 v184, v184, v185\n\t"
    "v_mfma_f32_32x32x16_bf16 v[144:159], a[92:95], a[60:63], v[144:159]\n\t"
    "ds_read_b128 a[88:91], v230 offset:40960\n\t"
    "v_exp_f32 v188, v188\n\t"
    "v_add_f32 v240, v240, v187\n\t"
    "s_waitcnt lgkmcnt(7)\n\t"
    "v_mfma_f32_32x32x16_bf16 v[0:15], a[96:99], v[160:163], v[0:15]\n\t"
    "v_exp_f32 v189, v189\n\t"
    "v_add_f32 v240, v240, v188\n\t"
    "v_cvt_pk_bf16_f32 v185, v186, v187\n\t"
    "v_mfma_f32_32x32x16_bf16 v[16:31], a[96:99], v[176:179], v[16:31]\n\t"
    "ds_read_b128 a[92:95], v231 offset:40960\n\t"
    "v_exp_f32 v190, v190\n\t"
    "v_add_f32 v240, v240, v189\n\t"
    "v_mfma_f32_32x32x16_bf16 v[32:47], a[100:103], v[160:163], v[32:47]\n\t"
    "ds_read_b64_tr_b16 a[96:97], v232 offset:32768\n\t"
    "v_exp_f32 v191, v191\n\t"
    "v_add_f32 v240, v240, v190\n\t"
    "v_cvt_pk_bf16_f32 v186, v188, v189\n\t"
    "v_mfma_f32_32x32x16_bf16 v[48:63], a[100:103], v[176:179], v[48:63]\n\t"
    "ds_read_b64_tr_b16 a[98:99], v232 offset:34816\n\t"
    "v_exp_f32 v128, v128\n\t"
    "v_add_f32 v240, v240, v191\n\t"
    "v_mfma_f32_32x32x16_bf16 v[64:79], a[104:107], v[160:163], v[64:79]\n\t"
    "ds_read_b64_tr_b16 a[100:101], v232 offset:33280\n\t"
    "v_exp_f32 v129, v129\n\t"
    "v_add_f32 v239, v239, v128\n\t"
    "v_cvt_pk_bf16_f32 v187, v190, v191\n\t"
    "v_mfma_f32_32x32x16_bf16 v[80:95], a[104:107], v[176:179], v[80:95]\n\t"
    "ds_read_b64_tr_b16 a[102:103], v232 offset:35328\n\t"
    "v_exp_f32 v130, v130\n\t"
    "v_add_f32 v239, v239, v129\n\t"
    "v_mfma_f32_32x32x16_bf16 v[96:111], a[108:111], v[160:163], v[96:111]\n\t"
    "ds_read_b64_tr_b16 a[104:105], v232 offset:33792\n\t"
    "v_exp_f32 v131, v131\n\t"
    "v_add_f32 v239, v239, v130\n\t"
    "v_cvt_pk_bf16_f32 v128, v128, v129\n\t"
    "v_mfma_f32_32x32x16_bf16 v[112:127], a[108:111], v[176:179], v[112:127]\n\t"
    "ds_read_b64_tr_b16 a[106:107], v232 offset:35840\n\t"
    "v_exp_f32 v132, v132\n\t"
    "v_add_f32 v239, v239, v131\n\t"
    "v_mfma_f32_32x32x16_bf16 v[0:15], a[112:115], v[168:171], v[0:15]\n\t"
    "ds_read_b64_tr_b16 a[108:109], v232 offset:34304\n\t"
    "v_exp_f32 v133, v133\n\t"
    "v_add_f32 v239, v239, v132\n\t"
    "v_cvt_pk_bf16_f32 v129, v130, v131\n\t"
    "v_mfma_f32_32x32x16_bf16 v[16:31], a[112:115], v[184:187], v[16:31]\n\t"
    "ds_read_b64_tr_b16 a[110:111], v232 offset:36352\n\t"
    "v_exp_f32 v134, v134\n\t"
    "v_add_f32 v239, v239, v133\n\t"
    "v_mfma_f32_32x32x16_bf16 v[32:47], a[116:119], v[168:171], v[32:47]\n\t"
    "ds_read_b64_tr_b16 a[112:113], v232 offset:36864\n\t"
    "v_exp_f32 v135, v135\n\t"
    "v_add_f32 v239, v239, v134\n\t"
    "v_cvt_pk_bf16_f32 v130, v132, v133\n\t"
    "v_mfma_f32_32x32x16_bf16 v[48:63], a[116:119], v[184:187], v[48:63]\n\t"
    "ds_read_b64_tr_b16 a[114:115], v232 offset:38912\n\t"
    "v_exp_f32 v144, v144\n\t"
    "v_add_f32 v239, v239, v135\n\t"
    "v_mfma_f32_32x32x16_bf16 v[64:79], a[120:123], v[168:171], v[64:79]\n\t"
    "ds_read_b64_tr_b16 a[116:117], v232 offset:37376\n\t"
    "v_exp_f32 v145, v145\n\t"
    "v_add_f32 v240, v240, v144\n\t"
    "v_cvt_pk_bf16_f32 v131, v134, v135\n\t"
    "v_mfma_f32_32x32x16_bf16 v[80:95], a[120:123], v[184:187], v[80:95]\n\t"
    "ds_read_b64_tr_b16 a[118:119], v232 offset:39424\n\t"
    "v_exp_f32 v146, v146\n\t"
    "v_add_f32 v240, v240, v145\n\t"
    "v_mfma_f32_32x32x16_bf16 v[96:111], a[124:127], v[168:171], v[96:111]\n\t"
    "ds_read_b64_tr_b16 a[120:121], v232 offset:37888\n\t"
    "v_exp_f32 v147, v147\n\t"
    "v_add_f32 v240, v240, v146\n\t"
    "v_cvt_pk_bf16_f32 v144, v144, v145\n\t"
    "v_mfma_f32_32x32x16_bf16 v[112:127], a[124:127], v[184:187], v[112:127]\n\t"
    "ds_read_b64_tr_b16 a[122:123], v232 offset:39936\n\t"
    "v_exp_f32 v148, v148\n\t"
    "v_add_f32 v240, v240, v147\n\t"
    "s_waitcnt lgkmcnt(14)\n\t"
    "v_mfma_f32_32x32x16_bf16 v[160:175], a[64:67], a[0:3], v[192:207]\n\t"
    "ds_read_b64_tr_b16 a[124:125], v232 offset:38400\n\t"
    "v_exp_f32 v149, v149\n\t"
    "v_add_f32 v240, v240, v148\n\t"
    "v_cvt_pk_bf16_f32 v145, v146, v147\n\t"
    "v_mfma_f32_32x32x16_bf16 v[176:191], a[64:67], a[32:35], v[208:223]\n\t"
    "ds_read_b64_tr_b16 a[126:127], v232 offset:40448\n\t"
    "v_exp_f32 v150, v150\n\t"
    "v_add_f32 v240, v240, v149\n\t"
    "v_mfma_f32_32x32x16_bf16 v[160:175], a[68:71], a[4:7], v[160:175]\n\t"
    "v_exp_f32 v151, v151\n\t"
    "v_add_f32 v240, v240, v150\n\t"
    "v_cvt_pk_bf16_f32 v146, v148, v149\n\t"
    "v_mfma_f32_32x32x16_bf16 v[176:191], a[68:71], a[36:39], v[176:191]\n\t"
    "ds_read_b128 a[64:67], v224 offset:49152\n\t"
    "v_exp_f32 v136, v136\n\t"
    "v_add_f32 v240, v240, v151\n\t"
    "v_mfma_f32_32x32x16_bf16 v[160:175], a[72:75], a[8:11], v[160:175]\n\t"
    "v_exp_f32 v137, v137\n\t"
    "v_add_f32 v239, v239, v136\n\t"
    "v_cvt_pk_bf16_f32 v147, v150, v151\n\t"
    "v_mfma_f32_32x32x16_bf16 v[176:191], a[72:75], a[40:43], v[176:191]\n\t"
    "ds_read_b128 a[68:71], v225 offset:49152\n\t"
    "v_exp_f32 v138, v138\n\t"
    "v_add_f32 v239, v239, v137\n\t"
    "v_mfma_f32_32x32x16_bf16 v[160:175], a[76:79], a[12:15], v[160:175]\n\t"
    "v_exp_f32 v139, v139\n\t"
    "v_add_f32 v239, v239, v138\n\t"
    "v_cvt_pk_bf16_f32 v136, v136, v137\n\t"
    "v_mfma_f32_32x32x16_bf16 v[176:191], a[76:79], a[44:47], v[176:191]\n\t"
    "ds_read_b128 a[72:75], v226 offset:49152\n\t"
    "v_exp_f32 v140, v140\n\t"
    "v_add_f32 v239, v239, v139\n\t"
    "v_mfma_f32_32x32x16_bf16 v[160:175], a[80:83], a[16:19], v[160:175]\n\t"
    "v_exp_f32 v141, v141\n\t"
    "v_add_f32 v239, v239, v140\n\t"
    "v_cvt_pk_bf16_f32 v137, v138, v139\n\t"
    "v_mfma_f32_32x32x16_bf16 v[176:191], a[80:83], a[48:51], v[176:191]\n\t"
    "ds_read_b128 a[76:79], v227 offset:49152\n\t"
    "v_exp_f32 v142, v142\n\t"
    "v_add_f32 v239, v239, v141\n\t"
    "v_mfma_f32_32x32x16_bf16 v[160:175], a[84:87], a[20:23], v[160:175]\n\t"
    "v_exp_f32 v143, v143\n\t"
    "v_add_f32 v239, v239, v142\n\t"
    "v_cvt_pk_bf16_f32 v138, v140, v141\n\t"
    "v_mfma_f32_32x32x16_bf16 v[176:191], a[84:87], a[52:55], v[176:191]\n\t"
    "ds_read_b128 a[80:83], v228 offset:49152\n\t"
    "v_exp_f32 v152, v152\n\t"
    "v_add_f32 v239, v239, v143\n\t"
    "v_mfma_f32_32x32x16_bf16 v[160:175], a[88:91], a[24:27], v[160:175]\n\t"
    "v_exp_f32 v153, v153\n\t"
    "v_add_f32 v240, v240, v152\n\t"
    "v_cvt_pk_bf16_f32 v139, v142, v143\n\t"
    "v_mfma_f32_32x32x16_bf16 v[176:191], a[88:91], a[56:59], v[176:191]\n\t"
    "ds_read_b128 a[84:87], v229 offset:49152\n\t"
    "v_exp_f32 v154, v154\n\t"
    "v_add_f32 v240, v240, v153\n\t"
    "v_mfma_f32_32x32x16_bf16 v[160:175], a[92:95], a[28:31], v[160:175]\n\t"
    "v_exp_f32 v155, v155\n\t"
    "v_add_f32 v240, v240, v154\n\t"
    "v_cvt_pk_bf16_f32 v152, v152, v153\n\t"
    "v_mfma_f32_32x32x16_bf16 v[176:191], a[92:95], a[60:63], v[176:191]\n\t"
    "ds_read_b128 a[88:91], v230 offset:49152\n\t"
    "v_exp_f32 v156, v156\n\t"
    "v_add_f32 v240, v240, v155\n\t"
    "s_waitcnt lgkmcnt(7)\n\t"
    "v_mfma_f32_32x32x16_bf16 v[0:15], a[96:99], v[128:131], v[0:15]\n\t"
    "v_exp_f32 v157, v157\n\t"
    "v_add_f32 v240, v240, v156\n\t"
    "v_cvt_pk_bf16_f32 v153, v154, v155\n\t"
    "v_mfma_f32_32x32x16_bf16 v[16:31], a[96:99], v[144:147], v[16:31]\n\t"
    "ds_read_b128 a[92:95], v231 offset:49152\n\t"
    "v_exp_f32 v158, v158\n\t"
    "v_add_f32 v240, v240, v157\n\t"
    "v_mfma_f32_32x32x16_bf16 v[32:47], a[100:103], v[128:131], v[32:47]\n\t"
    "ds_read_b64_tr_b16 a[96:97], v232 offset:40960\n\t"
    "v_exp_f32 v159, v159\n\t"
    "v_add_f32 v240, v240, v158\n\t"
    "v_cvt_pk_bf16_f32 v154, v156, v157\n\t"
    "v_mfma_f32_32x32x16_bf16 v[48:63], a[100:103], v[144:147], v[48:63]\n\t"
    "ds_read_b64_tr_b16 a[98:99], v232 offset:43008\n\t"
    "v_exp_f32 v160, v160\n\t"
    "v_add_f32 v240, v240, v159\n\t"
    "v_mfma_f32_32x32x16_bf16 v[64:79], a[104:107], v[128:131], v[64:79]\n\t"
    "ds_read_b64_tr_b16 a[100:101], v232 offset:41472\n\t"
    "v_exp_f32 v161, v161\n\t"
    "v_add_f32 v239, v239, v160\n\t"
    "v_cvt_pk_bf16_f32 v155, v158, v159\n\t"
    "v_mfma_f32_32x32x16_bf16 v[80:95], a[104:107], v[144:147], v[80:95]\n\t"
    "ds_read_b64_tr_b16 a[102:103], v232 offset:43520\n\t"
    "v_exp_f32 v162, v162\n\t"
    "v_add_f32 v239, v239, v161\n\t"
    "v_mfma_f32_32x32x16_bf16 v[96:111], a[108:111], v[128:131], v[96:111]\n\t"
    "ds_read_b64_tr_b16 a[104:105], v232 offset:41984\n\t"
    "v_exp_f32 v163, v163\n\t"
    "v_add_f32 v239, v239, v162\n\t"
    "v_cvt_pk_bf16_f32 v160, v160, v161\n\t"
    "v_mfma_f32_32x32x16_bf16 v[112:127], a[108:111], v[144:147], v[112:127]\n\t"
    "ds_read_b64_tr_b16 a[106:107], v232 offset:44032\n\t"
    "v_exp_f32 v164, v164\n\t"
    "v_add_f32 v239, v239, v163\n\t"
    "v_mfma_f32_32x32x16_bf16 v[0:15], a[112:115], v[136:139], v[0:15]\n\t"
    "ds_read_b64_tr_b16 a[108:109], v232 offset:42496\n\t"
    "v_exp_f32 v165, v165\n\t"
    "v_add_f32 v239, v239, v164\n\t"
    "v_cvt_pk_bf16_f32 v161, v162, v163\n\t"
    "v_mfma_f32_32x32x16_bf16 v[16:31], a[112:115], v[152:155], v[16:31]\n\t"
    "ds_read_b64_tr_b16 a[110:111], v232 offset:44544\n\t"
    "v_exp_f32 v166, v166\n\t"
    "v_add_f32 v239, v239, v165\n\t"
    "v_mfma_f32_32x32x16_bf16 v[32:47], a[116:119], v[136:139], v[32:47]\n\t"
    "ds_read_b64_tr_b16 a[112:113], v232 offset:45056\n\t"
    "v_exp_f32 v167, v167\n\t"
    "v_add_f32 v239, v239, v166\n\t"
    "v_cvt_pk_bf16_f32 v162, v164, v165\n\t"
    "v_mfma_f32_32x32x16_bf16 v[48:63], a[116:119], v[152:155], v[48:63]\n\t"
    "ds_read_b64_tr_b16 a[114:115], v232 offset:47104\n\t"
    "v_exp_f32 v176, v176\n\t"
    "v_add_f32 v239, v239, v167\n\t"
    "v_mfma_f32_32x32x16_bf16 v[64:79], a[120:123], v[136:139], v[64:79]\n\t"
    "ds_read_b64_tr_b16 a[116:117], v232 offset:45568\n\t"
    "v_exp_f32 v177, v177\n\t"
    "v_add_f32 v240, v240, v176\n\t"
    "v_cvt_pk_bf16_f32 v163, v166, v167\n\t"
    "v_mfma_f32_32x32x16_bf16 v[80:95], a[120:123], v[152:155], v[80:95]\n\t"
    "ds_read_b64_tr_b16 a[118:119], v232 offset:47616\n\t"
    "v_exp_f32 v178, v178\n\t"
    "v_add_f32 v240, v240, v177\n\t"
    "v_mfma_f32_32x32x16_bf16 v[96:111], a[124:127], v[136:139], v[96:111]\n\t"
    "ds_read_b64_tr_b16 a[120:121], v232 offset:46080\n\t"
    "v_exp_f32 v179, v179\n\t"
    "v_add_f32 v240, v240, v178\n\t"
    "v_cvt_pk_bf16_f32 v176, v176, v177\n\t"
    "v_mfma_f32_32x32x16_bf16 v[112:127], a[124:127], v[152:155], v[112:127]\n\t"
    "ds_read_b64_tr_b16 a[122:123], v232 offset:48128\n\t"
    "v_exp_f32 v180, v180\n\t"
    "v_add_f32 v240, v240, v179\n\t"
    "s_waitcnt lgkmcnt(14)\n\t"
    "v_mfma_f32_32x32x16_bf16 v[128:143], a[64:67], a[0:3], v[192:207]\n\t"
    "s_waitcnt vmcnt(0)\n\t"
    "s_barrier\n\t"
    "ds_read_b64_tr_b16 a[124:125], v232 offset:46592\n\t"
    "v_exp_f32 v181, v181\n\t"
    "v_add_f32 v240, v240, v180\n\t"
    "v_cvt_pk_bf16_f32 v177, v178, v179\n\t"
    "v_mfma_f32_32x32x16_bf16 v[144:159], a[64:67], a[32:35], v[208:223]\n\t"
    "ds_read_b64_tr_b16 a[126:127], v232 offset:48640\n\t"
    "v_exp_f32 v182, v182\n\t"
    "v_add_f32 v240, v240, v181\n\t"
    "v_mfma_f32_32x32x16_bf16 v[128:143], a[68:71], a[4:7], v[128:143]\n\t"
    "v_exp_f32 v183, v183\n\t"
    "v_add_f32 v240, v240, v182\n\t"
    "v_cvt_pk_bf16_f32 v178, v180, v181\n\t"
    "v_mfma_f32_32x32x16_bf16 v[144:159], a[68:71], a[36:39], v[144:159]\n\t"
    "ds_read_b128 a[64:67], v224 offset:57344\n\t"
    "v_exp_f32 v168, v168\n\t"
    "v_add_f32 v240, v240, v183\n\t"
    "v_mfma_f32_32x32x16_bf16 v[128:143], a[72:75], a[8:11], v[128:143]\n\t"
    "v_exp_f32 v169, v169\n\t"
    "v_add_f32 v239, v239, v168\n\t"
    "v_cvt_pk_bf16_f32 v179, v182, v183\n\t"
    "v_mfma_f32_32x32x16_bf16 v[144:159], a[72:75], a[40:43], v[144:159]\n\t"
    "ds_read_b128 a[68:71], v225 offset:57344\n\t"
    "v_exp_f32 v170, v170\n\t"
    "v_add_f32 v239, v239, v169\n\t"
    "v_mfma_f32_32x32x16_bf16 v[128:143], a[76:79], a[12:15], v[128:143]\n\t"
    "v_exp_f32 v171, v171\n\t"
    "v_add_f32 v239, v239, v170\n\t"
    "v_cvt_pk_bf16_f32 v168, v168, v169\n\t"
    "v_mfma_f32_32x32x16_bf16 v[144:159], a[76:79], a[44:47], v[144:159]\n\t"
    "ds_read_b128 a[72:75], v226 offset:57344\n\t"
    "v_exp_f32 v172, v172\n\t"
    "v_add_f32 v239, v239, v171\n\t"
    "v_mfma_f32_32x32x16_bf16 v[128:143], a[80:83], a[16:19], v[128:143]\n\t"
    "v_exp_f32 v173, v173\n\t"
    "v_add_f32 v239, v239, v172\n\t"
    "v_cvt_pk_bf16_f32 v169, v170, v171\n\t"
    "v_mfma_f32_32x32x16_bf16 v[144:159], a[80:83], a[48:51], v[144:159]\n\t"
    "ds_read_b128 a[76:79], v227 offset:57344\n\t"
    "v_exp_f32 v174, v174\n\t"
    "v_add_f32 v239, v239, v173\n\t"
    "v_mfma_f32_32x32x16_bf16 v[128:143], a[84:87], a[20:23], v[128:143]\n\t"
    "v_exp_f32 v175, v175\n\t"
    "v_add_f32 v239, v239, v174\n\t"
    "v_cvt_pk_bf16_f32 v170, v172, v173\n\t"
    "v_mfma_f32_32x32x16_bf16 v[144:159], a[84:87], a[52:55], v[144:159]\n\t"
    "ds_read_b128 a[80:83], v228 offset:57344\n\t"
    "v_exp_f32 v184, v184\n\t"
    "v_add_f32 v239, v239, v175\n\t"
    "v_mfma_f32_32x32x16_bf16 v[128:143], a[88:91], a[24:27], v[128:143]\n\t"
    "v_exp_f32 v185, v185\n\t"
    "v_add_f32 v240, v240, v184\n\t"
    "v_cvt_pk_bf16_f32 v171, v174, v175\n\t"
    "v_mfma_f32_32x32x16_bf16 v[144:159], a[88:91], a[56:59], v[144:159]\n\t"
    "ds_read_b128 a[84:87], v229 offset:57344\n\t"
    "v_exp_f32 v186, v186\n\t"
    "v_add_f32 v240, v240, v185\n\t"
    "v_mfma_f32_32x32x16_bf16 v[128:143], a[92:95], a[28:31], v[128:143]\n\t"
    "v_exp_f32 v187, v187\n\t"
    "v_add_f32 v240, v240, v186\n\t"
    "v_cvt_pk_bf16_f32 v184, v184, v185\n\t"
    "v_mfma_f32_32x32x16_bf16 v[144:159], a[92:95], a[60:63], v[144:159]\n\t"
    "ds_read_b128 a[88:91], v230 offset:57344\n\t"
    "v_exp_f32 v188, v188\n\t"
    "v_add_f32 v240, v240, v187\n\t"
    "s_waitcnt lgkmcnt(7)\n\t"
    "v_mfma_f32_32x32x16_bf16 v[0:15], a[96:99], v[160:163], v[0:15]\n\t"
    "v_exp_f32 v189, v189\n\t"
    "v_add_f32 v240, v240, v188\n\t"
    "v_cvt_pk_bf16_f32 v185, v186, v187\n\t"
    "v_mfma_f32_32x32x16_bf16 v[16:31], a[96:99], v[176:179], v[16:31]\n\t"
    "ds_read_b128 a[92:95], v231 offset:57344\n\t"
    "v_exp_f32 v190, v190\n\t"
    "v_add_f32 v240, v240, v189\n\t"
    "v_mfma_f32_32x32x16_bf16 v[32:47], a[100:103], v[160:163], v[32:47]\n\t"
    "ds_read_b64_tr_b16 a[96:97], v232 offset:49152\n\t"
    "v_exp_f32 v191, v191\n\t"
    "v_add_f32 v240, v240, v190\n\t"
    "v_cvt_pk_bf16_f32 v186, v188, v189\n\t"
    "v_mfma_f32_32x32x16_bf16 v[48:63], a[100:103], v[176:179], v[48:63]\n\t"
    "ds_read_b64_tr_b16 a[98:99], v232 offset:51200\n\t"
    "v_exp_f32 v128, v128\n\t"
    "v_add_f32 v240, v240, v191\n\t"
    "v_mfma_f32_32x32x16_bf16 v[64:79], a[104:107], v[160:163], v[64:79]\n\t"
    "ds_read_b64_tr_b16 a[100:101], v232 offset:49664\n\t"
    "v_exp_f32 v129, v129\n\t"
    "v_add_f32 v239, v239, v128\n\t"
    "v_cvt_pk_bf16_f32 v187, v190, v191\n\t"
    "v_mfma_f32_32x32x16_bf16 v[80:95], a[104:107], v[176:179], v[80:95]\n\t"
    "ds_read_b64_tr_b16 a[102:103], v232 offset:51712\n\t"
    "v_exp_f32 v130, v130\n\t"
    "v_add_f32 v239, v239, v129\n\t"
    "v_mfma_f32_32x32x16_bf16 v[96:111], a[108:111], v[160:163], v[96:111]\n\t"
    "ds_read_b64_tr_b16 a[104:105], v232 offset:50176\n\t"
    "v_exp_f32 v131, v131\n\t"
    "v_add_f32 v239, v239, v130\n\t"
    "v_cvt_pk_bf16_f32 v128, v128, v129\n\t"
    "v_mfma_f32_32x32x16_bf16 v[112:127], a[108:111], v[176:179], v[112:127]\n\t"
    "ds_read_b64_tr_b16 a[106:107], v232 offset:52224\n\t"
    "v_exp_f32 v132, v132\n\t"
    "v_add_f32 v239, v239, v131\n\t"
    "v_mfma_f32_32x32x16_bf16 v[0:15], a[112:115], v[168:171], v[0:15]\n\t"
    "ds_read_b64_tr_b16 a[108:109], v232 offset:50688\n\t"
    "v_exp_f32 v133, v133\n\t"
    "v_add_f32 v239, v239, v132\n\t"
    "v_cvt_pk_bf16_f32 v129, v130, v131\n\t"
    "v_mfma_f32_32x32x16_bf16 v[16:31], a[112:115], v[184:187], v[16:31]\n\t"
    "ds_read_b64_tr_b16 a[110:111], v232 offset:52736\n\t"
    "v_exp_f32 v134, v134\n\t"
    "v_add_f32 v239, v239, v133\n\t"
    "v_mfma_f32_32x32x16_bf16 v[32:47], a[116:119], v[168:171], v[32:47]\n\t"
    "ds_read_b64_tr_b16 a[112:113], v232 offset:53248\n\t"
    "v_exp_f32 v135, v135\n\t"
    "v_add_f32 v239, v239, v134\n\t"
    "v_cvt_pk_bf16_f32 v130, v132, v133\n\t"
    "v_mfma_f32_32x32x16_bf16 v[48:63], a[116:119], v[184:187], v[48:63]\n\t"
    "ds_read_b64_tr_b16 a[114:115], v232 offset:55296\n\t"
    "v_exp_f32 v144, v144\n\t"
    "v_add_f32 v239, v239, v135\n\t"
    "v_mfma_f32_32x32x16_bf16 v[64:79], a[120:123], v[168:171], v[64:79]\n\t"
    "ds_read_b64_tr_b16 a[116:117], v232 offset:53760\n\t"
    "v_exp_f32 v145, v145\n\t"
    "v_add_f32 v240, v240, v144\n\t"
    "v_cvt_pk_bf16_f32 v131, v134, v135\n\t"
    "v_mfma_f32_32x32x16_bf16 v[80:95], a[120:123], v[184:187], v[80:95]\n\t"
    "ds_read_b64_tr_b16 a[118:119], v232 offset:55808\n\t"
    "v_exp_f32 v146, v146\n\t"
    "v_add_f32 v240, v240, v145\n\t"
    "v_mfma_f32_32x32x16_bf16 v[96:111], a[124:127], v[168:171], v[96:111]\n\t"
    "ds_read_b64_tr_b16 a[120:121], v232 offset:54272\n\t"
    "v_exp_f32 v147, v147\n\t"
    "v_add_f32 v240, v240, v146\n\t"
    "v_cvt_pk_bf16_f32 v144, v144, v145\n\t"
    "v_mfma_f32_32x32x16_bf16 v[112:127], a[124:127], v[184:187], v[112:127]\n\t"
    "ds_read_b64_tr_b16 a[122:123], v232 offset:56320\n\t"
    "v_exp_f32 v148, v148\n\t"
    "v_add_f32 v240, v240, v147\n\t"
    "s_waitcnt lgkmcnt(14)\n\t"
    "v_mfma_f32_32x32x16_bf16 v[160:175], a[64:67], a[0:3], v[192:207]\n\t"
    "ds_read_b64_tr_b16 a[124:125], v232 offset:54784\n\t"
    "v_exp_f32 v149, v149\n\t"
    "v_add_f32 v240, v240, v148\n\t"
    "v_cvt_pk_bf16_f32 v145, v146, v147\n\t"
    "v_mfma_f32_32x32x16_bf16 v[176:191], a[64:67], a[32:35], v[208:223]\n\t"
    "ds_read_b64_tr_b16 a[126:127], v232 offset:56832\n\t"
    "v_exp_f32 v150, v150\n\t"
    "v_add_f32 v240, v240, v149\n\t"
    "v_mfma_f32_32x32x16_bf16 v[160:175], a[68:71], a[4:7], v[160:175]\n\t"
    "v_exp_f32 v151, v151\n\t"
    "v_add_f32 v240, v240, v150\n\t"
    "v_cvt_pk_bf16_f32 v146, v148, v149\n\t"
    "v_mfma_f32_32x32x16_bf16 v[176:191], a[68:71], a[36:39], v[176:191]\n\t"
    "v_exp_f32 v136, v136\n\t"
    "v_add_f32 v240, v240, v151\n\t"
    "v_mfma_f32_32x32x16_bf16 v[160:175], a[72:75], a[8:11], v[160:175]\n\t"
    "v_exp_f32 v137, v137\n\t"
    "v_add_f32 v239, v239, v136\n\t"
    "v_cvt_pk_bf16_f32 v147, v150, v151\n\t"
    "v_mfma_f32_32x32x16_bf16 v[176:191], a[72:75], a[40:43], v[176:191]\n\t"
    "v_exp_f32 v138, v138\n\t"
    "v_add_f32 v239, v239, v137\n\t"
    "v_mfma_f32_32x32x16_bf16 v[160:175], a[76:79], a[12:15], v[160:175]\n\t"
    "v_exp_f32 v139, v139\n\t"
    "v_add_f32 v239, v239, v138\n\t"
    "v_cvt_pk_bf16_f32 v136, v136, v137\n\t"
    "v_mfma_f32_32x32x16_bf16 v[176:191], a[76:79], a[44:47], v[176:191]\n\t"
    "v_exp_f32 v140, v140\n\t"
    "v_add_f32 v239, v239, v139\n\t"
    "v_mfma_f32_32x32x16_bf16 v[160:175], a[80:83], a[16:19], v[160:175]\n\t"
    "v_exp_f32 v141, v141\n\t"
    "v_add_f32 v239, v239, v140\n\t"
    "v_cvt_pk_bf16_f32 v137, v138, v139\n\t"
    "v_mfma_f32_32x32x16_bf16 v[176:191], a[80:83], a[48:51], v[176:191]\n\t"
    "v_exp_f32 v142, v142\n\t"
    "v_add_f32 v239, v239, v141\n\t"
    "v_mfma_f32_32x32x16_bf16 v[160:175], a[84:87], a[20:23], v[160:175]\n\t"
    "v_exp_f32 v143, v143\n\t"
    "v_add_f32 v239, v239, v142\n\t"
    "v_cvt_pk_bf16_f32 v138, v140, v141\n\t"
    "v_mfma_f32_32x32x16_bf16 v[176:191], a[84:87], a[52:55], v[176:191]\n\t"
    "v_exp_f32 v152, v152\n\t"
    "v_add_f32 v239, v239, v143\n\t"
    "v_mfma_f32_32x32x16_bf16 v[160:175], a[88:91], a[24:27], v[160:175]\n\t"
    "v_exp_f32 v153, v153\n\t"
    "v_add_f32 v240, v240, v152\n\t"
    "v_cvt_pk_bf16_f32 v139, v142, v143\n\t"
    "v_mfma_f32_32x32x16_bf16 v[176:191], a[88:91], a[56:59], v[176:191]\n\t"
    "v_exp_f32 v154, v154\n\t"
    "v_add_f32 v240, v240, v153\n\t"
    "v_mfma_f32_32x32x16_bf16 v[160:175], a[92:95], a[28:31], v[160:175]\n\t"
    "v_exp_f32 v155, v155\n\t"
    "v_add_f32 v240, v240, v154\n\t"
    "v_cvt_pk_bf16_f32 v152, v152, v153\n\t"
    "v_mfma_f32_32x32x16_bf16 v[176:191], a[92:95], a[60:63], v[176:191]\n\t"
    "v_exp_f32 v156, v156\n\t"
    "v_add_f32 v240, v240, v155\n\t"
    "s_waitcnt lgkmcnt(0)\n\t"
    "v_mfma_f32_32x32x16_bf16 v[0:15], a[96:99], v[128:131], v[0:15]\n\t"
    "v_exp_f32 v157, v157\n\t"
    "v_add_f32 v240, v240, v156\n\t"
    "v_cvt_pk_bf16_f32 v153, v154, v155\n\t"
    "v_mfma_f32_32x32x16_bf16 v[16:31], a[96:99], v[144:147], v[16:31]\n\t"
    "v_exp_f32 v158, v158\n\t"
    "v_add_f32 v240, v240, v157\n\t"
    "v_mfma_f32_32x32x16_bf16 v[32:47], a[100:103], v[128:131], v[32:47]\n\t"
    "ds_read_b64_tr_b16 a[96:97], v232 offset:57344\n\t"
    "v_exp_f32 v159, v159\n\t"
    "v_add_f32 v240, v240, v158\n\t"
    "v_cvt_pk_bf16_f32 v154, v156, v157\n\t"
    "v_mfma_f32_32x32x16_bf16 v[48:63], a[100:103], v[144:147], v[48:63]\n\t"
    "ds_read_b64_tr_b16 a[98:99], v232 offset:59392\n\t"
    "v_exp_f32 v160, v160\n\t"
    "v_add_f32 v240, v240, v159\n\t"
    "v_mfma_f32_32x32x16_bf16 v[64:79], a[104:107], v[128:131], v[64:79]\n\t"
    "ds_read_b64_tr_b16 a[100:101], v232 offset:57856\n\t"
    "v_exp_f32 v161, v161\n\t"
    "v_add_f32 v239, v239, v160\n\t"
    "v_cvt_pk_bf16_f32 v155, v158, v159\n\t"
    "v_mfma_f32_32x32x16_bf16 v[80:95], a[104:107], v[144:147], v[80:95]\n\t"
    "ds_read_b64_tr_b16 a[102:103], v232 offset:59904\n\t"
    "v_exp_f32 v162, v162\n\t"
    "v_add_f32 v239, v239, v161\n\t"
    "v_mfma_f32_32x32x16_bf16 v[96:111], a[108:111], v[128:131], v[96:111]\n\t"
    "ds_read_b64_tr_b16 a[104:105], v232 offset:58368\n\t"
    "v_exp_f32 v163, v163\n\t"
    "v_add_f32 v239, v239, v162\n\t"
    "v_cvt_pk_bf16_f32 v160, v160, v161\n\t"
    "v_mfma_f32_32x32x16_bf16 v[112:127], a[108:111], v[144:147], v[112:127]\n\t"
    "ds_read_b64_tr_b16 a[106:107], v232 offset:60416\n\t"
    "v_exp_f32 v164, v164\n\t"
    "v_add_f32 v239, v239, v163\n\t"
    "v_mfma_f32_32x32x16_bf16 v[0:15], a[112:115], v[136:139], v[0:15]\n\t"
    "ds_read_b64_tr_b16 a[108:109], v232 offset:58880\n\t"
    "v_exp_f32 v165, v165\n\t"
    "v_add_f32 v239, v239, v164\n\t"
    "v_cvt_pk_bf16_f32 v161, v162, v163\n\t"
    "v_mfma_f32_32x32x16_bf16 v[16:31], a[112:115], v[152:155], v[16:31]\n\t"
    "ds_read_b64_tr_b16 a[110:111], v232 offset:60928\n\t"
    "v_exp_f32 v166, v166\n\t"
    "v_add_f32 v239, v239, v165\n\t"
    "v_mfma_f32_32x32x16_bf16 v[32:47], a[116:119], v[136:139], v[32:47]\n\t"
    "ds_read_b64_tr_b16 a[112:113], v232 offset:61440\n\t"
    "v_exp_f32 v167, v167\n\t"
    "v_add_f32 v239, v239, v166\n\t"
    "v_cvt_pk_bf16_f32 v162, v164, v165\n\t"
    "v_mfma_f32_32x32x16_bf16 v[48:63], a[116:119], v[152:155], v[48:63]\n\t"
    "ds_read_b64_tr_b16 a[114:115], v232 offset:63488\n\t"
    "v_exp_f32 v176, v176\n\t"
    "v_add_f32 v239, v239, v167\n\t"
    "v_mfma_f32_32x32x16_bf16 v[64:79], a[120:123], v[136:139], v[64:79]\n\t"
    "ds_read_b64_tr_b16 a[116:117], v232 offset:61952\n\t"
    "v_exp_f32 v177, v177\n\t"
    "v_add_f32 v240, v240, v176\n\t"
    "v_cvt_pk_bf16_f32 v163, v166, v167\n\t"
    "v_mfma_f32_32x32x16_bf16 v[80:95], a[120:123], v[152:155], v[80:95]\n\t"
    "ds_read_b64_tr_b16 a[118:119], v232 offset:64000\n\t"
    "v_exp_f32 v178, v178\n\t"
    "v_add_f32 v240, v240, v177\n\t"
    "v_mfma_f32_32x32x16_bf16 v[96:111], a[124:127], v[136:139], v[96:111]\n\t"
    "ds_read_b64_tr_b16 a[120:121], v232 offset:62464\n\t"
    "v_exp_f32 v179, v179\n\t"
    "v_add_f32 v240, v240, v178\n\t"
    "v_cvt_pk_bf16_f32 v176, v176, v177\n\t"
    "v_mfma_f32_32x32x16_bf16 v[112:127], a[124:127], v[152:155], v[112:127]\n\t"
    "ds_read_b64_tr_b16 a[122:123], v232 offset:64512\n\t"
    "v_exp_f32 v180, v180\n\t"
    "v_add_f32 v240, v240, v179\n\t"
    "CODE_A_TAIL:\n\t"
    "ds_read_b64_tr_b16 a[124:125], v232 offset:62976\n\t"
    "v_exp_f32 v181, v181\n\t"
    "v_add_f32 v240, v240, v180\n\t"
    "v_cvt_pk_bf16_f32 v177, v178, v179\n\t"
    "ds_read_b64_tr_b16 a[126:127], v232 offset:65024\n\t"
    "v_exp_f32 v182, v182\n\t"
    "v_add_f32 v240, v240, v181\n\t"
    "v_exp_f32 v183, v183\n\t"
    "v_add_f32 v240, v240, v182\n\t"
    "v_cvt_pk_bf16_f32 v178, v180, v181\n\t"
    "v_exp_f32 v168, v168\n\t"
    "v_add_f32 v240, v240, v183\n\t"
    "v_exp_f32 v169, v169\n\t"
    "v_add_f32 v239, v239, v168\n\t"
    "v_cvt_pk_bf16_f32 v179, v182, v183\n\t"
    "v_exp_f32 v170, v170\n\t"
    "v_add_f32 v239, v239, v169\n\t"
    "v_exp_f32 v171, v171\n\t"
    "v_add_f32 v239, v239, v170\n\t"
    "v_cvt_pk_bf16_f32 v168, v168, v169\n\t"
    "v_exp_f32 v172, v172\n\t"
    "v_add_f32 v239, v239, v171\n\t"
    "v_exp_f32 v173, v173\n\t"
    "v_add_f32 v239, v239, v172\n\t"
    "v_cvt_pk_bf16_f32 v169, v170, v171\n\t"
    "v_exp_f32 v174, v174\n\t"
    "v_add_f32 v239, v239, v173\n\t"
    "v_exp_f32 v175, v175\n\t"
    "v_add_f32 v239, v239, v174\n\t"
    "v_cvt_pk_bf16_f32 v170, v172, v173\n\t"
    "v_exp_f32 v184, v184\n\t"
    "v_add_f32 v239, v239, v175\n\t"
    "v_exp_f32 v185, v185\n\t"
    "v_add_f32 v240, v240, v184\n\t"
    "v_cvt_pk_bf16_f32 v171, v174, v175\n\t"
    "v_exp_f32 v186, v186\n\t"
    "v_add_f32 v240, v240, v185\n\t"
    "v_exp_f32 v187, v187\n\t"
    "v_add_f32 v240, v240, v186\n\t"
    "v_cvt_pk_bf16_f32 v184, v184, v185\n\t"
    "v_exp_f32 v188, v188\n\t"
    "v_add_f32 v240, v240, v187\n\t"
    "s_waitcnt lgkmcnt(0)\n\t"
    "v_mfma_f32_32x32x16_bf16 v[0:15], a[96:99], v[160:163], v[0:15]\n\t"
    "v_exp_f32 v189, v189\n\t"
    "v_add_f32 v240, v240, v188\n\t"
    "v_cvt_pk_bf16_f32 v185, v186, v187\n\t"
    "v_mfma_f32_32x32x16_bf16 v[16:31], a[96:99], v[176:179], v[16:31]\n\t"
    "v_exp_f32 v190, v190\n\t"
    "v_add_f32 v240, v240, v189\n\t"
    "v_mfma_f32_32x32x16_bf16 v[32:47], a[100:103], v[160:163], v[32:47]\n\t"
    "v_exp_f32 v191, v191\n\t"
    "v_add_f32 v240, v240, v190\n\t"
    "v_cvt_pk_bf16_f32 v186, v188, v189\n\t"
    "v_mfma_f32_32x32x16_bf16 v[48:63], a[100:103], v[176:179], v[48:63]\n\t"
    "v_add_f32 v240, v240, v191\n\t"
    "v_mfma_f32_32x32x16_bf16 v[64:79], a[104:107], v[160:163], v[64:79]\n\t"
    "v_cvt_pk_bf16_f32 v187, v190, v191\n\t"
    "v_mfma_f32_32x32x16_bf16 v[80:95], a[104:107], v[176:179], v[80:95]\n\t"
    "v_mfma_f32_32x32x16_bf16 v[96:111], a[108:111], v[160:163], v[96:111]\n\t"
    "v_mfma_f32_32x32x16_bf16 v[112:127], a[108:111], v[176:179], v[112:127]\n\t"
    "v_mfma_f32_32x32x16_bf16 v[0:15], a[112:115], v[168:171], v[0:15]\n\t"
    "v_mfma_f32_32x32x16_bf16 v[16:31], a[112:115], v[184:187], v[16:31]\n\t"
    "v_mfma_f32_32x32x16_bf16 v[32:47], a[116:119], v[168:171], v[32:47]\n\t"
    "v_mfma_f32_32x32x16_bf16 v[48:63], a[116:119], v[184:187], v[48:63]\n\t"
    "v_mfma_f32_32x32x16_bf16 v[64:79], a[120:123], v[168:171], v[64:79]\n\t"
    "v_mfma_f32_32x32x16_bf16 v[80:95], a[120:123], v[184:187], v[80:95]\n\t"
    "v_mfma_f32_32x32x16_bf16 v[96:111], a[124:127], v[168:171], v[96:111]\n\t"
    "v_mfma_f32_32x32x16_bf16 v[112:127], a[124:127], v[184:187], v[112:127]\n\t"
    "s_mov_b32 s56, s44\n\t"
    "s_and_b32 s57, s45, 0xffff\n\t"
    "s_mov_b32 s58, 16777216\n\t"
    "s_mov_b32 s59, 0x20000\n\t"
    "v_mov_b32 v250, v239\n\t"
    "s_nop 1\n\t"
    "v_permlane32_swap_b32 v239, v250\n\t"
    "v_add_f32 v239, v239, v250\n\t"
    "v_mov_b32 v250, v240\n\t"
    "s_nop 1\n\t"
    "v_permlane32_swap_b32 v240, v250\n\t"
    "v_add_f32 v240, v240, v250\n\t"
    "v_mov_b32 v248, v239\n\t"
    "v_mov_b32 v249, v240\n\t"
    "v_rcp_f32 v239, v239\n\t"
    "v_rcp_f32 v240, v240\n\t"
    "v_lshlrev_b32 v241, 4, v251\n\t"
    "v_add_u32 v242, 4096, v241\n\t"
    "s_lshl_b32 s88, s91, 18\n\t"
    "s_lshl_b32 s89, s79, 13\n\t"
    "s_add_u32 s88, s88, s89\n\t"
    "s_add_u32 s89, s88, 262144\n\t"
    "s_and_b32 s86, s35, 3\n\t"
    "s_lshl_b32 s87, s86, 2\n\t"
    "s_lshr_b32 s90, s35, 3\n\t"
    "s_and_b32 s90, s90, 3\n\t"
    "s_add_u32 s87, s87, s90\n\t"
    "s_lshr_b32 s87, s87, 1\n\t"
    "s_lshl_b32 s87, s87, 5\n\t"
    "s_lshr_b32 s90, s35, 2\n\t"
    "s_and_b32 s90, s90, 1\n\t"
    "s_lshl_b32 s90, s90, 4\n\t"
    "s_add_u32 s87, s87, s90\n\t"
    "s_add_u32 s86, s86, 1\n\t"
    "s_and_b32 s86, s86, 3\n\t"
    "s_lshl_b32 s86, s86, 2\n\t"
    "s_add_u32 s87, s87, s86\n\t"
    "s_lshl_b32 s87, s87, 2\n\t"
    "s_add_u32 s87, s87, 1024\n\t"
    "v_and_b32 v160, 3, v251\n\t"
    "v_lshlrev_b32 v160, 2, v160\n\t"
    "v_add_u32 v160, s87, v160\n\t"
    "v_lshlrev_b32 v160, 4, v160\n\t"
    "v_lshlrev_b32 v161, 2, v251\n\t"
    "v_add_u32 v161, 8704, v161\n\t"
    "v_lshlrev_b32 v161, 4, v161\n\t"
    "v_add_u32 v162, 4096, v161\n\t"
    "global_load_dword v163, v160, s[54:55] sc1\n\t"
    "global_load_dword v164, v161, s[54:55] sc1\n\t"
    "global_load_dword v165, v162, s[54:55] sc1\n\t"
    "v_mul_f32 v0, v0, v239\n\t"
    "v_mul_f32 v1, v1, v239\n\t"
    "v_mul_f32 v2, v2, v239\n\t"
    "v_mul_f32 v3, v3, v239\n\t"
    "v_cvt_pk_bf16_f32 v128, v0, v1\n\t"
    "v_cvt_pk_bf16_f32 v129, v2, v3\n\t"
    "v_mul_f32 v4, v4, v239\n\t"
    "v_mul_f32 v5, v5, v239\n\t"
    "v_mul_f32 v6, v6, v239\n\t"
    "v_mul_f32 v7, v7, v239\n\t"
    "v_cvt_pk_bf16_f32 v130, v4, v5\n\t"
    "v_cvt_pk_bf16_f32 v131, v6, v7\n\t"
    "s_nop 0\n\t"
    "v_permlane32_swap_b32 v128, v130\n\t"
    "v_permlane32_swap_b32 v129, v131\n\t"
    "buffer_store_dwordx4 v[128:131], v241, s[56:59], s88 offen offset:0 sc1\n\t"
    "v_mul_f32 v8, v8, v239\n\t"
    "v_mul_f32 v9, v9, v239\n\t"
    "v_mul_f32 v10, v10, v239\n\t"
    "v_mul_f32 v11, v11, v239\n\t"
    "v_cvt_pk_bf16_f32 v132, v8, v9\n\t"
    "v_cvt_pk_bf16_f32 v133, v10, v11\n\t"
    "v_mul_f32 v12, v12, v239\n\t"
    "v_mul_f32 v13, v13, v239\n\t"
    "v_mul_f32 v14, v14, v239\n\t"
    "v_mul_f32 v15, v15, v239\n\t"
    "v_cvt_pk_bf16_f32 v134, v12, v13\n\t"
    "v_cvt_pk_bf16_f32 v135, v14, v15\n\t"
    "s_nop 0\n\t"
    "v_permlane32_swap_b32 v132, v134\n\t"
    "v_permlane32_swap_b32 v133, v135\n\t"
    "buffer_store_dwordx4 v[132:135], v241, s[56:59], s88 offen offset:1024 sc1\n\t"
    "v_mul_f32 v32, v32, v239\n\t"
    "v_mul_f32 v33, v33, v239\n\t"
    "v_mul_f32 v34, v34, v239\n\t"
    "v_mul_f32 v35, v35, v239\n\t"
    "v_cvt_pk_bf16_f32 v136, v32, v33\n\t"
    "v_cvt_pk_bf16_f32 v137, v34, v35\n\t"
    "v_mul_f32 v36, v36, v239\n\t"
    "v_mul_f32 v37, v37, v239\n\t"
    "v_mul_f32 v38, v38, v239\n\t"
    "v_mul_f32 v39, v39, v239\n\t"
    "v_cvt_pk_bf16_f32 v138, v36, v37\n\t"
    "v_cvt_pk_bf16_f32 v139, v38, v39\n\t"
    "s_nop 0\n\t"
    "v_permlane32_swap_b32 v136, v138\n\t"
    "v_permlane32_swap_b32 v137, v139\n\t"
    "buffer_store_dwordx4 v[136:139], v241, s[56:59], s88 offen offset:2048 sc1\n\t"
    "v_mul_f32 v40, v40, v239\n\t"
    "v_mul_f32 v41, v41, v239\n\t"
    "v_mul_f32 v42, v42, v239\n\t"
    "v_mul_f32 v43, v43, v239\n\t"
    "v_cvt_pk_bf16_f32 v140, v40, v41\n\t"
    "v_cvt_pk_bf16_f32 v141, v42, v43\n\t"
    "v_mul_f32 v44, v44, v239\n\t"
    "v_mul_f32 v45, v45, v239\n\t"
    "v_mul_f32 v46, v46, v239\n\t"
    "v_mul_f32 v47, v47, v239\n\t"
    "v_cvt_pk_bf16_f32 v142, v44, v45\n\t"
    "v_cvt_pk_bf16_f32 v143, v46, v47\n\t"
    "s_nop 0\n\t"
    "v_permlane32_swap_b32 v140, v142\n\t"
    "v_permlane32_swap_b32 v141, v143\n\t"
    "buffer_store_dwordx4 v[140:143], v241, s[56:59], s88 offen offset:3072 sc1\n\t"
    "v_mul_f32 v64, v64, v239\n\t"
    "v_mul_f32 v65, v65, v239\n\t"
    "v_mul_f32 v66, v66, v239\n\t"
    "v_mul_f32 v67, v67, v239\n\t"
    "v_cvt_pk_bf16_f32 v128, v64, v65\n\t"
    "v_cvt_pk_bf16_f32 v129, v66, v67\n\t"
    "v_mul_f32 v68, v68, v239\n\t"
    "v_mul_f32 v69, v69, v239\n\t"
    "v_mul_f32 v70, v70, v239\n\t"
    "v_mul_f32 v71, v71, v239\n\t"
    "v_cvt_pk_bf16_f32 v130, v68, v69\n\t"
    "v_cvt_pk_bf16_f32 v131, v70, v71\n\t"
    "s_nop 0\n\t"
    "v_permlane32_swap_b32 v128, v130\n\t"
    "v_permlane32_swap_b32 v129, v131\n\t"
    "buffer_store_dwordx4 v[128:131], v242, s[56:59], s88 offen offset:0 sc1\n\t"
    "v_mul_f32 v72, v72, v239\n\t"
    "v_mul_f32 v73, v73, v239\n\t"
    "v_mul_f32 v74, v74, v239\n\t"
    "v_mul_f32 v75, v75, v239\n\t"
    "v_cvt_pk_bf16_f32 v132, v72, v73\n\t"
    "v_cvt_pk_bf16_f32 v133, v74, v75\n\t"
    "v_mul_f32 v76, v76, v239\n\t"
    "v_mul_f32 v77, v77, v239\n\t"
    "v_mul_f32 v78, v78, v239\n\t"
    "v_mul_f32 v79, v79, v239\n\t"
    "v_cvt_pk_bf16_f32 v134, v76, v77\n\t"
    "v_cvt_pk_bf16_f32 v135, v78, v79\n\t"
    "s_nop 0\n\t"
    "v_permlane32_swap_b32 v132, v134\n\t"
    "v_permlane32_swap_b32 v133, v135\n\t"
    "buffer_store_dwordx4 v[132:135], v242, s[56:59], s88 offen offset:1024 sc1\n\t"
    "v_mul_f32 v96, v96, v239\n\t"
    "v_mul_f32 v97, v97, v239\n\t"
    "v_mul_f32 v98, v98, v239\n\t"
    "v_mul_f32 v99, v99, v239\n\t"
    "v_cvt_pk_bf16_f32 v136, v96, v97\n\t"
    "v_cvt_pk_bf16_f32 v137, v98, v99\n\t"
    "v_mul_f32 v100, v100, v239\n\t"
    "v_mul_f32 v101, v101, v239\n\t"
    "v_mul_f32 v102, v102, v239\n\t"
    "v_mul_f32 v103, v103, v239\n\t"
    "v_cvt_pk_bf16_f32 v138, v100, v101\n\t"
    "v_cvt_pk_bf16_f32 v139, v102, v103\n\t"
    "s_nop 0\n\t"
    "v_permlane32_swap_b32 v136, v138\n\t"
    "v_permlane32_swap_b32 v137, v139\n\t"
    "buffer_store_dwordx4 v[136:139], v242, s[56:59], s88 offen offset:2048 sc1\n\t"
    "v_mul_f32 v104, v104, v239\n\t"
    "v_mul_f32 v105, v105, v239\n\t"
    "v_mul_f32 v106, v106, v239\n\t"
    "v_mul_f32 v107, v107, v239\n\t"
    "v_cvt_pk_bf16_f32 v140, v104, v105\n\t"
    "v_cvt_pk_bf16_f32 v141, v106, v107\n\t"
    "v_mul_f32 v108, v108, v239\n\t"
    "v_mul_f32 v109, v109, v239\n\t"
    "v_mul_f32 v110, v110, v239\n\t"
    "v_mul_f32 v111, v111, v239\n\t"
    "v_cvt_pk_bf16_f32 v142, v108, v109\n\t"
    "v_cvt_pk_bf16_f32 v143, v110, v111\n\t"
    "s_nop 0\n\t"
    "v_permlane32_swap_b32 v140, v142\n\t"
    "v_permlane32_swap_b32 v141, v143\n\t"
    "buffer_store_dwordx4 v[140:143], v242, s[56:59], s88 offen offset:3072 sc1\n\t"
    "v_mul_f32 v16, v16, v240\n\t"
    "v_mul_f32 v17, v17, v240\n\t"
    "v_mul_f32 v18, v18, v240\n\t"
    "v_mul_f32 v19, v19, v240\n\t"
    "v_cvt_pk_bf16_f32 v128, v16, v17\n\t"
    "v_cvt_pk_bf16_f32 v129, v18, v19\n\t"
    "v_mul_f32 v20, v20, v240\n\t"
    "v_mul_f32 v21, v21, v240\n\t"
    "v_mul_f32 v22, v22, v240\n\t"
    "v_mul_f32 v23, v23, v240\n\t"
    "v_cvt_pk_bf16_f32 v130, v20, v21\n\t"
    "v_cvt_pk_bf16_f32 v131, v22, v23\n\t"
    "s_nop 0\n\t"
    "v_permlane32_swap_b32 v128, v130\n\t"
    "v_permlane32_swap_b32 v129, v131\n\t"
    "buffer_store_dwordx4 v[128:131], v241, s[56:59], s89 offen offset:0 sc1\n\t"
    "v_mul_f32 v24, v24, v240\n\t"
    "v_mul_f32 v25, v25, v240\n\t"
    "v_mul_f32 v26, v26, v240\n\t"
    "v_mul_f32 v27, v27, v240\n\t"
    "v_cvt_pk_bf16_f32 v132, v24, v25\n\t"
    "v_cvt_pk_bf16_f32 v133, v26, v27\n\t"
    "v_mul_f32 v28, v28, v240\n\t"
    "v_mul_f32 v29, v29, v240\n\t"
    "v_mul_f32 v30, v30, v240\n\t"
    "v_mul_f32 v31, v31, v240\n\t"
    "v_cvt_pk_bf16_f32 v134, v28, v29\n\t"
    "v_cvt_pk_bf16_f32 v135, v30, v31\n\t"
    "s_nop 0\n\t"
    "v_permlane32_swap_b32 v132, v134\n\t"
    "v_permlane32_swap_b32 v133, v135\n\t"
    "buffer_store_dwordx4 v[132:135], v241, s[56:59], s89 offen offset:1024 sc1\n\t"
    "v_mul_f32 v48, v48, v240\n\t"
    "v_mul_f32 v49, v49, v240\n\t"
    "v_mul_f32 v50, v50, v240\n\t"
    "v_mul_f32 v51, v51, v240\n\t"
    "v_cvt_pk_bf16_f32 v136, v48, v49\n\t"
    "v_cvt_pk_bf16_f32 v137, v50, v51\n\t"
    "v_mul_f32 v52, v52, v240\n\t"
    "v_mul_f32 v53, v53, v240\n\t"
    "v_mul_f32 v54, v54, v240\n\t"
    "v_mul_f32 v55, v55, v240\n\t"
    "v_cvt_pk_bf16_f32 v138, v52, v53\n\t"
    "v_cvt_pk_bf16_f32 v139, v54, v55\n\t"
    "s_nop 0\n\t"
    "v_permlane32_swap_b32 v136, v138\n\t"
    "v_permlane32_swap_b32 v137, v139\n\t"
    "buffer_store_dwordx4 v[136:139], v241, s[56:59], s89 offen offset:2048 sc1\n\t"
    "v_mul_f32 v56, v56, v240\n\t"
    "v_mul_f32 v57, v57, v240\n\t"
    "v_mul_f32 v58, v58, v240\n\t"
    "v_mul_f32 v59, v59, v240\n\t"
    "v_cvt_pk_bf16_f32 v140, v56, v57\n\t"
    "v_cvt_pk_bf16_f32 v141, v58, v59\n\t"
    "v_mul_f32 v60, v60, v240\n\t"
    "v_mul_f32 v61, v61, v240\n\t"
    "v_mul_f32 v62, v62, v240\n\t"
    "v_mul_f32 v63, v63, v240\n\t"
    "v_cvt_pk_bf16_f32 v142, v60, v61\n\t"
    "v_cvt_pk_bf16_f32 v143, v62, v63\n\t"
    "s_nop 0\n\t"
    "v_permlane32_swap_b32 v140, v142\n\t"
    "v_permlane32_swap_b32 v141, v143\n\t"
    "buffer_store_dwordx4 v[140:143], v241, s[56:59], s89 offen offset:3072 sc1\n\t"
    "v_mul_f32 v80, v80, v240\n\t"
    "v_mul_f32 v81, v81, v240\n\t"
    "v_mul_f32 v82, v82, v240\n\t"
    "v_mul_f32 v83, v83, v240\n\t"
    "v_cvt_pk_bf16_f32 v128, v80, v81\n\t"
    "v_cvt_pk_bf16_f32 v129, v82, v83\n\t"
    "v_mul_f32 v84, v84, v240\n\t"
    "v_mul_f32 v85, v85, v240\n\t"
    "v_mul_f32 v86, v86, v240\n\t"
    "v_mul_f32 v87, v87, v240\n\t"
    "v_cvt_pk_bf16_f32 v130, v84, v85\n\t"
    "v_cvt_pk_bf16_f32 v131, v86, v87\n\t"
    "s_nop 0\n\t"
    "v_permlane32_swap_b32 v128, v130\n\t"
    "v_permlane32_swap_b32 v129, v131\n\t"
    "buffer_store_dwordx4 v[128:131], v242, s[56:59], s89 offen offset:0 sc1\n\t"
    "v_mul_f32 v88, v88, v240\n\t"
    "v_mul_f32 v89, v89, v240\n\t"
    "v_mul_f32 v90, v90, v240\n\t"
    "v_mul_f32 v91, v91, v240\n\t"
    "v_cvt_pk_bf16_f32 v132, v88, v89\n\t"
    "v_cvt_pk_bf16_f32 v133, v90, v91\n\t"
    "v_mul_f32 v92, v92, v240\n\t"
    "v_mul_f32 v93, v93, v240\n\t"
    "v_mul_f32 v94, v94, v240\n\t"
    "v_mul_f32 v95, v95, v240\n\t"
    "v_cvt_pk_bf16_f32 v134, v92, v93\n\t"
    "v_cvt_pk_bf16_f32 v135, v94, v95\n\t"
    "s_nop 0\n\t"
    "v_permlane32_swap_b32 v132, v134\n\t"
    "v_permlane32_swap_b32 v133, v135\n\t"
    "buffer_store_dwordx4 v[132:135], v242, s[56:59], s89 offen offset:1024 sc1\n\t"
    "v_mul_f32 v112, v112, v240\n\t"
    "v_mul_f32 v113, v113, v240\n\t"
    "v_mul_f32 v114, v114, v240\n\t"
    "v_mul_f32 v115, v115, v240\n\t"
    "v_cvt_pk_bf16_f32 v136, v112, v113\n\t"
    "v_cvt_pk_bf16_f32 v137, v114, v115\n\t"
    "v_mul_f32 v116, v116, v240\n\t"
    "v_mul_f32 v117, v117, v240\n\t"
    "v_mul_f32 v118, v118, v240\n\t"
    "v_mul_f32 v119, v119, v240\n\t"
    "v_cvt_pk_bf16_f32 v138, v116, v117\n\t"
    "v_cvt_pk_bf16_f32 v139, v118, v119\n\t"
    "s_nop 0\n\t"
    "v_permlane32_swap_b32 v136, v138\n\t"
    "v_permlane32_swap_b32 v137, v139\n\t"
    "buffer_store_dwordx4 v[136:139], v242, s[56:59], s89 offen offset:2048 sc1\n\t"
    "v_mul_f32 v120, v120, v240\n\t"
    "v_mul_f32 v121, v121, v240\n\t"
    "v_mul_f32 v122, v122, v240\n\t"
    "v_mul_f32 v123, v123, v240\n\t"
    "v_cvt_pk_bf16_f32 v140, v120, v121\n\t"
    "v_cvt_pk_bf16_f32 v141, v122, v123\n\t"
    "v_mul_f32 v124, v124, v240\n\t"
    "v_mul_f32 v125, v125, v240\n\t"
    "v_mul_f32 v126, v126, v240\n\t"
    "v_mul_f32 v127, v127, v240\n\t"
    "v_cvt_pk_bf16_f32 v142, v124, v125\n\t"
    "v_cvt_pk_bf16_f32 v143, v126, v127\n\t"
    "s_nop 0\n\t"
    "v_permlane32_swap_b32 v140, v142\n\t"
    "v_permlane32_swap_b32 v141, v143\n\t"
    "buffer_store_dwordx4 v[140:143], v242, s[56:59], s89 offen offset:3072 sc1\n\t"
    "v_max_f32 v144, abs(v0), abs(v1)\n\t"
    "v_max3_f32 v144, abs(v2), abs(v3), v144\n\t"
    "v_max3_f32 v144, abs(v4), abs(v5), v144\n\t"
    "v_max3_f32 v144, abs(v6), abs(v7), v144\n\t"
    "v_max3_f32 v144, abs(v8), abs(v9), v144\n\t"
    "v_max3_f32 v144, abs(v10), abs(v11), v144\n\t"
    "v_max3_f32 v144, abs(v12), abs(v13), v144\n\t"
    "v_max3_f32 v144, abs(v14), abs(v15), v144\n\t"
    "v_max3_f32 v144, abs(v32), abs(v33), v144\n\t"
    "v_max3_f32 v144, abs(v34), abs(v35), v144\n\t"
    "v_max3_f32 v144, abs(v36), abs(v37), v144\n\t"
    "v_max3_f32 v144, abs(v38), abs(v39), v144\n\t"
    "v_max3_f32 v144, abs(v40), abs(v41), v144\n\t"
    "v_max3_f32 v144, abs(v42), abs(v43), v144\n\t"
    "v_max3_f32 v144, abs(v44), abs(v45), v144\n\t"
    "v_max3_f32 v144, abs(v46), abs(v47), v144\n\t"
    "v_max3_f32 v144, abs(v64), abs(v65), v144\n\t"
    "v_max3_f32 v144, abs(v66), abs(v67), v144\n\t"
    "v_max3_f32 v144, abs(v68), abs(v69), v144\n\t"
    "v_max3_f32 v144, abs(v70), abs(v71), v144\n\t"
    "v_max3_f32 v144, abs(v72), abs(v73), v144\n\t"
    "v_max3_f32 v144, abs(v74), abs(v75), v144\n\t"
    "v_max3_f32 v144, abs(v76), abs(v77), v144\n\t"
    "v_max3_f32 v144, abs(v78), abs(v79), v144\n\t"
    "v_max3_f32 v144, abs(v96), abs(v97), v144\n\t"
    "v_max3_f32 v144, abs(v98), abs(v99), v144\n\t"
    "v_max3_f32 v144, abs(v100), abs(v101), v144\n\t"
    "v_max3_f32 v144, abs(v102), abs(v103), v144\n\t"
    "v_max3_f32 v144, abs(v104), abs(v105), v144\n\t"
    "v_max3_f32 v144, abs(v106), abs(v107), v144\n\t"
    "v_max3_f32 v144, abs(v108), abs(v109), v144\n\t"
    "v_max3_f32 v144, abs(v110), abs(v111), v144\n\t"
    "v_max_f32 v145, abs(v16), abs(v17)\n\t"
    "v_max3_f32 v145, abs(v18), abs(v19), v145\n\t"
    "v_max3_f32 v145, abs(v20), abs(v21), v145\n\t"
    "v_max3_f32 v145, abs(v22), abs(v23), v145\n\t"
    "v_max3_f32 v145, abs(v24), abs(v25), v145\n\t"
    "v_max3_f32 v145, abs(v26), abs(v27), v145\n\t"
    "v_max3_f32 v145, abs(v28), abs(v29), v145\n\t"
    "v_max3_f32 v145, abs(v30), abs(v31), v145\n\t"
    "v_max3_f32 v145, abs(v48), abs(v49), v145\n\t"
    "v_max3_f32 v145, abs(v50), abs(v51), v145\n\t"
    "v_max3_f32 v145, abs(v52), abs(v53), v145\n\t"
    "v_max3_f32 v145, abs(v54), abs(v55), v145\n\t"
    "v_max3_f32 v145, abs(v56), abs(v57), v145\n\t"
    "v_max3_f32 v145, abs(v58), abs(v59), v145\n\t"
    "v_max3_f32 v145, abs(v60), abs(v61), v145\n\t"
    "v_max3_f32 v145, abs(v62), abs(v63), v145\n\t"
    "v_max3_f32 v145, abs(v80), abs(v81), v145\n\t"
    "v_max3_f32 v145, abs(v82), abs(v83), v145\n\t"
    "v_max3_f32 v145, abs(v84), abs(v85), v145\n\t"
    "v_max3_f32 v145, abs(v86), abs(v87), v145\n\t"
    "v_max3_f32 v145, abs(v88), abs(v89), v145\n\t"
    "v_max3_f32 v145, abs(v90), abs(v91), v145\n\t"
    "v_max3_f32 v145, abs(v92), abs(v93), v145\n\t"
    "v_max3_f32 v145, abs(v94), abs(v95), v145\n\t"
    "v_max3_f32 v145, abs(v112), abs(v113), v145\n\t"
    "v_max3_f32 v145, abs(v114), abs(v115), v145\n\t"
    "v_max3_f32 v145, abs(v116), abs(v117), v145\n\t"
    "v_max3_f32 v145, abs(v118), abs(v119), v145\n\t"
    "v_max3_f32 v145, abs(v120), abs(v121), v145\n\t"
    "v_max3_f32 v145, abs(v122), abs(v123), v145\n\t"
    "v_max3_f32 v145, abs(v124), abs(v125), v145\n\t"
    "v_max3_f32 v145, abs(v126), abs(v127), v145\n\t"
    "v_mov_b32 v146, v144\n\t"
    "s_nop 1\n\t"
    "v_permlane32_swap_b32 v144, v146\n\t"
    "v_max_f32 v144, v144, v146\n\t"
    "v_mul_f32 v144, v144, v248\n\t"
    "v_mov_b32 v146, v145\n\t"
    "s_nop 1\n\t"
    "v_permlane32_swap_b32 v145, v146\n\t"
    "v_max_f32 v145, v145, v146\n\t"
    "v_mul_f32 v145, v145, v249\n\t"
    "v_cmp_nle_f32 vcc, v248, 1.0\n\t"
    "v_cmp_nle_f32 s[90:91], v249, 1.0\n\t"
    "s_nop 0\n\t"
    "s_or_b64 vcc, vcc, s[90:91]\n\t"
    "s_mov_b32 s86, 0x3800000\n\t"
    "v_cmp_ngt_f32 s[90:91], v144, s86\n\t"
    "s_nop 0\n\t"
    "s_or_b64 vcc, vcc, s[90:91]\n\t"
    "v_cmp_ngt_f32 s[90:91], v145, s86\n\t"
    "s_nop 0\n\t"
    "s_or_b64 vcc, vcc, s[90:91]\n\t"
    "s_cmp_lg_u64 vcc, 0\n\t"
    "s_cselect_b32 s81, 1, 0\n\t"
    "s_waitcnt vmcnt(16)\n\t"
    "v_cmp_ne_u32 vcc, %5, v163\n\t"
    "v_cmp_ne_u32 s[90:91], %5, v164\n\t"
    "s_nop 0\n\t"
    "s_or_b64 vcc, vcc, s[90:91]\n\t"
    "v_cmp_ne_u32 s[90:91], %5, v165\n\t"
    "s_nop 0\n\t"
    "s_or_b64 vcc, vcc, s[90:91]\n\t"
    "s_cmp_eq_u64 vcc, 0\n\t"
    "s_cselect_b32 s82, 1, 0\n\t"
    "s_lshl_b32 s82, s82, 1\n\t"
    "s_or_b32 s81, s81, s82\n\t"
    "s_mov_b32 %0, s81\n\t"
    : "=s"(flag) : "s"(karg), "s"(wid), "s"(bid), "s"(ldsb), "s"(tag)
    : "s33", "v0", "v1", "v2", "v3", "v4", "v5", "v6", "v7", "v8", "v9", "v10", "v11", "v12", "v13", "v14", "v15", "v16", "v17", "v18", "v19", "v20", "v21", "v22", "v23", "v24", "v25", "v26", "v27", "v28", "v29", "v30", "v31", "v32", "v33", "v34", "v35", "v36", "v37", "v38", "v39", "v40", "v41", "v42", "v43", "v44", "v45", "v46", "v47", "v48", "v49", "v50", "v51", "v52", "v53", "v54", "v55", "v56", "v57", "v58", "v59", "v60", "v61", "v62", "v63", "v64", "v65", "v66", "v67", "v68", "v69", "v70", "v71", "v72", "v73", "v74", "v75", "v76", "v77", "v78", "v79", "v80", "v81", "v82", "v83", "v84", "v85", "v86", "v87", "v88", "v89", "v90", "v91", "v92", "v93", "v94", "v95", "v96", "v97", "v98", "v99", "v100", "v101", "v102", "v103", "v104", "v105", "v106", "v107", "v108", "v109", "v110", "v111", "v112", "v113", "v114", "v115", "v116", "v117", "v118", "v119", "v120", "v121", "v122", "v123", "v124", "v125", "v126", "v127", "v128", "v129", "v130", "v131", "v132", "v133", "v134", "v135", "v136", "v137", "v138", "v139", "v140", "v141", "v142", "v143", "v144", "v145", "v146", "v147", "v148", "v149", "v150", "v151", "v152", "v153", "v154", "v155", "v156", "v157", "v158", "v159", "v160", "v161", "v162", "v163", "v164", "v165", "v166", "v167", "v168", "v169", "v170", "v171", "v172", "v173", "v174", "v175", "v176", "v177", "v178", "v179", "v180", "v181", "v182", "v183", "v184", "v185", "v186", "v187", "v188", "v189", "v190", "v191", "v192", "v193", "v194", "v195", "v196", "v197", "v198", "v199", "v200", "v201", "v202", "v203", "v204", "v205", "v206", "v207", "v208", "v209", "v210", "v211", "v212", "v213", "v214", "v215", "v216", "v217", "v218", "v219", "v220", "v221", "v222", "v223", "v224", "v225", "v226", "v227", "v228", "v229", "v230", "v231", "v232", "v233", "v234", "v235", "v236", "v237", "v238", "v239", "v240", "v241", "v242", "v243", "v244", "v245", "v246", "v247", "v248", "v249", "v250", "v251", "v252", "v253", "v254", "v255", "a0", "a1", "a2", "a3", "a4", "a5", "a6", "a7", "a8", "a9", "a10", "a11", "a12", "a13", "a14", "a15", "a16", "a17", "a18", "a19", "a20", "a21", "a22", "a23", "a24", "a25", "a26", "a27", "a28", "a29", "a30", "a31", "a32", "a33", "a34", "a35", "a36", "a37", "a38", "a39", "a40", "a41", "a42", "a43", "a44", "a45", "a46", "a47", "a48", "a49", "a50", "a51", "a52", "a53", "a54", "a55", "a56", "a57", "a58", "a59", "a60", "a61", "a62", "a63", "a64", "a65", "a66", "a67", "a68", "a69", "a70", "a71", "a72", "a73", "a74", "a75", "a76", "a77", "a78", "a79", "a80", "a81", "a82", "a83", "a84", "a85", "a86", "a87", "a88", "a89", "a90", "a91", "a92", "a93", "a94", "a95", "a96", "a97", "a98", "a99", "a100", "a101", "a102", "a103", "a104", "a105", "a106", "a107", "a108", "a109", "a110", "a111", "a112", "a113", "a114", "a115", "a116", "a117", "a118", "a119", "a120", "a121", "a122", "a123", "a124", "a125", "a126", "a127", "a128", "a129", "a130", "a131", "a132", "a133", "a134", "a135", "a136", "a137", "a138", "a139", "a140", "a141", "a142", "a143", "a144", "a145", "a146", "a147", "a148", "a149", "a150", "a151", "a152", "a153", "a154", "a155", "a156", "a157", "a158", "a159", "a160", "a161", "a162", "a163", "a164", "a165", "a166", "a167", "a168", "a169", "a170", "a171", "a172", "a173", "a174", "a175", "a176", "a177", "a178", "a179", "a180", "a181", "a182", "a183", "a184", "a185", "a186", "a187", "a188", "a189", "a190", "a191", "a192", "a193", "a194", "a195", "a196", "a197", "a198", "a199", "a200", "a201", "a202", "a203", "a204", "a205", "a206", "a207", "a208", "a209", "a210", "a211", "a212", "a213", "a214", "a215", "a216", "a217", "a218", "a219", "a220", "a221", "a222", "a223", "a224", "a225", "a226", "a227", "a228", "a229", "a230", "a231", "a232", "a233", "a234", "a235", "a236", "a237", "a238", "a239", "a240", "a241", "a242", "a243", "a244", "a245", "a246", "a247", "a248", "a249", "a250", "a251", "a252", "a253", "a254", "a255", "s34", "s35", "s36", "s37", "s38", "s39", "s40", "s41", "s42", "s43", "s44", "s45", "s46", "s47", "s48", "s49", "s50", "s51", "s52", "s53", "s54", "s55", "s56", "s57", "s58", "s59", "s60", "s61", "s62", "s63", "s64", "s65", "s66", "s67", "s68", "s69", "s70", "s71", "s72", "s73", "s74", "s75", "s76", "s77", "s78", "s79", "s80", "s81", "s82", "s83", "s84", "s85", "s86", "s87", "s88", "s89", "s90", "s91", "s92", "s93", "s94", "s95", "s96", "s97", "s98", "s99", "vcc", "scc", "memory");

  asm volatile("" : "+s"(ka), "+s"(bid), "+s"(wid));
  if(__builtin_expect((flag & 1u) != 0u, 0)){ const int j_ = bid >> 3;
    attn_slow(((const u16*)(__attribute__((address_space(1))) const u16*)ka[0]), ((const u16*)(__attribute__((address_space(1))) const u16*)ka[1]), ((const u16*)(__attribute__((address_space(1))) const u16*)ka[2]), ((const u16*)(__attribute__((address_space(1))) const u16*)ka[3]), ((u16*)(__attribute__((address_space(1))) u16*)ka[4]), (int)(((bid & 7) << 2) + (j_ & 3)),
              (int)((j_ >> 2)*256 + wid*64 + lane_now())); }
  asm volatile(
    "s_load_dwordx2 s[36:37], %0, 32\n\t"
    "s_load_dwordx2 s[38:39], %0, 40\n\t"
    "s_load_dwordx2 s[40:41], %0, 96\n\t"
    "s_load_dwordx2 s[42:43], %0, 72\n\t"
    "s_mov_b32 s34, %1\n\t"
    "s_mov_b32 s35, %2\n\t"
    "s_mov_b32 s33, %3\n\t"
    "s_mov_b32 s82, %5\n\t"
    "s_waitcnt lgkmcnt(0)\n\t"
    "v_mbcnt_lo_u32_b32 v0, -1, 0\n\t"
    "v_mbcnt_hi_u32_b32 v0, -1, v0\n\t"
    "s_and_b32 s80, s35, 7\n\t"
    "s_lshr_b32 s81, s35, 3\n\t"
    "s_lshr_b32 s77, s80, 2\n\t"
    "s_and_b32 s98, s80, 3\n\t"
    "s_lshl_b32 s98, s98, 2\n\t"
    "s_and_b32 s99, s81, 3\n\t"
    "s_add_u32 s75, s98, s99\n\t"
    "s_lshr_b32 s76, s81, 2\n\t"
    "s_mov_b32 s52, s36\n\t"
    "s_and_b32 s53, s37, 0xffff\n\t"
    "s_mov_b32 s54, 16777216\n\t"
    "s_mov_b32 s55, 0x20000\n\t"
    "s_sub_u32 s56, s38, 4096\n\t"
    "s_subb_u32 s57, s39, 0\n\t"
    "s_and_b32 s57, s57, 0xffff\n\t"
    "s_mov_b32 s58, 8392704\n\t"
    "s_mov_b32 s59, 0x20000\n\t"
    "s_mul_i32 s73, s77, 4194304\n\t"
    "s_mul_i32 s80, s34, 32768\n\t"
    "s_add_u32 s73, s73, s80\n\t"
    "s_lshl_b32 s80, s76, 8\n\t"
    "s_add_u32 s73, s73, s80\n\t"
    "s_lshl_b32 s80, s34, 12\n\t"
    "s_add_u32 s74, s33, s80\n\t"
    "s_lshl_b32 s80, s75, 2\n\t"
    "s_add_u32 s78, s80, s34\n\t"
    "s_mul_i32 s72, s78, 262144\n\t"
    "s_mul_i32 s80, s77, 131072\n\t"
    "s_add_u32 s72, s72, s80\n\t"
    "v_mov_b32 v3, 4096\n\t"
    "v_bfe_u32 v253, v0, 0, 2\n\t"
    "v_lshl_add_u32 v3, v253, 4, v3\n\t"
    "v_bfe_u32 v253, v0, 2, 2\n\t"
    "v_lshl_add_u32 v3, v253, 11, v3\n\t"
    "v_bfe_u32 v253, v0, 4, 1\n\t"
    "s_mov_b32 s98, 16416\n\t"
    "v_mad_i32_i24 v3, v253, s98, v3\n\t"
    "v_bfe_u32 v253, v0, 5, 1\n\t"
    "v_lshl_add_u32 v3, v253, 6, v3\n\t"
    "v_bfe_u32 v253, v0, 1, 1\n\t"
    "v_bfe_u32 v254, v0, 4, 1\n\t"
    "v_and_b32 v253, v253, v254\n\t"
    "s_mov_b32 s98, 4294967232\n\t"
    "v_mad_i32_i24 v3, v253, s98, v3\n\t"
    "v_add_u32 v4, 4294966400, v3\n\t"
    "v_add_u32 v5, 6144, v3\n\t"
    "v_add_u32 v6, 5248, v3\n\t"
    "v_mov_b32 v1, 0\n\t"
    "v_bfe_u32 v253, v0, 0, 4\n\t"
    "v_lshl_add_u32 v1, v253, 4, v1\n\t"
    "v_bfe_u32 v253, v0, 4, 2\n\t"
    "v_lshl_add_u32 v1, v253, 9, v1\n\t"
    "v_mov_b32 v10, 0\n\t"
    "v_bfe_u32 v253, v0, 0, 2\n\t"
    "v_lshl_add_u32 v10, v253, 3, v10\n\t"
    "v_bfe_u32 v253, v0, 2, 2\n\t"
    "v_lshl_add_u32 v10, v253, 6, v10\n\t"
    "v_bfe_u32 v253, v0, 4, 1\n\t"
    "s_mov_b32 s98, 288\n\t"
    "v_mad_i32_i24 v10, v253, s98, v10\n\t"
    "v_bfe_u32 v253, v0, 5, 1\n\t"
    "v_lshl_add_u32 v10, v253, 12, v10\n\t"
    "v_add_u32 v10, s33, v10\n\t"
    "v_mov_b32 v11, 32\n\t"
    "v_bfe_u32 v253, v0, 0, 2\n\t"
    "v_lshl_add_u32 v11, v253, 3, v11\n\t"
    "v_bfe_u32 v253, v0, 2, 2\n\t"
    "v_lshl_add_u32 v11, v253, 6, v11\n\t"
    "v_bfe_u32 v253, v0, 4, 1\n\t"
    "s_mov_b32 s98, 224\n\t"
    "v_mad_i32_i24 v11, v253, s98, v11\n\t"
    "v_bfe_u32 v253, v0, 5, 1\n\t"
    "v_lshl_add_u32 v11, v253, 12, v11\n\t"
    "v_add_u32 v11, s33, v11\n\t"
    "s_mov_b32 s60, s40\n\t"
    "s_and_b32 s61, s41, 0xffff\n\t"
    "s_mov_b32 s62, 8388608\n\t"
    "s_mov_b32 s63, 0x20000\n\t"
    "s_lshl_b32 s88, s77, 22\n\t"
    "s_lshl_b32 s80, s78, 16\n\t"
    "s_add_u32 s88, s88, s80\n\t"
    "s_lshl_b32 s80, s76, 13\n\t"
    "s_add_u32 s88, s88, s80\n\t"
    "v_mov_b32 v240, 0\n\t"
    "v_bfe_u32 v253, v0, 0, 4\n\t"
    "v_lshl_add_u32 v240, v253, 4, v240\n\t"
    "v_bfe_u32 v253, v0, 4, 1\n\t"
    "v_lshl_add_u32 v240, v253, 10, v240\n\t"
    "v_bfe_u32 v253, v0, 5, 1\n\t"
    "v_lshl_add_u32 v240, v253, 9, v240\n\t"
    "v_add_u32 v241, 4096, v240\n\t"
    "s_and_b32 s80, s35, 7\n\t"
    "s_lshr_b32 s81, s35, 3\n\t"
    "s_lshl_b32 s80, s80, 2\n\t"
    "s_and_b32 s98, s81, 3\n\t"
    "s_add_u32 s80, s80, s98\n\t"
    "s_lshr_b32 s81, s81, 2\n\t"
    "s_lshl_b32 s81, s81, 5\n\t"
    "s_add_u32 s98, s80, s81\n\t"
    "s_mov_b32 s87, s98\n\t"
    "v_add_u32 v1, s72, v1\n\t"
    "v_add_u32 v3, s73, v3\n\t"
    "v_add_u32 v4, s73, v4\n\t"
    "v_add_u32 v5, s73, v5\n\t"
    "v_add_u32 v6, s73, v6\n\t"
    "s_and_b32 s99, s35, 3\n\t"
    "s_add_u32 s99, s99, 1\n\t"
    "s_and_b32 s50, s99, 3\n\t"
    "s_mul_i32 s72, s50, 32768\n\t"
    "s_mul_i32 s73, s50, 1048576\n\t"
    "s_lshr_b32 s98, s75, 1\n\t"
    "s_lshl_b32 s98, s98, 5\n\t"
    "s_lshl_b32 s99, s77, 4\n\t"
    "s_add_u32 s98, s98, s99\n\t"
    "s_lshl_b32 s98, s98, 2\n\t"
    "s_add_u32 s98, s98, 1024\n\t"
    "v_mbcnt_lo_u32_b32 v250, -1, 0\n\t"
    "v_mbcnt_hi_u32_b32 v250, -1, v250\n\t"
    "v_and_b32 v250, 3, v250\n\t"
    "v_lshlrev_b32 v250, 2, v250\n\t"
    "v_add_u32 v250, s98, v250\n\t"
    "v_lshlrev_b32 v250, 4, v250\n\t"
    "v_mov_b32 v245, s82\n\t"
    "s_lshl_b32 s80, s34, 2\n\t"
    "s_add_u32 s80, s80, 159744\n\t"
    "s_add_u32 s80, s80, s33\n\t"
    "v_mov_b32 v246, s80\n\t"
    "ds_write_b32 v246, v245\n\t"
    "s_waitcnt lgkmcnt(0)\n\t"
    "s_barrier\n\t"
    "v_mov_b32 v246, 159744\n\t"
    "v_add_u32 v246, s33, v246\n\t"
    "ds_read_b128 v[252:255], v246\n\t"
    "s_add_u32 m0, s74, 0\n\t"
    "s_nop 0\n\t"
    "buffer_load_dwordx4 v3, s[56:59], s73 offen offset:0 lds\n\t"
    "buffer_load_dwordx4 v4, s[56:59], s73 offen offset:1024 lds\n\t"
    "buffer_load_dwordx4 v5, s[56:59], s73 offen offset:2048 lds\n\t"
    "buffer_load_dwordx4 v6, s[56:59], s73 offen offset:3072 lds\n\t"
    "s_add_u32 s73, s73, 131072\n\t"
    "s_and_b32 s73, s73, 4194303\n\t"
    "s_add_u32 m0, s74, 16384\n\t"
    "s_nop 0\n\t"
    "buffer_load_dwordx4 v3, s[56:59], s73 offen offset:0 lds\n\t"
    "buffer_load_dwordx4 v4, s[56:59], s73 offen offset:1024 lds\n\t"
    "buffer_load_dwordx4 v5, s[56:59], s73 offen offset:2048 lds\n\t"
    "buffer_load_dwordx4 v6, s[56:59], s73 offen offset:3072 lds\n\t"
    "s_add_u32 s73, s73, 131072\n\t"
    "s_and_b32 s73, s73, 4194303\n\t"
    "s_add_u32 m0, s74, 32768\n\t"
    "s_nop 0\n\t"
    "buffer_load_dwordx4 v3, s[56:59], s73 offen offset:0 lds\n\t"
    "buffer_load_dwordx4 v4, s[56:59], s73 offen offset:1024 lds\n\t"
    "buffer_load_dwordx4 v5, s[56:59], s73 offen offset:2048 lds\n\t"
    "buffer_load_dwordx4 v6, s[56:59], s73 offen offset:3072 lds\n\t"
    "s_add_u32 s73, s73, 131072\n\t"
    "s_and_b32 s73, s73, 4194303\n\t"
    "s_waitcnt lgkmcnt(0)\n\t"
    "v_and_b32 v252, v252, v253\n\t"
    "v_and_b32 v254, v254, v255\n\t"
    "v_and_b32 v252, v252, v254\n\t"
    "s_nop 0\n\t"
    "v_readfirstlane_b32 s82, v252\n\t"
    "s_nop 0\n\t"
    "s_cmp_eq_u32 s82, 0\n\t"
    "s_cbranch_scc1 LLONG_m_oproj\n\t"
    "buffer_load_dwordx4 a[64:67], v1, s[52:55], s72 offen offset:0 sc1\n\t"
    "buffer_load_dwordx4 a[68:71], v1, s[52:55], s72 offen offset:256 sc1\n\t"
    "buffer_load_dwordx4 a[72:75], v1, s[52:55], s72 offen offset:2048 sc1\n\t"
    "buffer_load_dwordx4 a[76:79], v1, s[52:55], s72 offen offset:2304 sc1\n\t"
    "s_waitcnt vmcnt(16)\n\t"
    "s_barrier\n\t"
    "s_cmp_lg_u32 s34, 0\n\t"
    "s_cbranch_scc1 LPB_m_oproj_s\n\t"
    "s_lshl_b32 s86, s87, 2\n\t"
    "s_add_u32 s86, s86, 1024\n\t"
    "s_lshl_b32 s86, s86, 4\n\t"
    "v_mov_b32 v244, s86\n\t"
    "v_mov_b32 v245, %4\n\t"
    "s_mov_b64 exec, 1\n\t"
    "global_store_dword v244, v245, s[42:43] sc1\n\t"
    "s_mov_b64 exec, -1\n\t"
    "LPB_m_oproj_s:\n\t"
    "s_branch LSHORT_m_oproj\n\t"
    "LLONG_m_oproj:\n\t"
    "s_waitcnt vmcnt(12)\n\t"
    "s_barrier\n\t"
    "s_cmp_lg_u32 s34, 0\n\t"
    "s_cbranch_scc1 LPB_m_oproj\n\t"
    "s_lshl_b32 s86, s87, 2\n\t"
    "s_add_u32 s86, s86, 1024\n\t"
    "s_lshl_b32 s86, s86, 4\n\t"
    "v_mov_b32 v244, s86\n\t"
    "v_mov_b32 v245, %4\n\t"
    "s_mov_b64 exec, 1\n\t"
    "global_store_dword v244, v245, s[42:43] sc1\n\t"
    "s_mov_b64 exec, -1\n\t"
    "LPB_m_oproj:\n\t"
    "s_lshr_b32 s98, s75, 1\n\t"
    "s_lshl_b32 s98, s98, 5\n\t"
    "s_lshl_b32 s99, s77, 4\n\t"
    "s_add_u32 s98, s98, s99\n\t"
    "s_lshl_b32 s99, s50, 2\n\t"
    "s_add_u32 s98, s98, s99\n\t"
    "s_cmp_lg_u32 s34, 0\n\t"
    "s_cbranch_scc1 LPW_m_oproj\n\t"
    "v_mbcnt_lo_u32_b32 v245, -1, 0\n\t"
    "v_mbcnt_hi_u32_b32 v245, -1, v245\n\t"
    "s_lshl_b32 s86, s98, 2\n\t"
    "s_add_u32 s86, s86, 1024\n\t"
    "v_mov_b32 v244, v245\n\t"
    "v_min_u32 v244, 3, v244\n\t"
    "v_lshlrev_b32 v244, 2, v244\n\t"
    "v_add_u32 v246, s86, v244\n\t"
    "s_mov_b64 exec, -1\n\t"
    "v_lshlrev_b32 v246, 4, v246\n\t"
    "v_mbcnt_lo_u32_b32 v247, -1, 0\n\t"
    "v_mbcnt_hi_u32_b32 v247, -1, v247\n\t"
    "v_lshlrev_b32 v247, 2, v247\n\t"
    "v_add_u32 v247, 8704, v247\n\t"
    "v_lshlrev_b32 v247, 4, v247\n\t"
    "v_add_u32 v244, 4096, v247\n\t"
    "s_mov_b32 s51, 0x400000\n\t"
    "LPP_m_oproj:\n\t"
    "global_load_dword v245, v246, s[42:43] sc1\n\t"
    "global_load_dword v248, v247, s[42:43] sc1\n\t"
    "global_load_dword v249, v244, s[42:43] sc1\n\t"
    "s_waitcnt vmcnt(0)\n\t"
    "v_cmp_ne_u32 vcc, %4, v245\n\t"
    "v_cmp_ne_u32 s[84:85], %4, v248\n\t"
    "s_nop 0\n\t"
    "s_or_b64 vcc, vcc, s[84:85]\n\t"
    "v_cmp_ne_u32 s[84:85], %4, v249\n\t"
    "s_nop 0\n\t"
    "s_or_b64 vcc, vcc, s[84:85]\n\t"
    "s_sub_u32 s51, s51, 1\n\t"
    "s_cmp_eq_u32 s51, 0\n\t"
    "s_cbranch_scc1 LPD_m_oproj\n\t"
    "s_cmp_eq_u64 vcc, 0\n\t"
    "s_cbranch_scc1 LPD_m_oproj\n\t"
    "s_sleep 1\n\t"
    "s_branch LPP_m_oproj\n\t"
    "LPD_m_oproj:\n\t"
    "LPW_m_oproj:\n\t"
    "s_barrier\n\t"
    "buffer_load_dwordx4 a[64:67], v1, s[52:55], s72 offen offset:0 sc1\n\t"
    "buffer_load_dwordx4 a[68:71], v1, s[52:55], s72 offen offset:256 sc1\n\t"
    "buffer_load_dwordx4 a[72:75], v1, s[52:55], s72 offen offset:2048 sc1\n\t"
    "buffer_load_dwordx4 a[76:79], v1, s[52:55], s72 offen offset:2304 sc1\n\t"
    "LSHORT_m_oproj:\n\t"
    "s_add_u32 s72, s72, 4096\n\t"
    "s_and_b32 s72, s72, 131071\n\t"
    "s_waitcnt vmcnt(12)\n\t"
    "s_barrier\n\t"
    "ds_read_b64_tr_b16 a[0:1], v10 offset:0\n\t"
    "ds_read_b64_tr_b16 a[2:3], v10 offset:2048\n\t"
    "ds_read_b64_tr_b16 a[4:5], v11 offset:0\n\t"
    "ds_read_b64_tr_b16 a[6:7], v11 offset:2048\n\t"
    "ds_read_b64_tr_b16 a[8:9], v10 offset:512\n\t"
    "ds_read_b64_tr_b16 a[10:11], v10 offset:2560\n\t"
    "ds_read_b64_tr_b16 a[12:13], v11 offset:512\n\t"
    "ds_read_b64_tr_b16 a[14:15], v11 offset:2560\n\t"
    "ds_read_b64_tr_b16 a[16:17], v10 offset:1024\n\t"
    "ds_read_b64_tr_b16 a[18:19], v10 offset:3072\n\t"
    "ds_read_b64_tr_b16 a[20:21], v11 offset:1024\n\t"
    "ds_read_b64_tr_b16 a[22:23], v11 offset:3072\n\t"
    "ds_read_b64_tr_b16 a[24:25], v10 offset:1536\n\t"
    "ds_read_b64_tr_b16 a[26:27], v10 offset:3584\n\t"
    "ds_read_b64_tr_b16 a[28:29], v11 offset:1536\n\t"
    "ds_read_b64_tr_b16 a[30:31], v11 offset:3584\n\t"
    "s_mov_b32 s51, 4\n\t"
    "s_lshr_b32 s98, s51, 3\n\t"
    "s_add_u32 s98, s98, s50\n\t"
    "s_and_b32 s98, s98, 3\n\t"
    "s_lshl_b32 s98, s98, 8\n\t"
    "v_add_u32 v248, s98, v250\n\t"
    "global_load_dword v249, v248, s[42:43] sc1\n\t"
    "s_add_u32 s51, s51, 4\n\t"
    "s_waitcnt vmcnt(4) lgkmcnt(14)\n\t"
    "v_mfma_f32_16x16x32_bf16 v[16:19], a[0:3], a[64:67], 0\n\t"
    "buffer_load_dwordx4 a[80:83], v1, s[52:55], s72 offen offset:0 sc1\n\t"
    "s_waitcnt vmcnt(4)\n\t"
    "v_mfma_f32_16x16x32_bf16 v[20:23], a[0:3], a[68:71], 0\n\t"
    "buffer_load_dwordx4 a[84:87], v1, s[52:55], s72 offen offset:256 sc1\n\t"
    "s_waitcnt lgkmcnt(12)\n\t"
    "v_mfma_f32_16x16x32_bf16 v[24:27], a[4:7], a[64:67], 0\n\t"
    "buffer_load_dwordx4 a[88:91], v1, s[52:55], s72 offen offset:2048 sc1\n\t"
    "v_mfma_f32_16x16x32_bf16 v[28:31], a[4:7], a[68:71], 0\n\t"
    "s_waitcnt lgkmcnt(8)\n\t"
    "buffer_load_dwordx4 a[92:95], v1, s[52:55], s72 offen offset:2304 sc1\n\t"
    "v_mfma_f32_16x16x32_bf16 v[32:35], a[8:11], a[64:67], 0\n\t"
    "ds_read_b64_tr_b16 a[0:1], v10 offset:8192\n\t"
    "ds_read_b64_tr_b16 a[2:3], v10 offset:10240\n\t"
    "v_mfma_f32_16x16x32_bf16 v[36:39], a[8:11], a[68:71], 0\n\t"
    "ds_read_b64_tr_b16 a[4:5], v11 offset:8192\n\t"
    "ds_read_b64_tr_b16 a[6:7], v11 offset:10240\n\t"
    "s_add_u32 s72, s72, 4096\n\t"
    "v_mfma_f32_16x16x32_bf16 v[40:43], a[12:15], a[64:67], 0\n\t"
    "s_and_b32 s72, s72, 131071\n\t"
    "v_mfma_f32_16x16x32_bf16 v[44:47], a[12:15], a[68:71], 0\n\t"
    "s_waitcnt lgkmcnt(8)\n\t"
    "buffer_load_dwordx4 a[96:99], v1, s[52:55], s72 offen offset:0 sc1\n\t"
    "v_mfma_f32_16x16x32_bf16 v[48:51], a[16:19], a[64:67], 0\n\t"
    "ds_read_b64_tr_b16 a[8:9], v10 offset:8704\n\t"
    "ds_read_b64_tr_b16 a[10:11], v10 offset:10752\n\t"
    "buffer_load_dwordx4 a[100:103], v1, s[52:55], s72 offen offset:256 sc1\n\t"
    "v_mfma_f32_16x16x32_bf16 v[52:55], a[16:19], a[68:71], 0\n\t"
    "ds_read_b64_tr_b16 a[12:13], v11 offset:8704\n\t"
    "ds_read_b64_tr_b16 a[14:15], v11 offset:10752\n\t"
    "v_mfma_f32_16x16x32_bf16 v[56:59], a[20:23], a[64:67], 0\n\t"
    "buffer_load_dwordx4 a[104:107], v1, s[52:55], s72 offen offset:2048 sc1\n\t"
    "v_mfma_f32_16x16x32_bf16 v[60:63], a[20:23], a[68:71], 0\n\t"
    "s_waitcnt lgkmcnt(8)\n\t"
    "buffer_load_dwordx4 a[108:111], v1, s[52:55], s72 offen offset:2304 sc1\n\t"
    "v_mfma_f32_16x16x32_bf16 v[64:67], a[24:27], a[64:67], 0\n\t"
    "ds_read_b64_tr_b16 a[16:17], v10 offset:9216\n\t"
    "ds_read_b64_tr_b16 a[18:19], v10 offset:11264\n\t"
    "s_add_u32 s72, s72, 4096\n\t"
    "v_mfma_f32_16x16x32_bf16 v[68:71], a[24:27], a[68:71], 0\n\t"
    "ds_read_b64_tr_b16 a[20:21], v11 offset:9216\n\t"
    "ds_read_b64_tr_b16 a[22:23], v11 offset:11264\n\t"
    "s_and_b32 s72, s72, 131071\n\t"
    "v_mfma_f32_16x16x32_bf16 v[72:75], a[28:31], a[64:67], 0\n\t"
    "v_mfma_f32_16x16x32_bf16 v[76:79], a[28:31], a[68:71], 0\n\t"
    "s_waitcnt vmcnt(9) lgkmcnt(8)\n\t"
    "s_add_u32 m0, s74, 49152\n\t"
    "v_mfma_f32_16x16x32_bf16 v[16:19], a[0:3], a[72:75], v[16:19]\n\t"
    "s_barrier\n\t"
    "ds_read_b64_tr_b16 a[24:25], v10 offset:9728\n\t"
    "ds_read_b64_tr_b16 a[26:27], v10 offset:11776\n\t"
    "v_mfma_f32_16x16x32_bf16 v[20:23], a[0:3], a[76:79], v[20:23]\n\t"
    "ds_read_b64_tr_b16 a[28:29], v11 offset:9728\n\t"
    "ds_read_b64_tr_b16 a[30:31], v11 offset:11776\n\t"
    "v_mfma_f32_16x16x32_bf16 v[24:27], a[4:7], a[72:75], v[24:27]\n\t"
    "buffer_load_dwordx4 v3, s[56:59], s73 offen offset:0 lds\n\t"
    "v_mfma_f32_16x16x32_bf16 v[28:31], a[4:7], a[76:79], v[28:31]\n\t"
    "s_waitcnt lgkmcnt(8)\n\t"
    "v_mfma_f32_16x16x32_bf16 v[32:35], a[8:11], a[72:75], v[32:35]\n\t"
    "ds_read_b64_tr_b16 a[0:1], v10 offset:16384\n\t"
    "ds_read_b64_tr_b16 a[2:3], v10 offset:18432\n\t"
    "v_mfma_f32_16x16x32_bf16 v[36:39], a[8:11], a[76:79], v[36:39]\n\t"
    "ds_read_b64_tr_b16 a[4:5], v11 offset:16384\n\t"
    "ds_read_b64_tr_b16 a[6:7], v11 offset:18432\n\t"
    "v_mfma_f32_16x16x32_bf16 v[40:43], a[12:15], a[72:75], v[40:43]\n\t"
    "buffer_load_dwordx4 v4, s[56:59], s73 offen offset:1024 lds\n\t"
    "v_mfma_f32_16x16x32_bf16 v[44:47], a[12:15], a[76:79], v[44:47]\n\t"
    "s_waitcnt lgkmcnt(8)\n\t"
    "v_mfma_f32_16x16x32_bf16 v[48:51], a[16:19], a[72:75], v[48:51]\n\t"
    "ds_read_b64_tr_b16 a[8:9], v10 offset:16896\n\t"
    "ds_read_b64_tr_b16 a[10:11], v10 offset:18944\n\t"
    "v_mfma_f32_16x16x32_bf16 v[52:55], a[16:19], a[76:79], v[52:55]\n\t"
    "ds_read_b64_tr_b16 a[12:13], v11 offset:16896\n\t"
    "ds_read_b64_tr_b16 a[14:15], v11 offset:18944\n\t"
    "v_mfma_f32_16x16x32_bf16 v[56:59], a[20:23], a[72:75], v[56:59]\n\t"
    "buffer_load_dwordx4 v5, s[56:59], s73 offen offset:2048 lds\n\t"
    "v_mfma_f32_16x16x32_bf16 v[60:63], a[20:23], a[76:79], v[60:63]\n\t"
    "s_waitcnt lgkmcnt(8)\n\t"
    "v_mfma_f32_16x16x32_bf16 v[64:67], a[24:27], a[72:75], v[64:67]\n\t"
    "ds_read_b64_tr_b16 a[16:17], v10 offset:17408\n\t"
    "ds_read_b64_tr_b16 a[18:19], v10 offset:19456\n\t"
    "v_mfma_f32_16x16x32_bf16 v[68:71], a[24:27], a[76:79], v[68:71]\n\t"
    "ds_read_b64_tr_b16 a[20:21], v11 offset:17408\n\t"
    "ds_read_b64_tr_b16 a[22:23], v11 offset:19456\n\t"
    "v_mfma_f32_16x16x32_bf16 v[72:75], a[28:31], a[72:75], v[72:75]\n\t"
    "buffer_load_dwordx4 v6, s[56:59], s73 offen offset:3072 lds\n\t"
    "v_mfma_f32_16x16x32_bf16 v[76:79], a[28:31], a[76:79], v[76:79]\n\t"
    "s_waitcnt vmcnt(10) lgkmcnt(8)\n\t"
    "s_add_u32 s73, s73, 131072\n\t"
    "v_cmp_ne_u32 vcc, %4, v249\n\t"
    "s_cbranch_vccz 2f\n\t"
    "s_mov_b32 s86, 0x400000\n\t"
    "1:\n\t"
    "global_load_dword v249, v248, s[42:43] sc1\n\t"
    "s_waitcnt vmcnt(0)\n\t"
    "v_cmp_ne_u32 vcc, %4, v249\n\t"
    "s_sub_u32 s86, s86, 1\n\t"
    "s_cmp_eq_u32 s86, 0\n\t"
    "s_cbranch_scc1 2f\n\t"
    "s_cbranch_vccz 2f\n\t"
    "s_sleep 1\n\t"
    "s_branch 1b\n\t"
    "2:\n\t"
    "v_mfma_f32_16x16x32_bf16 v[16:19], a[0:3], a[80:83], v[16:19]\n\t"
    "ds_read_b64_tr_b16 a[24:25], v10 offset:17920\n\t"
    "ds_read_b64_tr_b16 a[26:27], v10 offset:19968\n\t"
    "v_mfma_f32_16x16x32_bf16 v[20:23], a[0:3], a[84:87], v[20:23]\n\t"
    "ds_read_b64_tr_b16 a[28:29], v11 offset:17920\n\t"
    "ds_read_b64_tr_b16 a[30:31], v11 offset:19968\n\t"
    "v_mfma_f32_16x16x32_bf16 v[24:27], a[4:7], a[80:83], v[24:27]\n\t"
    "buffer_load_dwordx4 a[112:115], v1, s[52:55], s72 offen offset:0 sc1\n\t"
    "v_mfma_f32_16x16x32_bf16 v[28:31], a[4:7], a[84:87], v[28:31]\n\t"
    "s_waitcnt lgkmcnt(8)\n\t"
    "s_and_b32 s73, s73, 4194303\n\t"
    "v_mfma_f32_16x16x32_bf16 v[32:35], a[8:11], a[80:83], v[32:35]\n\t"
    "ds_read_b64_tr_b16 a[0:1], v10 offset:24576\n\t"
    "ds_read_b64_tr_b16 a[2:3], v10 offset:26624\n\t"
    "v_mfma_f32_16x16x32_bf16 v[36:39], a[8:11], a[84:87], v[36:39]\n\t"
    "ds_read_b64_tr_b16 a[4:5], v11 offset:24576\n\t"
    "ds_read_b64_tr_b16 a[6:7], v11 offset:26624\n\t"
    "v_mfma_f32_16x16x32_bf16 v[40:43], a[12:15], a[80:83], v[40:43]\n\t"
    "buffer_load_dwordx4 a[116:119], v1, s[52:55], s72 offen offset:256 sc1\n\t"
    "v_mfma_f32_16x16x32_bf16 v[44:47], a[12:15], a[84:87], v[44:47]\n\t"
    "s_waitcnt lgkmcnt(8)\n\t"
    "v_mfma_f32_16x16x32_bf16 v[48:51], a[16:19], a[80:83], v[48:51]\n\t"
    "ds_read_b64_tr_b16 a[8:9], v10 offset:25088\n\t"
    "ds_read_b64_tr_b16 a[10:11], v10 offset:27136\n\t"
    "v_mfma_f32_16x16x32_bf16 v[52:55], a[16:19], a[84:87], v[52:55]\n\t"
    "ds_read_b64_tr_b16 a[12:13], v11 offset:25088\n\t"
    "ds_read_b64_tr_b16 a[14:15], v11 offset:27136\n\t"
    "v_mfma_f32_16x16x32_bf16 v[56:59], a[20:23], a[80:83], v[56:59]\n\t"
    "buffer_load_dwordx4 a[120:123], v1, s[52:55], s72 offen offset:2048 sc1\n\t"
    "v_mfma_f32_16x16x32_bf16 v[60:63], a[20:23], a[84:87], v[60:63]\n\t"
    "s_waitcnt lgkmcnt(8)\n\t"
    "v_mfma_f32_16x16x32_bf16 v[64:67], a[24:27], a[80:83], v[64:67]\n\t"
    "ds_read_b64_tr_b16 a[16:17], v10 offset:25600\n\t"
    "ds_read_b64_tr_b16 a[18:19], v10 offset:27648\n\t"
    "v_mfma_f32_16x16x32_bf16 v[68:71], a[24:27], a[84:87], v[68:71]\n\t"
    "ds_read_b64_tr_b16 a[20:21], v11 offset:25600\n\t"
    "ds_read_b64_tr_b16 a[22:23], v11 offset:27648\n\t"
    "v_mfma_f32_16x16x32_bf16 v[72:75], a[28:31], a[80:83], v[72:75]\n\t"
    "buffer_load_dwordx4 a[124:127], v1, s[52:55], s72 offen offset:2304 sc1\n\t"
    "v_mfma_f32_16x16x32_bf16 v[76:79], a[28:31], a[84:87], v[76:79]\n\t"
    "s_waitcnt vmcnt(12) lgkmcnt(8)\n\t"
    "s_add_u32 s72, s72, 4096\n\t"
    "v_mfma_f32_16x16x32_bf16 v[16:19], a[0:3], a[88:91], v[16:19]\n\t"
    "s_barrier\n\t"
    "ds_read_b64_tr_b16 a[24:25], v10 offset:26112\n\t"
    "ds_read_b64_tr_b16 a[26:27], v10 offset:28160\n\t"
    "v_mfma_f32_16x16x32_bf16 v[20:23], a[0:3], a[92:95], v[20:23]\n\t"
    "ds_read_b64_tr_b16 a[28:29], v11 offset:26112\n\t"
    "ds_read_b64_tr_b16 a[30:31], v11 offset:28160\n\t"
    "v_mfma_f32_16x16x32_bf16 v[24:27], a[4:7], a[88:91], v[24:27]\n\t"
    "s_add_u32 m0, s74, 0\n\t"
    "v_mfma_f32_16x16x32_bf16 v[28:31], a[4:7], a[92:95], v[28:31]\n\t"
    "s_waitcnt lgkmcnt(8)\n\t"
    "s_and_b32 s72, s72, 131071\n\t"
    "v_mfma_f32_16x16x32_bf16 v[32:35], a[8:11], a[88:91], v[32:35]\n\t"
    "ds_read_b64_tr_b16 a[0:1], v10 offset:32768\n\t"
    "ds_read_b64_tr_b16 a[2:3], v10 offset:34816\n\t"
    "v_mfma_f32_16x16x32_bf16 v[36:39], a[8:11], a[92:95], v[36:39]\n\t"
    "ds_read_b64_tr_b16 a[4:5], v11 offset:32768\n\t"
    "ds_read_b64_tr_b16 a[6:7], v11 offset:34816\n\t"
    "v_mfma_f32_16x16x32_bf16 v[40:43], a[12:15], a[88:91], v[40:43]\n\t"
    "buffer_load_dwordx4 v3, s[56:59], s73 offen offset:0 lds\n\t"
    "v_mfma_f32_16x16x32_bf16 v[44:47], a[12:15], a[92:95], v[44:47]\n\t"
    "s_waitcnt lgkmcnt(8)\n\t"
    "v_mfma_f32_16x16x32_bf16 v[48:51], a[16:19], a[88:91], v[48:51]\n\t"
    "ds_read_b64_tr_b16 a[8:9], v10 offset:33280\n\t"
    "ds_read_b64_tr_b16 a[10:11], v10 offset:35328\n\t"
    "v_mfma_f32_16x16x32_bf16 v[52:55], a[16:19], a[92:95], v[52:55]\n\t"
    "ds_read_b64_tr_b16 a[12:13], v11 offset:33280\n\t"
    "ds_read_b64_tr_b16 a[14:15], v11 offset:35328\n\t"
    "v_mfma_f32_16x16x32_bf16 v[56:59], a[20:23], a[88:91], v[56:59]\n\t"
    "buffer_load_dwordx4 v4, s[56:59], s73 offen offset:1024 lds\n\t"
    "v_mfma_f32_16x16x32_bf16 v[60:63], a[20:23], a[92:95], v[60:63]\n\t"
    "s_waitcnt lgkmcnt(8)\n\t"
    "v_mfma_f32_16x16x32_bf16 v[64:67], a[24:27], a[88:91], v[64:67]\n\t"
    "ds_read_b64_tr_b16 a[16:17], v10 offset:33792\n\t"
    "ds_read_b64_tr_b16 a[18:19], v10 offset:35840\n\t"
    "v_mfma_f32_16x16x32_bf16 v[68:71], a[24:27], a[92:95], v[68:71]\n\t"
    "ds_read_b64_tr_b16 a[20:21], v11 offset:33792\n\t"
    "ds_read_b64_tr_b16 a[22:23], v11 offset:35840\n\t"
    "v_mfma_f32_16x16x32_bf16 v[72:75], a[28:31], a[88:91], v[72:75]\n\t"
    "buffer_load_dwordx4 v5, s[56:59], s73 offen offset:2048 lds\n\t"
    "v_mfma_f32_16x16x32_bf16 v[76:79], a[28:31], a[92:95], v[76:79]\n\t"
    "s_waitcnt vmcnt(13) lgkmcnt(8)\n\t"
    "s_lshr_b32 s98, s51, 3\n\t"
    "s_add_u32 s98, s98, s50\n\t"
    "s_and_b32 s98, s98, 3\n\t"
    "s_lshl_b32 s98, s98, 8\n\t"
    "v_add_u32 v248, s98, v250\n\t"
    "global_load_dword v249, v248, s[42:43] sc1\n\t"
    "s_add_u32 s51, s51, 4\n\t"
    "v_mfma_f32_16x16x32_bf16 v[16:19], a[0:3], a[96:99], v[16:19]\n\t"
    "ds_read_b64_tr_b16 a[24:25], v10 offset:34304\n\t"
    "ds_read_b64_tr_b16 a[26:27], v10 offset:36352\n\t"
    "v_mfma_f32_16x16x32_bf16 v[20:23], a[0:3], a[100:103], v[20:23]\n\t"
    "ds_read_b64_tr_b16 a[28:29], v11 offset:34304\n\t"
    "ds_read_b64_tr_b16 a[30:31], v11 offset:36352\n\t"
    "v_mfma_f32_16x16x32_bf16 v[24:27], a[4:7], a[96:99], v[24:27]\n\t"
    "buffer_load_dwordx4 v6, s[56:59], s73 offen offset:3072 lds\n\t"
    "v_mfma_f32_16x16x32_bf16 v[28:31], a[4:7], a[100:103], v[28:31]\n\t"
    "s_waitcnt lgkmcnt(8)\n\t"
    "s_add_u32 s73, s73, 131072\n\t"
    "v_mfma_f32_16x16x32_bf16 v[32:35], a[8:11], a[96:99], v[32:35]\n\t"
    "ds_read_b64_tr_b16 a[0:1], v10 offset:40960\n\t"
    "ds_read_b64_tr_b16 a[2:3], v10 offset:43008\n\t"
    "v_mfma_f32_16x16x32_bf16 v[36:39], a[8:11], a[100:103], v[36:39]\n\t"
    "ds_read_b64_tr_b16 a[4:5], v11 offset:40960\n\t"
    "ds_read_b64_tr_b16 a[6:7], v11 offset:43008\n\t"
    "v_mfma_f32_16x16x32_bf16 v[40:43], a[12:15], a[96:99], v[40:43]\n\t"
    "buffer_load_dwordx4 a[64:67], v1, s[52:55], s72 offen offset:0 sc1\n\t"
    "v_mfma_f32_16x16x32_bf16 v[44:47], a[12:15], a[100:103], v[44:47]\n\t"
    "s_waitcnt lgkmcnt(8)\n\t"
    "s_and_b32 s73, s73, 4194303\n\t"
    "v_mfma_f32_16x16x32_bf16 v[48:51], a[16:19], a[96:99], v[48:51]\n\t"
    "ds_read_b64_tr_b16 a[8:9], v10 offset:41472\n\t"
    "ds_read_b64_tr_b16 a[10:11], v10 offset:43520\n\t"
    "v_mfma_f32_16x16x32_bf16 v[52:55], a[16:19], a[100:103], v[52:55]\n\t"
    "ds_read_b64_tr_b16 a[12:13], v11 offset:41472\n\t"
    "ds_read_b64_tr_b16 a[14:15], v11 offset:43520\n\t"
    "v_mfma_f32_16x16x32_bf16 v[56:59], a[20:23], a[96:99], v[56:59]\n\t"
    "buffer_load_dwordx4 a[68:71], v1, s[52:55], s72 offen offset:256 sc1\n\t"
    "v_mfma_f32_16x16x32_bf16 v[60:63], a[20:23], a[100:103], v[60:63]\n\t"
    "s_waitcnt lgkmcnt(8)\n\t"
    "v_mfma_f32_16x16x32_bf16 v[64:67], a[24:27], a[96:99], v[64:67]\n\t"
    "ds_read_b64_tr_b16 a[16:17], v10 offset:41984\n\t"
    "ds_read_b64_tr_b16 a[18:19], v10 offset:44032\n\t"
    "v_mfma_f32_16x16x32_bf16 v[68:71], a[24:27], a[100:103], v[68:71]\n\t"
    "ds_read_b64_tr_b16 a[20:21], v11 offset:41984\n\t"
    "ds_read_b64_tr_b16 a[22:23], v11 offset:44032\n\t"
    "v_mfma_f32_16x16x32_bf16 v[72:75], a[28:31], a[96:99], v[72:75]\n\t"
    "buffer_load_dwordx4 a[72:75], v1, s[52:55], s72 offen offset:2048 sc1\n\t"
    "v_mfma_f32_16x16x32_bf16 v[76:79], a[28:31], a[100:103], v[76:79]\n\t"
    "s_waitcnt vmcnt(16) lgkmcnt(8)\n\t"
    "s_add_u32 m0, s74, 16384\n\t"
    "v_mfma_f32_16x16x32_bf16 v[16:19], a[0:3], a[104:107], v[16:19]\n\t"
    "s_waitcnt vmcnt(12)\n\t"
    "s_barrier\n\t"
    "ds_read_b64_tr_b16 a[24:25], v10 offset:42496\n\t"
    "ds_read_b64_tr_b16 a[26:27], v10 offset:44544\n\t"
    "v_mfma_f32_16x16x32_bf16 v[20:23], a[0:3], a[108:111], v[20:23]\n\t"
    "ds_read_b64_tr_b16 a[28:29], v11 offset:42496\n\t"
    "ds_read_b64_tr_b16 a[30:31], v11 offset:44544\n\t"
    "v_mfma_f32_16x16x32_bf16 v[24:27], a[4:7], a[104:107], v[24:27]\n\t"
    "buffer_load_dwordx4 a[76:79], v1, s[52:55], s72 offen offset:2304 sc1\n\t"
    "v_mfma_f32_16x16x32_bf16 v[28:31], a[4:7], a[108:111], v[28:31]\n\t"
    "s_waitcnt lgkmcnt(8)\n\t"
    "s_add_u32 s72, s72, 4096\n\t"
    "v_mfma_f32_16x16x32_bf16 v[32:35], a[8:11], a[104:107], v[32:35]\n\t"
    "ds_read_b64_tr_b16 a[0:1], v10 offset:49152\n\t"
    "ds_read_b64_tr_b16 a[2:3], v10 offset:51200\n\t"
    "v_mfma_f32_16x16x32_bf16 v[36:39], a[8:11], a[108:111], v[36:39]\n\t"
    "ds_read_b64_tr_b16 a[4:5], v11 offset:49152\n\t"
    "ds_read_b64_tr_b16 a[6:7], v11 offset:51200\n\t"
    "v_mfma_f32_16x16x32_bf16 v[40:43], a[12:15], a[104:107], v[40:43]\n\t"
    "buffer_load_dwordx4 v3, s[56:59], s73 offen offset:0 lds\n\t"
    "v_mfma_f32_16x16x32_bf16 v[44:47], a[12:15], a[108:111], v[44:47]\n\t"
    "s_waitcnt lgkmcnt(8)\n\t"
    "s_and_b32 s72, s72, 131071\n\t"
    "v_mfma_f32_16x16x32_bf16 v[48:51], a[16:19], a[104:107], v[48:51]\n\t"
    "ds_read_b64_tr_b16 a[8:9], v10 offset:49664\n\t"
    "ds_read_b64_tr_b16 a[10:11], v10 offset:51712\n\t"
    "v_mfma_f32_16x16x32_bf16 v[52:55], a[16:19], a[108:111], v[52:55]\n\t"
    "ds_read_b64_tr_b16 a[12:13], v11 offset:49664\n\t"
    "ds_read_b64_tr_b16 a[14:15], v11 offset:51712\n\t"
    "v_mfma_f32_16x16x32_bf16 v[56:59], a[20:23], a[104:107], v[56:59]\n\t"
    "buffer_load_dwordx4 v4, s[56:59], s73 offen offset:1024 lds\n\t"
    "v_mfma_f32_16x16x32_bf16 v[60:63], a[20:23], a[108:111], v[60:63]\n\t"
    "s_waitcnt lgkmcnt(8)\n\t"
    "v_mfma_f32_16x16x32_bf16 v[64:67], a[24:27], a[104:107], v[64:67]\n\t"
    "ds_read_b64_tr_b16 a[16:17], v10 offset:50176\n\t"
    "ds_read_b64_tr_b16 a[18:19], v10 offset:52224\n\t"
    "v_mfma_f32_16x16x32_bf16 v[68:71], a[24:27], a[108:111], v[68:71]\n\t"
    "ds_read_b64_tr_b16 a[20:21], v11 offset:50176\n\t"
    "ds_read_b64_tr_b16 a[22:23], v11 offset:52224\n\t"
    "v_mfma_f32_16x16x32_bf16 v[72:75], a[28:31], a[104:107], v[72:75]\n\t"
    "buffer_load_dwordx4 v5, s[56:59], s73 offen offset:2048 lds\n\t"
    "v_mfma_f32_16x16x32_bf16 v[76:79], a[28:31], a[108:111], v[76:79]\n\t"
    "s_waitcnt vmcnt(14) lgkmcnt(8)\n\t"
    "v_mfma_f32_16x16x32_bf16 v[16:19], a[0:3], a[112:115], v[16:19]\n\t"
    "ds_read_b64_tr_b16 a[24:25], v10 offset:50688\n\t"
    "ds_read_b64_tr_b16 a[26:27], v10 offset:52736\n\t"
    "v_mfma_f32_16x16x32_bf16 v[20:23], a[0:3], a[116:119], v[20:23]\n\t"
    "ds_read_b64_tr_b16 a[28:29], v11 offset:50688\n\t"
    "ds_read_b64_tr_b16 a[30:31], v11 offset:52736\n\t"
    "v_mfma_f32_16x16x32_bf16 v[24:27], a[4:7], a[112:115], v[24:27]\n\t"
    "buffer_load_dwordx4 v6, s[56:59], s73 offen offset:3072 lds\n\t"
    "v_mfma_f32_16x16x32_bf16 v[28:31], a[4:7], a[116:119], v[28:31]\n\t"
    "s_waitcnt lgkmcnt(8)\n\t"
    "s_add_u32 s73, s73, 131072\n\t"
    "v_mfma_f32_16x16x32_bf16 v[32:35], a[8:11], a[112:115], v[32:35]\n\t"
    "ds_read_b64_tr_b16 a[0:1], v10 offset:57344\n\t"
    "ds_read_b64_tr_b16 a[2:3], v10 offset:59392\n\t"
    "v_mfma_f32_16x16x32_bf16 v[36:39], a[8:11], a[116:119], v[36:39]\n\t"
    "ds_read_b64_tr_b16 a[4:5], v11 offset:57344\n\t"
    "ds_read_b64_tr_b16 a[6:7], v11 offset:59392\n\t"
    "v_mfma_f32_16x16x32_bf16 v[40:43], a[12:15], a[112:115], v[40:43]\n\t"
    "buffer_load_dwordx4 a[80:83], v1, s[52:55], s72 offen offset:0 sc1\n\t"
    "v_mfma_f32_16x16x32_bf16 v[44:47], a[12:15], a[116:119], v[44:47]\n\t"
    "s_waitcnt lgkmcnt(8)\n\t"
    "s_and_b32 s73, s73, 4194303\n\t"
    "v_mfma_f32_16x16x32_bf16 v[48:51], a[16:19], a[112:115], v[48:51]\n\t"
    "ds_read_b64_tr_b16 a[8:9], v10 offset:57856\n\t"
    "ds_read_b64_tr_b16 a[10:11], v10 offset:59904\n\t"
    "v_mfma_f32_16x16x32_bf16 v[52:55], a[16:19], a[116:119], v[52:55]\n\t"
    "ds_read_b64_tr_b16 a[12:13], v11 offset:57856\n\t"
    "ds_read_b64_tr_b16 a[14:15], v11 offset:59904\n\t"
    "v_mfma_f32_16x16x32_bf16 v[56:59], a[20:23], a[112:115], v[56:59]\n\t"
    "buffer_load_dwordx4 a[84:87], v1, s[52:55], s72 offen offset:256 sc1\n\t"
    "v_mfma_f32_16x16x32_bf16 v[60:63], a[20:23], a[116:119], v[60:63]\n\t"
    "s_waitcnt lgkmcnt(8)\n\t"
    "v_mfma_f32_16x16x32_bf16 v[64:67], a[24:27], a[112:115], v[64:67]\n\t"
    "ds_read_b64_tr_b16 a[16:17], v10 offset:58368\n\t"
    "ds_read_b64_tr_b16 a[18:19], v10 offset:60416\n\t"
    "v_mfma_f32_16x16x32_bf16 v[68:71], a[24:27], a[116:119], v[68:71]\n\t"
    "ds_read_b64_tr_b16 a[20:21], v11 offset:58368\n\t"
    "ds_read_b64_tr_b16 a[22:23], v11 offset:60416\n\t"
    "v_mfma_f32_16x16x32_bf16 v[72:75], a[28:31], a[112:115], v[72:75]\n\t"
    "buffer_load_dwordx4 a[88:91], v1, s[52:55], s72 offen offset:2048 sc1\n\t"
    "v_mfma_f32_16x16x32_bf16 v[76:79], a[28:31], a[116:119], v[76:79]\n\t"
    "s_waitcnt vmcnt(16) lgkmcnt(8)\n\t"
    "s_add_u32 m0, s74, 32768\n\t"
    "v_mfma_f32_16x16x32_bf16 v[16:19], a[0:3], a[120:123], v[16:19]\n\t"
    "s_waitcnt vmcnt(11)\n\t"
    "s_barrier\n\t"
    "ds_read_b64_tr_b16 a[24:25], v10 offset:58880\n\t"
    "ds_read_b64_tr_b16 a[26:27], v10 offset:60928\n\t"
    "v_mfma_f32_16x16x32_bf16 v[20:23], a[0:3], a[124:127], v[20:23]\n\t"
    "ds_read_b64_tr_b16 a[28:29], v11 offset:58880\n\t"
    "ds_read_b64_tr_b16 a[30:31], v11 offset:60928\n\t"
    "v_mfma_f32_16x16x32_bf16 v[24:27], a[4:7], a[120:123], v[24:27]\n\t"
    "buffer_load_dwordx4 a[92:95], v1, s[52:55], s72 offen offset:2304 sc1\n\t"
    "v_mfma_f32_16x16x32_bf16 v[28:31], a[4:7], a[124:127], v[28:31]\n\t"
    "s_waitcnt lgkmcnt(8)\n\t"
    "s_add_u32 s72, s72, 4096\n\t"
    "v_mfma_f32_16x16x32_bf16 v[32:35], a[8:11], a[120:123], v[32:35]\n\t"
    "ds_read_b64_tr_b16 a[0:1], v10 offset:0\n\t"
    "ds_read_b64_tr_b16 a[2:3], v10 offset:2048\n\t"
    "v_mfma_f32_16x16x32_bf16 v[36:39], a[8:11], a[124:127], v[36:39]\n\t"
    "ds_read_b64_tr_b16 a[4:5], v11 offset:0\n\t"
    "ds_read_b64_tr_b16 a[6:7], v11 offset:2048\n\t"
    "v_mfma_f32_16x16x32_bf16 v[40:43], a[12:15], a[120:123], v[40:43]\n\t"
    "buffer_load_dwordx4 v3, s[56:59], s73 offen offset:0 lds\n\t"
    "v_mfma_f32_16x16x32_bf16 v[44:47], a[12:15], a[124:127], v[44:47]\n\t"
    "s_waitcnt lgkmcnt(8)\n\t"
    "s_and_b32 s72, s72, 131071\n\t"
    "v_mfma_f32_16x16x32_bf16 v[48:51], a[16:19], a[120:123], v[48:51]\n\t"
    "ds_read_b64_tr_b16 a[8:9], v10 offset:512\n\t"
    "ds_read_b64_tr_b16 a[10:11], v10 offset:2560\n\t"
    "v_mfma_f32_16x16x32_bf16 v[52:55], a[16:19], a[124:127], v[52:55]\n\t"
    "ds_read_b64_tr_b16 a[12:13], v11 offset:512\n\t"
    "ds_read_b64_tr_b16 a[14:15], v11 offset:2560\n\t"
    "v_mfma_f32_16x16x32_bf16 v[56:59], a[20:23], a[120:123], v[56:59]\n\t"
    "buffer_load_dwordx4 v4, s[56:59], s73 offen offset:1024 lds\n\t"
    "v_mfma_f32_16x16x32_bf16 v[60:63], a[20:23], a[124:127], v[60:63]\n\t"
    "s_waitcnt lgkmcnt(8)\n\t"
    "v_mfma_f32_16x16x32_bf16 v[64:67], a[24:27], a[120:123], v[64:67]\n\t"
    "ds_read_b64_tr_b16 a[16:17], v10 offset:1024\n\t"
    "ds_read_b64_tr_b16 a[18:19], v10 offset:3072\n\t"
    "v_mfma_f32_16x16x32_bf16 v[68:71], a[24:27], a[124:127], v[68:71]\n\t"
    "ds_read_b64_tr_b16 a[20:21], v11 offset:1024\n\t"
    "ds_read_b64_tr_b16 a[22:23], v11 offset:3072\n\t"
    "v_mfma_f32_16x16x32_bf16 v[72:75], a[28:31], a[120:123], v[72:75]\n\t"
    "buffer_load_dwordx4 v5, s[56:59], s73 offen offset:2048 lds\n\t"
    "v_mfma_f32_16x16x32_bf16 v[76:79], a[28:31], a[124:127], v[76:79]\n\t"
    "s_waitcnt vmcnt(13) lgkmcnt(8)\n\t"
    "v_mfma_f32_16x16x32_bf16 v[16:19], a[0:3], a[64:67], v[16:19]\n\t"
    "ds_read_b64_tr_b16 a[24:25], v10 offset:1536\n\t"
    "ds_read_b64_tr_b16 a[26:27], v10 offset:3584\n\t"
    "v_mfma_f32_16x16x32_bf16 v[20:23], a[0:3], a[68:71], v[20:23]\n\t"
    "ds_read_b64_tr_b16 a[28:29], v11 offset:1536\n\t"
    "ds_read_b64_tr_b16 a[30:31], v11 offset:3584\n\t"
    "v_mfma_f32_16x16x32_bf16 v[24:27], a[4:7], a[64:67], v[24:27]\n\t"
    "buffer_load_dwordx4 v6, s[56:59], s73 offen offset:3072 lds\n\t"
    "v_mfma_f32_16x16x32_bf16 v[28:31], a[4:7], a[68:71], v[28:31]\n\t"
    "s_waitcnt lgkmcnt(8)\n\t"
    "s_add_u32 s73, s73, 131072\n\t"
    "v_mfma_f32_16x16x32_bf16 v[32:35], a[8:11], a[64:67], v[32:35]\n\t"
    "ds_read_b64_tr_b16 a[0:1], v10 offset:8192\n\t"
    "ds_read_b64_tr_b16 a[2:3], v10 offset:10240\n\t"
    "v_mfma_f32_16x16x32_bf16 v[36:39], a[8:11], a[68:71], v[36:39]\n\t"
    "ds_read_b64_tr_b16 a[4:5], v11 offset:8192\n\t"
    "ds_read_b64_tr_b16 a[6:7], v11 offset:10240\n\t"
    "v_mfma_f32_16x16x32_bf16 v[40:43], a[12:15], a[64:67], v[40:43]\n\t"
    "buffer_load_dwordx4 a[96:99], v1, s[52:55], s72 offen offset:0 sc1\n\t"
    "v_mfma_f32_16x16x32_bf16 v[44:47], a[12:15], a[68:71], v[44:47]\n\t"
    "s_waitcnt lgkmcnt(8)\n\t"
    "s_and_b32 s73, s73, 4194303\n\t"
    "v_mfma_f32_16x16x32_bf16 v[48:51], a[16:19], a[64:67], v[48:51]\n\t"
    "ds_read_b64_tr_b16 a[8:9], v10 offset:8704\n\t"
    "ds_read_b64_tr_b16 a[10:11], v10 offset:10752\n\t"
    "v_mfma_f32_16x16x32_bf16 v[52:55], a[16:19], a[68:71], v[52:55]\n\t"
    "ds_read_b64_tr_b16 a[12:13], v11 offset:8704\n\t"
    "ds_read_b64_tr_b16 a[14:15], v11 offset:10752\n\t"
    "v_mfma_f32_16x16x32_bf16 v[56:59], a[20:23], a[64:67], v[56:59]\n\t"
    "buffer_load_dwordx4 a[100:103], v1, s[52:55], s72 offen offset:256 sc1\n\t"
    "v_mfma_f32_16x16x32_bf16 v[60:63], a[20:23], a[68:71], v[60:63]\n\t"
    "s_waitcnt lgkmcnt(8)\n\t"
    "v_mfma_f32_16x16x32_bf16 v[64:67], a[24:27], a[64:67], v[64:67]\n\t"
    "ds_read_b64_tr_b16 a[16:17], v10 offset:9216\n\t"
    "ds_read_b64_tr_b16 a[18:19], v10 offset:11264\n\t"
    "v_mfma_f32_16x16x32_bf16 v[68:71], a[24:27], a[68:71], v[68:71]\n\t"
    "ds_read_b64_tr_b16 a[20:21], v11 offset:9216\n\t"
    "ds_read_b64_tr_b16 a[22:23], v11 offset:11264\n\t"
    "v_mfma_f32_16x16x32_bf16 v[72:75], a[28:31], a[64:67], v[72:75]\n\t"
    "buffer_load_dwordx4 a[104:107], v1, s[52:55], s72 offen offset:2048 sc1\n\t"
    "v_mfma_f32_16x16x32_bf16 v[76:79], a[28:31], a[68:71], v[76:79]\n\t"
    "s_waitcnt vmcnt(15) lgkmcnt(8)\n\t"
    "s_add_u32 m0, s74, 49152\n\t"
    "v_mfma_f32_16x16x32_bf16 v[16:19], a[0:3], a[72:75], v[16:19]\n\t"
    "s_waitcnt vmcnt(11)\n\t"
    "s_barrier\n\t"
    "ds_read_b64_tr_b16 a[24:25], v10 offset:9728\n\t"
    "ds_read_b64_tr_b16 a[26:27], v10 offset:11776\n\t"
    "v_mfma_f32_16x16x32_bf16 v[20:23], a[0:3], a[76:79], v[20:23]\n\t"
    "ds_read_b64_tr_b16 a[28:29], v11 offset:9728\n\t"
    "ds_read_b64_tr_b16 a[30:31], v11 offset:11776\n\t"
    "v_mfma_f32_16x16x32_bf16 v[24:27], a[4:7], a[72:75], v[24:27]\n\t"
    "buffer_load_dwordx4 a[108:111], v1, s[52:55], s72 offen offset:2304 sc1\n\t"
    "v_mfma_f32_16x16x32_bf16 v[28:31], a[4:7], a[76:79], v[28:31]\n\t"
    "s_waitcnt lgkmcnt(8)\n\t"
    "s_add_u32 s72, s72, 4096\n\t"
    "v_mfma_f32_16x16x32_bf16 v[32:35], a[8:11], a[72:75], v[32:35]\n\t"
    "ds_read_b64_tr_b16 a[0:1], v10 offset:16384\n\t"
    "ds_read_b64_tr_b16 a[2:3], v10 offset:18432\n\t"
    "v_mfma_f32_16x16x32_bf16 v[36:39], a[8:11], a[76:79], v[36:39]\n\t"
    "ds_read_b64_tr_b16 a[4:5], v11 offset:16384\n\t"
    "ds_read_b64_tr_b16 a[6:7], v11 offset:18432\n\t"
    "v_mfma_f32_16x16x32_bf16 v[40:43], a[12:15], a[72:75], v[40:43]\n\t"
    "buffer_load_dwordx4 v3, s[56:59], s73 offen offset:0 lds\n\t"
    "v_mfma_f32_16x16x32_bf16 v[44:47], a[12:15], a[76:79], v[44:47]\n\t"
    "s_waitcnt lgkmcnt(8)\n\t"
    "s_and_b32 s72, s72, 131071\n\t"
    "v_mfma_f32_16x16x32_bf16 v[48:51], a[16:19], a[72:75], v[48:51]\n\t"
    "ds_read_b64_tr_b16 a[8:9], v10 offset:16896\n\t"
    "ds_read_b64_tr_b16 a[10:11], v10 offset:18944\n\t"
    "v_mfma_f32_16x16x32_bf16 v[52:55], a[16:19], a[76:79], v[52:55]\n\t"
    "ds_read_b64_tr_b16 a[12:13], v11 offset:16896\n\t"
    "ds_read_b64_tr_b16 a[14:15], v11 offset:18944\n\t"
    "v_mfma_f32_16x16x32_bf16 v[56:59], a[20:23], a[72:75], v[56:59]\n\t"
    "buffer_load_dwordx4 v4, s[56:59], s73 offen offset:1024 lds\n\t"
    "v_mfma_f32_16x16x32_bf16 v[60:63], a[20:23], a[76:79], v[60:63]\n\t"
    "s_waitcnt lgkmcnt(8)\n\t"
    "v_mfma_f32_16x16x32_bf16 v[64:67], a[24:27], a[72:75], v[64:67]\n\t"
    "ds_read_b64_tr_b16 a[16:17], v10 offset:17408\n\t"
    "ds_read_b64_tr_b16 a[18:19], v10 offset:19456\n\t"
    "v_mfma_f32_16x16x32_bf16 v[68:71], a[24:27], a[76:79], v[68:71]\n\t"
    "ds_read_b64_tr_b16 a[20:21], v11 offset:17408\n\t"
    "ds_read_b64_tr_b16 a[22:23], v11 offset:19456\n\t"
    "v_mfma_f32_16x16x32_bf16 v[72:75], a[28:31], a[72:75], v[72:75]\n\t"
    "buffer_load_dwordx4 v5, s[56:59], s73 offen offset:2048 lds\n\t"
    "v_mfma_f32_16x16x32_bf16 v[76:79], a[28:31], a[76:79], v[76:79]\n\t"
    "s_waitcnt vmcnt(13) lgkmcnt(8)\n\t"
    "s_mov_b32 s79, 6\n\t"
    ".p2align 6\n\t"
    "Lm_oproj_0_0:\n\t"
    "v_cmp_ne_u32 vcc, %4, v249\n\t"
    "s_cbranch_vccz 2f\n\t"
    "s_mov_b32 s86, 0x400000\n\t"
    "1:\n\t"
    "global_load_dword v249, v248, s[42:43] sc1\n\t"
    "s_waitcnt vmcnt(0)\n\t"
    "v_cmp_ne_u32 vcc, %4, v249\n\t"
    "s_sub_u32 s86, s86, 1\n\t"
    "s_cmp_eq_u32 s86, 0\n\t"
    "s_cbranch_scc1 2f\n\t"
    "s_cbranch_vccz 2f\n\t"
    "s_sleep 1\n\t"
    "s_branch 1b\n\t"
    "2:\n\t"
    "v_mfma_f32_16x16x32_bf16 v[16:19], a[0:3], a[80:83], v[16:19]\n\t"
    "ds_read_b64_tr_b16 a[24:25], v10 offset:17920\n\t"
    "ds_read_b64_tr_b16 a[26:27], v10 offset:19968\n\t"
    "v_mfma_f32_16x16x32_bf16 v[20:23], a[0:3], a[84:87], v[20:23]\n\t"
    "ds_read_b64_tr_b16 a[28:29], v11 offset:17920\n\t"
    "ds_read_b64_tr_b16 a[30:31], v11 offset:19968\n\t"
    "v_mfma_f32_16x16x32_bf16 v[24:27], a[4:7], a[80:83], v[24:27]\n\t"
    "buffer_load_dwordx4 v6, s[56:59], s73 offen offset:3072 lds\n\t"
    "v_mfma_f32_16x16x32_bf16 v[28:31], a[4:7], a[84:87], v[28:31]\n\t"
    "s_waitcnt lgkmcnt(8)\n\t"
    "s_add_u32 s73, s73, 131072\n\t"
    "v_mfma_f32_16x16x32_bf16 v[32:35], a[8:11], a[80:83], v[32:35]\n\t"
    "ds_read_b64_tr_b16 a[0:1], v10 offset:24576\n\t"
    "ds_read_b64_tr_b16 a[2:3], v10 offset:26624\n\t"
    "v_mfma_f32_16x16x32_bf16 v[36:39], a[8:11], a[84:87], v[36:39]\n\t"
    "ds_read_b64_tr_b16 a[4:5], v11 offset:24576\n\t"
    "ds_read_b64_tr_b16 a[6:7], v11 offset:26624\n\t"
    "v_mfma_f32_16x16x32_bf16 v[40:43], a[12:15], a[80:83], v[40:43]\n\t"
    "buffer_load_dwordx4 a[112:115], v1, s[52:55], s72 offen offset:0 sc1\n\t"
    "v_mfma_f32_16x16x32_bf16 v[44:47], a[12:15], a[84:87], v[44:47]\n\t"
    "s_waitcnt lgkmcnt(8)\n\t"
    "s_and_b32 s73, s73, 4194303\n\t"
    "v_mfma_f32_16x16x32_bf16 v[48:51], a[16:19], a[80:83], v[48:51]\n\t"
    "ds_read_b64_tr_b16 a[8:9], v10 offset:25088\n\t"
    "ds_read_b64_tr_b16 a[10:11], v10 offset:27136\n\t"
    "v_mfma_f32_16x16x32_bf16 v[52:55], a[16:19], a[84:87], v[52:55]\n\t"
    "ds_read_b64_tr_b16 a[12:13], v11 offset:25088\n\t"
    "ds_read_b64_tr_b16 a[14:15], v11 offset:27136\n\t"
    "v_mfma_f32_16x16x32_bf16 v[56:59], a[20:23], a[80:83], v[56:59]\n\t"
    "buffer_load_dwordx4 a[116:119], v1, s[52:55], s72 offen offset:256 sc1\n\t"
    "v_mfma_f32_16x16x32_bf16 v[60:63], a[20:23], a[84:87], v[60:63]\n\t"
    "s_waitcnt lgkmcnt(8)\n\t"
    "v_mfma_f32_16x16x32_bf16 v[64:67], a[24:27], a[80:83], v[64:67]\n\t"
    "ds_read_b64_tr_b16 a[16:17], v10 offset:25600\n\t"
    "ds_read_b64_tr_b16 a[18:19], v10 offset:27648\n\t"
    "v_mfma_f32_16x16x32_bf16 v[68:71], a[24:27], a[84:87], v[68:71]\n\t"
    "ds_read_b64_tr_b16 a[20:21], v11 offset:25600\n\t"
    "ds_read_b64_tr_b16 a[22:23], v11 offset:27648\n\t"
    "v_mfma_f32_16x16x32_bf16 v[72:75], a[28:31], a[80:83], v[72:75]\n\t"
    "buffer_load_dwordx4 a[120:123], v1, s[52:55], s72 offen offset:2048 sc1\n\t"
    "v_mfma_f32_16x16x32_bf16 v[76:79], a[28:31], a[84:87], v[76:79]\n\t"
    "s_waitcnt vmcnt(15) lgkmcnt(8)\n\t"
    "s_add_u32 m0, s74, 0\n\t"
    "v_mfma_f32_16x16x32_bf16 v[16:19], a[0:3], a[88:91], v[16:19]\n\t"
    "s_waitcnt vmcnt(11)\n\t"
    "s_barrier\n\t"
    "ds_read_b64_tr_b16 a[24:25], v10 offset:26112\n\t"
    "ds_read_b64_tr_b16 a[26:27], v10 offset:28160\n\t"
    "v_mfma_f32_16x16x32_bf16 v[20:23], a[0:3], a[92:95], v[20:23]\n\t"
    "ds_read_b64_tr_b16 a[28:29], v11 offset:26112\n\t"
    "ds_read_b64_tr_b16 a[30:31], v11 offset:28160\n\t"
    "v_mfma_f32_16x16x32_bf16 v[24:27], a[4:7], a[88:91], v[24:27]\n\t"
    "buffer_load_dwordx4 a[124:127], v1, s[52:55], s72 offen offset:2304 sc1\n\t"
    "v_mfma_f32_16x16x32_bf16 v[28:31], a[4:7], a[92:95], v[28:31]\n\t"
    "s_waitcnt lgkmcnt(8)\n\t"
    "s_add_u32 s72, s72, 4096\n\t"
    "v_mfma_f32_16x16x32_bf16 v[32:35], a[8:11], a[88:91], v[32:35]\n\t"
    "ds_read_b64_tr_b16 a[0:1], v10 offset:32768\n\t"
    "ds_read_b64_tr_b16 a[2:3], v10 offset:34816\n\t"
    "v_mfma_f32_16x16x32_bf16 v[36:39], a[8:11], a[92:95], v[36:39]\n\t"
    "ds_read_b64_tr_b16 a[4:5], v11 offset:32768\n\t"
    "ds_read_b64_tr_b16 a[6:7], v11 offset:34816\n\t"
    "v_mfma_f32_16x16x32_bf16 v[40:43], a[12:15], a[88:91], v[40:43]\n\t"
    "buffer_load_dwordx4 v3, s[56:59], s73 offen offset:0 lds\n\t"
    "v_mfma_f32_16x16x32_bf16 v[44:47], a[12:15], a[92:95], v[44:47]\n\t"
    "s_waitcnt lgkmcnt(8)\n\t"
    "s_and_b32 s72, s72, 131071\n\t"
    "v_mfma_f32_16x16x32_bf16 v[48:51], a[16:19], a[88:91], v[48:51]\n\t"
    "ds_read_b64_tr_b16 a[8:9], v10 offset:33280\n\t"
    "ds_read_b64_tr_b16 a[10:11], v10 offset:35328\n\t"
    "v_mfma_f32_16x16x32_bf16 v[52:55], a[16:19], a[92:95], v[52:55]\n\t"
    "ds_read_b64_tr_b16 a[12:13], v11 offset:33280\n\t"
    "ds_read_b64_tr_b16 a[14:15], v11 offset:35328\n\t"
    "v_mfma_f32_16x16x32_bf16 v[56:59], a[20:23], a[88:91], v[56:59]\n\t"
    "buffer_load_dwordx4 v4, s[56:59], s73 offen offset:1024 lds\n\t"
    "v_mfma_f32_16x16x32_bf16 v[60:63], a[20:23], a[92:95], v[60:63]\n\t"
    "s_waitcnt lgkmcnt(8)\n\t"
    "v_mfma_f32_16x16x32_bf16 v[64:67], a[24:27], a[88:91], v[64:67]\n\t"
    "ds_read_b64_tr_b16 a[16:17], v10 offset:33792\n\t"
    "ds_read_b64_tr_b16 a[18:19], v10 offset:35840\n\t"
    "v_mfma_f32_16x16x32_bf16 v[68:71], a[24:27], a[92:95], v[68:71]\n\t"
    "ds_read_b64_tr_b16 a[20:21], v11 offset:33792\n\t"
    "ds_read_b64_tr_b16 a[22:23], v11 offset:35840\n\t"
    "v_mfma_f32_16x16x32_bf16 v[72:75], a[28:31], a[88:91], v[72:75]\n\t"
    "buffer_load_dwordx4 v5, s[56:59], s73 offen offset:2048 lds\n\t"
    "v_mfma_f32_16x16x32_bf16 v[76:79], a[28:31], a[92:95], v[76:79]\n\t"
    "s_waitcnt vmcnt(13) lgkmcnt(8)\n\t"
    "s_lshr_b32 s98, s51, 3\n\t"
    "s_add_u32 s98, s98, s50\n\t"
    "s_and_b32 s98, s98, 3\n\t"
    "s_lshl_b32 s98, s98, 8\n\t"
    "v_add_u32 v248, s98, v250\n\t"
    "global_load_dword v249, v248, s[42:43] sc1\n\t"
    "s_add_u32 s51, s51, 4\n\t"
    "v_mfma_f32_16x16x32_bf16 v[16:19], a[0:3], a[96:99], v[16:19]\n\t"
    "ds_read_b64_tr_b16 a[24:25], v10 offset:34304\n\t"
    "ds_read_b64_tr_b16 a[26:27], v10 offset:36352\n\t"
    "v_mfma_f32_16x16x32_bf16 v[20:23], a[0:3], a[100:103], v[20:23]\n\t"
    "ds_read_b64_tr_b16 a[28:29], v11 offset:34304\n\t"
    "ds_read_b64_tr_b16 a[30:31], v11 offset:36352\n\t"
    "v_mfma_f32_16x16x32_bf16 v[24:27], a[4:7], a[96:99], v[24:27]\n\t"
    "buffer_load_dwordx4 v6, s[56:59], s73 offen offset:3072 lds\n\t"
    "v_mfma_f32_16x16x32_bf16 v[28:31], a[4:7], a[100:103], v[28:31]\n\t"
    "s_waitcnt lgkmcnt(8)\n\t"
    "s_add_u32 s73, s73, 131072\n\t"
    "v_mfma_f32_16x16x32_bf16 v[32:35], a[8:11], a[96:99], v[32:35]\n\t"
    "ds_read_b64_tr_b16 a[0:1], v10 offset:40960\n\t"
    "ds_read_b64_tr_b16 a[2:3], v10 offset:43008\n\t"
    "v_mfma_f32_16x16x32_bf16 v[36:39], a[8:11], a[100:103], v[36:39]\n\t"
    "ds_read_b64_tr_b16 a[4:5], v11 offset:40960\n\t"
    "ds_read_b64_tr_b16 a[6:7], v11 offset:43008\n\t"
    "v_mfma_f32_16x16x32_bf16 v[40:43], a[12:15], a[96:99], v[40:43]\n\t"
    "buffer_load_dwordx4 a[64:67], v1, s[52:55], s72 offen offset:0 sc1\n\t"
    "v_mfma_f32_16x16x32_bf16 v[44:47], a[12:15], a[100:103], v[44:47]\n\t"
    "s_waitcnt lgkmcnt(8)\n\t"
    "s_and_b32 s73, s73, 4194303\n\t"
    "v_mfma_f32_16x16x32_bf16 v[48:51], a[16:19], a[96:99], v[48:51]\n\t"
    "ds_read_b64_tr_b16 a[8:9], v10 offset:41472\n\t"
    "ds_read_b64_tr_b16 a[10:11], v10 offset:43520\n\t"
    "v_mfma_f32_16x16x32_bf16 v[52:55], a[16:19], a[100:103], v[52:55]\n\t"
    "ds_read_b64_tr_b16 a[12:13], v11 offset:41472\n\t"
    "ds_read_b64_tr_b16 a[14:15], v11 offset:43520\n\t"
    "v_mfma_f32_16x16x32_bf16 v[56:59], a[20:23], a[96:99], v[56:59]\n\t"
    "buffer_load_dwordx4 a[68:71], v1, s[52:55], s72 offen offset:256 sc1\n\t"
    "v_mfma_f32_16x16x32_bf16 v[60:63], a[20:23], a[100:103], v[60:63]\n\t"
    "s_waitcnt lgkmcnt(8)\n\t"
    "v_mfma_f32_16x16x32_bf16 v[64:67], a[24:27], a[96:99], v[64:67]\n\t"
    "ds_read_b64_tr_b16 a[16:17], v10 offset:41984\n\t"
    "ds_read_b64_tr_b16 a[18:19], v10 offset:44032\n\t"
    "v_mfma_f32_16x16x32_bf16 v[68:71], a[24:27], a[100:103], v[68:71]\n\t"
    "ds_read_b64_tr_b16 a[20:21], v11 offset:41984\n\t"
    "ds_read_b64_tr_b16 a[22:23], v11 offset:44032\n\t"
    "v_mfma_f32_16x16x32_bf16 v[72:75], a[28:31], a[96:99], v[72:75]\n\t"
    "buffer_load_dwordx4 a[72:75], v1, s[52:55], s72 offen offset:2048 sc1\n\t"
    "v_mfma_f32_16x16x32_bf16 v[76:79], a[28:31], a[100:103], v[76:79]\n\t"
    "s_waitcnt vmcnt(16) lgkmcnt(8)\n\t"
    "s_add_u32 m0, s74, 16384\n\t"
    "v_mfma_f32_16x16x32_bf16 v[16:19], a[0:3], a[104:107], v[16:19]\n\t"
    "s_waitcnt vmcnt(12)\n\t"
    "s_barrier\n\t"
    "ds_read_b64_tr_b16 a[24:25], v10 offset:42496\n\t"
    "ds_read_b64_tr_b16 a[26:27], v10 offset:44544\n\t"
    "v_mfma_f32_16x16x32_bf16 v[20:23], a[0:3], a[108:111], v[20:23]\n\t"
    "ds_read_b64_tr_b16 a[28:29], v11 offset:42496\n\t"
    "ds_read_b64_tr_b16 a[30:31], v11 offset:44544\n\t"
    "v_mfma_f32_16x16x32_bf16 v[24:27], a[4:7], a[104:107], v[24:27]\n\t"
    "buffer_load_dwordx4 a[76:79], v1, s[52:55], s72 offen offset:2304 sc1\n\t"
    "v_mfma_f32_16x16x32_bf16 v[28:31], a[4:7], a[108:111], v[28:31]\n\t"
    "s_waitcnt lgkmcnt(8)\n\t"
    "s_add_u32 s72, s72, 4096\n\t"
    "v_mfma_f32_16x16x32_bf16 v[32:35], a[8:11], a[104:107], v[32:35]\n\t"
    "ds_read_b64_tr_b16 a[0:1], v10 offset:49152\n\t"
    "ds_read_b64_tr_b16 a[2:3], v10 offset:51200\n\t"
    "v_mfma_f32_16x16x32_bf16 v[36:39], a[8:11], a[108:111], v[36:39]\n\t"
    "ds_read_b64_tr_b16 a[4:5], v11 offset:49152\n\t"
    "ds_read_b64_tr_b16 a[6:7], v11 offset:51200\n\t"
    "v_mfma_f32_16x16x32_bf16 v[40:43], a[12:15], a[104:107], v[40:43]\n\t"
    "buffer_load_dwordx4 v3, s[56:59], s73 offen offset:0 lds\n\t"
    "v_mfma_f32_16x16x32_bf16 v[44:47], a[12:15], a[108:111], v[44:47]\n\t"
    "s_waitcnt lgkmcnt(8)\n\t"
    "s_and_b32 s72, s72, 131071\n\t"
    "v_mfma_f32_16x16x32_bf16 v[48:51], a[16:19], a[104:107], v[48:51]\n\t"
    "ds_read_b64_tr_b16 a[8:9], v10 offset:49664\n\t"
    "ds_read_b64_tr_b16 a[10:11], v10 offset:51712\n\t"
    "v_mfma_f32_16x16x32_bf16 v[52:55], a[16:19], a[108:111], v[52:55]\n\t"
    "ds_read_b64_tr_b16 a[12:13], v11 offset:49664\n\t"
    "ds_read_b64_tr_b16 a[14:15], v11 offset:51712\n\t"
    "v_mfma_f32_16x16x32_bf16 v[56:59], a[20:23], a[104:107], v[56:59]\n\t"
    "buffer_load_dwordx4 v4, s[56:59], s73 offen offset:1024 lds\n\t"
    "v_mfma_f32_16x16x32_bf16 v[60:63], a[20:23], a[108:111], v[60:63]\n\t"
    "s_waitcnt lgkmcnt(8)\n\t"
    "v_mfma_f32_16x16x32_bf16 v[64:67], a[24:27], a[104:107], v[64:67]\n\t"
    "ds_read_b64_tr_b16 a[16:17], v10 offset:50176\n\t"
    "ds_read_b64_tr_b16 a[18:19], v10 offset:52224\n\t"
    "v_mfma_f32_16x16x32_bf16 v[68:71], a[24:27], a[108:111], v[68:71]\n\t"
    "ds_read_b64_tr_b16 a[20:21], v11 offset:50176\n\t"
    "ds_read_b64_tr_b16 a[22:23], v11 offset:52224\n\t"
    "v_mfma_f32_16x16x32_bf16 v[72:75], a[28:31], a[104:107], v[72:75]\n\t"
    "buffer_load_dwordx4 v5, s[56:59], s73 offen offset:2048 lds\n\t"
    "v_mfma_f32_16x16x32_bf16 v[76:79], a[28:31], a[108:111], v[76:79]\n\t"
    "s_waitcnt vmcnt(14) lgkmcnt(8)\n\t"
    "v_mfma_f32_16x16x32_bf16 v[16:19], a[0:3], a[112:115], v[16:19]\n\t"
    "ds_read_b64_tr_b16 a[24:25], v10 offset:50688\n\t"
    "ds_read_b64_tr_b16 a[26:27], v10 offset:52736\n\t"
    "v_mfma_f32_16x16x32_bf16 v[20:23], a[0:3], a[116:119], v[20:23]\n\t"
    "ds_read_b64_tr_b16 a[28:29], v11 offset:50688\n\t"
    "ds_read_b64_tr_b16 a[30:31], v11 offset:52736\n\t"
    "v_mfma_f32_16x16x32_bf16 v[24:27], a[4:7], a[112:115], v[24:27]\n\t"
    "buffer_load_dwordx4 v6, s[56:59], s73 offen offset:3072 lds\n\t"
    "v_mfma_f32_16x16x32_bf16 v[28:31], a[4:7], a[116:119], v[28:31]\n\t"
    "s_waitcnt lgkmcnt(8)\n\t"
    "s_add_u32 s73, s73, 131072\n\t"
    "v_mfma_f32_16x16x32_bf16 v[32:35], a[8:11], a[112:115], v[32:35]\n\t"
    "ds_read_b64_tr_b16 a[0:1], v10 offset:57344\n\t"
    "ds_read_b64_tr_b16 a[2:3], v10 offset:59392\n\t"
    "v_mfma_f32_16x16x32_bf16 v[36:39], a[8:11], a[116:119], v[36:39]\n\t"
    "ds_read_b64_tr_b16 a[4:5], v11 offset:57344\n\t"
    "ds_read_b64_tr_b16 a[6:7], v11 offset:59392\n\t"
    "v_mfma_f32_16x16x32_bf16 v[40:43], a[12:15], a[112:115], v[40:43]\n\t"
    "buffer_load_dwordx4 a[80:83], v1, s[52:55], s72 offen offset:0 sc1\n\t"
    "v_mfma_f32_16x16x32_bf16 v[44:47], a[12:15], a[116:119], v[44:47]\n\t"
    "s_waitcnt lgkmcnt(8)\n\t"
    "s_and_b32 s73, s73, 4194303\n\t"
    "v_mfma_f32_16x16x32_bf16 v[48:51], a[16:19], a[112:115], v[48:51]\n\t"
    "ds_read_b64_tr_b16 a[8:9], v10 offset:57856\n\t"
    "ds_read_b64_tr_b16 a[10:11], v10 offset:59904\n\t"
    "v_mfma_f32_16x16x32_bf16 v[52:55], a[16:19], a[116:119], v[52:55]\n\t"
    "ds_read_b64_tr_b16 a[12:13], v11 offset:57856\n\t"
    "ds_read_b64_tr_b16 a[14:15], v11 offset:59904\n\t"
    "v_mfma_f32_16x16x32_bf16 v[56:59], a[20:23], a[112:115], v[56:59]\n\t"
    "buffer_load_dwordx4 a[84:87], v1, s[52:55], s72 offen offset:256 sc1\n\t"
    "v_mfma_f32_16x16x32_bf16 v[60:63], a[20:23], a[116:119], v[60:63]\n\t"
    "s_waitcnt lgkmcnt(8)\n\t"
    "v_mfma_f32_16x16x32_bf16 v[64:67], a[24:27], a[112:115], v[64:67]\n\t"
    "ds_read_b64_tr_b16 a[16:17], v10 offset:58368\n\t"
    "ds_read_b64_tr_b16 a[18:19], v10 offset:60416\n\t"
    "v_mfma_f32_16x16x32_bf16 v[68:71], a[24:27], a[116:119], v[68:71]\n\t"
    "ds_read_b64_tr_b16 a[20:21], v11 offset:58368\n\t"
    "ds_read_b64_tr_b16 a[22:23], v11 offset:60416\n\t"
    "v_mfma_f32_16x16x32_bf16 v[72:75], a[28:31], a[112:115], v[72:75]\n\t"
    "buffer_load_dwordx4 a[88:91], v1, s[52:55], s72 offen offset:2048 sc1\n\t"
    "v_mfma_f32_16x16x32_bf16 v[76:79], a[28:31], a[116:119], v[76:79]\n\t"
    "s_waitcnt vmcnt(16) lgkmcnt(8)\n\t"
    "s_add_u32 m0, s74, 32768\n\t"
    "v_mfma_f32_16x16x32_bf16 v[16:19], a[0:3], a[120:123], v[16:19]\n\t"
    "s_waitcnt vmcnt(11)\n\t"
    "s_barrier\n\t"
    "ds_read_b64_tr_b16 a[24:25], v10 offset:58880\n\t"
    "ds_read_b64_tr_b16 a[26:27], v10 offset:60928\n\t"
    "v_mfma_f32_16x16x32_bf16 v[20:23], a[0:3], a[124:127], v[20:23]\n\t"
    "ds_read_b64_tr_b16 a[28:29], v11 offset:58880\n\t"
    "ds_read_b64_tr_b16 a[30:31], v11 offset:60928\n\t"
    "v_mfma_f32_16x16x32_bf16 v[24:27], a[4:7], a[120:123], v[24:27]\n\t"
    "buffer_load_dwordx4 a[92:95], v1, s[52:55], s72 offen offset:2304 sc1\n\t"
    "v_mfma_f32_16x16x32_bf16 v[28:31], a[4:7], a[124:127], v[28:31]\n\t"
    "s_waitcnt lgkmcnt(8)\n\t"
    "s_add_u32 s72, s72, 4096\n\t"
    "v_mfma_f32_16x16x32_bf16 v[32:35], a[8:11], a[120:123], v[32:35]\n\t"
    "ds_read_b64_tr_b16 a[0:1], v10 offset:0\n\t"
    "ds_read_b64_tr_b16 a[2:3], v10 offset:2048\n\t"
    "v_mfma_f32_16x16x32_bf16 v[36:39], a[8:11], a[124:127], v[36:39]\n\t"
    "ds_read_b64_tr_b16 a[4:5], v11 offset:0\n\t"
    "ds_read_b64_tr_b16 a[6:7], v11 offset:2048\n\t"
    "v_mfma_f32_16x16x32_bf16 v[40:43], a[12:15], a[120:123], v[40:43]\n\t"
    "buffer_load_dwordx4 v3, s[56:59], s73 offen offset:0 lds\n\t"
    "v_mfma_f32_16x16x32_bf16 v[44:47], a[12:15], a[124:127], v[44:47]\n\t"
    "s_waitcnt lgkmcnt(8)\n\t"
    "s_and_b32 s72, s72, 131071\n\t"
    "v_mfma_f32_16x16x32_bf16 v[48:51], a[16:19], a[120:123], v[48:51]\n\t"
    "ds_read_b64_tr_b16 a[8:9], v10 offset:512\n\t"
    "ds_read_b64_tr_b16 a[10:11], v10 offset:2560\n\t"
    "v_mfma_f32_16x16x32_bf16 v[52:55], a[16:19], a[124:127], v[52:55]\n\t"
    "ds_read_b64_tr_b16 a[12:13], v11 offset:512\n\t"
    "ds_read_b64_tr_b16 a[14:15], v11 offset:2560\n\t"
    "v_mfma_f32_16x16x32_bf16 v[56:59], a[20:23], a[120:123], v[56:59]\n\t"
    "buffer_load_dwordx4 v4, s[56:59], s73 offen offset:1024 lds\n\t"
    "v_mfma_f32_16x16x32_bf16 v[60:63], a[20:23], a[124:127], v[60:63]\n\t"
    "s_waitcnt lgkmcnt(8)\n\t"
    "v_mfma_f32_16x16x32_bf16 v[64:67], a[24:27], a[120:123], v[64:67]\n\t"
    "ds_read_b64_tr_b16 a[16:17], v10 offset:1024\n\t"
    "ds_read_b64_tr_b16 a[18:19], v10 offset:3072\n\t"
    "v_mfma_f32_16x16x32_bf16 v[68:71], a[24:27], a[124:127], v[68:71]\n\t"
    "ds_read_b64_tr_b16 a[20:21], v11 offset:1024\n\t"
    "ds_read_b64_tr_b16 a[22:23], v11 offset:3072\n\t"
    "v_mfma_f32_16x16x32_bf16 v[72:75], a[28:31], a[120:123], v[72:75]\n\t"
    "buffer_load_dwordx4 v5, s[56:59], s73 offen offset:2048 lds\n\t"
    "v_mfma_f32_16x16x32_bf16 v[76:79], a[28:31], a[124:127], v[76:79]\n\t"
    "s_waitcnt vmcnt(13) lgkmcnt(8)\n\t"
    "v_mfma_f32_16x16x32_bf16 v[16:19], a[0:3], a[64:67], v[16:19]\n\t"
    "ds_read_b64_tr_b16 a[24:25], v10 offset:1536\n\t"
    "ds_read_b64_tr_b16 a[26:27], v10 offset:3584\n\t"
    "v_mfma_f32_16x16x32_bf16 v[20:23], a[0:3], a[68:71], v[20:23]\n\t"
    "ds_read_b64_tr_b16 a[28:29], v11 offset:1536\n\t"
    "ds_read_b64_tr_b16 a[30:31], v11 offset:3584\n\t"
    "v_mfma_f32_16x16x32_bf16 v[24:27], a[4:7], a[64:67], v[24:27]\n\t"
    "buffer_load_dwordx4 v6, s[56:59], s73 offen offset:3072 lds\n\t"
    "v_mfma_f32_16x16x32_bf16 v[28:31], a[4:7], a[68:71], v[28:31]\n\t"
    "s_waitcnt lgkmcnt(8)\n\t"
    "s_add_u32 s73, s73, 131072\n\t"
    "v_mfma_f32_16x16x32_bf16 v[32:35], a[8:11], a[64:67], v[32:35]\n\t"
    "ds_read_b64_tr_b16 a[0:1], v10 offset:8192\n\t"
    "ds_read_b64_tr_b16 a[2:3], v10 offset:10240\n\t"
    "v_mfma_f32_16x16x32_bf16 v[36:39], a[8:11], a[68:71], v[36:39]\n\t"
    "ds_read_b64_tr_b16 a[4:5], v11 offset:8192\n\t"
    "ds_read_b64_tr_b16 a[6:7], v11 offset:10240\n\t"
    "v_mfma_f32_16x16x32_bf16 v[40:43], a[12:15], a[64:67], v[40:43]\n\t"
    "buffer_load_dwordx4 a[96:99], v1, s[52:55], s72 offen offset:0 sc1\n\t"
    "v_mfma_f32_16x16x32_bf16 v[44:47], a[12:15], a[68:71], v[44:47]\n\t"
    "s_waitcnt lgkmcnt(8)\n\t"
    "s_and_b32 s73, s73, 4194303\n\t"
    "v_mfma_f32_16x16x32_bf16 v[48:51], a[16:19], a[64:67], v[48:51]\n\t"
    "ds_read_b64_tr_b16 a[8:9], v10 offset:8704\n\t"
    "ds_read_b64_tr_b16 a[10:11], v10 offset:10752\n\t"
    "v_mfma_f32_16x16x32_bf16 v[52:55], a[16:19], a[68:71], v[52:55]\n\t"
    "ds_read_b64_tr_b16 a[12:13], v11 offset:8704\n\t"
    "ds_read_b64_tr_b16 a[14:15], v11 offset:10752\n\t"
    "v_mfma_f32_16x16x32_bf16 v[56:59], a[20:23], a[64:67], v[56:59]\n\t"
    "buffer_load_dwordx4 a[100:103], v1, s[52:55], s72 offen offset:256 sc1\n\t"
    "v_mfma_f32_16x16x32_bf16 v[60:63], a[20:23], a[68:71], v[60:63]\n\t"
    "s_waitcnt lgkmcnt(8)\n\t"
    "v_mfma_f32_16x16x32_bf16 v[64:67], a[24:27], a[64:67], v[64:67]\n\t"
    "ds_read_b64_tr_b16 a[16:17], v10 offset:9216\n\t"
    "ds_read_b64_tr_b16 a[18:19], v10 offset:11264\n\t"
    "v_mfma_f32_16x16x32_bf16 v[68:71], a[24:27], a[68:71], v[68:71]\n\t"
    "ds_read_b64_tr_b16 a[20:21], v11 offset:9216\n\t"
    "ds_read_b64_tr_b16 a[22:23], v11 offset:11264\n\t"
    "v_mfma_f32_16x16x32_bf16 v[72:75], a[28:31], a[64:67], v[72:75]\n\t"
    "buffer_load_dwordx4 a[104:107], v1, s[52:55], s72 offen offset:2048 sc1\n\t"
    "v_mfma_f32_16x16x32_bf16 v[76:79], a[28:31], a[68:71], v[76:79]\n\t"
    "s_waitcnt vmcnt(15) lgkmcnt(8)\n\t"
    "s_add_u32 m0, s74, 49152\n\t"
    "v_mfma_f32_16x16x32_bf16 v[16:19], a[0:3], a[72:75], v[16:19]\n\t"
    "s_waitcnt vmcnt(11)\n\t"
    "s_barrier\n\t"
    "ds_read_b64_tr_b16 a[24:25], v10 offset:9728\n\t"
    "ds_read_b64_tr_b16 a[26:27], v10 offset:11776\n\t"
    "v_mfma_f32_16x16x32_bf16 v[20:23], a[0:3], a[76:79], v[20:23]\n\t"
    "ds_read_b64_tr_b16 a[28:29], v11 offset:9728\n\t"
    "ds_read_b64_tr_b16 a[30:31], v11 offset:11776\n\t"
    "v_mfma_f32_16x16x32_bf16 v[24:27], a[4:7], a[72:75], v[24:27]\n\t"
    "buffer_load_dwordx4 a[108:111], v1, s[52:55], s72 offen offset:2304 sc1\n\t"
    "v_mfma_f32_16x16x32_bf16 v[28:31], a[4:7], a[76:79], v[28:31]\n\t"
    "s_waitcnt lgkmcnt(8)\n\t"
    "s_add_u32 s72, s72, 4096\n\t"
    "v_mfma_f32_16x16x32_bf16 v[32:35], a[8:11], a[72:75], v[32:35]\n\t"
    "ds_read_b64_tr_b16 a[0:1], v10 offset:16384\n\t"
    "ds_read_b64_tr_b16 a[2:3], v10 offset:18432\n\t"
    "v_mfma_f32_16x16x32_bf16 v[36:39], a[8:11], a[76:79], v[36:39]\n\t"
    "ds_read_b64_tr_b16 a[4:5], v11 offset:16384\n\t"
    "ds_read_b64_tr_b16 a[6:7], v11 offset:18432\n\t"
    "v_mfma_f32_16x16x32_bf16 v[40:43], a[12:15], a[72:75], v[40:43]\n\t"
    "buffer_load_dwordx4 v3, s[56:59], s73 offen offset:0 lds\n\t"
    "v_mfma_f32_16x16x32_bf16 v[44:47], a[12:15], a[76:79], v[44:47]\n\t"
    "s_waitcnt lgkmcnt(8)\n\t"
    "s_and_b32 s72, s72, 131071\n\t"
    "v_mfma_f32_16x16x32_bf16 v[48:51], a[16:19], a[72:75], v[48:51]\n\t"
    "ds_read_b64_tr_b16 a[8:9], v10 offset:16896\n\t"
    "ds_read_b64_tr_b16 a[10:11], v10 offset:18944\n\t"
    "v_mfma_f32_16x16x32_bf16 v[52:55], a[16:19], a[76:79], v[52:55]\n\t"
    "ds_read_b64_tr_b16 a[12:13], v11 offset:16896\n\t"
    "ds_read_b64_tr_b16 a[14:15], v11 offset:18944\n\t"
    "v_mfma_f32_16x16x32_bf16 v[56:59], a[20:23], a[72:75], v[56:59]\n\t"
    "buffer_load_dwordx4 v4, s[56:59], s73 offen offset:1024 lds\n\t"
    "v_mfma_f32_16x16x32_bf16 v[60:63], a[20:23], a[76:79], v[60:63]\n\t"
    "s_waitcnt lgkmcnt(8)\n\t"
    "v_mfma_f32_16x16x32_bf16 v[64:67], a[24:27], a[72:75], v[64:67]\n\t"
    "ds_read_b64_tr_b16 a[16:17], v10 offset:17408\n\t"
    "ds_read_b64_tr_b16 a[18:19], v10 offset:19456\n\t"
    "v_mfma_f32_16x16x32_bf16 v[68:71], a[24:27], a[76:79], v[68:71]\n\t"
    "ds_read_b64_tr_b16 a[20:21], v11 offset:17408\n\t"
    "ds_read_b64_tr_b16 a[22:23], v11 offset:19456\n\t"
    "v_mfma_f32_16x16x32_bf16 v[72:75], a[28:31], a[72:75], v[72:75]\n\t"
    "buffer_load_dwordx4 v5, s[56:59], s73 offen offset:2048 lds\n\t"
    "v_mfma_f32_16x16x32_bf16 v[76:79], a[28:31], a[76:79], v[76:79]\n\t"
    "s_waitcnt vmcnt(13) lgkmcnt(8)\n\t"
    "s_sub_u32 s79, s79, 1\n\t"
    "s_cmp_lg_u32 s79, 0\n\t"
    "s_cbranch_scc1 Lm_oproj_0_0\n\t"
    "v_mfma_f32_16x16x32_bf16 v[16:19], a[0:3], a[80:83], v[16:19]\n\t"
    "ds_read_b64_tr_b16 a[24:25], v10 offset:17920\n\t"
    "ds_read_b64_tr_b16 a[26:27], v10 offset:19968\n\t"
    "v_mfma_f32_16x16x32_bf16 v[20:23], a[0:3], a[84:87], v[20:23]\n\t"
    "ds_read_b64_tr_b16 a[28:29], v11 offset:17920\n\t"
    "ds_read_b64_tr_b16 a[30:31], v11 offset:19968\n\t"
    "v_mfma_f32_16x16x32_bf16 v[24:27], a[4:7], a[80:83], v[24:27]\n\t"
    "buffer_load_dwordx4 v6, s[56:59], s73 offen offset:3072 lds\n\t"
    "v_mfma_f32_16x16x32_bf16 v[28:31], a[4:7], a[84:87], v[28:31]\n\t"
    "s_waitcnt lgkmcnt(8)\n\t"
    "s_add_u32 s73, s73, 131072\n\t"
    "v_mfma_f32_16x16x32_bf16 v[32:35], a[8:11], a[80:83], v[32:35]\n\t"
    "ds_read_b64_tr_b16 a[0:1], v10 offset:24576\n\t"
    "ds_read_b64_tr_b16 a[2:3], v10 offset:26624\n\t"
    "v_mfma_f32_16x16x32_bf16 v[36:39], a[8:11], a[84:87], v[36:39]\n\t"
    "ds_read_b64_tr_b16 a[4:5], v11 offset:24576\n\t"
    "ds_read_b64_tr_b16 a[6:7], v11 offset:26624\n\t"
    "v_mfma_f32_16x16x32_bf16 v[40:43], a[12:15], a[80:83], v[40:43]\n\t"
    "buffer_load_dwordx4 a[112:115], v1, s[52:55], s72 offen offset:0 sc1\n\t"
    "v_mfma_f32_16x16x32_bf16 v[44:47], a[12:15], a[84:87], v[44:47]\n\t"
    "s_waitcnt lgkmcnt(8)\n\t"
    "s_and_b32 s73, s73, 4194303\n\t"
    "v_mfma_f32_16x16x32_bf16 v[48:51], a[16:19], a[80:83], v[48:51]\n\t"
    "ds_read_b64_tr_b16 a[8:9], v10 offset:25088\n\t"
    "ds_read_b64_tr_b16 a[10:11], v10 offset:27136\n\t"
    "v_mfma_f32_16x16x32_bf16 v[52:55], a[16:19], a[84:87], v[52:55]\n\t"
    "ds_read_b64_tr_b16 a[12:13], v11 offset:25088\n\t"
    "ds_read_b64_tr_b16 a[14:15], v11 offset:27136\n\t"
    "v_mfma_f32_16x16x32_bf16 v[56:59], a[20:23], a[80:83], v[56:59]\n\t"
    "buffer_load_dwordx4 a[116:119], v1, s[52:55], s72 offen offset:256 sc1\n\t"
    "v_mfma_f32_16x16x32_bf16 v[60:63], a[20:23], a[84:87], v[60:63]\n\t"
    "s_waitcnt lgkmcnt(8)\n\t"
    "v_mfma_f32_16x16x32_bf16 v[64:67], a[24:27], a[80:83], v[64:67]\n\t"
    "ds_read_b64_tr_b16 a[16:17], v10 offset:25600\n\t"
    "ds_read_b64_tr_b16 a[18:19], v10 offset:27648\n\t"
    "v_mfma_f32_16x16x32_bf16 v[68:71], a[24:27], a[84:87], v[68:71]\n\t"
    "ds_read_b64_tr_b16 a[20:21], v11 offset:25600\n\t"
    "ds_read_b64_tr_b16 a[22:23], v11 offset:27648\n\t"
    "v_mfma_f32_16x16x32_bf16 v[72:75], a[28:31], a[80:83], v[72:75]\n\t"
    "buffer_load_dwordx4 a[120:123], v1, s[52:55], s72 offen offset:2048 sc1\n\t"
    "v_mfma_f32_16x16x32_bf16 v[76:79], a[28:31], a[84:87], v[76:79]\n\t"
    "s_waitcnt vmcnt(15) lgkmcnt(8)\n\t"
    "v_mfma_f32_16x16x32_bf16 v[16:19], a[0:3], a[88:91], v[16:19]\n\t"
    "s_waitcnt vmcnt(11)\n\t"
    "s_barrier\n\t"
    "ds_read_b64_tr_b16 a[24:25], v10 offset:26112\n\t"
    "ds_read_b64_tr_b16 a[26:27], v10 offset:28160\n\t"
    "v_mfma_f32_16x16x32_bf16 v[20:23], a[0:3], a[92:95], v[20:23]\n\t"
    "ds_read_b64_tr_b16 a[28:29], v11 offset:26112\n\t"
    "ds_read_b64_tr_b16 a[30:31], v11 offset:28160\n\t"
    "v_mfma_f32_16x16x32_bf16 v[24:27], a[4:7], a[88:91], v[24:27]\n\t"
    "buffer_load_dwordx4 a[124:127], v1, s[52:55], s72 offen offset:2304 sc1\n\t"
    "v_mfma_f32_16x16x32_bf16 v[28:31], a[4:7], a[92:95], v[28:31]\n\t"
    "s_waitcnt lgkmcnt(8)\n\t"
    "s_add_u32 s72, s72, 4096\n\t"
    "v_mfma_f32_16x16x32_bf16 v[32:35], a[8:11], a[88:91], v[32:35]\n\t"
    "ds_read_b64_tr_b16 a[0:1], v10 offset:32768\n\t"
    "ds_read_b64_tr_b16 a[2:3], v10 offset:34816\n\t"
    "v_mfma_f32_16x16x32_bf16 v[36:39], a[8:11], a[92:95], v[36:39]\n\t"
    "ds_read_b64_tr_b16 a[4:5], v11 offset:32768\n\t"
    "ds_read_b64_tr_b16 a[6:7], v11 offset:34816\n\t"
    "v_mfma_f32_16x16x32_bf16 v[40:43], a[12:15], a[88:91], v[40:43]\n\t"
    "v_mfma_f32_16x16x32_bf16 v[44:47], a[12:15], a[92:95], v[44:47]\n\t"
    "s_waitcnt lgkmcnt(8)\n\t"
    "s_and_b32 s72, s72, 131071\n\t"
    "v_mfma_f32_16x16x32_bf16 v[48:51], a[16:19], a[88:91], v[48:51]\n\t"
    "ds_read_b64_tr_b16 a[8:9], v10 offset:33280\n\t"
    "ds_read_b64_tr_b16 a[10:11], v10 offset:35328\n\t"
    "v_mfma_f32_16x16x32_bf16 v[52:55], a[16:19], a[92:95], v[52:55]\n\t"
    "ds_read_b64_tr_b16 a[12:13], v11 offset:33280\n\t"
    "ds_read_b64_tr_b16 a[14:15], v11 offset:35328\n\t"
    "v_mfma_f32_16x16x32_bf16 v[56:59], a[20:23], a[88:91], v[56:59]\n\t"
    "v_mfma_f32_16x16x32_bf16 v[60:63], a[20:23], a[92:95], v[60:63]\n\t"
    "s_waitcnt lgkmcnt(8)\n\t"
    "v_mfma_f32_16x16x32_bf16 v[64:67], a[24:27], a[88:91], v[64:67]\n\t"
    "ds_read_b64_tr_b16 a[16:17], v10 offset:33792\n\t"
    "ds_read_b64_tr_b16 a[18:19], v10 offset:35840\n\t"
    "v_mfma_f32_16x16x32_bf16 v[68:71], a[24:27], a[92:95], v[68:71]\n\t"
    "ds_read_b64_tr_b16 a[20:21], v11 offset:33792\n\t"
    "ds_read_b64_tr_b16 a[22:23], v11 offset:35840\n\t"
    "v_mfma_f32_16x16x32_bf16 v[72:75], a[28:31], a[88:91], v[72:75]\n\t"
    "v_mfma_f32_16x16x32_bf16 v[76:79], a[28:31], a[92:95], v[76:79]\n\t"
    "s_waitcnt vmcnt(10) lgkmcnt(8)\n\t"
    "v_mfma_f32_16x16x32_bf16 v[16:19], a[0:3], a[96:99], v[16:19]\n\t"
    "ds_read_b64_tr_b16 a[24:25], v10 offset:34304\n\t"
    "ds_read_b64_tr_b16 a[26:27], v10 offset:36352\n\t"
    "v_mfma_f32_16x16x32_bf16 v[20:23], a[0:3], a[100:103], v[20:23]\n\t"
    "ds_read_b64_tr_b16 a[28:29], v11 offset:34304\n\t"
    "ds_read_b64_tr_b16 a[30:31], v11 offset:36352\n\t"
    "v_mfma_f32_16x16x32_bf16 v[24:27], a[4:7], a[96:99], v[24:27]\n\t"
    "v_mfma_f32_16x16x32_bf16 v[28:31], a[4:7], a[100:103], v[28:31]\n\t"
    "s_waitcnt lgkmcnt(8)\n\t"
    "v_mfma_f32_16x16x32_bf16 v[32:35], a[8:11], a[96:99], v[32:35]\n\t"
    "ds_read_b64_tr_b16 a[0:1], v10 offset:40960\n\t"
    "ds_read_b64_tr_b16 a[2:3], v10 offset:43008\n\t"
    "v_mfma_f32_16x16x32_bf16 v[36:39], a[8:11], a[100:103], v[36:39]\n\t"
    "ds_read_b64_tr_b16 a[4:5], v11 offset:40960\n\t"
    "ds_read_b64_tr_b16 a[6:7], v11 offset:43008\n\t"
    "v_mfma_f32_16x16x32_bf16 v[40:43], a[12:15], a[96:99], v[40:43]\n\t"
    "v_mfma_f32_16x16x32_bf16 v[44:47], a[12:15], a[100:103], v[44:47]\n\t"
    "s_waitcnt lgkmcnt(8)\n\t"
    "v_mfma_f32_16x16x32_bf16 v[48:51], a[16:19], a[96:99], v[48:51]\n\t"
    "ds_read_b64_tr_b16 a[8:9], v10 offset:41472\n\t"
    "ds_read_b64_tr_b16 a[10:11], v10 offset:43520\n\t"
    "v_mfma_f32_16x16x32_bf16 v[52:55], a[16:19], a[100:103], v[52:55]\n\t"
    "ds_read_b64_tr_b16 a[12:13], v11 offset:41472\n\t"
    "ds_read_b64_tr_b16 a[14:15], v11 offset:43520\n\t"
    "v_mfma_f32_16x16x32_bf16 v[56:59], a[20:23], a[96:99], v[56:59]\n\t"
    "v_mfma_f32_16x16x32_bf16 v[60:63], a[20:23], a[100:103], v[60:63]\n\t"
    "s_waitcnt lgkmcnt(8)\n\t"
    "v_mfma_f32_16x16x32_bf16 v[64:67], a[24:27], a[96:99], v[64:67]\n\t"
    "ds_read_b64_tr_b16 a[16:17], v10 offset:41984\n\t"
    "ds_read_b64_tr_b16 a[18:19], v10 offset:44032\n\t"
    "v_mfma_f32_16x16x32_bf16 v[68:71], a[24:27], a[100:103], v[68:71]\n\t"
    "ds_read_b64_tr_b16 a[20:21], v11 offset:41984\n\t"
    "ds_read_b64_tr_b16 a[22:23], v11 offset:44032\n\t"
    "v_mfma_f32_16x16x32_bf16 v[72:75], a[28:31], a[96:99], v[72:75]\n\t"
    "v_mfma_f32_16x16x32_bf16 v[76:79], a[28:31], a[100:103], v[76:79]\n\t"
    "s_waitcnt vmcnt(8) lgkmcnt(8)\n\t"
    "v_mfma_f32_16x16x32_bf16 v[16:19], a[0:3], a[104:107], v[16:19]\n\t"
    "s_waitcnt vmcnt(4)\n\t"
    "s_barrier\n\t"
    "ds_read_b64_tr_b16 a[24:25], v10 offset:42496\n\t"
    "ds_read_b64_tr_b16 a[26:27], v10 offset:44544\n\t"
    "v_mfma_f32_16x16x32_bf16 v[20:23], a[0:3], a[108:111], v[20:23]\n\t"
    "ds_read_b64_tr_b16 a[28:29], v11 offset:42496\n\t"
    "ds_read_b64_tr_b16 a[30:31], v11 offset:44544\n\t"
    "v_mfma_f32_16x16x32_bf16 v[24:27], a[4:7], a[104:107], v[24:27]\n\t"
    "v_mfma_f32_16x16x32_bf16 v[28:31], a[4:7], a[108:111], v[28:31]\n\t"
    "s_waitcnt lgkmcnt(8)\n\t"
    "v_mfma_f32_16x16x32_bf16 v[32:35], a[8:11], a[104:107], v[32:35]\n\t"
    "ds_read_b64_tr_b16 a[0:1], v10 offset:49152\n\t"
    "ds_read_b64_tr_b16 a[2:3], v10 offset:51200\n\t"
    "v_mfma_f32_16x16x32_bf16 v[36:39], a[8:11], a[108:111], v[36:39]\n\t"
    "ds_read_b64_tr_b16 a[4:5], v11 offset:49152\n\t"
    "ds_read_b64_tr_b16 a[6:7], v11 offset:51200\n\t"
    "v_mfma_f32_16x16x32_bf16 v[40:43], a[12:15], a[104:107], v[40:43]\n\t"
    "v_mfma_f32_16x16x32_bf16 v[44:47], a[12:15], a[108:111], v[44:47]\n\t"
    "s_waitcnt lgkmcnt(8)\n\t"
    "v_mfma_f32_16x16x32_bf16 v[48:51], a[16:19], a[104:107], v[48:51]\n\t"
    "ds_read_b64_tr_b16 a[8:9], v10 offset:49664\n\t"
    "ds_read_b64_tr_b16 a[10:11], v10 offset:51712\n\t"
    "v_mfma_f32_16x16x32_bf16 v[52:55], a[16:19], a[108:111], v[52:55]\n\t"
    "ds_read_b64_tr_b16 a[12:13], v11 offset:49664\n\t"
    "ds_read_b64_tr_b16 a[14:15], v11 offset:51712\n\t"
    "v_mfma_f32_16x16x32_bf16 v[56:59], a[20:23], a[104:107], v[56:59]\n\t"
    "v_mfma_f32_16x16x32_bf16 v[60:63], a[20:23], a[108:111], v[60:63]\n\t"
    "s_waitcnt lgkmcnt(8)\n\t"
    "v_mfma_f32_16x16x32_bf16 v[64:67], a[24:27], a[104:107], v[64:67]\n\t"
    "ds_read_b64_tr_b16 a[16:17], v10 offset:50176\n\t"
    "ds_read_b64_tr_b16 a[18:19], v10 offset:52224\n\t"
    "v_mfma_f32_16x16x32_bf16 v[68:71], a[24:27], a[108:111], v[68:71]\n\t"
    "ds_read_b64_tr_b16 a[20:21], v11 offset:50176\n\t"
    "ds_read_b64_tr_b16 a[22:23], v11 offset:52224\n\t"
    "v_mfma_f32_16x16x32_bf16 v[72:75], a[28:31], a[104:107], v[72:75]\n\t"
    "v_mfma_f32_16x16x32_bf16 v[76:79], a[28:31], a[108:111], v[76:79]\n\t"
    "s_waitcnt vmcnt(2) lgkmcnt(8)\n\t"
    "v_mfma_f32_16x16x32_bf16 v[16:19], a[0:3], a[112:115], v[16:19]\n\t"
    "ds_read_b64_tr_b16 a[24:25], v10 offset:50688\n\t"
    "ds_read_b64_tr_b16 a[26:27], v10 offset:52736\n\t"
    "v_mfma_f32_16x16x32_bf16 v[20:23], a[0:3], a[116:119], v[20:23]\n\t"
    "ds_read_b64_tr_b16 a[28:29], v11 offset:50688\n\t"
    "ds_read_b64_tr_b16 a[30:31], v11 offset:52736\n\t"
    "v_mfma_f32_16x16x32_bf16 v[24:27], a[4:7], a[112:115], v[24:27]\n\t"
    "v_mfma_f32_16x16x32_bf16 v[28:31], a[4:7], a[116:119], v[28:31]\n\t"
    "s_waitcnt lgkmcnt(8)\n\t"
    "v_mfma_f32_16x16x32_bf16 v[32:35], a[8:11], a[112:115], v[32:35]\n\t"
    "ds_read_b64_tr_b16 a[0:1], v10 offset:57344\n\t"
    "ds_read_b64_tr_b16 a[2:3], v10 offset:59392\n\t"
    "v_mfma_f32_16x16x32_bf16 v[36:39], a[8:11], a[116:119], v[36:39]\n\t"
    "ds_read_b64_tr_b16 a[4:5], v11 offset:57344\n\t"
    "ds_read_b64_tr_b16 a[6:7], v11 offset:59392\n\t"
    "v_mfma_f32_16x16x32_bf16 v[40:43], a[12:15], a[112:115], v[40:43]\n\t"
    "v_mfma_f32_16x16x32_bf16 v[44:47], a[12:15], a[116:119], v[44:47]\n\t"
    "s_waitcnt lgkmcnt(8)\n\t"
    "v_mfma_f32_16x16x32_bf16 v[48:51], a[16:19], a[112:115], v[48:51]\n\t"
    "ds_read_b64_tr_b16 a[8:9], v10 offset:57856\n\t"
    "ds_read_b64_tr_b16 a[10:11], v10 offset:59904\n\t"
    "v_mfma_f32_16x16x32_bf16 v[52:55], a[16:19], a[116:119], v[52:55]\n\t"
    "ds_read_b64_tr_b16 a[12:13], v11 offset:57856\n\t"
    "ds_read_b64_tr_b16 a[14:15], v11 offset:59904\n\t"
    "v_mfma_f32_16x16x32_bf16 v[56:59], a[20:23], a[112:115], v[56:59]\n\t"
    "v_mfma_f32_16x16x32_bf16 v[60:63], a[20:23], a[116:119], v[60:63]\n\t"
    "s_waitcnt lgkmcnt(8)\n\t"
    "v_mfma_f32_16x16x32_bf16 v[64:67], a[24:27], a[112:115], v[64:67]\n\t"
    "ds_read_b64_tr_b16 a[16:17], v10 offset:58368\n\t"
    "ds_read_b64_tr_b16 a[18:19], v10 offset:60416\n\t"
    "v_mfma_f32_16x16x32_bf16 v[68:71], a[24:27], a[116:119], v[68:71]\n\t"
    "ds_read_b64_tr_b16 a[20:21], v11 offset:58368\n\t"
    "ds_read_b64_tr_b16 a[22:23], v11 offset:60416\n\t"
    "v_mfma_f32_16x16x32_bf16 v[72:75], a[28:31], a[112:115], v[72:75]\n\t"
    "v_mfma_f32_16x16x32_bf16 v[76:79], a[28:31], a[116:119], v[76:79]\n\t"
    "s_waitcnt vmcnt(0) lgkmcnt(8)\n\t"
    "v_mfma_f32_16x16x32_bf16 v[16:19], a[0:3], a[120:123], v[16:19]\n\t"
    "ds_read_b64_tr_b16 a[24:25], v10 offset:58880\n\t"
    "ds_read_b64_tr_b16 a[26:27], v10 offset:60928\n\t"
    "v_mfma_f32_16x16x32_bf16 v[20:23], a[0:3], a[124:127], v[20:23]\n\t"
    "ds_read_b64_tr_b16 a[28:29], v11 offset:58880\n\t"
    "ds_read_b64_tr_b16 a[30:31], v11 offset:60928\n\t"
    "v_mfma_f32_16x16x32_bf16 v[24:27], a[4:7], a[120:123], v[24:27]\n\t"
    "v_mfma_f32_16x16x32_bf16 v[28:31], a[4:7], a[124:127], v[28:31]\n\t"
    "s_waitcnt lgkmcnt(8)\n\t"
    "v_mfma_f32_16x16x32_bf16 v[32:35], a[8:11], a[120:123], v[32:35]\n\t"
    "v_mfma_f32_16x16x32_bf16 v[36:39], a[8:11], a[124:127], v[36:39]\n\t"
    "v_mfma_f32_16x16x32_bf16 v[40:43], a[12:15], a[120:123], v[40:43]\n\t"
    "v_mfma_f32_16x16x32_bf16 v[44:47], a[12:15], a[124:127], v[44:47]\n\t"
    "s_waitcnt lgkmcnt(4)\n\t"
    "v_mfma_f32_16x16x32_bf16 v[48:51], a[16:19], a[120:123], v[48:51]\n\t"
    "v_mfma_f32_16x16x32_bf16 v[52:55], a[16:19], a[124:127], v[52:55]\n\t"
    "v_mfma_f32_16x16x32_bf16 v[56:59], a[20:23], a[120:123], v[56:59]\n\t"
    "v_mfma_f32_16x16x32_bf16 v[60:63], a[20:23], a[124:127], v[60:63]\n\t"
    "s_waitcnt lgkmcnt(0)\n\t"
    "v_mfma_f32_16x16x32_bf16 v[64:67], a[24:27], a[120:123], v[64:67]\n\t"
    "v_mfma_f32_16x16x32_bf16 v[68:71], a[24:27], a[124:127], v[68:71]\n\t"
    "v_mfma_f32_16x16x32_bf16 v[72:75], a[28:31], a[120:123], v[72:75]\n\t"
    "v_mfma_f32_16x16x32_bf16 v[76:79], a[28:31], a[124:127], v[76:79]\n\t"
    "v_cvt_pk_bf16_f32 v176, v16, v17\n\t"
    "v_cvt_pk_bf16_f32 v177, v18, v19\n\t"
    "v_cvt_pk_bf16_f32 v178, v24, v25\n\t"
    "v_cvt_pk_bf16_f32 v179, v26, v27\n\t"
    "s_nop 0\n\t"
    "v_permlane16_swap_b32 v176, v178\n\t"
    "v_permlane16_swap_b32 v177, v179\n\t"
    "buffer_store_dwordx4 v[176:179], v240, s[60:63], s88 offen offset:0 sc1\n\t"
    "v_cvt_pk_bf16_f32 v180, v32, v33\n\t"
    "v_cvt_pk_bf16_f32 v181, v34, v35\n\t"
    "v_cvt_pk_bf16_f32 v182, v40, v41\n\t"
    "v_cvt_pk_bf16_f32 v183, v42, v43\n\t"
    "s_nop 0\n\t"
    "v_permlane16_swap_b32 v180, v182\n\t"
    "v_permlane16_swap_b32 v181, v183\n\t"
    "buffer_store_dwordx4 v[180:183], v240, s[60:63], s88 offen offset:2048 sc1\n\t"
    "v_cvt_pk_bf16_f32 v184, v48, v49\n\t"
    "v_cvt_pk_bf16_f32 v185, v50, v51\n\t"
    "v_cvt_pk_bf16_f32 v186, v56, v57\n\t"
    "v_cvt_pk_bf16_f32 v187, v58, v59\n\t"
    "s_nop 0\n\t"
    "v_permlane16_swap_b32 v184, v186\n\t"
    "v_permlane16_swap_b32 v185, v187\n\t"
    "buffer_store_dwordx4 v[184:187], v241, s[60:63], s88 offen offset:0 sc1\n\t"
    "v_cvt_pk_bf16_f32 v188, v64, v65\n\t"
    "v_cvt_pk_bf16_f32 v189, v66, v67\n\t"
    "v_cvt_pk_bf16_f32 v190, v72, v73\n\t"
    "v_cvt_pk_bf16_f32 v191, v74, v75\n\t"
    "s_nop 0\n\t"
    "v_permlane16_swap_b32 v188, v190\n\t"
    "v_permlane16_swap_b32 v189, v191\n\t"
    "buffer_store_dwordx4 v[188:191], v241, s[60:63], s88 offen offset:2048 sc1\n\t"
    "v_cvt_pk_bf16_f32 v192, v20, v21\n\t"
    "v_cvt_pk_bf16_f32 v193, v22, v23\n\t"
    "v_cvt_pk_bf16_f32 v194, v28, v29\n\t"
    "v_cvt_pk_bf16_f32 v195, v30, v31\n\t"
    "s_nop 0\n\t"
    "v_permlane16_swap_b32 v192, v194\n\t"
    "v_permlane16_swap_b32 v193, v195\n\t"
    "buffer_store_dwordx4 v[192:195], v240, s[60:63], s88 offen offset:256 sc1\n\t"
    "v_cvt_pk_bf16_f32 v196, v36, v37\n\t"
    "v_cvt_pk_bf16_f32 v197, v38, v39\n\t"
    "v_cvt_pk_bf16_f32 v198, v44, v45\n\t"
    "v_cvt_pk_bf16_f32 v199, v46, v47\n\t"
    "s_nop 0\n\t"
    "v_permlane16_swap_b32 v196, v198\n\t"
    "v_permlane16_swap_b32 v197, v199\n\t"
    "buffer_store_dwordx4 v[196:199], v240, s[60:63], s88 offen offset:2304 sc1\n\t"
    "v_cvt_pk_bf16_f32 v200, v52, v53\n\t"
    "v_cvt_pk_bf16_f32 v201, v54, v55\n\t"
    "v_cvt_pk_bf16_f32 v202, v60, v61\n\t"
    "v_cvt_pk_bf16_f32 v203, v62, v63\n\t"
    "s_nop 0\n\t"
    "v_permlane16_swap_b32 v200, v202\n\t"
    "v_permlane16_swap_b32 v201, v203\n\t"
    "buffer_store_dwordx4 v[200:203], v241, s[60:63], s88 offen offset:256 sc1\n\t"
    "v_cvt_pk_bf16_f32 v204, v68, v69\n\t"
    "v_cvt_pk_bf16_f32 v205, v70, v71\n\t"
    "v_cvt_pk_bf16_f32 v206, v76, v77\n\t"
    "v_cvt_pk_bf16_f32 v207, v78, v79\n\t"
    "s_nop 0\n\t"
    "v_permlane16_swap_b32 v204, v206\n\t"
    "v_permlane16_swap_b32 v205, v207\n\t"
    "buffer_store_dwordx4 v[204:207], v241, s[60:63], s88 offen offset:2304 sc1\n\t"
    "s_lshl_b32 s98, s75, 3\n\t"
    "s_add_u32 s98, s98, s76\n\t"
    "s_lshl_b32 s98, s98, 1\n\t"
    "s_add_u32 s98, s98, s77\n\t"
    "s_waitcnt vmcnt(0)\n\t"
    "s_barrier\n\t"
    "s_cmp_lg_u32 s34, 0\n\t"
    "s_cbranch_scc1 LPB_m_oproj_p\n\t"
    "s_lshl_b32 s86, s98, 2\n\t"
    "s_add_u32 s86, s86, 2048\n\t"
    "s_lshl_b32 s86, s86, 4\n\t"
    "v_mov_b32 v244, s86\n\t"
    "v_mov_b32 v245, %4\n\t"
    "s_mov_b64 exec, 1\n\t"
    "global_store_dword v244, v245, s[42:43] sc1\n\t"
    "s_mov_b64 exec, -1\n\t"
    "LPB_m_oproj_p:\n\t"
    "CODE_LN2:\n\t"
    :  : "s"(karg), "s"(wid), "s"(bid), "s"(ldsb), "s"(tag), "s"(((flag >> 1) & 1u))
    : "s33", "v0", "v1", "v2", "v3", "v4", "v5", "v6", "v7", "v8", "v9", "v10", "v11", "v12", "v13", "v14", "v15", "v16", "v17", "v18", "v19", "v20", "v21", "v22", "v23", "v24", "v25", "v26", "v27", "v28", "v29", "v30", "v31", "v32", "v33", "v34", "v35", "v36", "v37", "v38", "v39", "v40", "v41", "v42", "v43", "v44", "v45", "v46", "v47", "v48", "v49", "v50", "v51", "v52", "v53", "v54", "v55", "v56", "v57", "v58", "v59", "v60", "v61", "v62", "v63", "v64", "v65", "v66", "v67", "v68", "v69", "v70", "v71", "v72", "v73", "v74", "v75", "v76", "v77", "v78", "v79", "v80", "v81", "v82", "v83", "v84", "v85", "v86", "v87", "v88", "v89", "v90", "v91", "v92", "v93", "v94", "v95", "v96", "v97", "v98", "v99", "v100", "v101", "v102", "v103", "v104", "v105", "v106", "v107", "v108", "v109", "v110", "v111", "v112", "v113", "v114", "v115", "v116", "v117", "v118", "v119", "v120", "v121", "v122", "v123", "v124", "v125", "v126", "v127", "v128", "v129", "v130", "v131", "v132", "v133", "v134", "v135", "v136", "v137", "v138", "v139", "v140", "v141", "v142", "v143", "v144", "v145", "v146", "v147", "v148", "v149", "v150", "v151", "v152", "v153", "v154", "v155", "v156", "v157", "v158", "v159", "v160", "v161", "v162", "v163", "v164", "v165", "v166", "v167", "v168", "v169", "v170", "v171", "v172", "v173", "v174", "v175", "v176", "v177", "v178", "v179", "v180", "v181", "v182", "v183", "v184", "v185", "v186", "v187", "v188", "v189", "v190", "v191", "v192", "v193", "v194", "v195", "v196", "v197", "v198", "v199", "v200", "v201", "v202", "v203", "v204", "v205", "v206", "v207", "v208", "v209", "v210", "v211", "v212", "v213", "v214", "v215", "v216", "v217", "v218", "v219", "v220", "v221", "v222", "v223", "v224", "v225", "v226", "v227", "v228", "v229", "v230", "v231", "v232", "v233", "v234", "v235", "v236", "v237", "v238", "v239", "v240", "v241", "v242", "v243", "v244", "v245", "v246", "v247", "v248", "v249", "v250", "v251", "v252", "v253", "v254", "v255", "a0", "a1", "a2", "a3", "a4", "a5", "a6", "a7", "a8", "a9", "a10", "a11", "a12", "a13", "a14", "a15", "a16", "a17", "a18", "a19", "a20", "a21", "a22", "a23", "a24", "a25", "a26", "a27", "a28", "a29", "a30", "a31", "a32", "a33", "a34", "a35", "a36", "a37", "a38", "a39", "a40", "a41", "a42", "a43", "a44", "a45", "a46", "a47", "a48", "a49", "a50", "a51", "a52", "a53", "a54", "a55", "a56", "a57", "a58", "a59", "a60", "a61", "a62", "a63", "a64", "a65", "a66", "a67", "a68", "a69", "a70", "a71", "a72", "a73", "a74", "a75", "a76", "a77", "a78", "a79", "a80", "a81", "a82", "a83", "a84", "a85", "a86", "a87", "a88", "a89", "a90", "a91", "a92", "a93", "a94", "a95", "a96", "a97", "a98", "a99", "a100", "a101", "a102", "a103", "a104", "a105", "a106", "a107", "a108", "a109", "a110", "a111", "a112", "a113", "a114", "a115", "a116", "a117", "a118", "a119", "a120", "a121", "a122", "a123", "a124", "a125", "a126", "a127", "s34", "s35", "s36", "s37", "s38", "s39", "s40", "s41", "s42", "s43", "s44", "s45", "s46", "s47", "s48", "s49", "s50", "s51", "s52", "s53", "s54", "s55", "s56", "s57", "s58", "s59", "s60", "s61", "s62", "s63", "s64", "s65", "s66", "s67", "s68", "s69", "s70", "s71", "s72", "s73", "s74", "s75", "s76", "s77", "s78", "s79", "s80", "s81", "s82", "s83", "s84", "s85", "s86", "s87", "s88", "s89", "s90", "s91", "s92", "s93", "s94", "s95", "s96", "s97", "s98", "s99", "vcc", "scc", "memory");

  asm volatile("" : "+s"(ka), "+s"(bid), "+s"(wid));
  { const int t = wid*64 + lane_now();
    const int b = bid; (void)b;
    Ln2Regs lnr; ln2_load_a(lnr, ((const u16*)(__attribute__((address_space(1))) const u16*)ka[10]), ((const float*)(__attribute__((address_space(1))) const float*)ka[11]), t, bid);
    xch_clear(((void*)(__attribute__((address_space(1))) void*)ka[16]), t, bid);
    ln2_stage(lnr, smem + 114688, t);
    if(bid == 0 && wid == 1){
      const unsigned* fl_ = ((const unsigned*)(__attribute__((address_space(1))) const unsigned*)ka[9]);
      unsigned spins = 0;
      while(true){
        bool ok = true;
#pragma unroll
        for(int i = 0; i < 4; i++) ok &= __hip_atomic_load((gu32*)FLG(fl_, 2048 + i*64 + (t & 63)), __ATOMIC_RELAXED, __HIP_MEMORY_SCOPE_AGENT) == tag;
        if(__all(ok) || ++spins > (1u<<22)) break;
        __builtin_amdgcn_s_sleep(1);
      }
      if(t == 64) __hip_atomic_store((gu32*)FLG(fl_, 3072), tag, __ATOMIC_RELAXED, __HIP_MEMORY_SCOPE_AGENT);
    }
    wait_flags(((const unsigned*)(__attribute__((address_space(1))) const unsigned*)ka[9]), 512 + (bid >> 4)*16, 16, t, tag);
    ln2_load_b<true>(lnr, ((const u16*)(__attribute__((address_space(1))) const u16*)ka[12]), t, bid);
    ln2_phase<true>(lnr, ((u16*)(__attribute__((address_space(1))) u16*)ka[15]), ((u16*)(__attribute__((address_space(1))) u16*)ka[13]), ((unsigned*)(__attribute__((address_space(1))) unsigned*)ka[9]), 2304, smem + 114688, t, bid, tag);
  }
  asm volatile(
    "s_load_dwordx2 s[36:37], %0, 104\n\t"
    "s_load_dwordx2 s[38:39], %0, 48\n\t"
    "s_load_dwordx2 s[40:41], %0, 112\n\t"
    "s_load_dwordx2 s[42:43], %0, 56\n\t"
    "s_load_dwordx2 s[44:45], %0, 72\n\t"
    "s_mov_b32 s34, %1\n\t"
    "s_mov_b32 s35, %2\n\t"
    "s_mov_b32 s33, %3\n\t"
    "s_waitcnt lgkmcnt(0)\n\t"
    "v_mbcnt_lo_u32_b32 v0, -1, 0\n\t"
    "v_mbcnt_hi_u32_b32 v0, -1, v0\n\t"
    "s_and_b32 s80, s35, 7\n\t"
    "s_lshr_b32 s81, s35, 3\n\t"
    "s_mov_b32 s77, 0\n\t"
    "s_and_b32 s75, s81, 7\n\t"
    "s_lshr_b32 s98, s81, 3\n\t"
    "s_lshl_b32 s99, s80, 2\n\t"
    "s_add_u32 s76, s98, s99\n\t"
    "s_mov_b32 s52, s36\n\t"
    "s_and_b32 s53, s37, 0xffff\n\t"
    "s_mov_b32 s54, 4194304\n\t"
    "s_mov_b32 s55, 0x20000\n\t"
    "s_cmp_ge_u32 s34, 2\n\t"
    "s_cselect_b32 s98, s42, s38\n\t"
    "s_cselect_b32 s99, s43, s39\n\t"
    "s_sub_u32 s56, s98, 4096\n\t"
    "s_subb_u32 s57, s99, 0\n\t"
    "s_and_b32 s57, s57, 0xffff\n\t"
    "s_mov_b32 s58, 7344128\n\t"
    "s_mov_b32 s59, 0x20000\n\t"
    "s_and_b32 s80, s34, 1\n\t"
    "s_mul_i32 s73, s80, 229376\n\t"
    "s_mul_i32 s81, s76, 224\n\t"
    "s_add_u32 s73, s73, s81\n\t"
    "s_mul_i32 s80, s80, 7168\n\t"
    "s_lshr_b32 s81, s34, 1\n\t"
    "s_mul_i32 s81, s81, 14336\n\t"
    "s_add_u32 s80, s80, s81\n\t"
    "s_add_u32 s74, s33, s80\n\t"
    "s_lshl_b32 s80, s75, 3\n\t"
    "s_lshl_b32 s81, s34, 1\n\t"
    "s_add_u32 s78, s80, s81\n\t"
    "s_mul_i32 s72, s78, 65536\n\t"
    "s_mul_i32 s80, s77, 65536\n\t"
    "s_add_u32 s72, s72, s80\n\t"
    "v_add_u32 v253, 0, v0\n\t"
    "v_mul_u32_u24 v254, 4682, v253\n\t"
    "v_lshrrev_b32 v254, 16, v254\n\t"
    "v_mul_u32_u24 v3, 14, v254\n\t"
    "v_sub_u32 v253, v253, v3\n\t"
    "v_and_b32 v3, 0x13, v254\n\t"
    "v_bfe_u32 v255, v254, 2, 1\n\t"
    "v_lshl_or_b32 v3, v255, 3, v3\n\t"
    "v_bfe_u32 v255, v254, 3, 1\n\t"
    "v_lshl_or_b32 v3, v255, 2, v3\n\t"
    "v_mul_u32_u24 v3, 7168, v3\n\t"
    "v_lshl_add_u32 v3, v253, 4, v3\n\t"
    "v_add_u32 v3, 4096, v3\n\t"
    "v_add_u32 v253, 64, v0\n\t"
    "v_mul_u32_u24 v254, 4682, v253\n\t"
    "v_lshrrev_b32 v254, 16, v254\n\t"
    "v_mul_u32_u24 v4, 14, v254\n\t"
    "v_sub_u32 v253, v253, v4\n\t"
    "v_and_b32 v4, 0x13, v254\n\t"
    "v_bfe_u32 v255, v254, 2, 1\n\t"
    "v_lshl_or_b32 v4, v255, 3, v4\n\t"
    "v_bfe_u32 v255, v254, 3, 1\n\t"
    "v_lshl_or_b32 v4, v255, 2, v4\n\t"
    "v_mul_u32_u24 v4, 7168, v4\n\t"
    "v_lshl_add_u32 v4, v253, 4, v4\n\t"
    "v_add_u32 v4, 3072, v4\n\t"
    "v_add_u32 v253, 128, v0\n\t"
    "v_mul_u32_u24 v254, 4682, v253\n\t"
    "v_lshrrev_b32 v254, 16, v254\n\t"
    "v_mul_u32_u24 v5, 14, v254\n\t"
    "v_sub_u32 v253, v253, v5\n\t"
    "v_and_b32 v5, 0x13, v254\n\t"
    "v_bfe_u32 v255, v254, 2, 1\n\t"
    "v_lshl_or_b32 v5, v255, 3, v5\n\t"
    "v_bfe_u32 v255, v254, 3, 1\n\t"
    "v_lshl_or_b32 v5, v255, 2, v5\n\t"
    "v_mul_u32_u24 v5, 7168, v5\n\t"
    "v_lshl_add_u32 v5, v253, 4, v5\n\t"
    "v_add_u32 v5, 2048, v5\n\t"
    "v_add_u32 v253, 192, v0\n\t"
    "v_mul_u32_u24 v254, 4682, v253\n\t"
    "v_lshrrev_b32 v254, 16, v254\n\t"
    "v_mul_u32_u24 v6, 14, v254\n\t"
    "v_sub_u32 v253, v253, v6\n\t"
    "v_and_b32 v6, 0x13, v254\n\t"
    "v_bfe_u32 v255, v254, 2, 1\n\t"
    "v_lshl_or_b32 v6, v255, 3, v6\n\t"
    "v_bfe_u32 v255, v254, 3, 1\n\t"
    "v_lshl_or_b32 v6, v255, 2, v6\n\t"
    "v_mul_u32_u24 v6, 7168, v6\n\t"
    "v_lshl_add_u32 v6, v253, 4, v6\n\t"
    "v_add_u32 v6, 1024, v6\n\t"
    "v_add_u32 v253, 256, v0\n\t"
    "v_mul_u32_u24 v254, 4682, v253\n\t"
    "v_lshrrev_b32 v254, 16, v254\n\t"
    "v_mul_u32_u24 v7, 14, v254\n\t"
    "v_sub_u32 v253, v253, v7\n\t"
    "v_and_b32 v7, 0x13, v254\n\t"
    "v_bfe_u32 v255, v254, 2, 1\n\t"
    "v_lshl_or_b32 v7, v255, 3, v7\n\t"
    "v_bfe_u32 v255, v254, 3, 1\n\t"
    "v_lshl_or_b32 v7, v255, 2, v7\n\t"
    "v_mul_u32_u24 v7, 7168, v7\n\t"
    "v_lshl_add_u32 v7, v253, 4, v7\n\t"
    "v_add_u32 v7, 4096, v7\n\t"
    "v_add_u32 v253, 320, v0\n\t"
    "v_mul_u32_u24 v254, 4682, v253\n\t"
    "v_lshrrev_b32 v254, 16, v254\n\t"
    "v_mul_u32_u24 v8, 14, v254\n\t"
    "v_sub_u32 v253, v253, v8\n\t"
    "v_and_b32 v8, 0x13, v254\n\t"
    "v_bfe_u32 v255, v254, 2, 1\n\t"
    "v_lshl_or_b32 v8, v255, 3, v8\n\t"
    "v_bfe_u32 v255, v254, 3, 1\n\t"
    "v_lshl_or_b32 v8, v255, 2, v8\n\t"
    "v_mul_u32_u24 v8, 7168, v8\n\t"
    "v_lshl_add_u32 v8, v253, 4, v8\n\t"
    "v_add_u32 v8, 3072, v8\n\t"
    "v_add_u32 v253, 384, v0\n\t"
    "v_mul_u32_u24 v254, 4682, v253\n\t"
    "v_lshrrev_b32 v254, 16, v254\n\t"
    "v_mul_u32_u24 v9, 14, v254\n\t"
    "v_sub_u32 v253, v253, v9\n\t"
    "v_and_b32 v9, 0x13, v254\n\t"
    "v_bfe_u32 v255, v254, 2, 1\n\t"
    "v_lshl_or_b32 v9, v255, 3, v9\n\t"
    "v_bfe_u32 v255, v254, 3, 1\n\t"
    "v_lshl_or_b32 v9, v255, 2, v9\n\t"
    "v_mul_u32_u24 v9, 7168, v9\n\t"
    "v_lshl_add_u32 v9, v253, 4, v9\n\t"
    "v_add_u32 v9, 2048, v9\n\t"
    "v_mov_b32 v1, 0\n\t"
    "v_bfe_u32 v253, v0, 0, 4\n\t"
    "v_lshl_add_u32 v1, v253, 4, v1\n\t"
    "v_bfe_u32 v253, v0, 4, 2\n\t"
    "v_lshl_add_u32 v1, v253, 9, v1\n\t"
    "v_add_u32 v2, 65536, v1\n\t"
    "s_add_u32 m0, s74, 0\n\t"
    "s_nop 0\n\t"
    "buffer_load_dwordx4 v3, s[56:59], s73 offen offset:0 lds\n\t"
    "buffer_load_dwordx4 v4, s[56:59], s73 offen offset:1024 lds\n\t"
    "buffer_load_dwordx4 v5, s[56:59], s73 offen offset:2048 lds\n\t"
    "buffer_load_dwordx4 v6, s[56:59], s73 offen offset:3072 lds\n\t"
    "s_add_u32 m0, s74, 4096\n\t"
    "s_nop 0\n\t"
    "buffer_load_dwordx4 v7, s[56:59], s73 offen offset:0 lds\n\t"
    "buffer_load_dwordx4 v8, s[56:59], s73 offen offset:1024 lds\n\t"
    "buffer_load_dwordx4 v9, s[56:59], s73 offen offset:2048 lds\n\t"
    "s_add_u32 s73, s73, 458752\n\t"
    "s_add_u32 m0, s74, 28672\n\t"
    "s_nop 0\n\t"
    "buffer_load_dwordx4 v3, s[56:59], s73 offen offset:0 lds\n\t"
    "buffer_load_dwordx4 v4, s[56:59], s73 offen offset:1024 lds\n\t"
    "buffer_load_dwordx4 v5, s[56:59], s73 offen offset:2048 lds\n\t"
    "buffer_load_dwordx4 v6, s[56:59], s73 offen offset:3072 lds\n\t"
    "s_add_u32 m0, s74, 32768\n\t"
    "s_nop 0\n\t"
    "buffer_load_dwordx4 v7, s[56:59], s73 offen offset:0 lds\n\t"
    "buffer_load_dwordx4 v8, s[56:59], s73 offen offset:1024 lds\n\t"
    "buffer_load_dwordx4 v9, s[56:59], s73 offen offset:2048 lds\n\t"
    "s_add_u32 s73, s73, 458752\n\t"
    "s_add_u32 m0, s74, 57344\n\t"
    "s_nop 0\n\t"
    "buffer_load_dwordx4 v3, s[56:59], s73 offen offset:0 lds\n\t"
    "buffer_load_dwordx4 v4, s[56:59], s73 offen offset:1024 lds\n\t"
    "buffer_load_dwordx4 v5, s[56:59], s73 offen offset:2048 lds\n\t"
    "buffer_load_dwordx4 v6, s[56:59], s73 offen offset:3072 lds\n\t"
    "s_add_u32 m0, s74, 61440\n\t"
    "s_nop 0\n\t"
    "buffer_load_dwordx4 v7, s[56:59], s73 offen offset:0 lds\n\t"
    "buffer_load_dwordx4 v8, s[56:59], s73 offen offset:1024 lds\n\t"
    "buffer_load_dwordx4 v9, s[56:59], s73 offen offset:2048 lds\n\t"
    "s_add_u32 s73, s73, 458752\n\t"
    "v_mov_b32 v10, 0\n\t"
    "v_bfe_u32 v253, v0, 0, 2\n\t"
    "v_lshl_add_u32 v10, v253, 3, v10\n\t"
    "v_bfe_u32 v253, v0, 2, 3\n\t"
    "s_mov_b32 s98, 224\n\t"
    "v_mad_i32_i24 v10, v253, s98, v10\n\t"
    "v_bfe_u32 v253, v0, 5, 1\n\t"
    "s_mov_b32 s98, 3584\n\t"
    "v_mad_i32_i24 v10, v253, s98, v10\n\t"
    "v_add_u32 v10, s33, v10\n\t"
    "v_add_u32 v11, 65536, v10\n\t"
    "s_mov_b32 s60, s40\n\t"
    "s_and_b32 s61, s41, 0xffff\n\t"
    "s_mov_b32 s62, 14680064\n\t"
    "s_mov_b32 s63, 0x20000\n\t"
    "s_mul_i32 s88, s78, 229376\n\t"
    "s_lshr_b32 s80, s76, 4\n\t"
    "s_mul_i32 s80, s80, 114688\n\t"
    "s_add_u32 s88, s88, s80\n\t"
    "s_and_b32 s80, s76, 15\n\t"
    "s_lshl_b32 s90, s80, 10\n\t"
    "s_lshl_b32 s80, s80, 11\n\t"
    "s_add_u32 s88, s88, s80\n\t"
    "s_add_u32 s89, s88, 229376\n\t"
    "v_mov_b32 v240, 0\n\t"
    "v_bfe_u32 v253, v0, 0, 4\n\t"
    "v_lshl_add_u32 v240, v253, 4, v240\n\t"
    "v_bfe_u32 v253, v0, 4, 1\n\t"
    "v_lshl_add_u32 v240, v253, 10, v240\n\t"
    "v_bfe_u32 v253, v0, 5, 1\n\t"
    "v_lshl_add_u32 v240, v253, 9, v240\n\t"
    "v_mov_b32 v242, 0\n\t"
    "v_bfe_u32 v253, v0, 0, 4\n\t"
    "v_lshl_add_u32 v242, v253, 4, v242\n\t"
    "v_bfe_u32 v253, v0, 4, 1\n\t"
    "v_lshl_add_u32 v242, v253, 3, v242\n\t"
    "v_bfe_u32 v253, v0, 5, 1\n\t"
    "v_lshl_add_u32 v242, v253, 9, v242\n\t"
    "s_lshl_b32 s91, s75, 5\n\t"
    "s_add_u32 s91, s91, s76\n\t"
    "s_lshl_b32 s91, s91, 2\n\t"
    "s_add_u32 s91, s91, 16384\n\t"
    "s_lshl_b32 s98, s75, 5\n\t"
    "s_cmp_lg_u32 s34, 0\n\t"
    "s_cbranch_scc1 LPW_m_gu\n\t"
    "v_mbcnt_lo_u32_b32 v245, -1, 0\n\t"
    "v_mbcnt_hi_u32_b32 v245, -1, v245\n\t"
    "s_lshl_b32 s86, s98, 2\n\t"
    "s_add_u32 s86, s86, 9216\n\t"
    "v_mov_b32 v244, v245\n\t"
    "v_min_u32 v244, 31, v244\n\t"
    "v_lshlrev_b32 v244, 2, v244\n\t"
    "v_add_u32 v246, s86, v244\n\t"
    "s_mov_b64 exec, -1\n\t"
    "v_lshlrev_b32 v246, 4, v246\n\t"
    "s_mov_b32 s87, 0x400000\n\t"
    "LPP_m_gu:\n\t"
    "global_load_dword v245, v246, s[44:45] sc1\n\t"
    "s_waitcnt vmcnt(0)\n\t"
    "v_cmp_ne_u32 vcc, %4, v245\n\t"
    "s_sub_u32 s87, s87, 1\n\t"
    "s_cmp_eq_u32 s87, 0\n\t"
    "s_cbranch_scc1 LPD_m_gu\n\t"
    "s_cmp_eq_u64 vcc, 0\n\t"
    "s_cbranch_scc1 LPD_m_gu\n\t"
    "s_sleep 1\n\t"
    "s_branch LPP_m_gu\n\t"
    "LPD_m_gu:\n\t"
    "LPW_m_gu:\n\t"
    "s_barrier\n\t"
    "buffer_load_dwordx4 a[64:67], v1, s[52:55], s72 offen offset:0 sc1\n\t"
    "buffer_load_dwordx4 a[68:71], v1, s[52:55], s72 offen offset:256 sc1\n\t"
    "buffer_load_dwordx4 a[72:75], v2, s[52:55], s72 offen offset:0 sc1\n\t"
    "buffer_load_dwordx4 a[76:79], v2, s[52:55], s72 offen offset:256 sc1\n\t"
    "buffer_load_dwordx4 a[80:83], v1, s[52:55], s72 offen offset:2048 sc1\n\t"
    "buffer_load_dwordx4 a[84:87], v1, s[52:55], s72 offen offset:2304 sc1\n\t"
    "buffer_load_dwordx4 a[88:91], v2, s[52:55], s72 offen offset:2048 sc1\n\t"
    "buffer_load_dwordx4 a[92:95], v2, s[52:55], s72 offen offset:2304 sc1\n\t"
    "s_add_u32 s72, s72, 4096\n\t"
    "s_waitcnt vmcnt(22)\n\t"
    "s_barrier\n\t"
    "ds_read_b64_tr_b16 a[0:1], v10 offset:0\n\t"
    "ds_read_b64_tr_b16 a[2:3], v10 offset:1792\n\t"
    "ds_read_b64_tr_b16 a[4:5], v10 offset:32\n\t"
    "ds_read_b64_tr_b16 a[6:7], v10 offset:1824\n\t"
    "ds_read_b64_tr_b16 a[8:9], v10 offset:64\n\t"
    "ds_read_b64_tr_b16 a[10:11], v10 offset:1856\n\t"
    "ds_read_b64_tr_b16 a[12:13], v10 offset:96\n\t"
    "ds_read_b64_tr_b16 a[14:15], v10 offset:1888\n\t"
    "ds_read_b64_tr_b16 a[16:17], v10 offset:128\n\t"
    "ds_read_b64_tr_b16 a[18:19], v10 offset:1920\n\t"
    "ds_read_b64_tr_b16 a[20:21], v10 offset:160\n\t"
    "ds_read_b64_tr_b16 a[22:23], v10 offset:1952\n\t"
    "ds_read_b64_tr_b16 a[24:25], v10 offset:192\n\t"
    "ds_read_b64_tr_b16 a[26:27], v10 offset:1984\n\t"
    "ds_read_b64_tr_b16 a[28:29], v10 offset:14336\n\t"
    "ds_read_b64_tr_b16 a[30:31], v10 offset:16128\n\t"
    "ds_read_b64_tr_b16 a[32:33], v10 offset:14368\n\t"
    "ds_read_b64_tr_b16 a[34:35], v10 offset:16160\n\t"
    "ds_read_b64_tr_b16 a[36:37], v10 offset:14400\n\t"
    "ds_read_b64_tr_b16 a[38:39], v10 offset:16192\n\t"
    "ds_read_b64_tr_b16 a[40:41], v10 offset:14432\n\t"
    "ds_read_b64_tr_b16 a[42:43], v10 offset:16224\n\t"
    "ds_read_b64_tr_b16 a[44:45], v10 offset:14464\n\t"
    "ds_read_b64_tr_b16 a[46:47], v10 offset:16256\n\t"
    "ds_read_b64_tr_b16 a[48:49], v10 offset:14496\n\t"
    "ds_read_b64_tr_b16 a[50:51], v10 offset:16288\n\t"
    "ds_read_b64_tr_b16 a[52:53], v10 offset:14528\n\t"
    "ds_read_b64_tr_b16 a[54:55], v10 offset:16320\n\t"
    "s_waitcnt vmcnt(7) lgkmcnt(15)\n\t"
    "v_mfma_f32_16x16x32_bf16 v[16:19], a[0:3], a[64:67], 0\n\t"
    "buffer_load_dwordx4 a[96:99], v1, s[52:55], s72 offen offset:0 sc1\n\t"
    "s_waitcnt vmcnt(7)\n\t"
    "v_mfma_f32_16x16x32_bf16 v[20:23], a[0:3], a[68:71], 0\n\t"
    "s_waitcnt vmcnt(6)\n\t"
    "v_mfma_f32_16x16x32_bf16 v[24:27], a[0:3], a[72:75], 0\n\t"
    "s_waitcnt vmcnt(5)\n\t"
    "v_mfma_f32_16x16x32_bf16 v[28:31], a[0:3], a[76:79], 0\n\t"
    "buffer_load_dwordx4 a[100:103], v1, s[52:55], s72 offen offset:256 sc1\n\t"
    "v_mfma_f32_16x16x32_bf16 v[32:35], a[4:7], a[64:67], 0\n\t"
    "ds_read_b64_tr_b16 a[0:1], v10 offset:7168\n\t"
    "v_mfma_f32_16x16x32_bf16 v[36:39], a[4:7], a[68:71], 0\n\t"
    "ds_read_b64_tr_b16 a[2:3], v10 offset:8960\n\t"
    "v_mfma_f32_16x16x32_bf16 v[40:43], a[4:7], a[72:75], 0\n\t"
    "buffer_load_dwordx4 a[104:107], v2, s[52:55], s72 offen offset:0 sc1\n\t"
    "v_mfma_f32_16x16x32_bf16 v[44:47], a[4:7], a[76:79], 0\n\t"
    "v_mfma_f32_16x16x32_bf16 v[48:51], a[8:11], a[64:67], 0\n\t"
    "ds_read_b64_tr_b16 a[4:5], v10 offset:7200\n\t"
    "v_mfma_f32_16x16x32_bf16 v[52:55], a[8:11], a[68:71], 0\n\t"
    "ds_read_b64_tr_b16 a[6:7], v10 offset:8992\n\t"
    "buffer_load_dwordx4 a[108:111], v2, s[52:55], s72 offen offset:256 sc1\n\t"
    "v_mfma_f32_16x16x32_bf16 v[56:59], a[8:11], a[72:75], 0\n\t"
    "v_mfma_f32_16x16x32_bf16 v[60:63], a[8:11], a[76:79], 0\n\t"
    "v_mfma_f32_16x16x32_bf16 v[64:67], a[12:15], a[64:67], 0\n\t"
    "ds_read_b64_tr_b16 a[8:9], v10 offset:7232\n\t"
    "buffer_load_dwordx4 a[112:115], v1, s[52:55], s72 offen offset:2048 sc1\n\t"
    "v_mfma_f32_16x16x32_bf16 v[68:71], a[12:15], a[68:71], 0\n\t"
    "ds_read_b64_tr_b16 a[10:11], v10 offset:9024\n\t"
    "v_mfma_f32_16x16x32_bf16 v[72:75], a[12:15], a[72:75], 0\n\t"
    "v_mfma_f32_16x16x32_bf16 v[76:79], a[12:15], a[76:79], 0\n\t"
    "buffer_load_dwordx4 a[116:119], v1, s[52:55], s72 offen offset:2304 sc1\n\t"
    "v_mfma_f32_16x16x32_bf16 v[80:83], a[16:19], a[64:67], 0\n\t"
    "ds_read_b64_tr_b16 a[12:13], v10 offset:7264\n\t"
    "v_mfma_f32_16x16x32_bf16 v[84:87], a[16:19], a[68:71], 0\n\t"
    "ds_read_b64_tr_b16 a[14:15], v10 offset:9056\n\t"
    "v_mfma_f32_16x16x32_bf16 v[88:91], a[16:19], a[72:75], 0\n\t"
    "buffer_load_dwordx4 a[120:123], v2, s[52:55], s72 offen offset:2048 sc1\n\t"
    "v_mfma_f32_16x16x32_bf16 v[92:95], a[16:19], a[76:79], 0\n\t"
    "v_mfma_f32_16x16x32_bf16 v[96:99], a[20:23], a[64:67], 0\n\t"
    "ds_read_b64_tr_b16 a[16:17], v10 offset:7296\n\t"
    "v_mfma_f32_16x16x32_bf16 v[100:103], a[20:23], a[68:71], 0\n\t"
    "ds_read_b64_tr_b16 a[18:19], v10 offset:9088\n\t"
    "buffer_load_dwordx4 a[124:127], v2, s[52:55], s72 offen offset:2304 sc1\n\t"
    "v_mfma_f32_16x16x32_bf16 v[104:107], a[20:23], a[72:75], 0\n\t"
    "v_mfma_f32_16x16x32_bf16 v[108:111], a[20:23], a[76:79], 0\n\t"
    "s_waitcnt lgkmcnt(15)\n\t"
    "v_mfma_f32_16x16x32_bf16 v[112:115], a[24:27], a[64:67], 0\n\t"
    "ds_read_b64_tr_b16 a[20:21], v10 offset:7328\n\t"
    "s_add_u32 s72, s72, 4096\n\t"
    "v_mfma_f32_16x16x32_bf16 v[116:119], a[24:27], a[68:71], 0\n\t"
    "ds_read_b64_tr_b16 a[22:23], v10 offset:9120\n\t"
    "v_mfma_f32_16x16x32_bf16 v[120:123], a[24:27], a[72:75], 0\n\t"
    "v_mfma_f32_16x16x32_bf16 v[124:127], a[24:27], a[76:79], 0\n\t"
    "buffer_load_dwordx4 a[128:131], v1, s[52:55], s72 offen offset:0 sc1\n\t"
    "v_mfma_f32_16x16x32_bf16 v[128:131], a[28:31], a[64:67], 0\n\t"
    "ds_read_b64_tr_b16 a[24:25], v10 offset:7360\n\t"
    "v_mfma_f32_16x16x32_bf16 v[132:135], a[28:31], a[68:71], 0\n\t"
    "ds_read_b64_tr_b16 a[26:27], v10 offset:9152\n\t"
    "v_mfma_f32_16x16x32_bf16 v[136:139], a[28:31], a[72:75], 0\n\t"
    "buffer_load_dwordx4 a[132:135], v1, s[52:55], s72 offen offset:256 sc1\n\t"
    "v_mfma_f32_16x16x32_bf16 v[140:143], a[28:31], a[76:79], 0\n\t"
    "v_mfma_f32_16x16x32_bf16 v[144:147], a[32:35], a[64:67], 0\n\t"
    "ds_read_b64_tr_b16 a[28:29], v10 offset:21504\n\t"
    "v_mfma_f32_16x16x32_bf16 v[148:151], a[32:35], a[68:71], 0\n\t"
    "ds_read_b64_tr_b16 a[30:31], v10 offset:23296\n\t"
    "buffer_load_dwordx4 a[136:139], v2, s[52:55], s72 offen offset:0 sc1\n\t"
    "v_mfma_f32_16x16x32_bf16 v[152:155], a[32:35], a[72:75], 0\n\t"
    "v_mfma_f32_16x16x32_bf16 v[156:159], a[32:35], a[76:79], 0\n\t"
    "v_mfma_f32_16x16x32_bf16 v[160:163], a[36:39], a[64:67], 0\n\t"
    "ds_read_b64_tr_b16 a[32:33], v10 offset:21536\n\t"
    "buffer_load_dwordx4 a[140:143], v2, s[52:55], s72 offen offset:256 sc1\n\t"
    "v_mfma_f32_16x16x32_bf16 v[164:167], a[36:39], a[68:71], 0\n\t"
    "ds_read_b64_tr_b16 a[34:35], v10 offset:23328\n\t"
    "v_mfma_f32_16x16x32_bf16 v[168:171], a[36:39], a[72:75], 0\n\t"
    "v_mfma_f32_16x16x32_bf16 v[172:175], a[36:39], a[76:79], 0\n\t"
    "s_waitcnt lgkmcnt(15)\n\t"
    "buffer_load_dwordx4 a[144:147], v1, s[52:55], s72 offen offset:2048 sc1\n\t"
    "v_mfma_f32_16x16x32_bf16 v[176:179], a[40:43], a[64:67], 0\n\t"
    "ds_read_b64_tr_b16 a[36:37], v10 offset:21568\n\t"
    "v_mfma_f32_16x16x32_bf16 v[180:183], a[40:43], a[68:71], 0\n\t"
    "ds_read_b64_tr_b16 a[38:39], v10 offset:23360\n\t"
    "v_mfma_f32_16x16x32_bf16 v[184:187], a[40:43], a[72:75], 0\n\t"
    "buffer_load_dwordx4 a[148:151], v1, s[52:55], s72 offen offset:2304 sc1\n\t"
    "v_mfma_f32_16x16x32_bf16 v[188:191], a[40:43], a[76:79], 0\n\t"
    "v_mfma_f32_16x16x32_bf16 v[192:195], a[44:47], a[64:67], 0\n\t"
    "ds_read_b64_tr_b16 a[40:41], v10 offset:21600\n\t"
    "v_mfma_f32_16x16x32_bf16 v[196:199], a[44:47], a[68:71], 0\n\t"
    "ds_read_b64_tr_b16 a[42:43], v10 offset:23392\n\t"
    "buffer_load_dwordx4 a[152:155], v2, s[52:55], s72 offen offset:2048 sc1\n\t"
    "v_mfma_f32_16x16x32_bf16 v[200:203], a[44:47], a[72:75], 0\n\t"
    "v_mfma_f32_16x16x32_bf16 v[204:207], a[44:47], a[76:79], 0\n\t"
    "v_mfma_f32_16x16x32_bf16 v[208:211], a[48:51], a[64:67], 0\n\t"
    "ds_read_b64_tr_b16 a[44:45], v10 offset:21632\n\t"
    "buffer_load_dwordx4 a[156:159], v2, s[52:55], s72 offen offset:2304 sc1\n\t"
    "v_mfma_f32_16x16x32_bf16 v[212:215], a[48:51], a[68:71], 0\n\t"
    "ds_read_b64_tr_b16 a[46:47], v10 offset:23424\n\t"
    "v_mfma_f32_16x16x32_bf16 v[216:219], a[48:51], a[72:75], 0\n\t"
    "v_mfma_f32_16x16x32_bf16 v[220:223], a[48:51], a[76:79], 0\n\t"
    "s_add_u32 s72, s72, 4096\n\t"
    "v_mfma_f32_16x16x32_bf16 v[224:227], a[52:55], a[64:67], 0\n\t"
    "ds_read_b64_tr_b16 a[48:49], v10 offset:21664\n\t"
    "v_mfma_f32_16x16x32_bf16 v[228:231], a[52:55], a[68:71], 0\n\t"
    "ds_read_b64_tr_b16 a[50:51], v10 offset:23456\n\t"
    "s_add_u32 m0, s74, 86016\n\t"
    "v_mfma_f32_16x16x32_bf16 v[232:235], a[52:55], a[72:75], 0\n\t"
    "v_mfma_f32_16x16x32_bf16 v[236:239], a[52:55], a[76:79], 0\n\t"
    "s_waitcnt vmcnt(16) lgkmcnt(15)\n\t"
    "v_mfma_f32_16x16x32_bf16 v[16:19], a[0:3], a[80:83], v[16:19]\n\t"
    "s_barrier\n\t"
    "ds_read_b64_tr_b16 a[52:53], v10 offset:21696\n\t"
    "v_mfma_f32_16x16x32_bf16 v[20:23], a[0:3], a[84:87], v[20:23]\n\t"
    "ds_read_b64_tr_b16 a[54:55], v10 offset:23488\n\t"
    "v_mfma_f32_16x16x32_bf16 v[24:27], a[0:3], a[88:91], v[24:27]\n\t"
    "buffer_load_dwordx4 v3, s[56:59], s73 offen offset:0 lds\n\t"
    "v_mfma_f32_16x16x32_bf16 v[28:31], a[0:3], a[92:95], v[28:31]\n\t"
    "v_mfma_f32_16x16x32_bf16 v[32:35], a[4:7], a[80:83], v[32:35]\n\t"
    "ds_read_b64_tr_b16 a[0:1], v10 offset:28672\n\t"
    "v_mfma_f32_16x16x32_bf16 v[36:39], a[4:7], a[84:87], v[36:39]\n\t"
    "ds_read_b64_tr_b16 a[2:3], v10 offset:30464\n\t"
    "v_mfma_f32_16x16x32_bf16 v[40:43], a[4:7], a[88:91], v[40:43]\n\t"
    "v_mfma_f32_16x16x32_bf16 v[44:47], a[4:7], a[92:95], v[44:47]\n\t"
    "v_mfma_f32_16x16x32_bf16 v[48:51], a[8:11], a[80:83], v[48:51]\n\t"
    "ds_read_b64_tr_b16 a[4:5], v10 offset:28704\n\t"
    "v_mfma_f32_16x16x32_bf16 v[52:55], a[8:11], a[84:87], v[52:55]\n\t"
    "ds_read_b64_tr_b16 a[6:7], v10 offset:30496\n\t"
    "v_mfma_f32_16x16x32_bf16 v[56:59], a[8:11], a[88:91], v[56:59]\n\t"
    "buffer_load_dwordx4 v4, s[56:59], s73 offen offset:1024 lds\n\t"
    "v_mfma_f32_16x16x32_bf16 v[60:63], a[8:11], a[92:95], v[60:63]\n\t"
    "v_mfma_f32_16x16x32_bf16 v[64:67], a[12:15], a[80:83], v[64:67]\n\t"
    "ds_read_b64_tr_b16 a[8:9], v10 offset:28736\n\t"
    "v_mfma_f32_16x16x32_bf16 v[68:71], a[12:15], a[84:87], v[68:71]\n\t"
    "ds_read_b64_tr_b16 a[10:11], v10 offset:30528\n\t"
    "v_mfma_f32_16x16x32_bf16 v[72:75], a[12:15], a[88:91], v[72:75]\n\t"
    "v_mfma_f32_16x16x32_bf16 v[76:79], a[12:15], a[92:95], v[76:79]\n\t"
    "s_waitcnt lgkmcnt(15)\n\t"
    "v_mfma_f32_16x16x32_bf16 v[80:83], a[16:19], a[80:83], v[80:83]\n\t"
    "ds_read_b64_tr_b16 a[12:13], v10 offset:28768\n\t"
    "v_mfma_f32_16x16x32_bf16 v[84:87], a[16:19], a[84:87], v[84:87]\n\t"
    "ds_read_b64_tr_b16 a[14:15], v10 offset:30560\n\t"
    "v_mfma_f32_16x16x32_bf16 v[88:91], a[16:19], a[88:91], v[88:91]\n\t"
    "buffer_load_dwordx4 v5, s[56:59], s73 offen offset:2048 lds\n\t"
    "v_mfma_f32_16x16x32_bf16 v[92:95], a[16:19], a[92:95], v[92:95]\n\t"
    "v_mfma_f32_16x16x32_bf16 v[96:99], a[20:23], a[80:83], v[96:99]\n\t"
    "ds_read_b64_tr_b16 a[16:17], v10 offset:28800\n\t"
    "v_mfma_f32_16x16x32_bf16 v[100:103], a[20:23], a[84:87], v[100:103]\n\t"
    "ds_read_b64_tr_b16 a[18:19], v10 offset:30592\n\t"
    "v_mfma_f32_16x16x32_bf16 v[104:107], a[20:23], a[88:91], v[104:107]\n\t"
    "v_mfma_f32_16x16x32_bf16 v[108:111], a[20:23], a[92:95], v[108:111]\n\t"
    "v_mfma_f32_16x16x32_bf16 v[112:115], a[24:27], a[80:83], v[112:115]\n\t"
    "ds_read_b64_tr_b16 a[20:21], v10 offset:28832\n\t"
    "v_mfma_f32_16x16x32_bf16 v[116:119], a[24:27], a[84:87], v[116:119]\n\t"
    "ds_read_b64_tr_b16 a[22:23], v10 offset:30624\n\t"
    "v_mfma_f32_16x16x32_bf16 v[120:123], a[24:27], a[88:91], v[120:123]\n\t"
    "buffer_load_dwordx4 v6, s[56:59], s73 offen offset:3072 lds\n\t"
    "v_mfma_f32_16x16x32_bf16 v[124:127], a[24:27], a[92:95], v[124:127]\n\t"
    "v_mfma_f32_16x16x32_bf16 v[128:131], a[28:31], a[80:83], v[128:131]\n\t"
    "ds_read_b64_tr_b16 a[24:25], v10 offset:28864\n\t"
    "s_add_u32 m0, s74, 90112\n\t"
    "v_mfma_f32_16x16x32_bf16 v[132:135], a[28:31], a[84:87], v[132:135]\n\t"
    "ds_read_b64_tr_b16 a[26:27], v10 offset:30656\n\t"
    "v_mfma_f32_16x16x32_bf16 v[136:139], a[28:31], a[88:91], v[136:139]\n\t"
    "buffer_load_dwordx4 v7, s[56:59], s73 offen offset:0 lds\n\t"
    "v_mfma_f32_16x16x32_bf16 v[140:143], a[28:31], a[92:95], v[140:143]\n\t"
    "s_waitcnt lgkmcnt(15)\n\t"
    "v_mfma_f32_16x16x32_bf16 v[144:147], a[32:35], a[80:83], v[144:147]\n\t"
    "ds_read_b64_tr_b16 a[28:29], v10 offset:43008\n\t"
    "v_mfma_f32_16x16x32_bf16 v[148:151], a[32:35], a[84:87], v[148:151]\n\t"
    "ds_read_b64_tr_b16 a[30:31], v10 offset:44800\n\t"
    "v_mfma_f32_16x16x32_bf16 v[152:155], a[32:35], a[88:91], v[152:155]\n\t"
    "v_mfma_f32_16x16x32_bf16 v[156:159], a[32:35], a[92:95], v[156:159]\n\t"
    "v_mfma_f32_16x16x32_bf16 v[160:163], a[36:39], a[80:83], v[160:163]\n\t"
    "ds_read_b64_tr_b16 a[32:33], v10 offset:43040\n\t"
    "v_mfma_f32_16x16x32_bf16 v[164:167], a[36:39], a[84:87], v[164:167]\n\t"
    "ds_read_b64_tr_b16 a[34:35], v10 offset:44832\n\t"
    "v_mfma_f32_16x16x32_bf16 v[168:171], a[36:39], a[88:91], v[168:171]\n\t"
    "buffer_load_dwordx4 v8, s[56:59], s73 offen offset:1024 lds\n\t"
    "v_mfma_f32_16x16x32_bf16 v[172:175], a[36:39], a[92:95], v[172:175]\n\t"
    "v_mfma_f32_16x16x32_bf16 v[176:179], a[40:43], a[80:83], v[176:179]\n\t"
    "ds_read_b64_tr_b16 a[36:37], v10 offset:43072\n\t"
    "v_mfma_f32_16x16x32_bf16 v[180:183], a[40:43], a[84:87], v[180:183]\n\t"
    "ds_read_b64_tr_b16 a[38:39], v10 offset:44864\n\t"
    "v_mfma_f32_16x16x32_bf16 v[184:187], a[40:43], a[88:91], v[184:187]\n\t"
    "v_mfma_f32_16x16x32_bf16 v[188:191], a[40:43], a[92:95], v[188:191]\n\t"
    "v_mfma_f32_16x16x32_bf16 v[192:195], a[44:47], a[80:83], v[192:195]\n\t"
    "ds_read_b64_tr_b16 a[40:41], v10 offset:43104\n\t"
    "v_mfma_f32_16x16x32_bf16 v[196:199], a[44:47], a[84:87], v[196:199]\n\t"
    "ds_read_b64_tr_b16 a[42:43], v10 offset:44896\n\t"
    "v_mfma_f32_16x16x32_bf16 v[200:203], a[44:47], a[88:91], v[200:203]\n\t"
    "buffer_load_dwordx4 v9, s[56:59], s73 offen offset:2048 lds\n\t"
    "v_mfma_f32_16x16x32_bf16 v[204:207], a[44:47], a[92:95], v[204:207]\n\t"
    "s_waitcnt lgkmcnt(15)\n\t"
    "s_add_u32 s73, s73, 458752\n\t"
    "v_mfma_f32_16x16x32_bf16 v[208:211], a[48:51], a[80:83], v[208:211]\n\t"
    "ds_read_b64_tr_b16 a[44:45], v10 offset:43136\n\t"
    "v_mfma_f32_16x16x32_bf16 v[212:215], a[48:51], a[84:87], v[212:215]\n\t"
    "ds_read_b64_tr_b16 a[46:47], v10 offset:44928\n\t"
    "v_mfma_f32_16x16x32_bf16 v[216:219], a[48:51], a[88:91], v[216:219]\n\t"
    "v_mfma_f32_16x16x32_bf16 v[220:223], a[48:51], a[92:95], v[220:223]\n\t"
    "v_mfma_f32_16x16x32_bf16 v[224:227], a[52:55], a[80:83], v[224:227]\n\t"
    "ds_read_b64_tr_b16 a[48:49], v10 offset:43168\n\t"
    "v_mfma_f32_16x16x32_bf16 v[228:231], a[52:55], a[84:87], v[228:231]\n\t"
    "ds_read_b64_tr_b16 a[50:51], v10 offset:44960\n\t"
    "v_mfma_f32_16x16x32_bf16 v[232:235], a[52:55], a[88:91], v[232:235]\n\t"
    "buffer_load_dwordx4 a[160:163], v1, s[52:55], s72 offen offset:0 sc1\n\t"
    "v_mfma_f32_16x16x32_bf16 v[236:239], a[52:55], a[92:95], v[236:239]\n\t"
    "s_waitcnt vmcnt(20)\n\t"
    "v_mfma_f32_16x16x32_bf16 v[16:19], a[0:3], a[96:99], v[16:19]\n\t"
    "ds_read_b64_tr_b16 a[52:53], v10 offset:43200\n\t"
    "v_mfma_f32_16x16x32_bf16 v[20:23], a[0:3], a[100:103], v[20:23]\n\t"
    "ds_read_b64_tr_b16 a[54:55], v10 offset:44992\n\t"
    "v_mfma_f32_16x16x32_bf16 v[24:27], a[0:3], a[104:107], v[24:27]\n\t"
    "v_mfma_f32_16x16x32_bf16 v[28:31], a[0:3], a[108:111], v[28:31]\n\t"
    "buffer_load_dwordx4 a[164:167], v1, s[52:55], s72 offen offset:256 sc1\n\t"
    "v_mfma_f32_16x16x32_bf16 v[32:35], a[4:7], a[96:99], v[32:35]\n\t"
    "ds_read_b64_tr_b16 a[0:1], v10 offset:35840\n\t"
    "v_mfma_f32_16x16x32_bf16 v[36:39], a[4:7], a[100:103], v[36:39]\n\t"
    "ds_read_b64_tr_b16 a[2:3], v10 offset:37632\n\t"
    "v_mfma_f32_16x16x32_bf16 v[40:43], a[4:7], a[104:107], v[40:43]\n\t"
    "v_mfma_f32_16x16x32_bf16 v[44:47], a[4:7], a[108:111], v[44:47]\n\t"
    "s_waitcnt lgkmcnt(15)\n\t"
    "v_mfma_f32_16x16x32_bf16 v[48:51], a[8:11], a[96:99], v[48:51]\n\t"
    "ds_read_b64_tr_b16 a[4:5], v10 offset:35872\n\t"
    "v_mfma_f32_16x16x32_bf16 v[52:55], a[8:11], a[100:103], v[52:55]\n\t"
    "ds_read_b64_tr_b16 a[6:7], v10 offset:37664\n\t"
    "v_mfma_f32_16x16x32_bf16 v[56:59], a[8:11], a[104:107], v[56:59]\n\t"
    "v_mfma_f32_16x16x32_bf16 v[60:63], a[8:11], a[108:111], v[60:63]\n\t"
    "buffer_load_dwordx4 a[168:171], v2, s[52:55], s72 offen offset:0 sc1\n\t"
    "v_mfma_f32_16x16x32_bf16 v[64:67], a[12:15], a[96:99], v[64:67]\n\t"
    "ds_read_b64_tr_b16 a[8:9], v10 offset:35904\n\t"
    "v_mfma_f32_16x16x32_bf16 v[68:71], a[12:15], a[100:103], v[68:71]\n\t"
    "ds_read_b64_tr_b16 a[10:11], v10 offset:37696\n\t"
    "v_mfma_f32_16x16x32_bf16 v[72:75], a[12:15], a[104:107], v[72:75]\n\t"
    "v_mfma_f32_16x16x32_bf16 v[76:79], a[12:15], a[108:111], v[76:79]\n\t"
    "v_mfma_f32_16x16x32_bf16 v[80:83], a[16:19], a[96:99], v[80:83]\n\t"
    "ds_read_b64_tr_b16 a[12:13], v10 offset:35936\n\t"
    "v_mfma_f32_16x16x32_bf16 v[84:87], a[16:19], a[100:103], v[84:87]\n\t"
    "ds_read_b64_tr_b16 a[14:15], v10 offset:37728\n\t"
    "v_mfma_f32_16x16x32_bf16 v[88:91], a[16:19], a[104:107], v[88:91]\n\t"
    "buffer_load_dwordx4 a[172:175], v2, s[52:55], s72 offen offset:256 sc1\n\t"
    "v_mfma_f32_16x16x32_bf16 v[92:95], a[16:19], a[108:111], v[92:95]\n\t"
    "v_mfma_f32_16x16x32_bf16 v[96:99], a[20:23], a[96:99], v[96:99]\n\t"
    "ds_read_b64_tr_b16 a[16:17], v10 offset:35968\n\t"
    "v_mfma_f32_16x16x32_bf16 v[100:103], a[20:23], a[100:103], v[100:103]\n\t"
    "ds_read_b64_tr_b16 a[18:19], v10 offset:37760\n\t"
    "v_mfma_f32_16x16x32_bf16 v[104:107], a[20:23], a[104:107], v[104:107]\n\t"
    "v_mfma_f32_16x16x32_bf16 v[108:111], a[20:23], a[108:111], v[108:111]\n\t"
    "s_waitcnt lgkmcnt(15)\n\t"
    "v_mfma_f32_16x16x32_bf16 v[112:115], a[24:27], a[96:99], v[112:115]\n\t"
    "ds_read_b64_tr_b16 a[20:21], v10 offset:36000\n\t"
    "v_mfma_f32_16x16x32_bf16 v[116:119], a[24:27], a[100:103], v[116:119]\n\t"
    "ds_read_b64_tr_b16 a[22:23], v10 offset:37792\n\t"
    "v_mfma_f32_16x16x32_bf16 v[120:123], a[24:27], a[104:107], v[120:123]\n\t"
    "buffer_load_dwordx4 a[176:179], v1, s[52:55], s72 offen offset:2048 sc1\n\t"
    "v_mfma_f32_16x16x32_bf16 v[124:127], a[24:27], a[108:111], v[124:127]\n\t"
    "v_mfma_f32_16x16x32_bf16 v[128:131], a[28:31], a[96:99], v[128:131]\n\t"
    "ds_read_b64_tr_b16 a[24:25], v10 offset:36032\n\t"
    "v_mfma_f32_16x16x32_bf16 v[132:135], a[28:31], a[100:103], v[132:135]\n\t"
    "ds_read_b64_tr_b16 a[26:27], v10 offset:37824\n\t"
    "v_mfma_f32_16x16x32_bf16 v[136:139], a[28:31], a[104:107], v[136:139]\n\t"
    "v_mfma_f32_16x16x32_bf16 v[140:143], a[28:31], a[108:111], v[140:143]\n\t"
    "v_mfma_f32_16x16x32_bf16 v[144:147], a[32:35], a[96:99], v[144:147]\n\t"
    "ds_read_b64_tr_b16 a[28:29], v10 offset:50176\n\t"
    "v_mfma_f32_16x16x32_bf16 v[148:151], a[32:35], a[100:103], v[148:151]\n\t"
    "ds_read_b64_tr_b16 a[30:31], v10 offset:51968\n\t"
    "v_mfma_f32_16x16x32_bf16 v[152:155], a[32:35], a[104:107], v[152:155]\n\t"
    "buffer_load_dwordx4 a[180:183], v1, s[52:55], s72 offen offset:2304 sc1\n\t"
    "v_mfma_f32_16x16x32_bf16 v[156:159], a[32:35], a[108:111], v[156:159]\n\t"
    "v_mfma_f32_16x16x32_bf16 v[160:163], a[36:39], a[96:99], v[160:163]\n\t"
    "ds_read_b64_tr_b16 a[32:33], v10 offset:50208\n\t"
    "v_mfma_f32_16x16x32_bf16 v[164:167], a[36:39], a[100:103], v[164:167]\n\t"
    "ds_read_b64_tr_b16 a[34:35], v10 offset:52000\n\t"
    "v_mfma_f32_16x16x32_bf16 v[168:171], a[36:39], a[104:107], v[168:171]\n\t"
    "v_mfma_f32_16x16x32_bf16 v[172:175], a[36:39], a[108:111], v[172:175]\n\t"
    "s_waitcnt lgkmcnt(15)\n\t"
    "v_mfma_f32_16x16x32_bf16 v[176:179], a[40:43], a[96:99], v[176:179]\n\t"
    "ds_read_b64_tr_b16 a[36:37], v10 offset:50240\n\t"
    "v_mfma_f32_16x16x32_bf16 v[180:183], a[40:43], a[100:103], v[180:183]\n\t"
    "ds_read_b64_tr_b16 a[38:39], v10 offset:52032\n\t"
    "v_mfma_f32_16x16x32_bf16 v[184:187], a[40:43], a[104:107], v[184:187]\n\t"
    "buffer_load_dwordx4 a[184:187], v2, s[52:55], s72 offen offset:2048 sc1\n\t"
    "v_mfma_f32_16x16x32_bf16 v[188:191], a[40:43], a[108:111], v[188:191]\n\t"
    "v_mfma_f32_16x16x32_bf16 v[192:195], a[44:47], a[96:99], v[192:195]\n\t"
    "ds_read_b64_tr_b16 a[40:41], v10 offset:50272\n\t"
    "v_mfma_f32_16x16x32_bf16 v[196:199], a[44:47], a[100:103], v[196:199]\n\t"
    "ds_read_b64_tr_b16 a[42:43], v10 offset:52064\n\t"
    "v_mfma_f32_16x16x32_bf16 v[200:203], a[44:47], a[104:107], v[200:203]\n\t"
    "v_mfma_f32_16x16x32_bf16 v[204:207], a[44:47], a[108:111], v[204:207]\n\t"
    "v_mfma_f32_16x16x32_bf16 v[208:211], a[48:51], a[96:99], v[208:211]\n\t"
    "ds_read_b64_tr_b16 a[44:45], v10 offset:50304\n\t"
    "v_mfma_f32_16x16x32_bf16 v[212:215], a[48:51], a[100:103], v[212:215]\n\t"
    "ds_read_b64_tr_b16 a[46:47], v10 offset:52096\n\t"
    "v_mfma_f32_16x16x32_bf16 v[216:219], a[48:51], a[104:107], v[216:219]\n\t"
    "buffer_load_dwordx4 a[188:191], v2, s[52:55], s72 offen offset:2304 sc1\n\t"
    "v_mfma_f32_16x16x32_bf16 v[220:223], a[48:51], a[108:111], v[220:223]\n\t"
    "v_mfma_f32_16x16x32_bf16 v[224:227], a[52:55], a[96:99], v[224:227]\n\t"
    "ds_read_b64_tr_b16 a[48:49], v10 offset:50336\n\t"
    "s_add_u32 s72, s72, 4096\n\t"
    "v_mfma_f32_16x16x32_bf16 v[228:231], a[52:55], a[100:103], v[228:231]\n\t"
    "ds_read_b64_tr_b16 a[50:51], v10 offset:52128\n\t"
    "s_add_u32 m0, s74, 0\n\t"
    "v_mfma_f32_16x16x32_bf16 v[232:235], a[52:55], a[104:107], v[232:235]\n\t"
    "v_mfma_f32_16x16x32_bf16 v[236:239], a[52:55], a[108:111], v[236:239]\n\t"
    "s_waitcnt vmcnt(23) lgkmcnt(15)\n\t"
    "v_mfma_f32_16x16x32_bf16 v[16:19], a[0:3], a[112:115], v[16:19]\n\t"
    "s_barrier\n\t"
    "ds_read_b64_tr_b16 a[52:53], v10 offset:50368\n\t"
    "v_mfma_f32_16x16x32_bf16 v[20:23], a[0:3], a[116:119], v[20:23]\n\t"
    "ds_read_b64_tr_b16 a[54:55], v10 offset:52160\n\t"
    "v_mfma_f32_16x16x32_bf16 v[24:27], a[0:3], a[120:123], v[24:27]\n\t"
    "buffer_load_dwordx4 v3, s[56:59], s73 offen offset:0 lds\n\t"
    "v_mfma_f32_16x16x32_bf16 v[28:31], a[0:3], a[124:127], v[28:31]\n\t"
    "v_mfma_f32_16x16x32_bf16 v[32:35], a[4:7], a[112:115], v[32:35]\n\t"
    "ds_read_b64_tr_b16 a[0:1], v10 offset:57344\n\t"
    "v_mfma_f32_16x16x32_bf16 v[36:39], a[4:7], a[116:119], v[36:39]\n\t"
    "ds_read_b64_tr_b16 a[2:3], v10 offset:59136\n\t"
    "v_mfma_f32_16x16x32_bf16 v[40:43], a[4:7], a[120:123], v[40:43]\n\t"
    "v_mfma_f32_16x16x32_bf16 v[44:47], a[4:7], a[124:127], v[44:47]\n\t"
    "v_mfma_f32_16x16x32_bf16 v[48:51], a[8:11], a[112:115], v[48:51]\n\t"
    "ds_read_b64_tr_b16 a[4:5], v10 offset:57376\n\t"
    "v_mfma_f32_16x16x32_bf16 v[52:55], a[8:11], a[116:119], v[52:55]\n\t"
    "ds_read_b64_tr_b16 a[6:7], v10 offset:59168\n\t"
    "v_mfma_f32_16x16x32_bf16 v[56:59], a[8:11], a[120:123], v[56:59]\n\t"
    "buffer_load_dwordx4 v4, s[56:59], s73 offen offset:1024 lds\n\t"
    "v_mfma_f32_16x16x32_bf16 v[60:63], a[8:11], a[124:127], v[60:63]\n\t"
    "v_mfma_f32_16x16x32_bf16 v[64:67], a[12:15], a[112:115], v[64:67]\n\t"
    "ds_read_b64_tr_b16 a[8:9], v10 offset:57408\n\t"
    "v_mfma_f32_16x16x32_bf16 v[68:71], a[12:15], a[116:119], v[68:71]\n\t"
    "ds_read_b64_tr_b16 a[10:11], v10 offset:59200\n\t"
    "v_mfma_f32_16x16x32_bf16 v[72:75], a[12:15], a[120:123], v[72:75]\n\t"
    "v_mfma_f32_16x16x32_bf16 v[76:79], a[12:15], a[124:127], v[76:79]\n\t"
    "s_waitcnt lgkmcnt(15)\n\t"
    "v_mfma_f32_16x16x32_bf16 v[80:83], a[16:19], a[112:115], v[80:83]\n\t"
    "ds_read_b64_tr_b16 a[12:13], v10 offset:57440\n\t"
    "v_mfma_f32_16x16x32_bf16 v[84:87], a[16:19], a[116:119], v[84:87]\n\t"
    "ds_read_b64_tr_b16 a[14:15], v10 offset:59232\n\t"
    "v_mfma_f32_16x16x32_bf16 v[88:91], a[16:19], a[120:123], v[88:91]\n\t"
    "buffer_load_dwordx4 v5, s[56:59], s73 offen offset:2048 lds\n\t"
    "v_mfma_f32_16x16x32_bf16 v[92:95], a[16:19], a[124:127], v[92:95]\n\t"
    "v_mfma_f32_16x16x32_bf16 v[96:99], a[20:23], a[112:115], v[96:99]\n\t"
    "ds_read_b64_tr_b16 a[16:17], v10 offset:57472\n\t"
    "v_mfma_f32_16x16x32_bf16 v[100:103], a[20:23], a[116:119], v[100:103]\n\t"
    "ds_read_b64_tr_b16 a[18:19], v10 offset:59264\n\t"
    "v_mfma_f32_16x16x32_bf16 v[104:107], a[20:23], a[120:123], v[104:107]\n\t"
    "v_mfma_f32_16x16x32_bf16 v[108:111], a[20:23], a[124:127], v[108:111]\n\t"
    "v_mfma_f32_16x16x32_bf16 v[112:115], a[24:27], a[112:115], v[112:115]\n\t"
    "ds_read_b64_tr_b16 a[20:21], v10 offset:57504\n\t"
    "v_mfma_f32_16x16x32_bf16 v[116:119], a[24:27], a[116:119], v[116:119]\n\t"
    "ds_read_b64_tr_b16 a[22:23], v10 offset:59296\n\t"
    "v_mfma_f32_16x16x32_bf16 v[120:123], a[24:27], a[120:123], v[120:123]\n\t"
    "buffer_load_dwordx4 v6, s[56:59], s73 offen offset:3072 lds\n\t"
    "v_mfma_f32_16x16x32_bf16 v[124:127], a[24:27], a[124:127], v[124:127]\n\t"
    "v_mfma_f32_16x16x32_bf16 v[128:131], a[28:31], a[112:115], v[128:131]\n\t"
    "ds_read_b64_tr_b16 a[24:25], v10 offset:57536\n\t"
    "s_add_u32 m0, s74, 4096\n\t"
    "v_mfma_f32_16x16x32_bf16 v[132:135], a[28:31], a[116:119], v[132:135]\n\t"
    "ds_read_b64_tr_b16 a[26:27], v10 offset:59328\n\t"
    "v_mfma_f32_16x16x32_bf16 v[136:139], a[28:31], a[120:123], v[136:139]\n\t"
    "buffer_load_dwordx4 v7, s[56:59], s73 offen offset:0 lds\n\t"
    "v_mfma_f32_16x16x32_bf16 v[140:143], a[28:31], a[124:127], v[140:143]\n\t"
    "s_waitcnt lgkmcnt(15)\n\t"
    "v_mfma_f32_16x16x32_bf16 v[144:147], a[32:35], a[112:115], v[144:147]\n\t"
    "ds_read_b64_tr_b16 a[28:29], v11 offset:6144\n\t"
    "v_mfma_f32_16x16x32_bf16 v[148:151], a[32:35], a[116:119], v[148:151]\n\t"
    "ds_read_b64_tr_b16 a[30:31], v11 offset:7936\n\t"
    "v_mfma_f32_16x16x32_bf16 v[152:155], a[32:35], a[120:123], v[152:155]\n\t"
    "v_mfma_f32_16x16x32_bf16 v[156:159], a[32:35], a[124:127], v[156:159]\n\t"
    "v_mfma_f32_16x16x32_bf16 v[160:163], a[36:39], a[112:115], v[160:163]\n\t"
    "ds_read_b64_tr_b16 a[32:33], v11 offset:6176\n\t"
    "v_mfma_f32_16x16x32_bf16 v[164:167], a[36:39], a[116:119], v[164:167]\n\t"
    "ds_read_b64_tr_b16 a[34:35], v11 offset:7968\n\t"
    "v_mfma_f32_16x16x32_bf16 v[168:171], a[36:39], a[120:123], v[168:171]\n\t"
    "buffer_load_dwordx4 v8, s[56:59], s73 offen offset:1024 lds\n\t"
    "v_mfma_f32_16x16x32_bf16 v[172:175], a[36:39], a[124:127], v[172:175]\n\t"
    "v_mfma_f32_16x16x32_bf16 v[176:179], a[40:43], a[112:115], v[176:179]\n\t"
    "ds_read_b64_tr_b16 a[36:37], v11 offset:6208\n\t"
    "v_mfma_f32_16x16x32_bf16 v[180:183], a[40:43], a[116:119], v[180:183]\n\t"
    "ds_read_b64_tr_b16 a[38:39], v11 offset:8000\n\t"
    "v_mfma_f32_16x16x32_bf16 v[184:187], a[40:43], a[120:123], v[184:187]\n\t"
    "v_mfma_f32_16x16x32_bf16 v[188:191], a[40:43], a[124:127], v[188:191]\n\t"
    "v_mfma_f32_16x16x32_bf16 v[192:195], a[44:47], a[112:115], v[192:195]\n\t"
    "ds_read_b64_tr_b16 a[40:41], v11 offset:6240\n\t"
    "v_mfma_f32_16x16x32_bf16 v[196:199], a[44:47], a[116:119], v[196:199]\n\t"
    "ds_read_b64_tr_b16 a[42:43], v11 offset:8032\n\t"
    "v_mfma_f32_16x16x32_bf16 v[200:203], a[44:47], a[120:123], v[200:203]\n\t"
    "buffer_load_dwordx4 v9, s[56:59], s73 offen offset:2048 lds\n\t"
    "v_mfma_f32_16x16x32_bf16 v[204:207], a[44:47], a[124:127], v[204:207]\n\t"
    "s_waitcnt lgkmcnt(15)\n\t"
    "s_add_u32 s73, s73, 458752\n\t"
    "v_mfma_f32_16x16x32_bf16 v[208:211], a[48:51], a[112:115], v[208:211]\n\t"
    "ds_read_b64_tr_b16 a[44:45], v11 offset:6272\n\t"
    "v_mfma_f32_16x16x32_bf16 v[212:215], a[48:51], a[116:119], v[212:215]\n\t"
    "ds_read_b64_tr_b16 a[46:47], v11 offset:8064\n\t"
    "v_mfma_f32_16x16x32_bf16 v[216:219], a[48:51], a[120:123], v[216:219]\n\t"
    "v_mfma_f32_16x16x32_bf16 v[220:223], a[48:51], a[124:127], v[220:223]\n\t"
    "v_mfma_f32_16x16x32_bf16 v[224:227], a[52:55], a[112:115], v[224:227]\n\t"
    "ds_read_b64_tr_b16 a[48:49], v11 offset:6304\n\t"
    "v_mfma_f32_16x16x32_bf16 v[228:231], a[52:55], a[116:119], v[228:231]\n\t"
    "ds_read_b64_tr_b16 a[50:51], v11 offset:8096\n\t"
    "v_mfma_f32_16x16x32_bf16 v[232:235], a[52:55], a[120:123], v[232:235]\n\t"
    "buffer_load_dwordx4 a[64:67], v1, s[52:55], s72 offen offset:0 sc1\n\t"
    "v_mfma_f32_16x16x32_bf16 v[236:239], a[52:55], a[124:127], v[236:239]\n\t"
    "s_waitcnt vmcnt(27)\n\t"
    "v_mfma_f32_16x16x32_bf16 v[16:19], a[0:3], a[128:131], v[16:19]\n\t"
    "ds_read_b64_tr_b16 a[52:53], v11 offset:6336\n\t"
    "v_mfma_f32_16x16x32_bf16 v[20:23], a[0:3], a[132:135], v[20:23]\n\t"
    "ds_read_b64_tr_b16 a[54:55], v11 offset:8128\n\t"
    "v_mfma_f32_16x16x32_bf16 v[24:27], a[0:3], a[136:139], v[24:27]\n\t"
    "v_mfma_f32_16x16x32_bf16 v[28:31], a[0:3], a[140:143], v[28:31]\n\t"
    "buffer_load_dwordx4 a[68:71], v1, s[52:55], s72 offen offset:256 sc1\n\t"
    "v_mfma_f32_16x16x32_bf16 v[32:35], a[4:7], a[128:131], v[32:35]\n\t"
    "ds_read_b64_tr_b16 a[0:1], v10 offset:64512\n\t"
    "v_mfma_f32_16x16x32_bf16 v[36:39], a[4:7], a[132:135], v[36:39]\n\t"
    "ds_read_b64_tr_b16 a[2:3], v11 offset:768\n\t"
    "v_mfma_f32_16x16x32_bf16 v[40:43], a[4:7], a[136:139], v[40:43]\n\t"
    "v_mfma_f32_16x16x32_bf16 v[44:47], a[4:7], a[140:143], v[44:47]\n\t"
    "s_waitcnt lgkmcnt(15)\n\t"
    "v_mfma_f32_16x16x32_bf16 v[48:51], a[8:11], a[128:131], v[48:51]\n\t"
    "ds_read_b64_tr_b16 a[4:5], v10 offset:64544\n\t"
    "v_mfma_f32_16x16x32_bf16 v[52:55], a[8:11], a[132:135], v[52:55]\n\t"
    "ds_read_b64_tr_b16 a[6:7], v11 offset:800\n\t"
    "v_mfma_f32_16x16x32_bf16 v[56:59], a[8:11], a[136:139], v[56:59]\n\t"
    "v_mfma_f32_16x16x32_bf16 v[60:63], a[8:11], a[140:143], v[60:63]\n\t"
    "buffer_load_dwordx4 a[72:75], v2, s[52:55], s72 offen offset:0 sc1\n\t"
    "v_mfma_f32_16x16x32_bf16 v[64:67], a[12:15], a[128:131], v[64:67]\n\t"
    "ds_read_b64_tr_b16 a[8:9], v10 offset:64576\n\t"
    "v_mfma_f32_16x16x32_bf16 v[68:71], a[12:15], a[132:135], v[68:71]\n\t"
    "ds_read_b64_tr_b16 a[10:11], v11 offset:832\n\t"
    "v_mfma_f32_16x16x32_bf16 v[72:75], a[12:15], a[136:139], v[72:75]\n\t"
    "v_mfma_f32_16x16x32_bf16 v[76:79], a[12:15], a[140:143], v[76:79]\n\t"
    "v_mfma_f32_16x16x32_bf16 v[80:83], a[16:19], a[128:131], v[80:83]\n\t"
    "ds_read_b64_tr_b16 a[12:13], v10 offset:64608\n\t"
    "v_mfma_f32_16x16x32_bf16 v[84:87], a[16:19], a[132:135], v[84:87]\n\t"
    "ds_read_b64_tr_b16 a[14:15], v11 offset:864\n\t"
    "v_mfma_f32_16x16x32_bf16 v[88:91], a[16:19], a[136:139], v[88:91]\n\t"
    "buffer_load_dwordx4 a[76:79], v2, s[52:55], s72 offen offset:256 sc1\n\t"
    "v_mfma_f32_16x16x32_bf16 v[92:95], a[16:19], a[140:143], v[92:95]\n\t"
    "v_mfma_f32_16x16x32_bf16 v[96:99], a[20:23], a[128:131], v[96:99]\n\t"
    "ds_read_b64_tr_b16 a[16:17], v10 offset:64640\n\t"
    "v_mfma_f32_16x16x32_bf16 v[100:103], a[20:23], a[132:135], v[100:103]\n\t"
    "ds_read_b64_tr_b16 a[18:19], v11 offset:896\n\t"
    "v_mfma_f32_16x16x32_bf16 v[104:107], a[20:23], a[136:139], v[104:107]\n\t"
    "v_mfma_f32_16x16x32_bf16 v[108:111], a[20:23], a[140:143], v[108:111]\n\t"
    "s_waitcnt lgkmcnt(15)\n\t"
    "v_mfma_f32_16x16x32_bf16 v[112:115], a[24:27], a[128:131], v[112:115]\n\t"
    "ds_read_b64_tr_b16 a[20:21], v10 offset:64672\n\t"
    "v_mfma_f32_16x16x32_bf16 v[116:119], a[24:27], a[132:135], v[116:119]\n\t"
    "ds_read_b64_tr_b16 a[22:23], v11 offset:928\n\t"
    "v_mfma_f32_16x16x32_bf16 v[120:123], a[24:27], a[136:139], v[120:123]\n\t"
    "buffer_load_dwordx4 a[80:83], v1, s[52:55], s72 offen offset:2048 sc1\n\t"
    "v_mfma_f32_16x16x32_bf16 v[124:127], a[24:27], a[140:143], v[124:127]\n\t"
    "v_mfma_f32_16x16x32_bf16 v[128:131], a[28:31], a[128:131], v[128:131]\n\t"
    "ds_read_b64_tr_b16 a[24:25], v10 offset:64704\n\t"
    "v_mfma_f32_16x16x32_bf16 v[132:135], a[28:31], a[132:135], v[132:135]\n\t"
    "ds_read_b64_tr_b16 a[26:27], v11 offset:960\n\t"
    "v_mfma_f32_16x16x32_bf16 v[136:139], a[28:31], a[136:139], v[136:139]\n\t"
    "v_mfma_f32_16x16x32_bf16 v[140:143], a[28:31], a[140:143], v[140:143]\n\t"
    "v_mfma_f32_16x16x32_bf16 v[144:147], a[32:35], a[128:131], v[144:147]\n\t"
    "ds_read_b64_tr_b16 a[28:29], v11 offset:13312\n\t"
    "v_mfma_f32_16x16x32_bf16 v[148:151], a[32:35], a[132:135], v[148:151]\n\t"
    "ds_read_b64_tr_b16 a[30:31], v11 offset:15104\n\t"
    "v_mfma_f32_16x16x32_bf16 v[152:155], a[32:35], a[136:139], v[152:155]\n\t"
    "buffer_load_dwordx4 a[84:87], v1, s[52:55], s72 offen offset:2304 sc1\n\t"
    "v_mfma_f32_16x16x32_bf16 v[156:159], a[32:35], a[140:143], v[156:159]\n\t"
    "v_mfma_f32_16x16x32_bf16 v[160:163], a[36:39], a[128:131], v[160:163]\n\t"
    "ds_read_b64_tr_b16 a[32:33], v11 offset:13344\n\t"
    "v_mfma_f32_16x16x32_bf16 v[164:167], a[36:39], a[132:135], v[164:167]\n\t"
    "ds_read_b64_tr_b16 a[34:35], v11 offset:15136\n\t"
    "v_mfma_f32_16x16x32_bf16 v[168:171], a[36:39], a[136:139], v[168:171]\n\t"
    "v_mfma_f32_16x16x32_bf16 v[172:175], a[36:39], a[140:143], v[172:175]\n\t"
    "s_waitcnt lgkmcnt(15)\n\t"
    "v_mfma_f32_16x16x32_bf16 v[176:179], a[40:43], a[128:131], v[176:179]\n\t"
    "ds_read_b64_tr_b16 a[36:37], v11 offset:13376\n\t"
    "v_mfma_f32_16x16x32_bf16 v[180:183], a[40:43], a[132:135], v[180:183]\n\t"
    "ds_read_b64_tr_b16 a[38:39], v11 offset:15168\n\t"
    "v_mfma_f32_16x16x32_bf16 v[184:187], a[40:43], a[136:139], v[184:187]\n\t"
    "buffer_load_dwordx4 a[88:91], v2, s[52:55], s72 offen offset:2048 sc1\n\t"
    "v_mfma_f32_16x16x32_bf16 v[188:191], a[40:43], a[140:143], v[188:191]\n\t"
    "v_mfma_f32_16x16x32_bf16 v[192:195], a[44:47], a[128:131], v[192:195]\n\t"
    "ds_read_b64_tr_b16 a[40:41], v11 offset:13408\n\t"
    "v_mfma_f32_16x16x32_bf16 v[196:199], a[44:47], a[132:135], v[196:199]\n\t"
    "ds_read_b64_tr_b16 a[42:43], v11 offset:15200\n\t"
    "v_mfma_f32_16x16x32_bf16 v[200:203], a[44:47], a[136:139], v[200:203]\n\t"
    "v_mfma_f32_16x16x32_bf16 v[204:207], a[44:47], a[140:143], v[204:207]\n\t"
    "v_mfma_f32_16x16x32_bf16 v[208:211], a[48:51], a[128:131], v[208:211]\n\t"
    "ds_read_b64_tr_b16 a[44:45], v11 offset:13440\n\t"
    "v_mfma_f32_16x16x32_bf16 v[212:215], a[48:51], a[132:135], v[212:215]\n\t"
    "ds_read_b64_tr_b16 a[46:47], v11 offset:15232\n\t"
    "v_mfma_f32_16x16x32_bf16 v[216:219], a[48:51], a[136:139], v[216:219]\n\t"
    "buffer_load_dwordx4 a[92:95], v2, s[52:55], s72 offen offset:2304 sc1\n\t"
    "v_mfma_f32_16x16x32_bf16 v[220:223], a[48:51], a[140:143], v[220:223]\n\t"
    "v_mfma_f32_16x16x32_bf16 v[224:227], a[52:55], a[128:131], v[224:227]\n\t"
    "ds_read_b64_tr_b16 a[48:49], v11 offset:13472\n\t"
    "s_add_u32 s72, s72, 4096\n\t"
    "v_mfma_f32_16x16x32_bf16 v[228:231], a[52:55], a[132:135], v[228:231]\n\t"
    "ds_read_b64_tr_b16 a[50:51], v11 offset:15264\n\t"
    "s_add_u32 m0, s74, 28672\n\t"
    "v_mfma_f32_16x16x32_bf16 v[232:235], a[52:55], a[136:139], v[232:235]\n\t"
    "v_mfma_f32_16x16x32_bf16 v[236:239], a[52:55], a[140:143], v[236:239]\n\t"
    "s_waitcnt vmcnt(30) lgkmcnt(15)\n\t"
    "v_mfma_f32_16x16x32_bf16 v[16:19], a[0:3], a[144:147], v[16:19]\n\t"
    "s_waitcnt vmcnt(23)\n\t"
    "s_barrier\n\t"
    "ds_read_b64_tr_b16 a[52:53], v11 offset:13504\n\t"
    "v_mfma_f32_16x16x32_bf16 v[20:23], a[0:3], a[148:151], v[20:23]\n\t"
    "ds_read_b64_tr_b16 a[54:55], v11 offset:15296\n\t"
    "v_mfma_f32_16x16x32_bf16 v[24:27], a[0:3], a[152:155], v[24:27]\n\t"
    "buffer_load_dwordx4 v3, s[56:59], s73 offen offset:0 lds\n\t"
    "v_mfma_f32_16x16x32_bf16 v[28:31], a[0:3], a[156:159], v[28:31]\n\t"
    "v_mfma_f32_16x16x32_bf16 v[32:35], a[4:7], a[144:147], v[32:35]\n\t"
    "ds_read_b64_tr_b16 a[0:1], v11 offset:20480\n\t"
    "v_mfma_f32_16x16x32_bf16 v[36:39], a[4:7], a[148:151], v[36:39]\n\t"
    "ds_read_b64_tr_b16 a[2:3], v11 offset:22272\n\t"
    "v_mfma_f32_16x16x32_bf16 v[40:43], a[4:7], a[152:155], v[40:43]\n\t"
    "v_mfma_f32_16x16x32_bf16 v[44:47], a[4:7], a[156:159], v[44:47]\n\t"
    "v_mfma_f32_16x16x32_bf16 v[48:51], a[8:11], a[144:147], v[48:51]\n\t"
    "ds_read_b64_tr_b16 a[4:5], v11 offset:20512\n\t"
    "v_mfma_f32_16x16x32_bf16 v[52:55], a[8:11], a[148:151], v[52:55]\n\t"
    "ds_read_b64_tr_b16 a[6:7], v11 offset:22304\n\t"
    "v_mfma_f32_16x16x32_bf16 v[56:59], a[8:11], a[152:155], v[56:59]\n\t"
    "buffer_load_dwordx4 v4, s[56:59], s73 offen offset:1024 lds\n\t"
    "v_mfma_f32_16x16x32_bf16 v[60:63], a[8:11], a[156:159], v[60:63]\n\t"
    "v_mfma_f32_16x16x32_bf16 v[64:67], a[12:15], a[144:147], v[64:67]\n\t"
    "ds_read_b64_tr_b16 a[8:9], v11 offset:20544\n\t"
    "v_mfma_f32_16x16x32_bf16 v[68:71], a[12:15], a[148:151], v[68:71]\n\t"
    "ds_read_b64_tr_b16 a[10:11], v11 offset:22336\n\t"
    "v_mfma_f32_16x16x32_bf16 v[72:75], a[12:15], a[152:155], v[72:75]\n\t"
    "v_mfma_f32_16x16x32_bf16 v[76:79], a[12:15], a[156:159], v[76:79]\n\t"
    "s_waitcnt lgkmcnt(15)\n\t"
    "v_mfma_f32_16x16x32_bf16 v[80:83], a[16:19], a[144:147], v[80:83]\n\t"
    "ds_read_b64_tr_b16 a[12:13], v11 offset:20576\n\t"
    "v_mfma_f32_16x16x32_bf16 v[84:87], a[16:19], a[148:151], v[84:87]\n\t"
    "ds_read_b64_tr_b16 a[14:15], v11 offset:22368\n\t"
    "v_mfma_f32_16x16x32_bf16 v[88:91], a[16:19], a[152:155], v[88:91]\n\t"
    "buffer_load_dwordx4 v5, s[56:59], s73 offen offset:2048 lds\n\t"
    "v_mfma_f32_16x16x32_bf16 v[92:95], a[16:19], a[156:159], v[92:95]\n\t"
    "v_mfma_f32_16x16x32_bf16 v[96:99], a[20:23], a[144:147], v[96:99]\n\t"
    "ds_read_b64_tr_b16 a[16:17], v11 offset:20608\n\t"
    "v_mfma_f32_16x16x32_bf16 v[100:103], a[20:23], a[148:151], v[100:103]\n\t"
    "ds_read_b64_tr_b16 a[18:19], v11 offset:22400\n\t"
    "v_mfma_f32_16x16x32_bf16 v[104:107], a[20:23], a[152:155], v[104:107]\n\t"
    "v_mfma_f32_16x16x32_bf16 v[108:111], a[20:23], a[156:159], v[108:111]\n\t"
    "v_mfma_f32_16x16x32_bf16 v[112:115], a[24:27], a[144:147], v[112:115]\n\t"
    "ds_read_b64_tr_b16 a[20:21], v11 offset:20640\n\t"
    "v_mfma_f32_16x16x32_bf16 v[116:119], a[24:27], a[148:151], v[116:119]\n\t"
    "ds_read_b64_tr_b16 a[22:23], v11 offset:22432\n\t"
    "v_mfma_f32_16x16x32_bf16 v[120:123], a[24:27], a[152:155], v[120:123]\n\t"
    "buffer_load_dwordx4 v6, s[56:59], s73 offen offset:3072 lds\n\t"
    "v_mfma_f32_16x16x32_bf16 v[124:127], a[24:27], a[156:159], v[124:127]\n\t"
    "v_mfma_f32_16x16x32_bf16 v[128:131], a[28:31], a[144:147], v[128:131]\n\t"
    "ds_read_b64_tr_b16 a[24:25], v11 offset:20672\n\t"
    "s_add_u32 m0, s74, 32768\n\t"
    "v_mfma_f32_16x16x32_bf16 v[132:135], a[28:31], a[148:151], v[132:135]\n\t"
    "ds_read_b64_tr_b16 a[26:27], v11 offset:22464\n\t"
    "v_mfma_f32_16x16x32_bf16 v[136:139], a[28:31], a[152:155], v[136:139]\n\t"
    "buffer_load_dwordx4 v7, s[56:59], s73 offen offset:0 lds\n\t"
    "v_mfma_f32_16x16x32_bf16 v[140:143], a[28:31], a[156:159], v[140:143]\n\t"
    "s_waitcnt lgkmcnt(15)\n\t"
    "v_mfma_f32_16x16x32_bf16 v[144:147], a[32:35], a[144:147], v[144:147]\n\t"
    "ds_read_b64_tr_b16 a[28:29], v11 offset:34816\n\t"
    "v_mfma_f32_16x16x32_bf16 v[148:151], a[32:35], a[148:151], v[148:151]\n\t"
    "ds_read_b64_tr_b16 a[30:31], v11 offset:36608\n\t"
    "v_mfma_f32_16x16x32_bf16 v[152:155], a[32:35], a[152:155], v[152:155]\n\t"
    "v_mfma_f32_16x16x32_bf16 v[156:159], a[32:35], a[156:159], v[156:159]\n\t"
    "v_mfma_f32_16x16x32_bf16 v[160:163], a[36:39], a[144:147], v[160:163]\n\t"
    "ds_read_b64_tr_b16 a[32:33], v11 offset:34848\n\t"
    "v_mfma_f32_16x16x32_bf16 v[164:167], a[36:39], a[148:151], v[164:167]\n\t"
    "ds_read_b64_tr_b16 a[34:35], v11 offset:36640\n\t"
    "v_mfma_f32_16x16x32_bf16 v[168:171], a[36:39], a[152:155], v[168:171]\n\t"
    "buffer_load_dwordx4 v8, s[56:59], s73 offen offset:1024 lds\n\t"
    "v_mfma_f32_16x16x32_bf16 v[172:175], a[36:39], a[156:159], v[172:175]\n\t"
    "v_mfma_f32_16x16x32_bf16 v[176:179], a[40:43], a[144:147], v[176:179]\n\t"
    "ds_read_b64_tr_b16 a[36:37], v11 offset:34880\n\t"
    "v_mfma_f32_16x16x32_bf16 v[180:183], a[40:43], a[148:151], v[180:183]\n\t"
    "ds_read_b64_tr_b16 a[38:39], v11 offset:36672\n\t"
    "v_mfma_f32_16x16x32_bf16 v[184:187], a[40:43], a[152:155], v[184:187]\n\t"
    "v_mfma_f32_16x16x32_bf16 v[188:191], a[40:43], a[156:159], v[188:191]\n\t"
    "v_mfma_f32_16x16x32_bf16 v[192:195], a[44:47], a[144:147], v[192:195]\n\t"
    "ds_read_b64_tr_b16 a[40:41], v11 offset:34912\n\t"
    "v_mfma_f32_16x16x32_bf16 v[196:199], a[44:47], a[148:151], v[196:199]\n\t"
    "ds_read_b64_tr_b16 a[42:43], v11 offset:36704\n\t"
    "v_mfma_f32_16x16x32_bf16 v[200:203], a[44:47], a[152:155], v[200:203]\n\t"
    "buffer_load_dwordx4 v9, s[56:59], s73 offen offset:2048 lds\n\t"
    "v_mfma_f32_16x16x32_bf16 v[204:207], a[44:47], a[156:159], v[204:207]\n\t"
    "s_waitcnt lgkmcnt(15)\n\t"
    "s_add_u32 s73, s73, 458752\n\t"
    "v_mfma_f32_16x16x32_bf16 v[208:211], a[48:51], a[144:147], v[208:211]\n\t"
    "ds_read_b64_tr_b16 a[44:45], v11 offset:34944\n\t"
    "v_mfma_f32_16x16x32_bf16 v[212:215], a[48:51], a[148:151], v[212:215]\n\t"
    "ds_read_b64_tr_b16 a[46:47], v11 offset:36736\n\t"
    "v_mfma_f32_16x16x32_bf16 v[216:219], a[48:51], a[152:155], v[216:219]\n\t"
    "v_mfma_f32_16x16x32_bf16 v[220:223], a[48:51], a[156:159], v[220:223]\n\t"
    "v_mfma_f32_16x16x32_bf16 v[224:227], a[52:55], a[144:147], v[224:227]\n\t"
    "ds_read_b64_tr_b16 a[48:49], v11 offset:34976\n\t"
    "v_mfma_f32_16x16x32_bf16 v[228:231], a[52:55], a[148:151], v[228:231]\n\t"
    "ds_read_b64_tr_b16 a[50:51], v11 offset:36768\n\t"
    "v_mfma_f32_16x16x32_bf16 v[232:235], a[52:55], a[152:155], v[232:235]\n\t"
    "buffer_load_dwordx4 a[96:99], v1, s[52:55], s72 offen offset:0 sc1\n\t"
    "v_mfma_f32_16x16x32_bf16 v[236:239], a[52:55], a[156:159], v[236:239]\n\t"
    "s_waitcnt vmcnt(27)\n\t"
    "v_mfma_f32_16x16x32_bf16 v[16:19], a[0:3], a[160:163], v[16:19]\n\t"
    "ds_read_b64_tr_b16 a[52:53], v11 offset:35008\n\t"
    "v_mfma_f32_16x16x32_bf16 v[20:23], a[0:3], a[164:167], v[20:23]\n\t"
    "ds_read_b64_tr_b16 a[54:55], v11 offset:36800\n\t"
    "v_mfma_f32_16x16x32_bf16 v[24:27], a[0:3], a[168:171], v[24:27]\n\t"
    "v_mfma_f32_16x16x32_bf16 v[28:31], a[0:3], a[172:175], v[28:31]\n\t"
    "buffer_load_dwordx4 a[100:103], v1, s[52:55], s72 offen offset:256 sc1\n\t"
    "v_mfma_f32_16x16x32_bf16 v[32:35], a[4:7], a[160:163], v[32:35]\n\t"
    "ds_read_b64_tr_b16 a[0:1], v11 offset:27648\n\t"
    "v_mfma_f32_16x16x32_bf16 v[36:39], a[4:7], a[164:167], v[36:39]\n\t"
    "ds_read_b64_tr_b16 a[2:3], v11 offset:29440\n\t"
    "v_mfma_f32_16x16x32_bf16 v[40:43], a[4:7], a[168:171], v[40:43]\n\t"
    "v_mfma_f32_16x16x32_bf16 v[44:47], a[4:7], a[172:175], v[44:47]\n\t"
    "s_waitcnt lgkmcnt(15)\n\t"
    "v_mfma_f32_16x16x32_bf16 v[48:51], a[8:11], a[160:163], v[48:51]\n\t"
    "ds_read_b64_tr_b16 a[4:5], v11 offset:27680\n\t"
    "v_mfma_f32_16x16x32_bf16 v[52:55], a[8:11], a[164:167], v[52:55]\n\t"
    "ds_read_b64_tr_b16 a[6:7], v11 offset:29472\n\t"
    "v_mfma_f32_16x16x32_bf16 v[56:59], a[8:11], a[168:171], v[56:59]\n\t"
    "v_mfma_f32_16x16x32_bf16 v[60:63], a[8:11], a[172:175], v[60:63]\n\t"
    "buffer_load_dwordx4 a[104:107], v2, s[52:55], s72 offen offset:0 sc1\n\t"
    "v_mfma_f32_16x16x32_bf16 v[64:67], a[12:15], a[160:163], v[64:67]\n\t"
    "ds_read_b64_tr_b16 a[8:9], v11 offset:27712\n\t"
    "v_mfma_f32_16x16x32_bf16 v[68:71], a[12:15], a[164:167], v[68:71]\n\t"
    "ds_read_b64_tr_b16 a[10:11], v11 offset:29504\n\t"
    "v_mfma_f32_16x16x32_bf16 v[72:75], a[12:15], a[168:171], v[72:75]\n\t"
    "v_mfma_f32_16x16x32_bf16 v[76:79], a[12:15], a[172:175], v[76:79]\n\t"
    "v_mfma_f32_16x16x32_bf16 v[80:83], a[16:19], a[160:163], v[80:83]\n\t"
    "ds_read_b64_tr_b16 a[12:13], v11 offset:27744\n\t"
    "v_mfma_f32_16x16x32_bf16 v[84:87], a[16:19], a[164:167], v[84:87]\n\t"
    "ds_read_b64_tr_b16 a[14:15], v11 offset:29536\n\t"
    "v_mfma_f32_16x16x32_bf16 v[88:91], a[16:19], a[168:171], v[88:91]\n\t"
    "buffer_load_dwordx4 a[108:111], v2, s[52:55], s72 offen offset:256 sc1\n\t"
    "v_mfma_f32_16x16x32_bf16 v[92:95], a[16:19], a[172:175], v[92:95]\n\t"
    "v_mfma_f32_16x16x32_bf16 v[96:99], a[20:23], a[160:163], v[96:99]\n\t"
    "ds_read_b64_tr_b16 a[16:17], v11 offset:27776\n\t"
    "v_mfma_f32_16x16x32_bf16 v[100:103], a[20:23], a[164:167], v[100:103]\n\t"
    "ds_read_b64_tr_b16 a[18:19], v11 offset:29568\n\t"
    "v_mfma_f32_16x16x32_bf16 v[104:107], a[20:23], a[168:171], v[104:107]\n\t"
    "v_mfma_f32_16x16x32_bf16 v[108:111], a[20:23], a[172:175], v[108:111]\n\t"
    "s_waitcnt lgkmcnt(15)\n\t"
    "v_mfma_f32_16x16x32_bf16 v[112:115], a[24:27], a[160:163], v[112:115]\n\t"
    "ds_read_b64_tr_b16 a[20:21], v11 offset:27808\n\t"
    "v_mfma_f32_16x16x32_bf16 v[116:119], a[24:27], a[164:167], v[116:119]\n\t"
    "ds_read_b64_tr_b16 a[22:23], v11 offset:29600\n\t"
    "v_mfma_f32_16x16x32_bf16 v[120:123], a[24:27], a[168:171], v[120:123]\n\t"
    "buffer_load_dwordx4 a[112:115], v1, s[52:55], s72 offen offset:2048 sc1\n\t"
    "v_mfma_f32_16x16x32_bf16 v[124:127], a[24:27], a[172:175], v[124:127]\n\t"
    "v_mfma_f32_16x16x32_bf16 v[128:131], a[28:31], a[160:163], v[128:131]\n\t"
    "ds_read_b64_tr_b16 a[24:25], v11 offset:27840\n\t"
    "v_mfma_f32_16x16x32_bf16 v[132:135], a[28:31], a[164:167], v[132:135]\n\t"
    "ds_read_b64_tr_b16 a[26:27], v11 offset:29632\n\t"
    "v_mfma_f32_16x16x32_bf16 v[136:139], a[28:31], a[168:171], v[136:139]\n\t"
    "v_mfma_f32_16x16x32_bf16 v[140:143], a[28:31], a[172:175], v[140:143]\n\t"
    "v_mfma_f32_16x16x32_bf16 v[144:147], a[32:35], a[160:163], v[144:147]\n\t"
    "ds_read_b64_tr_b16 a[28:29], v11 offset:41984\n\t"
    "v_mfma_f32_16x16x32_bf16 v[148:151], a[32:35], a[164:167], v[148:151]\n\t"
    "ds_read_b64_tr_b16 a[30:31], v11 offset:43776\n\t"
    "v_mfma_f32_16x16x32_bf16 v[152:155], a[32:35], a[168:171], v[152:155]\n\t"
    "buffer_load_dwordx4 a[116:119], v1, s[52:55], s72 offen offset:2304 sc1\n\t"
    "v_mfma_f32_16x16x32_bf16 v[156:159], a[32:35], a[172:175], v[156:159]\n\t"
    "v_mfma_f32_16x16x32_bf16 v[160:163], a[36:39], a[160:163], v[160:163]\n\t"
    "ds_read_b64_tr_b16 a[32:33], v11 offset:42016\n\t"
    "v_mfma_f32_16x16x32_bf16 v[164:167], a[36:39], a[164:167], v[164:167]\n\t"
    "ds_read_b64_tr_b16 a[34:35], v11 offset:43808\n\t"
    "v_mfma_f32_16x16x32_bf16 v[168:171], a[36:39], a[168:171], v[168:171]\n\t"
    "v_mfma_f32_16x16x32_bf16 v[172:175], a[36:39], a[172:175], v[172:175]\n\t"
    "s_waitcnt lgkmcnt(15)\n\t"
    "v_mfma_f32_16x16x32_bf16 v[176:179], a[40:43], a[160:163], v[176:179]\n\t"
    "ds_read_b64_tr_b16 a[36:37], v11 offset:42048\n\t"
    "v_mfma_f32_16x16x32_bf16 v[180:183], a[40:43], a[164:167], v[180:183]\n\t"
    "ds_read_b64_tr_b16 a[38:39], v11 offset:43840\n\t"
    "v_mfma_f32_16x16x32_bf16 v[184:187], a[40:43], a[168:171], v[184:187]\n\t"
    "buffer_load_dwordx4 a[120:123], v2, s[52:55], s72 offen offset:2048 sc1\n\t"
    "v_mfma_f32_16x16x32_bf16 v[188:191], a[40:43], a[172:175], v[188:191]\n\t"
    "v_mfma_f32_16x16x32_bf16 v[192:195], a[44:47], a[160:163], v[192:195]\n\t"
    "ds_read_b64_tr_b16 a[40:41], v11 offset:42080\n\t"
    "v_mfma_f32_16x16x32_bf16 v[196:199], a[44:47], a[164:167], v[196:199]\n\t"
    "ds_read_b64_tr_b16 a[42:43], v11 offset:43872\n\t"
    "v_mfma_f32_16x16x32_bf16 v[200:203], a[44:47], a[168:171], v[200:203]\n\t"
    "v_mfma_f32_16x16x32_bf16 v[204:207], a[44:47], a[172:175], v[204:207]\n\t"
    "v_mfma_f32_16x16x32_bf16 v[208:211], a[48:51], a[160:163], v[208:211]\n\t"
    "ds_read_b64_tr_b16 a[44:45], v11 offset:42112\n\t"
    "v_mfma_f32_16x16x32_bf16 v[212:215], a[48:51], a[164:167], v[212:215]\n\t"
    "ds_read_b64_tr_b16 a[46:47], v11 offset:43904\n\t"
    "v_mfma_f32_16x16x32_bf16 v[216:219], a[48:51], a[168:171], v[216:219]\n\t"
    "buffer_load_dwordx4 a[124:127], v2, s[52:55], s72 offen offset:2304 sc1\n\t"
    "v_mfma_f32_16x16x32_bf16 v[220:223], a[48:51], a[172:175], v[220:223]\n\t"
    "v_mfma_f32_16x16x32_bf16 v[224:227], a[52:55], a[160:163], v[224:227]\n\t"
    "ds_read_b64_tr_b16 a[48:49], v11 offset:42144\n\t"
    "s_add_u32 s72, s72, 4096\n\t"
    "v_mfma_f32_16x16x32_bf16 v[228:231], a[52:55], a[164:167], v[228:231]\n\t"
    "ds_read_b64_tr_b16 a[50:51], v11 offset:43936\n\t"
    "s_add_u32 m0, s74, 57344\n\t"
    "v_mfma_f32_16x16x32_bf16 v[232:235], a[52:55], a[168:171], v[232:235]\n\t"
    "v_mfma_f32_16x16x32_bf16 v[236:239], a[52:55], a[172:175], v[236:239]\n\t"
    "s_waitcnt vmcnt(30) lgkmcnt(15)\n\t"
    "v_mfma_f32_16x16x32_bf16 v[16:19], a[0:3], a[176:179], v[16:19]\n\t"
    "s_waitcnt vmcnt(23)\n\t"
    "s_barrier\n\t"
    "ds_read_b64_tr_b16 a[52:53], v11 offset:42176\n\t"
    "v_mfma_f32_16x16x32_bf16 v[20:23], a[0:3], a[180:183], v[20:23]\n\t"
    "ds_read_b64_tr_b16 a[54:55], v11 offset:43968\n\t"
    "v_mfma_f32_16x16x32_bf16 v[24:27], a[0:3], a[184:187], v[24:27]\n\t"
    "buffer_load_dwordx4 v3, s[56:59], s73 offen offset:0 lds\n\t"
    "v_mfma_f32_16x16x32_bf16 v[28:31], a[0:3], a[188:191], v[28:31]\n\t"
    "v_mfma_f32_16x16x32_bf16 v[32:35], a[4:7], a[176:179], v[32:35]\n\t"
    "ds_read_b64_tr_b16 a[0:1], v10 offset:0\n\t"
    "v_mfma_f32_16x16x32_bf16 v[36:39], a[4:7], a[180:183], v[36:39]\n\t"
    "ds_read_b64_tr_b16 a[2:3], v10 offset:1792\n\t"
    "v_mfma_f32_16x16x32_bf16 v[40:43], a[4:7], a[184:187], v[40:43]\n\t"
    "v_mfma_f32_16x16x32_bf16 v[44:47], a[4:7], a[188:191], v[44:47]\n\t"
    "v_mfma_f32_16x16x32_bf16 v[48:51], a[8:11], a[176:179], v[48:51]\n\t"
    "ds_read_b64_tr_b16 a[4:5], v10 offset:32\n\t"
    "v_mfma_f32_16x16x32_bf16 v[52:55], a[8:11], a[180:183], v[52:55]\n\t"
    "ds_read_b64_tr_b16 a[6:7], v10 offset:1824\n\t"
    "v_mfma_f32_16x16x32_bf16 v[56:59], a[8:11], a[184:187], v[56:59]\n\t"
    "buffer_load_dwordx4 v4, s[56:59], s73 offen offset:1024 lds\n\t"
    "v_mfma_f32_16x16x32_bf16 v[60:63], a[8:11], a[188:191], v[60:63]\n\t"
    "v_mfma_f32_16x16x32_bf16 v[64:67], a[12:15], a[176:179], v[64:67]\n\t"
    "ds_read_b64_tr_b16 a[8:9], v10 offset:64\n\t"
    "v_mfma_f32_16x16x32_bf16 v[68:71], a[12:15], a[180:183], v[68:71]\n\t"
    "ds_read_b64_tr_b16 a[10:11], v10 offset:1856\n\t"
    "v_mfma_f32_16x16x32_bf16 v[72:75], a[12:15], a[184:187], v[72:75]\n\t"
    "v_mfma_f32_16x16x32_bf16 v[76:79], a[12:15], a[188:191], v[76:79]\n\t"
    "s_waitcnt lgkmcnt(15)\n\t"
    "v_mfma_f32_16x16x32_bf16 v[80:83], a[16:19], a[176:179], v[80:83]\n\t"
    "ds_read_b64_tr_b16 a[12:13], v10 offset:96\n\t"
    "v_mfma_f32_16x16x32_bf16 v[84:87], a[16:19], a[180:183], v[84:87]\n\t"
    "ds_read_b64_tr_b16 a[14:15], v10 offset:1888\n\t"
    "v_mfma_f32_16x16x32_bf16 v[88:91], a[16:19], a[184:187], v[88:91]\n\t"
    "buffer_load_dwordx4 v5, s[56:59], s73 offen offset:2048 lds\n\t"
    "v_mfma_f32_16x16x32_bf16 v[92:95], a[16:19], a[188:191], v[92:95]\n\t"
    "v_mfma_f32_16x16x32_bf16 v[96:99], a[20:23], a[176:179], v[96:99]\n\t"
    "ds_read_b64_tr_b16 a[16:17], v10 offset:128\n\t"
    "v_mfma_f32_16x16x32_bf16 v[100:103], a[20:23], a[180:183], v[100:103]\n\t"
    "ds_read_b64_tr_b16 a[18:19], v10 offset:1920\n\t"
    "v_mfma_f32_16x16x32_bf16 v[104:107], a[20:23], a[184:187], v[104:107]\n\t"
    "v_mfma_f32_16x16x32_bf16 v[108:111], a[20:23], a[188:191], v[108:111]\n\t"
    "v_mfma_f32_16x16x32_bf16 v[112:115], a[24:27], a[176:179], v[112:115]\n\t"
    "ds_read_b64_tr_b16 a[20:21], v10 offset:160\n\t"
    "v_mfma_f32_16x16x32_bf16 v[116:119], a[24:27], a[180:183], v[116:119]\n\t"
    "ds_read_b64_tr_b16 a[22:23], v10 offset:1952\n\t"
    "v_mfma_f32_16x16x32_bf16 v[120:123], a[24:27], a[184:187], v[120:123]\n\t"
    "buffer_load_dwordx4 v6, s[56:59], s73 offen offset:3072 lds\n\t"
    "v_mfma_f32_16x16x32_bf16 v[124:127], a[24:27], a[188:191], v[124:127]\n\t"
    "v_mfma_f32_16x16x32_bf16 v[128:131], a[28:31], a[176:179], v[128:131]\n\t"
    "ds_read_b64_tr_b16 a[24:25], v10 offset:192\n\t"
    "s_add_u32 m0, s74, 61440\n\t"
    "v_mfma_f32_16x16x32_bf16 v[132:135], a[28:31], a[180:183], v[132:135]\n\t"
    "ds_read_b64_tr_b16 a[26:27], v10 offset:1984\n\t"
    "v_mfma_f32_16x16x32_bf16 v[136:139], a[28:31], a[184:187], v[136:139]\n\t"
    "buffer_load_dwordx4 v7, s[56:59], s73 offen offset:0 lds\n\t"
    "v_mfma_f32_16x16x32_bf16 v[140:143], a[28:31], a[188:191], v[140:143]\n\t"
    "s_waitcnt lgkmcnt(15)\n\t"
    "v_mfma_f32_16x16x32_bf16 v[144:147], a[32:35], a[176:179], v[144:147]\n\t"
    "ds_read_b64_tr_b16 a[28:29], v10 offset:14336\n\t"
    "v_mfma_f32_16x16x32_bf16 v[148:151], a[32:35], a[180:183], v[148:151]\n\t"
    "ds_read_b64_tr_b16 a[30:31], v10 offset:16128\n\t"
    "v_mfma_f32_16x16x32_bf16 v[152:155], a[32:35], a[184:187], v[152:155]\n\t"
    "v_mfma_f32_16x16x32_bf16 v[156:159], a[32:35], a[188:191], v[156:159]\n\t"
    "v_mfma_f32_16x16x32_bf16 v[160:163], a[36:39], a[176:179], v[160:163]\n\t"
    "ds_read_b64_tr_b16 a[32:33], v10 offset:14368\n\t"
    "v_mfma_f32_16x16x32_bf16 v[164:167], a[36:39], a[180:183], v[164:167]\n\t"
    "ds_read_b64_tr_b16 a[34:35], v10 offset:16160\n\t"
    "v_mfma_f32_16x16x32_bf16 v[168:171], a[36:39], a[184:187], v[168:171]\n\t"
    "buffer_load_dwordx4 v8, s[56:59], s73 offen offset:1024 lds\n\t"
    "v_mfma_f32_16x16x32_bf16 v[172:175], a[36:39], a[188:191], v[172:175]\n\t"
    "v_mfma_f32_16x16x32_bf16 v[176:179], a[40:43], a[176:179], v[176:179]\n\t"
    "ds_read_b64_tr_b16 a[36:37], v10 offset:14400\n\t"
    "v_mfma_f32_16x16x32_bf16 v[180:183], a[40:43], a[180:183], v[180:183]\n\t"
    "ds_read_b64_tr_b16 a[38:39], v10 offset:16192\n\t"
    "v_mfma_f32_16x16x32_bf16 v[184:187], a[40:43], a[184:187], v[184:187]\n\t"
    "v_mfma_f32_16x16x32_bf16 v[188:191], a[40:43], a[188:191], v[188:191]\n\t"
    "v_mfma_f32_16x16x32_bf16 v[192:195], a[44:47], a[176:179], v[192:195]\n\t"
    "ds_read_b64_tr_b16 a[40:41], v10 offset:14432\n\t"
    "v_mfma_f32_16x16x32_bf16 v[196:199], a[44:47], a[180:183], v[196:199]\n\t"
    "ds_read_b64_tr_b16 a[42:43], v10 offset:16224\n\t"
    "v_mfma_f32_16x16x32_bf16 v[200:203], a[44:47], a[184:187], v[200:203]\n\t"
    "buffer_load_dwordx4 v9, s[56:59], s73 offen offset:2048 lds\n\t"
    "v_mfma_f32_16x16x32_bf16 v[204:207], a[44:47], a[188:191], v[204:207]\n\t"
    "s_waitcnt lgkmcnt(15)\n\t"
    "s_add_u32 s73, s73, 458752\n\t"
    "v_mfma_f32_16x16x32_bf16 v[208:211], a[48:51], a[176:179], v[208:211]\n\t"
    "ds_read_b64_tr_b16 a[44:45], v10 offset:14464\n\t"
    "v_mfma_f32_16x16x32_bf16 v[212:215], a[48:51], a[180:183], v[212:215]\n\t"
    "ds_read_b64_tr_b16 a[46:47], v10 offset:16256\n\t"
    "v_mfma_f32_16x16x32_bf16 v[216:219], a[48:51], a[184:187], v[216:219]\n\t"
    "v_mfma_f32_16x16x32_bf16 v[220:223], a[48:51], a[188:191], v[220:223]\n\t"
    "v_mfma_f32_16x16x32_bf16 v[224:227], a[52:55], a[176:179], v[224:227]\n\t"
    "ds_read_b64_tr_b16 a[48:49], v10 offset:14496\n\t"
    "v_mfma_f32_16x16x32_bf16 v[228:231], a[52:55], a[180:183], v[228:231]\n\t"
    "ds_read_b64_tr_b16 a[50:51], v10 offset:16288\n\t"
    "v_mfma_f32_16x16x32_bf16 v[232:235], a[52:55], a[184:187], v[232:235]\n\t"
    "buffer_load_dwordx4 a[128:131], v1, s[52:55], s72 offen offset:0 sc1\n\t"
    "v_mfma_f32_16x16x32_bf16 v[236:239], a[52:55], a[188:191], v[236:239]\n\t"
    "s_waitcnt vmcnt(27)\n\t"
    "v_mfma_f32_16x16x32_bf16 v[16:19], a[0:3], a[64:67], v[16:19]\n\t"
    "ds_read_b64_tr_b16 a[52:53], v10 offset:14528\n\t"
    "v_mfma_f32_16x16x32_bf16 v[20:23], a[0:3], a[68:71], v[20:23]\n\t"
    "ds_read_b64_tr_b16 a[54:55], v10 offset:16320\n\t"
    "v_mfma_f32_16x16x32_bf16 v[24:27], a[0:3], a[72:75], v[24:27]\n\t"
    "v_mfma_f32_16x16x32_bf16 v[28:31], a[0:3], a[76:79], v[28:31]\n\t"
    "buffer_load_dwordx4 a[132:135], v1, s[52:55], s72 offen offset:256 sc1\n\t"
    "v_mfma_f32_16x16x32_bf16 v[32:35], a[4:7], a[64:67], v[32:35]\n\t"
    "ds_read_b64_tr_b16 a[0:1], v10 offset:7168\n\t"
    "v_mfma_f32_16x16x32_bf16 v[36:39], a[4:7], a[68:71], v[36:39]\n\t"
    "ds_read_b64_tr_b16 a[2:3], v10 offset:8960\n\t"
    "v_mfma_f32_16x16x32_bf16 v[40:43], a[4:7], a[72:75], v[40:43]\n\t"
    "v_mfma_f32_16x16x32_bf16 v[44:47], a[4:7], a[76:79], v[44:47]\n\t"
    "s_waitcnt lgkmcnt(15)\n\t"
    "v_mfma_f32_16x16x32_bf16 v[48:51], a[8:11], a[64:67], v[48:51]\n\t"
    "ds_read_b64_tr_b16 a[4:5], v10 offset:7200\n\t"
    "v_mfma_f32_16x16x32_bf16 v[52:55], a[8:11], a[68:71], v[52:55]\n\t"
    "ds_read_b64_tr_b16 a[6:7], v10 offset:8992\n\t"
    "v_mfma_f32_16x16x32_bf16 v[56:59], a[8:11], a[72:75], v[56:59]\n\t"
    "v_mfma_f32_16x16x32_bf16 v[60:63], a[8:11], a[76:79], v[60:63]\n\t"
    "buffer_load_dwordx4 a[136:139], v2, s[52:55], s72 offen offset:0 sc1\n\t"
    "v_mfma_f32_16x16x32_bf16 v[64:67], a[12:15], a[64:67], v[64:67]\n\t"
    "ds_read_b64_tr_b16 a[8:9], v10 offset:7232\n\t"
    "v_mfma_f32_16x16x32_bf16 v[68:71], a[12:15], a[68:71], v[68:71]\n\t"
    "ds_read_b64_tr_b16 a[10:11], v10 offset:9024\n\t"
    "v_mfma_f32_16x16x32_bf16 v[72:75], a[12:15], a[72:75], v[72:75]\n\t"
    "v_mfma_f32_16x16x32_bf16 v[76:79], a[12:15], a[76:79], v[76:79]\n\t"
    "v_mfma_f32_16x16x32_bf16 v[80:83], a[16:19], a[64:67], v[80:83]\n\t"
    "ds_read_b64_tr_b16 a[12:13], v10 offset:7264\n\t"
    "v_mfma_f32_16x16x32_bf16 v[84:87], a[16:19], a[68:71], v[84:87]\n\t"
    "ds_read_b64_tr_b16 a[14:15], v10 offset:9056\n\t"
    "v_mfma_f32_16x16x32_bf16 v[88:91], a[16:19], a[72:75], v[88:91]\n\t"
    "buffer_load_dwordx4 a[140:143], v2, s[52:55], s72 offen offset:256 sc1\n\t"
    "v_mfma_f32_16x16x32_bf16 v[92:95], a[16:19], a[76:79], v[92:95]\n\t"
    "v_mfma_f32_16x16x32_bf16 v[96:99], a[20:23], a[64:67], v[96:99]\n\t"
    "ds_read_b64_tr_b16 a[16:17], v10 offset:7296\n\t"
    "v_mfma_f32_16x16x32_bf16 v[100:103], a[20:23], a[68:71], v[100:103]\n\t"
    "ds_read_b64_tr_b16 a[18:19], v10 offset:9088\n\t"
    "v_mfma_f32_16x16x32_bf16 v[104:107], a[20:23], a[72:75], v[104:107]\n\t"
    "v_mfma_f32_16x16x32_bf16 v[108:111], a[20:23], a[76:79], v[108:111]\n\t"
    "s_waitcnt lgkmcnt(15)\n\t"
    "v_mfma_f32_16x16x32_bf16 v[112:115], a[24:27], a[64:67], v[112:115]\n\t"
    "ds_read_b64_tr_b16 a[20:21], v10 offset:7328\n\t"
    "v_mfma_f32_16x16x32_bf16 v[116:119], a[24:27], a[68:71], v[116:119]\n\t"
    "ds_read_b64_tr_b16 a[22:23], v10 offset:9120\n\t"
    "v_mfma_f32_16x16x32_bf16 v[120:123], a[24:27], a[72:75], v[120:123]\n\t"
    "buffer_load_dwordx4 a[144:147], v1, s[52:55], s72 offen offset:2048 sc1\n\t"
    "v_mfma_f32_16x16x32_bf16 v[124:127], a[24:27], a[76:79], v[124:127]\n\t"
    "v_mfma_f32_16x16x32_bf16 v[128:131], a[28:31], a[64:67], v[128:131]\n\t"
    "ds_read_b64_tr_b16 a[24:25], v10 offset:7360\n\t"
    "v_mfma_f32_16x16x32_bf16 v[132:135], a[28:31], a[68:71], v[132:135]\n\t"
    "ds_read_b64_tr_b16 a[26:27], v10 offset:9152\n\t"
    "v_mfma_f32_16x16x32_bf16 v[136:139], a[28:31], a[72:75], v[136:139]\n\t"
    "v_mfma_f32_16x16x32_bf16 v[140:143], a[28:31], a[76:79], v[140:143]\n\t"
    "v_mfma_f32_16x16x32_bf16 v[144:147], a[32:35], a[64:67], v[144:147]\n\t"
    "ds_read_b64_tr_b16 a[28:29], v10 offset:21504\n\t"
    "v_mfma_f32_16x16x32_bf16 v[148:151], a[32:35], a[68:71], v[148:151]\n\t"
    "ds_read_b64_tr_b16 a[30:31], v10 offset:23296\n\t"
    "v_mfma_f32_16x16x32_bf16 v[152:155], a[32:35], a[72:75], v[152:155]\n\t"
    "buffer_load_dwordx4 a[148:151], v1, s[52:55], s72 offen offset:2304 sc1\n\t"
    "v_mfma_f32_16x16x32_bf16 v[156:159], a[32:35], a[76:79], v[156:159]\n\t"
    "v_mfma_f32_16x16x32_bf16 v[160:163], a[36:39], a[64:67], v[160:163]\n\t"
    "ds_read_b64_tr_b16 a[32:33], v10 offset:21536\n\t"
    "v_mfma_f32_16x16x32_bf16 v[164:167], a[36:39], a[68:71], v[164:167]\n\t"
    "ds_read_b64_tr_b16 a[34:35], v10 offset:23328\n\t"
    "v_mfma_f32_16x16x32_bf16 v[168:171], a[36:39], a[72:75], v[168:171]\n\t"
    "v_mfma_f32_16x16x32_bf16 v[172:175], a[36:39], a[76:79], v[172:175]\n\t"
    "s_waitcnt lgkmcnt(15)\n\t"
    "v_mfma_f32_16x16x32_bf16 v[176:179], a[40:43], a[64:67], v[176:179]\n\t"
    "ds_read_b64_tr_b16 a[36:37], v10 offset:21568\n\t"
    "v_mfma_f32_16x16x32_bf16 v[180:183], a[40:43], a[68:71], v[180:183]\n\t"
    "ds_read_b64_tr_b16 a[38:39], v10 offset:23360\n\t"
    "v_mfma_f32_16x16x32_bf16 v[184:187], a[40:43], a[72:75], v[184:187]\n\t"
    "buffer_load_dwordx4 a[152:155], v2, s[52:55], s72 offen offset:2048 sc1\n\t"
    "v_mfma_f32_16x16x32_bf16 v[188:191], a[40:43], a[76:79], v[188:191]\n\t"
    "v_mfma_f32_16x16x32_bf16 v[192:195], a[44:47], a[64:67], v[192:195]\n\t"
    "ds_read_b64_tr_b16 a[40:41], v10 offset:21600\n\t"
    "v_mfma_f32_16x16x32_bf16 v[196:199], a[44:47], a[68:71], v[196:199]\n\t"
    "ds_read_b64_tr_b16 a[42:43], v10 offset:23392\n\t"
    "v_mfma_f32_16x16x32_bf16 v[200:203], a[44:47], a[72:75], v[200:203]\n\t"
    "v_mfma_f32_16x16x32_bf16 v[204:207], a[44:47], a[76:79], v[204:207]\n\t"
    "v_mfma_f32_16x16x32_bf16 v[208:211], a[48:51], a[64:67], v[208:211]\n\t"
    "ds_read_b64_tr_b16 a[44:45], v10 offset:21632\n\t"
    "v_mfma_f32_16x16x32_bf16 v[212:215], a[48:51], a[68:71], v[212:215]\n\t"
    "ds_read_b64_tr_b16 a[46:47], v10 offset:23424\n\t"
    "v_mfma_f32_16x16x32_bf16 v[216:219], a[48:51], a[72:75], v[216:219]\n\t"
    "buffer_load_dwordx4 a[156:159], v2, s[52:55], s72 offen offset:2304 sc1\n\t"
    "v_mfma_f32_16x16x32_bf16 v[220:223], a[48:51], a[76:79], v[220:223]\n\t"
    "v_mfma_f32_16x16x32_bf16 v[224:227], a[52:55], a[64:67], v[224:227]\n\t"
    "ds_read_b64_tr_b16 a[48:49], v10 offset:21664\n\t"
    "s_add_u32 s72, s72, 4096\n\t"
    "v_mfma_f32_16x16x32_bf16 v[228:231], a[52:55], a[68:71], v[228:231]\n\t"
    "ds_read_b64_tr_b16 a[50:51], v10 offset:23456\n\t"
    "s_add_u32 m0, s74, 86016\n\t"
    "v_mfma_f32_16x16x32_bf16 v[232:235], a[52:55], a[72:75], v[232:235]\n\t"
    "v_mfma_f32_16x16x32_bf16 v[236:239], a[52:55], a[76:79], v[236:239]\n\t"
    "s_waitcnt vmcnt(30) lgkmcnt(15)\n\t"
    "v_mfma_f32_16x16x32_bf16 v[16:19], a[0:3], a[80:83], v[16:19]\n\t"
    "s_waitcnt vmcnt(23)\n\t"
    "s_barrier\n\t"
    "ds_read_b64_tr_b16 a[52:53], v10 offset:21696\n\t"
    "v_mfma_f32_16x16x32_bf16 v[20:23], a[0:3], a[84:87], v[20:23]\n\t"
    "ds_read_b64_tr_b16 a[54:55], v10 offset:23488\n\t"
    "v_mfma_f32_16x16x32_bf16 v[24:27], a[0:3], a[88:91], v[24:27]\n\t"
    "buffer_load_dwordx4 v3, s[56:59], s73 offen offset:0 lds\n\t"
    "v_mfma_f32_16x16x32_bf16 v[28:31], a[0:3], a[92:95], v[28:31]\n\t"
    "v_mfma_f32_16x16x32_bf16 v[32:35], a[4:7], a[80:83], v[32:35]\n\t"
    "ds_read_b64_tr_b16 a[0:1], v10 offset:28672\n\t"
    "v_mfma_f32_16x16x32_bf16 v[36:39], a[4:7], a[84:87], v[36:39]\n\t"
    "ds_read_b64_tr_b16 a[2:3], v10 offset:30464\n\t"
    "v_mfma_f32_16x16x32_bf16 v[40:43], a[4:7], a[88:91], v[40:43]\n\t"
    "v_mfma_f32_16x16x32_bf16 v[44:47], a[4:7], a[92:95], v[44:47]\n\t"
    "v_mfma_f32_16x16x32_bf16 v[48:51], a[8:11], a[80:83], v[48:51]\n\t"
    "ds_read_b64_tr_b16 a[4:5], v10 offset:28704\n\t"
    "v_mfma_f32_16x16x32_bf16 v[52:55], a[8:11], a[84:87], v[52:55]\n\t"
    "ds_read_b64_tr_b16 a[6:7], v10 offset:30496\n\t"
    "v_mfma_f32_16x16x32_bf16 v[56:59], a[8:11], a[88:91], v[56:59]\n\t"
    "buffer_load_dwordx4 v4, s[56:59], s73 offen offset:1024 lds\n\t"
    "v_mfma_f32_16x16x32_bf16 v[60:63], a[8:11], a[92:95], v[60:63]\n\t"
    "v_mfma_f32_16x16x32_bf16 v[64:67], a[12:15], a[80:83], v[64:67]\n\t"
    "ds_read_b64_tr_b16 a[8:9], v10 offset:28736\n\t"
    "v_mfma_f32_16x16x32_bf16 v[68:71], a[12:15], a[84:87], v[68:71]\n\t"
    "ds_read_b64_tr_b16 a[10:11], v10 offset:30528\n\t"
    "v_mfma_f32_16x16x32_bf16 v[72:75], a[12:15], a[88:91], v[72:75]\n\t"
    "v_mfma_f32_16x16x32_bf16 v[76:79], a[12:15], a[92:95], v[76:79]\n\t"
    "s_waitcnt lgkmcnt(15)\n\t"
    "v_mfma_f32_16x16x32_bf16 v[80:83], a[16:19], a[80:83], v[80:83]\n\t"
    "ds_read_b64_tr_b16 a[12:13], v10 offset:28768\n\t"
    "v_mfma_f32_16x16x32_bf16 v[84:87], a[16:19], a[84:87], v[84:87]\n\t"
    "ds_read_b64_tr_b16 a[14:15], v10 offset:30560\n\t"
    "v_mfma_f32_16x16x32_bf16 v[88:91], a[16:19], a[88:91], v[88:91]\n\t"
    "buffer_load_dwordx4 v5, s[56:59], s73 offen offset:2048 lds\n\t"
    "v_mfma_f32_16x16x32_bf16 v[92:95], a[16:19], a[92:95], v[92:95]\n\t"
    "v_mfma_f32_16x16x32_bf16 v[96:99], a[20:23], a[80:83], v[96:99]\n\t"
    "ds_read_b64_tr_b16 a[16:17], v10 offset:28800\n\t"
    "v_mfma_f32_16x16x32_bf16 v[100:103], a[20:23], a[84:87], v[100:103]\n\t"
    "ds_read_b64_tr_b16 a[18:19], v10 offset:30592\n\t"
    "v_mfma_f32_16x16x32_bf16 v[104:107], a[20:23], a[88:91], v[104:107]\n\t"
    "v_mfma_f32_16x16x32_bf16 v[108:111], a[20:23], a[92:95], v[108:111]\n\t"
    "v_mfma_f32_16x16x32_bf16 v[112:115], a[24:27], a[80:83], v[112:115]\n\t"
    "ds_read_b64_tr_b16 a[20:21], v10 offset:28832\n\t"
    "v_mfma_f32_16x16x32_bf16 v[116:119], a[24:27], a[84:87], v[116:119]\n\t"
    "ds_read_b64_tr_b16 a[22:23], v10 offset:30624\n\t"
    "v_mfma_f32_16x16x32_bf16 v[120:123], a[24:27], a[88:91], v[120:123]\n\t"
    "buffer_load_dwordx4 v6, s[56:59], s73 offen offset:3072 lds\n\t"
    "v_mfma_f32_16x16x32_bf16 v[124:127], a[24:27], a[92:95], v[124:127]\n\t"
    "v_mfma_f32_16x16x32_bf16 v[128:131], a[28:31], a[80:83], v[128:131]\n\t"
    "ds_read_b64_tr_b16 a[24:25], v10 offset:28864\n\t"
    "s_add_u32 m0, s74, 90112\n\t"
    "v_mfma_f32_16x16x32_bf16 v[132:135], a[28:31], a[84:87], v[132:135]\n\t"
    "ds_read_b64_tr_b16 a[26:27], v10 offset:30656\n\t"
    "v_mfma_f32_16x16x32_bf16 v[136:139], a[28:31], a[88:91], v[136:139]\n\t"
    "buffer_load_dwordx4 v7, s[56:59], s73 offen offset:0 lds\n\t"
    "v_mfma_f32_16x16x32_bf16 v[140:143], a[28:31], a[92:95], v[140:143]\n\t"
    "s_waitcnt lgkmcnt(15)\n\t"
    "v_mfma_f32_16x16x32_bf16 v[144:147], a[32:35], a[80:83], v[144:147]\n\t"
    "ds_read_b64_tr_b16 a[28:29], v10 offset:43008\n\t"
    "v_mfma_f32_16x16x32_bf16 v[148:151], a[32:35], a[84:87], v[148:151]\n\t"
    "ds_read_b64_tr_b16 a[30:31], v10 offset:44800\n\t"
    "v_mfma_f32_16x16x32_bf16 v[152:155], a[32:35], a[88:91], v[152:155]\n\t"
    "v_mfma_f32_16x16x32_bf16 v[156:159], a[32:35], a[92:95], v[156:159]\n\t"
    "v_mfma_f32_16x16x32_bf16 v[160:163], a[36:39], a[80:83], v[160:163]\n\t"
    "ds_read_b64_tr_b16 a[32:33], v10 offset:43040\n\t"
    "v_mfma_f32_16x16x32_bf16 v[164:167], a[36:39], a[84:87], v[164:167]\n\t"
    "ds_read_b64_tr_b16 a[34:35], v10 offset:44832\n\t"
    "v_mfma_f32_16x16x32_bf16 v[168:171], a[36:39], a[88:91], v[168:171]\n\t"
    "buffer_load_dwordx4 v8, s[56:59], s73 offen offset:1024 lds\n\t"
    "v_mfma_f32_16x16x32_bf16 v[172:175], a[36:39], a[92:95], v[172:175]\n\t"
    "v_mfma_f32_16x16x32_bf16 v[176:179], a[40:43], a[80:83], v[176:179]\n\t"
    "ds_read_b64_tr_b16 a[36:37], v10 offset:43072\n\t"
    "v_mfma_f32_16x16x32_bf16 v[180:183], a[40:43], a[84:87], v[180:183]\n\t"
    "ds_read_b64_tr_b16 a[38:39], v10 offset:44864\n\t"
    "v_mfma_f32_16x16x32_bf16 v[184:187], a[40:43], a[88:91], v[184:187]\n\t"
    "v_mfma_f32_16x16x32_bf16 v[188:191], a[40:43], a[92:95], v[188:191]\n\t"
    "v_mfma_f32_16x16x32_bf16 v[192:195], a[44:47], a[80:83], v[192:195]\n\t"
    "ds_read_b64_tr_b16 a[40:41], v10 offset:43104\n\t"
    "v_mfma_f32_16x16x32_bf16 v[196:199], a[44:47], a[84:87], v[196:199]\n\t"
    "ds_read_b64_tr_b16 a[42:43], v10 offset:44896\n\t"
    "v_mfma_f32_16x16x32_bf16 v[200:203], a[44:47], a[88:91], v[200:203]\n\t"
    "buffer_load_dwordx4 v9, s[56:59], s73 offen offset:2048 lds\n\t"
    "v_mfma_f32_16x16x32_bf16 v[204:207], a[44:47], a[92:95], v[204:207]\n\t"
    "s_waitcnt lgkmcnt(15)\n\t"
    "s_add_u32 s73, s73, 458752\n\t"
    "v_mfma_f32_16x16x32_bf16 v[208:211], a[48:51], a[80:83], v[208:211]\n\t"
    "ds_read_b64_tr_b16 a[44:45], v10 offset:43136\n\t"
    "v_mfma_f32_16x16x32_bf16 v[212:215], a[48:51], a[84:87], v[212:215]\n\t"
    "ds_read_b64_tr_b16 a[46:47], v10 offset:44928\n\t"
    "v_mfma_f32_16x16x32_bf16 v[216:219], a[48:51], a[88:91], v[216:219]\n\t"
    "v_mfma_f32_16x16x32_bf16 v[220:223], a[48:51], a[92:95], v[220:223]\n\t"
    "v_mfma_f32_16x16x32_bf16 v[224:227], a[52:55], a[80:83], v[224:227]\n\t"
    "ds_read_b64_tr_b16 a[48:49], v10 offset:43168\n\t"
    "v_mfma_f32_16x16x32_bf16 v[228:231], a[52:55], a[84:87], v[228:231]\n\t"
    "ds_read_b64_tr_b16 a[50:51], v10 offset:44960\n\t"
    "v_mfma_f32_16x16x32_bf16 v[232:235], a[52:55], a[88:91], v[232:235]\n\t"
    "buffer_load_dwordx4 a[160:163], v1, s[52:55], s72 offen offset:0 sc1\n\t"
    "v_mfma_f32_16x16x32_bf16 v[236:239], a[52:55], a[92:95], v[236:239]\n\t"
    "s_waitcnt vmcnt(27)\n\t"
    "s_mov_b32 s79, 2\n\t"
    ".p2align 6\n\t"
    "Lm_gu_0_0:\n\t"
    "v_mfma_f32_16x16x32_bf16 v[16:19], a[0:3], a[96:99], v[16:19]\n\t"
    "ds_read_b64_tr_b16 a[52:53], v10 offset:43200\n\t"
    "v_mfma_f32_16x16x32_bf16 v[20:23], a[0:3], a[100:103], v[20:23]\n\t"
    "ds_read_b64_tr_b16 a[54:55], v10 offset:44992\n\t"
    "v_mfma_f32_16x16x32_bf16 v[24:27], a[0:3], a[104:107], v[24:27]\n\t"
    "v_mfma_f32_16x16x32_bf16 v[28:31], a[0:3], a[108:111], v[28:31]\n\t"
    "buffer_load_dwordx4 a[164:167], v1, s[52:55], s72 offen offset:256 sc1\n\t"
    "v_mfma_f32_16x16x32_bf16 v[32:35], a[4:7], a[96:99], v[32:35]\n\t"
    "ds_read_b64_tr_b16 a[0:1], v10 offset:35840\n\t"
    "v_mfma_f32_16x16x32_bf16 v[36:39], a[4:7], a[100:103], v[36:39]\n\t"
    "ds_read_b64_tr_b16 a[2:3], v10 offset:37632\n\t"
    "v_mfma_f32_16x16x32_bf16 v[40:43], a[4:7], a[104:107], v[40:43]\n\t"
    "v_mfma_f32_16x16x32_bf16 v[44:47], a[4:7], a[108:111], v[44:47]\n\t"
    "s_waitcnt lgkmcnt(15)\n\t"
    "v_mfma_f32_16x16x32_bf16 v[48:51], a[8:11], a[96:99], v[48:51]\n\t"
    "ds_read_b64_tr_b16 a[4:5], v10 offset:35872\n\t"
    "v_mfma_f32_16x16x32_bf16 v[52:55], a[8:11], a[100:103], v[52:55]\n\t"
    "ds_read_b64_tr_b16 a[6:7], v10 offset:37664\n\t"
    "v_mfma_f32_16x16x32_bf16 v[56:59], a[8:11], a[104:107], v[56:59]\n\t"
    "v_mfma_f32_16x16x32_bf16 v[60:63], a[8:11], a[108:111], v[60:63]\n\t"
    "buffer_load_dwordx4 a[168:171], v2, s[52:55], s72 offen offset:0 sc1\n\t"
    "v_mfma_f32_16x16x32_bf16 v[64:67], a[12:15], a[96:99], v[64:67]\n\t"
    "ds_read_b64_tr_b16 a[8:9], v10 offset:35904\n\t"
    "v_mfma_f32_16x16x32_bf16 v[68:71], a[12:15], a[100:103], v[68:71]\n\t"
    "ds_read_b64_tr_b16 a[10:11], v10 offset:37696\n\t"
    "v_mfma_f32_16x16x32_bf16 v[72:75], a[12:15], a[104:107], v[72:75]\n\t"
    "v_mfma_f32_16x16x32_bf16 v[76:79], a[12:15], a[108:111], v[76:79]\n\t"
    "v_mfma_f32_16x16x32_bf16 v[80:83], a[16:19], a[96:99], v[80:83]\n\t"
    "ds_read_b64_tr_b16 a[12:13], v10 offset:35936\n\t"
    "v_mfma_f32_16x16x32_bf16 v[84:87], a[16:19], a[100:103], v[84:87]\n\t"
    "ds_read_b64_tr_b16 a[14:15], v10 offset:37728\n\t"
    "v_mfma_f32_16x16x32_bf16 v[88:91], a[16:19], a[104:107], v[88:91]\n\t"
    "buffer_load_dwordx4 a[172:175], v2, s[52:55], s72 offen offset:256 sc1\n\t"
    "v_mfma_f32_16x16x32_bf16 v[92:95], a[16:19], a[108:111], v[92:95]\n\t"
    "v_mfma_f32_16x16x32_bf16 v[96:99], a[20:23], a[96:99], v[96:99]\n\t"
    "ds_read_b64_tr_b16 a[16:17], v10 offset:35968\n\t"
    "v_mfma_f32_16x16x32_bf16 v[100:103], a[20:23], a[100:103], v[100:103]\n\t"
    "ds_read_b64_tr_b16 a[18:19], v10 offset:37760\n\t"
    "v_mfma_f32_16x16x32_bf16 v[104:107], a[20:23], a[104:107], v[104:107]\n\t"
    "v_mfma_f32_16x16x32_bf16 v[108:111], a[20:23], a[108:111], v[108:111]\n\t"
    "s_waitcnt lgkmcnt(15)\n\t"
    "v_mfma_f32_16x16x32_bf16 v[112:115], a[24:27], a[96:99], v[112:115]\n\t"
    "ds_read_b64_tr_b16 a[20:21], v10 offset:36000\n\t"
    "v_mfma_f32_16x16x32_bf16 v[116:119], a[24:27], a[100:103], v[116:119]\n\t"
    "ds_read_b64_tr_b16 a[22:23], v10 offset:37792\n\t"
    "v_mfma_f32_16x16x32_bf16 v[120:123], a[24:27], a[104:107], v[120:123]\n\t"
    "buffer_load_dwordx4 a[176:179], v1, s[52:55], s72 offen offset:2048 sc1\n\t"
    "v_mfma_f32_16x16x32_bf16 v[124:127], a[24:27], a[108:111], v[124:127]\n\t"
    "v_mfma_f32_16x16x32_bf16 v[128:131], a[28:31], a[96:99], v[128:131]\n\t"
    "ds_read_b64_tr_b16 a[24:25], v10 offset:36032\n\t"
    "v_mfma_f32_16x16x32_bf16 v[132:135], a[28:31], a[100:103], v[132:135]\n\t"
    "ds_read_b64_tr_b16 a[26:27], v10 offset:37824\n\t"
    "v_mfma_f32_16x16x32_bf16 v[136:139], a[28:31], a[104:107], v[136:139]\n\t"
    "v_mfma_f32_16x16x32_bf16 v[140:143], a[28:31], a[108:111], v[140:143]\n\t"
    "v_mfma_f32_16x16x32_bf16 v[144:147], a[32:35], a[96:99], v[144:147]\n\t"
    "ds_read_b64_tr_b16 a[28:29], v10 offset:50176\n\t"
    "v_mfma_f32_16x16x32_bf16 v[148:151], a[32:35], a[100:103], v[148:151]\n\t"
    "ds_read_b64_tr_b16 a[30:31], v10 offset:51968\n\t"
    "v_mfma_f32_16x16x32_bf16 v[152:155], a[32:35], a[104:107], v[152:155]\n\t"
    "buffer_load_dwordx4 a[180:183], v1, s[52:55], s72 offen offset:2304 sc1\n\t"
    "v_mfma_f32_16x16x32_bf16 v[156:159], a[32:35], a[108:111], v[156:159]\n\t"
    "v_mfma_f32_16x16x32_bf16 v[160:163], a[36:39], a[96:99], v[160:163]\n\t"
    "ds_read_b64_tr_b16 a[32:33], v10 offset:50208\n\t"
    "v_mfma_f32_16x16x32_bf16 v[164:167], a[36:39], a[100:103], v[164:167]\n\t"
    "ds_read_b64_tr_b16 a[34:35], v10 offset:52000\n\t"
    "v_mfma_f32_16x16x32_bf16 v[168:171], a[36:39], a[104:107], v[168:171]\n\t"
    "v_mfma_f32_16x16x32_bf16 v[172:175], a[36:39], a[108:111], v[172:175]\n\t"
    "s_waitcnt lgkmcnt(15)\n\t"
    "v_mfma_f32_16x16x32_bf16 v[176:179], a[40:43], a[96:99], v[176:179]\n\t"
    "ds_read_b64_tr_b16 a[36:37], v10 offset:50240\n\t"
    "v_mfma_f32_16x16x32_bf16 v[180:183], a[40:43], a[100:103], v[180:183]\n\t"
    "ds_read_b64_tr_b16 a[38:39], v10 offset:52032\n\t"
    "v_mfma_f32_16x16x32_bf16 v[184:187], a[40:43], a[104:107], v[184:187]\n\t"
    "buffer_load_dwordx4 a[184:187], v2, s[52:55], s72 offen offset:2048 sc1\n\t"
    "v_mfma_f32_16x16x32_bf16 v[188:191], a[40:43], a[108:111], v[188:191]\n\t"
    "v_mfma_f32_16x16x32_bf16 v[192:195], a[44:47], a[96:99], v[192:195]\n\t"
    "ds_read_b64_tr_b16 a[40:41], v10 offset:50272\n\t"
    "v_mfma_f32_16x16x32_bf16 v[196:199], a[44:47], a[100:103], v[196:199]\n\t"
    "ds_read_b64_tr_b16 a[42:43], v10 offset:52064\n\t"
    "v_mfma_f32_16x16x32_bf16 v[200:203], a[44:47], a[104:107], v[200:203]\n\t"
    "v_mfma_f32_16x16x32_bf16 v[204:207], a[44:47], a[108:111], v[204:207]\n\t"
    "v_mfma_f32_16x16x32_bf16 v[208:211], a[48:51], a[96:99], v[208:211]\n\t"
    "ds_read_b64_tr_b16 a[44:45], v10 offset:50304\n\t"
    "v_mfma_f32_16x16x32_bf16 v[212:215], a[48:51], a[100:103], v[212:215]\n\t"
    "ds_read_b64_tr_b16 a[46:47], v10 offset:52096\n\t"
    "v_mfma_f32_16x16x32_bf16 v[216:219], a[48:51], a[104:107], v[216:219]\n\t"
    "buffer_load_dwordx4 a[188:191], v2, s[52:55], s72 offen offset:2304 sc1\n\t"
    "v_mfma_f32_16x16x32_bf16 v[220:223], a[48:51], a[108:111], v[220:223]\n\t"
    "v_mfma_f32_16x16x32_bf16 v[224:227], a[52:55], a[96:99], v[224:227]\n\t"
    "ds_read_b64_tr_b16 a[48:49], v10 offset:50336\n\t"
    "s_add_u32 s72, s72, 4096\n\t"
    "v_mfma_f32_16x16x32_bf16 v[228:231], a[52:55], a[100:103], v[228:231]\n\t"
    "ds_read_b64_tr_b16 a[50:51], v10 offset:52128\n\t"
    "s_add_u32 m0, s74, 0\n\t"
    "v_mfma_f32_16x16x32_bf16 v[232:235], a[52:55], a[104:107], v[232:235]\n\t"
    "v_mfma_f32_16x16x32_bf16 v[236:239], a[52:55], a[108:111], v[236:239]\n\t"
    "s_waitcnt vmcnt(30) lgkmcnt(15)\n\t"
    "v_mfma_f32_16x16x32_bf16 v[16:19], a[0:3], a[112:115], v[16:19]\n\t"
    "s_waitcnt vmcnt(23)\n\t"
    "s_barrier\n\t"
    "ds_read_b64_tr_b16 a[52:53], v10 offset:50368\n\t"
    "v_mfma_f32_16x16x32_bf16 v[20:23], a[0:3], a[116:119], v[20:23]\n\t"
    "ds_read_b64_tr_b16 a[54:55], v10 offset:52160\n\t"
    "v_mfma_f32_16x16x32_bf16 v[24:27], a[0:3], a[120:123], v[24:27]\n\t"
    "buffer_load_dwordx4 v3, s[56:59], s73 offen offset:0 lds\n\t"
    "v_mfma_f32_16x16x32_bf16 v[28:31], a[0:3], a[124:127], v[28:31]\n\t"
    "v_mfma_f32_16x16x32_bf16 v[32:35], a[4:7], a[112:115], v[32:35]\n\t"
    "ds_read_b64_tr_b16 a[0:1], v10 offset:57344\n\t"
    "v_mfma_f32_16x16x32_bf16 v[36:39], a[4:7], a[116:119], v[36:39]\n\t"
    "ds_read_b64_tr_b16 a[2:3], v10 offset:59136\n\t"
    "v_mfma_f32_16x16x32_bf16 v[40:43], a[4:7], a[120:123], v[40:43]\n\t"
    "v_mfma_f32_16x16x32_bf16 v[44:47], a[4:7], a[124:127], v[44:47]\n\t"
    "v_mfma_f32_16x16x32_bf16 v[48:51], a[8:11], a[112:115], v[48:51]\n\t"
    "ds_read_b64_tr_b16 a[4:5], v10 offset:57376\n\t"
    "v_mfma_f32_16x16x32_bf16 v[52:55], a[8:11], a[116:119], v[52:55]\n\t"
    "ds_read_b64_tr_b16 a[6:7], v10 offset:59168\n\t"
    "v_mfma_f32_16x16x32_bf16 v[56:59], a[8:11], a[120:123], v[56:59]\n\t"
    "buffer_load_dwordx4 v4, s[56:59], s73 offen offset:1024 lds\n\t"
    "v_mfma_f32_16x16x32_bf16 v[60:63], a[8:11], a[124:127], v[60:63]\n\t"
    "v_mfma_f32_16x16x32_bf16 v[64:67], a[12:15], a[112:115], v[64:67]\n\t"
    "ds_read_b64_tr_b16 a[8:9], v10 offset:57408\n\t"
    "v_mfma_f32_16x16x32_bf16 v[68:71], a[12:15], a[116:119], v[68:71]\n\t"
    "ds_read_b64_tr_b16 a[10:11], v10 offset:59200\n\t"
    "v_mfma_f32_16x16x32_bf16 v[72:75], a[12:15], a[120:123], v[72:75]\n\t"
    "v_mfma_f32_16x16x32_bf16 v[76:79], a[12:15], a[124:127], v[76:79]\n\t"
    "s_waitcnt lgkmcnt(15)\n\t"
    "v_mfma_f32_16x16x32_bf16 v[80:83], a[16:19], a[112:115], v[80:83]\n\t"
    "ds_read_b64_tr_b16 a[12:13], v10 offset:57440\n\t"
    "v_mfma_f32_16x16x32_bf16 v[84:87], a[16:19], a[116:119], v[84:87]\n\t"
    "ds_read_b64_tr_b16 a[14:15], v10 offset:59232\n\t"
    "v_mfma_f32_16x16x32_bf16 v[88:91], a[16:19], a[120:123], v[88:91]\n\t"
    "buffer_load_dwordx4 v5, s[56:59], s73 offen offset:2048 lds\n\t"
    "v_mfma_f32_16x16x32_bf16 v[92:95], a[16:19], a[124:127], v[92:95]\n\t"
    "v_mfma_f32_16x16x32_bf16 v[96:99], a[20:23], a[112:115], v[96:99]\n\t"
    "ds_read_b64_tr_b16 a[16:17], v10 offset:57472\n\t"
    "v_mfma_f32_16x16x32_bf16 v[100:103], a[20:23], a[116:119], v[100:103]\n\t"
    "ds_read_b64_tr_b16 a[18:19], v10 offset:59264\n\t"
    "v_mfma_f32_16x16x32_bf16 v[104:107], a[20:23], a[120:123], v[104:107]\n\t"
    "v_mfma_f32_16x16x32_bf16 v[108:111], a[20:23], a[124:127], v[108:111]\n\t"
    "v_mfma_f32_16x16x32_bf16 v[112:115], a[24:27], a[112:115], v[112:115]\n\t"
    "ds_read_b64_tr_b16 a[20:21], v10 offset:57504\n\t"
    "v_mfma_f32_16x16x32_bf16 v[116:119], a[24:27], a[116:119], v[116:119]\n\t"
    "ds_read_b64_tr_b16 a[22:23], v10 offset:59296\n\t"
    "v_mfma_f32_16x16x32_bf16 v[120:123], a[24:27], a[120:123], v[120:123]\n\t"
    "buffer_load_dwordx4 v6, s[56:59], s73 offen offset:3072 lds\n\t"
    "v_mfma_f32_16x16x32_bf16 v[124:127], a[24:27], a[124:127], v[124:127]\n\t"
    "v_mfma_f32_16x16x32_bf16 v[128:131], a[28:31], a[112:115], v[128:131]\n\t"
    "ds_read_b64_tr_b16 a[24:25], v10 offset:57536\n\t"
    "s_add_u32 m0, s74, 4096\n\t"
    "v_mfma_f32_16x16x32_bf16 v[132:135], a[28:31], a[116:119], v[132:135]\n\t"
    "ds_read_b64_tr_b16 a[26:27], v10 offset:59328\n\t"
    "v_mfma_f32_16x16x32_bf16 v[136:139], a[28:31], a[120:123], v[136:139]\n\t"
    "buffer_load_dwordx4 v7, s[56:59], s73 offen offset:0 lds\n\t"
    "v_mfma_f32_16x16x32_bf16 v[140:143], a[28:31], a[124:127], v[140:143]\n\t"
    "s_waitcnt lgkmcnt(15)\n\t"
    "v_mfma_f32_16x16x32_bf16 v[144:147], a[32:35], a[112:115], v[144:147]\n\t"
    "ds_read_b64_tr_b16 a[28:29], v11 offset:6144\n\t"
    "v_mfma_f32_16x16x32_bf16 v[148:151], a[32:35], a[116:119], v[148:151]\n\t"
    "ds_read_b64_tr_b16 a[30:31], v11 offset:7936\n\t"
    "v_mfma_f32_16x16x32_bf16 v[152:155], a[32:35], a[120:123], v[152:155]\n\t"
    "v_mfma_f32_16x16x32_bf16 v[156:159], a[32:35], a[124:127], v[156:159]\n\t"
    "v_mfma_f32_16x16x32_bf16 v[160:163], a[36:39], a[112:115], v[160:163]\n\t"
    "ds_read_b64_tr_b16 a[32:33], v11 offset:6176\n\t"
    "v_mfma_f32_16x16x32_bf16 v[164:167], a[36:39], a[116:119], v[164:167]\n\t"
    "ds_read_b64_tr_b16 a[34:35], v11 offset:7968\n\t"
    "v_mfma_f32_16x16x32_bf16 v[168:171], a[36:39], a[120:123], v[168:171]\n\t"
    "buffer_load_dwordx4 v8, s[56:59], s73 offen offset:1024 lds\n\t"
    "v_mfma_f32_16x16x32_bf16 v[172:175], a[36:39], a[124:127], v[172:175]\n\t"
    "v_mfma_f32_16x16x32_bf16 v[176:179], a[40:43], a[112:115], v[176:179]\n\t"
    "ds_read_b64_tr_b16 a[36:37], v11 offset:6208\n\t"
    "v_mfma_f32_16x16x32_bf16 v[180:183], a[40:43], a[116:119], v[180:183]\n\t"
    "ds_read_b64_tr_b16 a[38:39], v11 offset:8000\n\t"
    "v_mfma_f32_16x16x32_bf16 v[184:187], a[40:43], a[120:123], v[184:187]\n\t"
    "v_mfma_f32_16x16x32_bf16 v[188:191], a[40:43], a[124:127], v[188:191]\n\t"
    "v_mfma_f32_16x16x32_bf16 v[192:195], a[44:47], a[112:115], v[192:195]\n\t"
    "ds_read_b64_tr_b16 a[40:41], v11 offset:6240\n\t"
    "v_mfma_f32_16x16x32_bf16 v[196:199], a[44:47], a[116:119], v[196:199]\n\t"
    "ds_read_b64_tr_b16 a[42:43], v11 offset:8032\n\t"
    "v_mfma_f32_16x16x32_bf16 v[200:203], a[44:47], a[120:123], v[200:203]\n\t"
    "buffer_load_dwordx4 v9, s[56:59], s73 offen offset:2048 lds\n\t"
    "v_mfma_f32_16x16x32_bf16 v[204:207], a[44:47], a[124:127], v[204:207]\n\t"
    "s_waitcnt lgkmcnt(15)\n\t"
    "s_add_u32 s73, s73, 458752\n\t"
    "v_mfma_f32_16x16x32_bf16 v[208:211], a[48:51], a[112:115], v[208:211]\n\t"
    "ds_read_b64_tr_b16 a[44:45], v11 offset:6272\n\t"
    "v_mfma_f32_16x16x32_bf16 v[212:215], a[48:51], a[116:119], v[212:215]\n\t"
    "ds_read_b64_tr_b16 a[46:47], v11 offset:8064\n\t"
    "v_mfma_f32_16x16x32_bf16 v[216:219], a[48:51], a[120:123], v[216:219]\n\t"
    "v_mfma_f32_16x16x32_bf16 v[220:223], a[48:51], a[124:127], v[220:223]\n\t"
    "v_mfma_f32_16x16x32_bf16 v[224:227], a[52:55], a[112:115], v[224:227]\n\t"
    "ds_read_b64_tr_b16 a[48:49], v11 offset:6304\n\t"
    "v_mfma_f32_16x16x32_bf16 v[228:231], a[52:55], a[116:119], v[228:231]\n\t"
    "ds_read_b64_tr_b16 a[50:51], v11 offset:8096\n\t"
    "v_mfma_f32_16x16x32_bf16 v[232:235], a[52:55], a[120:123], v[232:235]\n\t"
    "buffer_load_dwordx4 a[64:67], v1, s[52:55], s72 offen offset:0 sc1\n\t"
    "v_mfma_f32_16x16x32_bf16 v[236:239], a[52:55], a[124:127], v[236:239]\n\t"
    "s_waitcnt vmcnt(27)\n\t"
    "v_mfma_f32_16x16x32_bf16 v[16:19], a[0:3], a[128:131], v[16:19]\n\t"
    "ds_read_b64_tr_b16 a[52:53], v11 offset:6336\n\t"
    "v_mfma_f32_16x16x32_bf16 v[20:23], a[0:3], a[132:135], v[20:23]\n\t"
    "ds_read_b64_tr_b16 a[54:55], v11 offset:8128\n\t"
    "v_mfma_f32_16x16x32_bf16 v[24:27], a[0:3], a[136:139], v[24:27]\n\t"
    "v_mfma_f32_16x16x32_bf16 v[28:31], a[0:3], a[140:143], v[28:31]\n\t"
    "buffer_load_dwordx4 a[68:71], v1, s[52:55], s72 offen offset:256 sc1\n\t"
    "v_mfma_f32_16x16x32_bf16 v[32:35], a[4:7], a[128:131], v[32:35]\n\t"
    "ds_read_b64_tr_b16 a[0:1], v10 offset:64512\n\t"
    "v_mfma_f32_16x16x32_bf16 v[36:39], a[4:7], a[132:135], v[36:39]\n\t"
    "ds_read_b64_tr_b16 a[2:3], v11 offset:768\n\t"
    "v_mfma_f32_16x16x32_bf16 v[40:43], a[4:7], a[136:139], v[40:43]\n\t"
    "v_mfma_f32_16x16x32_bf16 v[44:47], a[4:7], a[140:143], v[44:47]\n\t"
    "s_waitcnt lgkmcnt(15)\n\t"
    "v_mfma_f32_16x16x32_bf16 v[48:51], a[8:11], a[128:131], v[48:51]\n\t"
    "ds_read_b64_tr_b16 a[4:5], v10 offset:64544\n\t"
    "v_mfma_f32_16x16x32_bf16 v[52:55], a[8:11], a[132:135], v[52:55]\n\t"
    "ds_read_b64_tr_b16 a[6:7], v11 offset:800\n\t"
    "v_mfma_f32_16x16x32_bf16 v[56:59], a[8:11], a[136:139], v[56:59]\n\t"
    "v_mfma_f32_16x16x32_bf16 v[60:63], a[8:11], a[140:143], v[60:63]\n\t"
    "buffer_load_dwordx4 a[72:75], v2, s[52:55], s72 offen offset:0 sc1\n\t"
    "v_mfma_f32_16x16x32_bf16 v[64:67], a[12:15], a[128:131], v[64:67]\n\t"
    "ds_read_b64_tr_b16 a[8:9], v10 offset:64576\n\t"
    "v_mfma_f32_16x16x32_bf16 v[68:71], a[12:15], a[132:135], v[68:71]\n\t"
    "ds_read_b64_tr_b16 a[10:11], v11 offset:832\n\t"
    "v_mfma_f32_16x16x32_bf16 v[72:75], a[12:15], a[136:139], v[72:75]\n\t"
    "v_mfma_f32_16x16x32_bf16 v[76:79], a[12:15], a[140:143], v[76:79]\n\t"
    "v_mfma_f32_16x16x32_bf16 v[80:83], a[16:19], a[128:131], v[80:83]\n\t"
    "ds_read_b64_tr_b16 a[12:13], v10 offset:64608\n\t"
    "v_mfma_f32_16x16x32_bf16 v[84:87], a[16:19], a[132:135], v[84:87]\n\t"
    "ds_read_b64_tr_b16 a[14:15], v11 offset:864\n\t"
    "v_mfma_f32_16x16x32_bf16 v[88:91], a[16:19], a[136:139], v[88:91]\n\t"
    "buffer_load_dwordx4 a[76:79], v2, s[52:55], s72 offen offset:256 sc1\n\t"
    "v_mfma_f32_16x16x32_bf16 v[92:95], a[16:19], a[140:143], v[92:95]\n\t"
    "v_mfma_f32_16x16x32_bf16 v[96:99], a[20:23], a[128:131], v[96:99]\n\t"
    "ds_read_b64_tr_b16 a[16:17], v10 offset:64640\n\t"
    "v_mfma_f32_16x16x32_bf16 v[100:103], a[20:23], a[132:135], v[100:103]\n\t"
    "ds_read_b64_tr_b16 a[18:19], v11 offset:896\n\t"
    "v_mfma_f32_16x16x32_bf16 v[104:107], a[20:23], a[136:139], v[104:107]\n\t"
    "v_mfma_f32_16x16x32_bf16 v[108:111], a[20:23], a[140:143], v[108:111]\n\t"
    "s_waitcnt lgkmcnt(15)\n\t"
    "v_mfma_f32_16x16x32_bf16 v[112:115], a[24:27], a[128:131], v[112:115]\n\t"
    "ds_read_b64_tr_b16 a[20:21], v10 offset:64672\n\t"
    "v_mfma_f32_16x16x32_bf16 v[116:119], a[24:27], a[132:135], v[116:119]\n\t"
    "ds_read_b64_tr_b16 a[22:23], v11 offset:928\n\t"
    "v_mfma_f32_16x16x32_bf16 v[120:123], a[24:27], a[136:139], v[120:123]\n\t"
    "buffer_load_dwordx4 a[80:83], v1, s[52:55], s72 offen offset:2048 sc1\n\t"
    "v_mfma_f32_16x16x32_bf16 v[124:127], a[24:27], a[140:143], v[124:127]\n\t"
    "v_mfma_f32_16x16x32_bf16 v[128:131], a[28:31], a[128:131], v[128:131]\n\t"
    "ds_read_b64_tr_b16 a[24:25], v10 offset:64704\n\t"
    "v_mfma_f32_16x16x32_bf16 v[132:135], a[28:31], a[132:135], v[132:135]\n\t"
    "ds_read_b64_tr_b16 a[26:27], v11 offset:960\n\t"
    "v_mfma_f32_16x16x32_bf16 v[136:139], a[28:31], a[136:139], v[136:139]\n\t"
    "v_mfma_f32_16x16x32_bf16 v[140:143], a[28:31], a[140:143], v[140:143]\n\t"
    "v_mfma_f32_16x16x32_bf16 v[144:147], a[32:35], a[128:131], v[144:147]\n\t"
    "ds_read_b64_tr_b16 a[28:29], v11 offset:13312\n\t"
    "v_mfma_f32_16x16x32_bf16 v[148:151], a[32:35], a[132:135], v[148:151]\n\t"
    "ds_read_b64_tr_b16 a[30:31], v11 offset:15104\n\t"
    "v_mfma_f32_16x16x32_bf16 v[152:155], a[32:35], a[136:139], v[152:155]\n\t"
    "buffer_load_dwordx4 a[84:87], v1, s[52:55], s72 offen offset:2304 sc1\n\t"
    "v_mfma_f32_16x16x32_bf16 v[156:159], a[32:35], a[140:143], v[156:159]\n\t"
    "v_mfma_f32_16x16x32_bf16 v[160:163], a[36:39], a[128:131], v[160:163]\n\t"
    "ds_read_b64_tr_b16 a[32:33], v11 offset:13344\n\t"
    "v_mfma_f32_16x16x32_bf16 v[164:167], a[36:39], a[132:135], v[164:167]\n\t"
    "ds_read_b64_tr_b16 a[34:35], v11 offset:15136\n\t"
    "v_mfma_f32_16x16x32_bf16 v[168:171], a[36:39], a[136:139], v[168:171]\n\t"
    "v_mfma_f32_16x16x32_bf16 v[172:175], a[36:39], a[140:143], v[172:175]\n\t"
    "s_waitcnt lgkmcnt(15)\n\t"
    "v_mfma_f32_16x16x32_bf16 v[176:179], a[40:43], a[128:131], v[176:179]\n\t"
    "ds_read_b64_tr_b16 a[36:37], v11 offset:13376\n\t"
    "v_mfma_f32_16x16x32_bf16 v[180:183], a[40:43], a[132:135], v[180:183]\n\t"
    "ds_read_b64_tr_b16 a[38:39], v11 offset:15168\n\t"
    "v_mfma_f32_16x16x32_bf16 v[184:187], a[40:43], a[136:139], v[184:187]\n\t"
    "buffer_load_dwordx4 a[88:91], v2, s[52:55], s72 offen offset:2048 sc1\n\t"
    "v_mfma_f32_16x16x32_bf16 v[188:191], a[40:43], a[140:143], v[188:191]\n\t"
    "v_mfma_f32_16x16x32_bf16 v[192:195], a[44:47], a[128:131], v[192:195]\n\t"
    "ds_read_b64_tr_b16 a[40:41], v11 offset:13408\n\t"
    "v_mfma_f32_16x16x32_bf16 v[196:199], a[44:47], a[132:135], v[196:199]\n\t"
    "ds_read_b64_tr_b16 a[42:43], v11 offset:15200\n\t"
    "v_mfma_f32_16x16x32_bf16 v[200:203], a[44:47], a[136:139], v[200:203]\n\t"
    "v_mfma_f32_16x16x32_bf16 v[204:207], a[44:47], a[140:143], v[204:207]\n\t"
    "v_mfma_f32_16x16x32_bf16 v[208:211], a[48:51], a[128:131], v[208:211]\n\t"
    "ds_read_b64_tr_b16 a[44:45], v11 offset:13440\n\t"
    "v_mfma_f32_16x16x32_bf16 v[212:215], a[48:51], a[132:135], v[212:215]\n\t"
    "ds_read_b64_tr_b16 a[46:47], v11 offset:15232\n\t"
    "v_mfma_f32_16x16x32_bf16 v[216:219], a[48:51], a[136:139], v[216:219]\n\t"
    "buffer_load_dwordx4 a[92:95], v2, s[52:55], s72 offen offset:2304 sc1\n\t"
    "v_mfma_f32_16x16x32_bf16 v[220:223], a[48:51], a[140:143], v[220:223]\n\t"
    "v_mfma_f32_16x16x32_bf16 v[224:227], a[52:55], a[128:131], v[224:227]\n\t"
    "ds_read_b64_tr_b16 a[48:49], v11 offset:13472\n\t"
    "s_add_u32 s72, s72, 4096\n\t"
    "v_mfma_f32_16x16x32_bf16 v[228:231], a[52:55], a[132:135], v[228:231]\n\t"
    "ds_read_b64_tr_b16 a[50:51], v11 offset:15264\n\t"
    "s_add_u32 m0, s74, 28672\n\t"
    "v_mfma_f32_16x16x32_bf16 v[232:235], a[52:55], a[136:139], v[232:235]\n\t"
    "v_mfma_f32_16x16x32_bf16 v[236:239], a[52:55], a[140:143], v[236:239]\n\t"
    "s_waitcnt vmcnt(30) lgkmcnt(15)\n\t"
    "v_mfma_f32_16x16x32_bf16 v[16:19], a[0:3], a[144:147], v[16:19]\n\t"
    "s_waitcnt vmcnt(23)\n\t"
    "s_barrier\n\t"
    "ds_read_b64_tr_b16 a[52:53], v11 offset:13504\n\t"
    "v_mfma_f32_16x16x32_bf16 v[20:23], a[0:3], a[148:151], v[20:23]\n\t"
    "ds_read_b64_tr_b16 a[54:55], v11 offset:15296\n\t"
    "v_mfma_f32_16x16x32_bf16 v[24:27], a[0:3], a[152:155], v[24:27]\n\t"
    "buffer_load_dwordx4 v3, s[56:59], s73 offen offset:0 lds\n\t"
    "v_mfma_f32_16x16x32_bf16 v[28:31], a[0:3], a[156:159], v[28:31]\n\t"
    "v_mfma_f32_16x16x32_bf16 v[32:35], a[4:7], a[144:147], v[32:35]\n\t"
    "ds_read_b64_tr_b16 a[0:1], v11 offset:20480\n\t"
    "v_mfma_f32_16x16x32_bf16 v[36:39], a[4:7], a[148:151], v[36:39]\n\t"
    "ds_read_b64_tr_b16 a[2:3], v11 offset:22272\n\t"
    "v_mfma_f32_16x16x32_bf16 v[40:43], a[4:7], a[152:155], v[40:43]\n\t"
    "v_mfma_f32_16x16x32_bf16 v[44:47], a[4:7], a[156:159], v[44:47]\n\t"
    "v_mfma_f32_16x16x32_bf16 v[48:51], a[8:11], a[144:147], v[48:51]\n\t"
    "ds_read_b64_tr_b16 a[4:5], v11 offset:20512\n\t"
    "v_mfma_f32_16x16x32_bf16 v[52:55], a[8:11], a[148:151], v[52:55]\n\t"
    "ds_read_b64_tr_b16 a[6:7], v11 offset:22304\n\t"
    "v_mfma_f32_16x16x32_bf16 v[56:59], a[8:11], a[152:155], v[56:59]\n\t"
    "buffer_load_dwordx4 v4, s[56:59], s73 offen offset:1024 lds\n\t"
    "v_mfma_f32_16x16x32_bf16 v[60:63], a[8:11], a[156:159], v[60:63]\n\t"
    "v_mfma_f32_16x16x32_bf16 v[64:67], a[12:15], a[144:147], v[64:67]\n\t"
    "ds_read_b64_tr_b16 a[8:9], v11 offset:20544\n\t"
    "v_mfma_f32_16x16x32_bf16 v[68:71], a[12:15], a[148:151], v[68:71]\n\t"
    "ds_read_b64_tr_b16 a[10:11], v11 offset:22336\n\t"
    "v_mfma_f32_16x16x32_bf16 v[72:75], a[12:15], a[152:155], v[72:75]\n\t"
    "v_mfma_f32_16x16x32_bf16 v[76:79], a[12:15], a[156:159], v[76:79]\n\t"
    "s_waitcnt lgkmcnt(15)\n\t"
    "v_mfma_f32_16x16x32_bf16 v[80:83], a[16:19], a[144:147], v[80:83]\n\t"
    "ds_read_b64_tr_b16 a[12:13], v11 offset:20576\n\t"
    "v_mfma_f32_16x16x32_bf16 v[84:87], a[16:19], a[148:151], v[84:87]\n\t"
    "ds_read_b64_tr_b16 a[14:15], v11 offset:22368\n\t"
    "v_mfma_f32_16x16x32_bf16 v[88:91], a[16:19], a[152:155], v[88:91]\n\t"
    "buffer_load_dwordx4 v5, s[56:59], s73 offen offset:2048 lds\n\t"
    "v_mfma_f32_16x16x32_bf16 v[92:95], a[16:19], a[156:159], v[92:95]\n\t"
    "v_mfma_f32_16x16x32_bf16 v[96:99], a[20:23], a[144:147], v[96:99]\n\t"
    "ds_read_b64_tr_b16 a[16:17], v11 offset:20608\n\t"
    "v_mfma_f32_16x16x32_bf16 v[100:103], a[20:23], a[148:151], v[100:103]\n\t"
    "ds_read_b64_tr_b16 a[18:19], v11 offset:22400\n\t"
    "v_mfma_f32_16x16x32_bf16 v[104:107], a[20:23], a[152:155], v[104:107]\n\t"
    "v_mfma_f32_16x16x32_bf16 v[108:111], a[20:23], a[156:159], v[108:111]\n\t"
    "v_mfma_f32_16x16x32_bf16 v[112:115], a[24:27], a[144:147], v[112:115]\n\t"
    "ds_read_b64_tr_b16 a[20:21], v11 offset:20640\n\t"
    "v_mfma_f32_16x16x32_bf16 v[116:119], a[24:27], a[148:151], v[116:119]\n\t"
    "ds_read_b64_tr_b16 a[22:23], v11 offset:22432\n\t"
    "v_mfma_f32_16x16x32_bf16 v[120:123], a[24:27], a[152:155], v[120:123]\n\t"
    "buffer_load_dwordx4 v6, s[56:59], s73 offen offset:3072 lds\n\t"
    "v_mfma_f32_16x16x32_bf16 v[124:127], a[24:27], a[156:159], v[124:127]\n\t"
    "v_mfma_f32_16x16x32_bf16 v[128:131], a[28:31], a[144:147], v[128:131]\n\t"
    "ds_read_b64_tr_b16 a[24:25], v11 offset:20672\n\t"
    "s_add_u32 m0, s74, 32768\n\t"
    "v_mfma_f32_16x16x32_bf16 v[132:135], a[28:31], a[148:151], v[132:135]\n\t"
    "ds_read_b64_tr_b16 a[26:27], v11 offset:22464\n\t"
    "v_mfma_f32_16x16x32_bf16 v[136:139], a[28:31], a[152:155], v[136:139]\n\t"
    "buffer_load_dwordx4 v7, s[56:59], s73 offen offset:0 lds\n\t"
    "v_mfma_f32_16x16x32_bf16 v[140:143], a[28:31], a[156:159], v[140:143]\n\t"
    "s_waitcnt lgkmcnt(15)\n\t"
    "v_mfma_f32_16x16x32_bf16 v[144:147], a[32:35], a[144:147], v[144:147]\n\t"
    "ds_read_b64_tr_b16 a[28:29], v11 offset:34816\n\t"
    "v_mfma_f32_16x16x32_bf16 v[148:151], a[32:35], a[148:151], v[148:151]\n\t"
    "ds_read_b64_tr_b16 a[30:31], v11 offset:36608\n\t"
    "v_mfma_f32_16x16x32_bf16 v[152:155], a[32:35], a[152:155], v[152:155]\n\t"
    "v_mfma_f32_16x16x32_bf16 v[156:159], a[32:35], a[156:159], v[156:159]\n\t"
    "v_mfma_f32_16x16x32_bf16 v[160:163], a[36:39], a[144:147], v[160:163]\n\t"
    "ds_read_b64_tr_b16 a[32:33], v11 offset:34848\n\t"
    "v_mfma_f32_16x16x32_bf16 v[164:167], a[36:39], a[148:151], v[164:167]\n\t"
    "ds_read_b64_tr_b16 a[34:35], v11 offset:36640\n\t"
    "v_mfma_f32_16x16x32_bf16 v[168:171], a[36:39], a[152:155], v[168:171]\n\t"
    "buffer_load_dwordx4 v8, s[56:59], s73 offen offset:1024 lds\n\t"
    "v_mfma_f32_16x16x32_bf16 v[172:175], a[36:39], a[156:159], v[172:175]\n\t"
    "v_mfma_f32_16x16x32_bf16 v[176:179], a[40:43], a[144:147], v[176:179]\n\t"
    "ds_read_b64_tr_b16 a[36:37], v11 offset:34880\n\t"
    "v_mfma_f32_16x16x32_bf16 v[180:183], a[40:43], a[148:151], v[180:183]\n\t"
    "ds_read_b64_tr_b16 a[38:39], v11 offset:36672\n\t"
    "v_mfma_f32_16x16x32_bf16 v[184:187], a[40:43], a[152:155], v[184:187]\n\t"
    "v_mfma_f32_16x16x32_bf16 v[188:191], a[40:43], a[156:159], v[188:191]\n\t"
    "v_mfma_f32_16x16x32_bf16 v[192:195], a[44:47], a[144:147], v[192:195]\n\t"
    "ds_read_b64_tr_b16 a[40:41], v11 offset:34912\n\t"
    "v_mfma_f32_16x16x32_bf16 v[196:199], a[44:47], a[148:151], v[196:199]\n\t"
    "ds_read_b64_tr_b16 a[42:43], v11 offset:36704\n\t"
    "v_mfma_f32_16x16x32_bf16 v[200:203], a[44:47], a[152:155], v[200:203]\n\t"
    "buffer_load_dwordx4 v9, s[56:59], s73 offen offset:2048 lds\n\t"
    "v_mfma_f32_16x16x32_bf16 v[204:207], a[44:47], a[156:159], v[204:207]\n\t"
    "s_waitcnt lgkmcnt(15)\n\t"
    "s_add_u32 s73, s73, 458752\n\t"
    "v_mfma_f32_16x16x32_bf16 v[208:211], a[48:51], a[144:147], v[208:211]\n\t"
    "ds_read_b64_tr_b16 a[44:45], v11 offset:34944\n\t"
    "v_mfma_f32_16x16x32_bf16 v[212:215], a[48:51], a[148:151], v[212:215]\n\t"
    "ds_read_b64_tr_b16 a[46:47], v11 offset:36736\n\t"
    "v_mfma_f32_16x16x32_bf16 v[216:219], a[48:51], a[152:155], v[216:219]\n\t"
    "v_mfma_f32_16x16x32_bf16 v[220:223], a[48:51], a[156:159], v[220:223]\n\t"
    "v_mfma_f32_16x16x32_bf16 v[224:227], a[52:55], a[144:147], v[224:227]\n\t"
    "ds_read_b64_tr_b16 a[48:49], v11 offset:34976\n\t"
    "v_mfma_f32_16x16x32_bf16 v[228:231], a[52:55], a[148:151], v[228:231]\n\t"
    "ds_read_b64_tr_b16 a[50:51], v11 offset:36768\n\t"
    "v_mfma_f32_16x16x32_bf16 v[232:235], a[52:55], a[152:155], v[232:235]\n\t"
    "buffer_load_dwordx4 a[96:99], v1, s[52:55], s72 offen offset:0 sc1\n\t"
    "v_mfma_f32_16x16x32_bf16 v[236:239], a[52:55], a[156:159], v[236:239]\n\t"
    "s_waitcnt vmcnt(27)\n\t"
    "v_mfma_f32_16x16x32_bf16 v[16:19], a[0:3], a[160:163], v[16:19]\n\t"
    "ds_read_b64_tr_b16 a[52:53], v11 offset:35008\n\t"
    "v_mfma_f32_16x16x32_bf16 v[20:23], a[0:3], a[164:167], v[20:23]\n\t"
    "ds_read_b64_tr_b16 a[54:55], v11 offset:36800\n\t"
    "v_mfma_f32_16x16x32_bf16 v[24:27], a[0:3], a[168:171], v[24:27]\n\t"
    "v_mfma_f32_16x16x32_bf16 v[28:31], a[0:3], a[172:175], v[28:31]\n\t"
    "buffer_load_dwordx4 a[100:103], v1, s[52:55], s72 offen offset:256 sc1\n\t"
    "v_mfma_f32_16x16x32_bf16 v[32:35], a[4:7], a[160:163], v[32:35]\n\t"
    "ds_read_b64_tr_b16 a[0:1], v11 offset:27648\n\t"
    "v_mfma_f32_16x16x32_bf16 v[36:39], a[4:7], a[164:167], v[36:39]\n\t"
    "ds_read_b64_tr_b16 a[2:3], v11 offset:29440\n\t"
    "v_mfma_f32_16x16x32_bf16 v[40:43], a[4:7], a[168:171], v[40:43]\n\t"
    "v_mfma_f32_16x16x32_bf16 v[44:47], a[4:7], a[172:175], v[44:47]\n\t"
    "s_waitcnt lgkmcnt(15)\n\t"
    "v_mfma_f32_16x16x32_bf16 v[48:51], a[8:11], a[160:163], v[48:51]\n\t"
    "ds_read_b64_tr_b16 a[4:5], v11 offset:27680\n\t"
    "v_mfma_f32_16x16x32_bf16 v[52:55], a[8:11], a[164:167], v[52:55]\n\t"
    "ds_read_b64_tr_b16 a[6:7], v11 offset:29472\n\t"
    "v_mfma_f32_16x16x32_bf16 v[56:59], a[8:11], a[168:171], v[56:59]\n\t"
    "v_mfma_f32_16x16x32_bf16 v[60:63], a[8:11], a[172:175], v[60:63]\n\t"
    "buffer_load_dwordx4 a[104:107], v2, s[52:55], s72 offen offset:0 sc1\n\t"
    "v_mfma_f32_16x16x32_bf16 v[64:67], a[12:15], a[160:163], v[64:67]\n\t"
    "ds_read_b64_tr_b16 a[8:9], v11 offset:27712\n\t"
    "v_mfma_f32_16x16x32_bf16 v[68:71], a[12:15], a[164:167], v[68:71]\n\t"
    "ds_read_b64_tr_b16 a[10:11], v11 offset:29504\n\t"
    "v_mfma_f32_16x16x32_bf16 v[72:75], a[12:15], a[168:171], v[72:75]\n\t"
    "v_mfma_f32_16x16x32_bf16 v[76:79], a[12:15], a[172:175], v[76:79]\n\t"
    "v_mfma_f32_16x16x32_bf16 v[80:83], a[16:19], a[160:163], v[80:83]\n\t"
    "ds_read_b64_tr_b16 a[12:13], v11 offset:27744\n\t"
    "v_mfma_f32_16x16x32_bf16 v[84:87], a[16:19], a[164:167], v[84:87]\n\t"
    "ds_read_b64_tr_b16 a[14:15], v11 offset:29536\n\t"
    "v_mfma_f32_16x16x32_bf16 v[88:91], a[16:19], a[168:171], v[88:91]\n\t"
    "buffer_load_dwordx4 a[108:111], v2, s[52:55], s72 offen offset:256 sc1\n\t"
    "v_mfma_f32_16x16x32_bf16 v[92:95], a[16:19], a[172:175], v[92:95]\n\t"
    "v_mfma_f32_16x16x32_bf16 v[96:99], a[20:23], a[160:163], v[96:99]\n\t"
    "ds_read_b64_tr_b16 a[16:17], v11 offset:27776\n\t"
    "v_mfma_f32_16x16x32_bf16 v[100:103], a[20:23], a[164:167], v[100:103]\n\t"
    "ds_read_b64_tr_b16 a[18:19], v11 offset:29568\n\t"
    "v_mfma_f32_16x16x32_bf16 v[104:107], a[20:23], a[168:171], v[104:107]\n\t"
    "v_mfma_f32_16x16x32_bf16 v[108:111], a[20:23], a[172:175], v[108:111]\n\t"
    "s_waitcnt lgkmcnt(15)\n\t"
    "v_mfma_f32_16x16x32_bf16 v[112:115], a[24:27], a[160:163], v[112:115]\n\t"
    "ds_read_b64_tr_b16 a[20:21], v11 offset:27808\n\t"
    "v_mfma_f32_16x16x32_bf16 v[116:119], a[24:27], a[164:167], v[116:119]\n\t"
    "ds_read_b64_tr_b16 a[22:23], v11 offset:29600\n\t"
    "v_mfma_f32_16x16x32_bf16 v[120:123], a[24:27], a[168:171], v[120:123]\n\t"
    "buffer_load_dwordx4 a[112:115], v1, s[52:55], s72 offen offset:2048 sc1\n\t"
    "v_mfma_f32_16x16x32_bf16 v[124:127], a[24:27], a[172:175], v[124:127]\n\t"
    "v_mfma_f32_16x16x32_bf16 v[128:131], a[28:31], a[160:163], v[128:131]\n\t"
    "ds_read_b64_tr_b16 a[24:25], v11 offset:27840\n\t"
    "v_mfma_f32_16x16x32_bf16 v[132:135], a[28:31], a[164:167], v[132:135]\n\t"
    "ds_read_b64_tr_b16 a[26:27], v11 offset:29632\n\t"
    "v_mfma_f32_16x16x32_bf16 v[136:139], a[28:31], a[168:171], v[136:139]\n\t"
    "v_mfma_f32_16x16x32_bf16 v[140:143], a[28:31], a[172:175], v[140:143]\n\t"
    "v_mfma_f32_16x16x32_bf16 v[144:147], a[32:35], a[160:163], v[144:147]\n\t"
    "ds_read_b64_tr_b16 a[28:29], v11 offset:41984\n\t"
    "v_mfma_f32_16x16x32_bf16 v[148:151], a[32:35], a[164:167], v[148:151]\n\t"
    "ds_read_b64_tr_b16 a[30:31], v11 offset:43776\n\t"
    "v_mfma_f32_16x16x32_bf16 v[152:155], a[32:35], a[168:171], v[152:155]\n\t"
    "buffer_load_dwordx4 a[116:119], v1, s[52:55], s72 offen offset:2304 sc1\n\t"
    "v_mfma_f32_16x16x32_bf16 v[156:159], a[32:35], a[172:175], v[156:159]\n\t"
    "v_mfma_f32_16x16x32_bf16 v[160:163], a[36:39], a[160:163], v[160:163]\n\t"
    "ds_read_b64_tr_b16 a[32:33], v11 offset:42016\n\t"
    "v_mfma_f32_16x16x32_bf16 v[164:167], a[36:39], a[164:167], v[164:167]\n\t"
    "ds_read_b64_tr_b16 a[34:35], v11 offset:43808\n\t"
    "v_mfma_f32_16x16x32_bf16 v[168:171], a[36:39], a[168:171], v[168:171]\n\t"
    "v_mfma_f32_16x16x32_bf16 v[172:175], a[36:39], a[172:175], v[172:175]\n\t"
    "s_waitcnt lgkmcnt(15)\n\t"
    "v_mfma_f32_16x16x32_bf16 v[176:179], a[40:43], a[160:163], v[176:179]\n\t"
    "ds_read_b64_tr_b16 a[36:37], v11 offset:42048\n\t"
    "v_mfma_f32_16x16x32_bf16 v[180:183], a[40:43], a[164:167], v[180:183]\n\t"
    "ds_read_b64_tr_b16 a[38:39], v11 offset:43840\n\t"
    "v_mfma_f32_16x16x32_bf16 v[184:187], a[40:43], a[168:171], v[184:187]\n\t"
    "buffer_load_dwordx4 a[120:123], v2, s[52:55], s72 offen offset:2048 sc1\n\t"
    "v_mfma_f32_16x16x32_bf16 v[188:191], a[40:43], a[172:175], v[188:191]\n\t"
    "v_mfma_f32_16x16x32_bf16 v[192:195], a[44:47], a[160:163], v[192:195]\n\t"
    "ds_read_b64_tr_b16 a[40:41], v11 offset:42080\n\t"
    "v_mfma_f32_16x16x32_bf16 v[196:199], a[44:47], a[164:167], v[196:199]\n\t"
    "ds_read_b64_tr_b16 a[42:43], v11 offset:43872\n\t"
    "v_mfma_f32_16x16x32_bf16 v[200:203], a[44:47], a[168:171], v[200:203]\n\t"
    "v_mfma_f32_16x16x32_bf16 v[204:207], a[44:47], a[172:175], v[204:207]\n\t"
    "v_mfma_f32_16x16x32_bf16 v[208:211], a[48:51], a[160:163], v[208:211]\n\t"
    "ds_read_b64_tr_b16 a[44:45], v11 offset:42112\n\t"
    "v_mfma_f32_16x16x32_bf16 v[212:215], a[48:51], a[164:167], v[212:215]\n\t"
    "ds_read_b64_tr_b16 a[46:47], v11 offset:43904\n\t"
    "v_mfma_f32_16x16x32_bf16 v[216:219], a[48:51], a[168:171], v[216:219]\n\t"
    "buffer_load_dwordx4 a[124:127], v2, s[52:55], s72 offen offset:2304 sc1\n\t"
    "v_mfma_f32_16x16x32_bf16 v[220:223], a[48:51], a[172:175], v[220:223]\n\t"
    "v_mfma_f32_16x16x32_bf16 v[224:227], a[52:55], a[160:163], v[224:227]\n\t"
    "ds_read_b64_tr_b16 a[48:49], v11 offset:42144\n\t"
    "s_add_u32 s72, s72, 4096\n\t"
    "v_mfma_f32_16x16x32_bf16 v[228:231], a[52:55], a[164:167], v[228:231]\n\t"
    "ds_read_b64_tr_b16 a[50:51], v11 offset:43936\n\t"
    "s_add_u32 m0, s74, 57344\n\t"
    "v_mfma_f32_16x16x32_bf16 v[232:235], a[52:55], a[168:171], v[232:235]\n\t"
    "v_mfma_f32_16x16x32_bf16 v[236:239], a[52:55], a[172:175], v[236:239]\n\t"
    "s_waitcnt vmcnt(30) lgkmcnt(15)\n\t"
    "v_mfma_f32_16x16x32_bf16 v[16:19], a[0:3], a[176:179], v[16:19]\n\t"
    "s_waitcnt vmcnt(23)\n\t"
    "s_barrier\n\t"
    "ds_read_b64_tr_b16 a[52:53], v11 offset:42176\n\t"
    "v_mfma_f32_16x16x32_bf16 v[20:23], a[0:3], a[180:183], v[20:23]\n\t"
    "ds_read_b64_tr_b16 a[54:55], v11 offset:43968\n\t"
    "v_mfma_f32_16x16x32_bf16 v[24:27], a[0:3], a[184:187], v[24:27]\n\t"
    "buffer_load_dwordx4 v3, s[56:59], s73 offen offset:0 lds\n\t"
    "v_mfma_f32_16x16x32_bf16 v[28:31], a[0:3], a[188:191], v[28:31]\n\t"
    "v_mfma_f32_16x16x32_bf16 v[32:35], a[4:7], a[176:179], v[32:35]\n\t"
    "ds_read_b64_tr_b16 a[0:1], v10 offset:0\n\t"
    "v_mfma_f32_16x16x32_bf16 v[36:39], a[4:7], a[180:183], v[36:39]\n\t"
    "ds_read_b64_tr_b16 a[2:3], v10 offset:1792\n\t"
    "v_mfma_f32_16x16x32_bf16 v[40:43], a[4:7], a[184:187], v[40:43]\n\t"
    "v_mfma_f32_16x16x32_bf16 v[44:47], a[4:7], a[188:191], v[44:47]\n\t"
    "v_mfma_f32_16x16x32_bf16 v[48:51], a[8:11], a[176:179], v[48:51]\n\t"
    "ds_read_b64_tr_b16 a[4:5], v10 offset:32\n\t"
    "v_mfma_f32_16x16x32_bf16 v[52:55], a[8:11], a[180:183], v[52:55]\n\t"
    "ds_read_b64_tr_b16 a[6:7], v10 offset:1824\n\t"
    "v_mfma_f32_16x16x32_bf16 v[56:59], a[8:11], a[184:187], v[56:59]\n\t"
    "buffer_load_dwordx4 v4, s[56:59], s73 offen offset:1024 lds\n\t"
    "v_mfma_f32_16x16x32_bf16 v[60:63], a[8:11], a[188:191], v[60:63]\n\t"
    "v_mfma_f32_16x16x32_bf16 v[64:67], a[12:15], a[176:179], v[64:67]\n\t"
    "ds_read_b64_tr_b16 a[8:9], v10 offset:64\n\t"
    "v_mfma_f32_16x16x32_bf16 v[68:71], a[12:15], a[180:183], v[68:71]\n\t"
    "ds_read_b64_tr_b16 a[10:11], v10 offset:1856\n\t"
    "v_mfma_f32_16x16x32_bf16 v[72:75], a[12:15], a[184:187], v[72:75]\n\t"
    "v_mfma_f32_16x16x32_bf16 v[76:79], a[12:15], a[188:191], v[76:79]\n\t"
    "s_waitcnt lgkmcnt(15)\n\t"
    "v_mfma_f32_16x16x32_bf16 v[80:83], a[16:19], a[176:179], v[80:83]\n\t"
    "ds_read_b64_tr_b16 a[12:13], v10 offset:96\n\t"
    "v_mfma_f32_16x16x32_bf16 v[84:87], a[16:19], a[180:183], v[84:87]\n\t"
    "ds_read_b64_tr_b16 a[14:15], v10 offset:1888\n\t"
    "v_mfma_f32_16x16x32_bf16 v[88:91], a[16:19], a[184:187], v[88:91]\n\t"
    "buffer_load_dwordx4 v5, s[56:59], s73 offen offset:2048 lds\n\t"
    "v_mfma_f32_16x16x32_bf16 v[92:95], a[16:19], a[188:191], v[92:95]\n\t"
    "v_mfma_f32_16x16x32_bf16 v[96:99], a[20:23], a[176:179], v[96:99]\n\t"
    "ds_read_b64_tr_b16 a[16:17], v10 offset:128\n\t"
    "v_mfma_f32_16x16x32_bf16 v[100:103], a[20:23], a[180:183], v[100:103]\n\t"
    "ds_read_b64_tr_b16 a[18:19], v10 offset:1920\n\t"
    "v_mfma_f32_16x16x32_bf16 v[104:107], a[20:23], a[184:187], v[104:107]\n\t"
    "v_mfma_f32_16x16x32_bf16 v[108:111], a[20:23], a[188:191], v[108:111]\n\t"
    "v_mfma_f32_16x16x32_bf16 v[112:115], a[24:27], a[176:179], v[112:115]\n\t"
    "ds_read_b64_tr_b16 a[20:21], v10 offset:160\n\t"
    "v_mfma_f32_16x16x32_bf16 v[116:119], a[24:27], a[180:183], v[116:119]\n\t"
    "ds_read_b64_tr_b16 a[22:23], v10 offset:1952\n\t"
    "v_mfma_f32_16x16x32_bf16 v[120:123], a[24:27], a[184:187], v[120:123]\n\t"
    "buffer_load_dwordx4 v6, s[56:59], s73 offen offset:3072 lds\n\t"
    "v_mfma_f32_16x16x32_bf16 v[124:127], a[24:27], a[188:191], v[124:127]\n\t"
    "v_mfma_f32_16x16x32_bf16 v[128:131], a[28:31], a[176:179], v[128:131]\n\t"
    "ds_read_b64_tr_b16 a[24:25], v10 offset:192\n\t"
    "s_add_u32 m0, s74, 61440\n\t"
    "v_mfma_f32_16x16x32_bf16 v[132:135], a[28:31], a[180:183], v[132:135]\n\t"
    "ds_read_b64_tr_b16 a[26:27], v10 offset:1984\n\t"
    "v_mfma_f32_16x16x32_bf16 v[136:139], a[28:31], a[184:187], v[136:139]\n\t"
    "buffer_load_dwordx4 v7, s[56:59], s73 offen offset:0 lds\n\t"
    "v_mfma_f32_16x16x32_bf16 v[140:143], a[28:31], a[188:191], v[140:143]\n\t"
    "s_waitcnt lgkmcnt(15)\n\t"
    "v_mfma_f32_16x16x32_bf16 v[144:147], a[32:35], a[176:179], v[144:147]\n\t"
    "ds_read_b64_tr_b16 a[28:29], v10 offset:14336\n\t"
    "v_mfma_f32_16x16x32_bf16 v[148:151], a[32:35], a[180:183], v[148:151]\n\t"
    "ds_read_b64_tr_b16 a[30:31], v10 offset:16128\n\t"
    "v_mfma_f32_16x16x32_bf16 v[152:155], a[32:35], a[184:187], v[152:155]\n\t"
    "v_mfma_f32_16x16x32_bf16 v[156:159], a[32:35], a[188:191], v[156:159]\n\t"
    "v_mfma_f32_16x16x32_bf16 v[160:163], a[36:39], a[176:179], v[160:163]\n\t"
    "ds_read_b64_tr_b16 a[32:33], v10 offset:14368\n\t"
    "v_mfma_f32_16x16x32_bf16 v[164:167], a[36:39], a[180:183], v[164:167]\n\t"
    "ds_read_b64_tr_b16 a[34:35], v10 offset:16160\n\t"
    "v_mfma_f32_16x16x32_bf16 v[168:171], a[36:39], a[184:187], v[168:171]\n\t"
    "buffer_load_dwordx4 v8, s[56:59], s73 offen offset:1024 lds\n\t"
    "v_mfma_f32_16x16x32_bf16 v[172:175], a[36:39], a[188:191], v[172:175]\n\t"
    "v_mfma_f32_16x16x32_bf16 v[176:179], a[40:43], a[176:179], v[176:179]\n\t"
    "ds_read_b64_tr_b16 a[36:37], v10 offset:14400\n\t"
    "v_mfma_f32_16x16x32_bf16 v[180:183], a[40:43], a[180:183], v[180:183]\n\t"
    "ds_read_b64_tr_b16 a[38:39], v10 offset:16192\n\t"
    "v_mfma_f32_16x16x32_bf16 v[184:187], a[40:43], a[184:187], v[184:187]\n\t"
    "v_mfma_f32_16x16x32_bf16 v[188:191], a[40:43], a[188:191], v[188:191]\n\t"
    "v_mfma_f32_16x16x32_bf16 v[192:195], a[44:47], a[176:179], v[192:195]\n\t"
    "ds_read_b64_tr_b16 a[40:41], v10 offset:14432\n\t"
    "v_mfma_f32_16x16x32_bf16 v[196:199], a[44:47], a[180:183], v[196:199]\n\t"
    "ds_read_b64_tr_b16 a[42:43], v10 offset:16224\n\t"
    "v_mfma_f32_16x16x32_bf16 v[200:203], a[44:47], a[184:187], v[200:203]\n\t"
    "buffer_load_dwordx4 v9, s[56:59], s73 offen offset:2048 lds\n\t"
    "v_mfma_f32_16x16x32_bf16 v[204:207], a[44:47], a[188:191], v[204:207]\n\t"
    "s_waitcnt lgkmcnt(15)\n\t"
    "s_add_u32 s73, s73, 458752\n\t"
    "v_mfma_f32_16x16x32_bf16 v[208:211], a[48:51], a[176:179], v[208:211]\n\t"
    "ds_read_b64_tr_b16 a[44:45], v10 offset:14464\n\t"
    "v_mfma_f32_16x16x32_bf16 v[212:215], a[48:51], a[180:183], v[212:215]\n\t"
    "ds_read_b64_tr_b16 a[46:47], v10 offset:16256\n\t"
    "v_mfma_f32_16x16x32_bf16 v[216:219], a[48:51], a[184:187], v[216:219]\n\t"
    "v_mfma_f32_16x16x32_bf16 v[220:223], a[48:51], a[188:191], v[220:223]\n\t"
    "v_mfma_f32_16x16x32_bf16 v[224:227], a[52:55], a[176:179], v[224:227]\n\t"
    "ds_read_b64_tr_b16 a[48:49], v10 offset:14496\n\t"
    "v_mfma_f32_16x16x32_bf16 v[228:231], a[52:55], a[180:183], v[228:231]\n\t"
    "ds_read_b64_tr_b16 a[50:51], v10 offset:16288\n\t"
    "v_mfma_f32_16x16x32_bf16 v[232:235], a[52:55], a[184:187], v[232:235]\n\t"
    "buffer_load_dwordx4 a[128:131], v1, s[52:55], s72 offen offset:0 sc1\n\t"
    "v_mfma_f32_16x16x32_bf16 v[236:239], a[52:55], a[188:191], v[236:239]\n\t"
    "s_waitcnt vmcnt(27)\n\t"
    "v_mfma_f32_16x16x32_bf16 v[16:19], a[0:3], a[64:67], v[16:19]\n\t"
    "ds_read_b64_tr_b16 a[52:53], v10 offset:14528\n\t"
    "v_mfma_f32_16x16x32_bf16 v[20:23], a[0:3], a[68:71], v[20:23]\n\t"
    "ds_read_b64_tr_b16 a[54:55], v10 offset:16320\n\t"
    "v_mfma_f32_16x16x32_bf16 v[24:27], a[0:3], a[72:75], v[24:27]\n\t"
    "v_mfma_f32_16x16x32_bf16 v[28:31], a[0:3], a[76:79], v[28:31]\n\t"
    "buffer_load_dwordx4 a[132:135], v1, s[52:55], s72 offen offset:256 sc1\n\t"
    "v_mfma_f32_16x16x32_bf16 v[32:35], a[4:7], a[64:67], v[32:35]\n\t"
    "ds_read_b64_tr_b16 a[0:1], v10 offset:7168\n\t"
    "v_mfma_f32_16x16x32_bf16 v[36:39], a[4:7], a[68:71], v[36:39]\n\t"
    "ds_read_b64_tr_b16 a[2:3], v10 offset:8960\n\t"
    "v_mfma_f32_16x16x32_bf16 v[40:43], a[4:7], a[72:75], v[40:43]\n\t"
    "v_mfma_f32_16x16x32_bf16 v[44:47], a[4:7], a[76:79], v[44:47]\n\t"
    "s_waitcnt lgkmcnt(15)\n\t"
    "v_mfma_f32_16x16x32_bf16 v[48:51], a[8:11], a[64:67], v[48:51]\n\t"
    "ds_read_b64_tr_b16 a[4:5], v10 offset:7200\n\t"
    "v_mfma_f32_16x16x32_bf16 v[52:55], a[8:11], a[68:71], v[52:55]\n\t"
    "ds_read_b64_tr_b16 a[6:7], v10 offset:8992\n\t"
    "v_mfma_f32_16x16x32_bf16 v[56:59], a[8:11], a[72:75], v[56:59]\n\t"
    "v_mfma_f32_16x16x32_bf16 v[60:63], a[8:11], a[76:79], v[60:63]\n\t"
    "buffer_load_dwordx4 a[136:139], v2, s[52:55], s72 offen offset:0 sc1\n\t"
    "v_mfma_f32_16x16x32_bf16 v[64:67], a[12:15], a[64:67], v[64:67]\n\t"
    "ds_read_b64_tr_b16 a[8:9], v10 offset:7232\n\t"
    "v_mfma_f32_16x16x32_bf16 v[68:71], a[12:15], a[68:71], v[68:71]\n\t"
    "ds_read_b64_tr_b16 a[10:11], v10 offset:9024\n\t"
    "v_mfma_f32_16x16x32_bf16 v[72:75], a[12:15], a[72:75], v[72:75]\n\t"
    "v_mfma_f32_16x16x32_bf16 v[76:79], a[12:15], a[76:79], v[76:79]\n\t"
    "v_mfma_f32_16x16x32_bf16 v[80:83], a[16:19], a[64:67], v[80:83]\n\t"
    "ds_read_b64_tr_b16 a[12:13], v10 offset:7264\n\t"
    "v_mfma_f32_16x16x32_bf16 v[84:87], a[16:19], a[68:71], v[84:87]\n\t"
    "ds_read_b64_tr_b16 a[14:15], v10 offset:9056\n\t"
    "v_mfma_f32_16x16x32_bf16 v[88:91], a[16:19], a[72:75], v[88:91]\n\t"
    "buffer_load_dwordx4 a[140:143], v2, s[52:55], s72 offen offset:256 sc1\n\t"
    "v_mfma_f32_16x16x32_bf16 v[92:95], a[16:19], a[76:79], v[92:95]\n\t"
    "v_mfma_f32_16x16x32_bf16 v[96:99], a[20:23], a[64:67], v[96:99]\n\t"
    "ds_read_b64_tr_b16 a[16:17], v10 offset:7296\n\t"
    "v_mfma_f32_16x16x32_bf16 v[100:103], a[20:23], a[68:71], v[100:103]\n\t"
    "ds_read_b64_tr_b16 a[18:19], v10 offset:9088\n\t"
    "v_mfma_f32_16x16x32_bf16 v[104:107], a[20:23], a[72:75], v[104:107]\n\t"
    "v_mfma_f32_16x16x32_bf16 v[108:111], a[20:23], a[76:79], v[108:111]\n\t"
    "s_waitcnt lgkmcnt(15)\n\t"
    "v_mfma_f32_16x16x32_bf16 v[112:115], a[24:27], a[64:67], v[112:115]\n\t"
    "ds_read_b64_tr_b16 a[20:21], v10 offset:7328\n\t"
    "v_mfma_f32_16x16x32_bf16 v[116:119], a[24:27], a[68:71], v[116:119]\n\t"
    "ds_read_b64_tr_b16 a[22:23], v10 offset:9120\n\t"
    "v_mfma_f32_16x16x32_bf16 v[120:123], a[24:27], a[72:75], v[120:123]\n\t"
    "buffer_load_dwordx4 a[144:147], v1, s[52:55], s72 offen offset:2048 sc1\n\t"
    "v_mfma_f32_16x16x32_bf16 v[124:127], a[24:27], a[76:79], v[124:127]\n\t"
    "v_mfma_f32_16x16x32_bf16 v[128:131], a[28:31], a[64:67], v[128:131]\n\t"
    "ds_read_b64_tr_b16 a[24:25], v10 offset:7360\n\t"
    "v_mfma_f32_16x16x32_bf16 v[132:135], a[28:31], a[68:71], v[132:135]\n\t"
    "ds_read_b64_tr_b16 a[26:27], v10 offset:9152\n\t"
    "v_mfma_f32_16x16x32_bf16 v[136:139], a[28:31], a[72:75], v[136:139]\n\t"
    "v_mfma_f32_16x16x32_bf16 v[140:143], a[28:31], a[76:79], v[140:143]\n\t"
    "v_mfma_f32_16x16x32_bf16 v[144:147], a[32:35], a[64:67], v[144:147]\n\t"
    "ds_read_b64_tr_b16 a[28:29], v10 offset:21504\n\t"
    "v_mfma_f32_16x16x32_bf16 v[148:151], a[32:35], a[68:71], v[148:151]\n\t"
    "ds_read_b64_tr_b16 a[30:31], v10 offset:23296\n\t"
    "v_mfma_f32_16x16x32_bf16 v[152:155], a[32:35], a[72:75], v[152:155]\n\t"
    "buffer_load_dwordx4 a[148:151], v1, s[52:55], s72 offen offset:2304 sc1\n\t"
    "v_mfma_f32_16x16x32_bf16 v[156:159], a[32:35], a[76:79], v[156:159]\n\t"
    "v_mfma_f32_16x16x32_bf16 v[160:163], a[36:39], a[64:67], v[160:163]\n\t"
    "ds_read_b64_tr_b16 a[32:33], v10 offset:21536\n\t"
    "v_mfma_f32_16x16x32_bf16 v[164:167], a[36:39], a[68:71], v[164:167]\n\t"
    "ds_read_b64_tr_b16 a[34:35], v10 offset:23328\n\t"
    "v_mfma_f32_16x16x32_bf16 v[168:171], a[36:39], a[72:75], v[168:171]\n\t"
    "v_mfma_f32_16x16x32_bf16 v[172:175], a[36:39], a[76:79], v[172:175]\n\t"
    "s_waitcnt lgkmcnt(15)\n\t"
    "v_mfma_f32_16x16x32_bf16 v[176:179], a[40:43], a[64:67], v[176:179]\n\t"
    "ds_read_b64_tr_b16 a[36:37], v10 offset:21568\n\t"
    "v_mfma_f32_16x16x32_bf16 v[180:183], a[40:43], a[68:71], v[180:183]\n\t"
    "ds_read_b64_tr_b16 a[38:39], v10 offset:23360\n\t"
    "v_mfma_f32_16x16x32_bf16 v[184:187], a[40:43], a[72:75], v[184:187]\n\t"
    "buffer_load_dwordx4 a[152:155], v2, s[52:55], s72 offen offset:2048 sc1\n\t"
    "v_mfma_f32_16x16x32_bf16 v[188:191], a[40:43], a[76:79], v[188:191]\n\t"
    "v_mfma_f32_16x16x32_bf16 v[192:195], a[44:47], a[64:67], v[192:195]\n\t"
    "ds_read_b64_tr_b16 a[40:41], v10 offset:21600\n\t"
    "v_mfma_f32_16x16x32_bf16 v[196:199], a[44:47], a[68:71], v[196:199]\n\t"
    "ds_read_b64_tr_b16 a[42:43], v10 offset:23392\n\t"
    "v_mfma_f32_16x16x32_bf16 v[200:203], a[44:47], a[72:75], v[200:203]\n\t"
    "v_mfma_f32_16x16x32_bf16 v[204:207], a[44:47], a[76:79], v[204:207]\n\t"
    "v_mfma_f32_16x16x32_bf16 v[208:211], a[48:51], a[64:67], v[208:211]\n\t"
    "ds_read_b64_tr_b16 a[44:45], v10 offset:21632\n\t"
    "v_mfma_f32_16x16x32_bf16 v[212:215], a[48:51], a[68:71], v[212:215]\n\t"
    "ds_read_b64_tr_b16 a[46:47], v10 offset:23424\n\t"
    "v_mfma_f32_16x16x32_bf16 v[216:219], a[48:51], a[72:75], v[216:219]\n\t"
    "buffer_load_dwordx4 a[156:159], v2, s[52:55], s72 offen offset:2304 sc1\n\t"
    "v_mfma_f32_16x16x32_bf16 v[220:223], a[48:51], a[76:79], v[220:223]\n\t"
    "v_mfma_f32_16x16x32_bf16 v[224:227], a[52:55], a[64:67], v[224:227]\n\t"
    "ds_read_b64_tr_b16 a[48:49], v10 offset:21664\n\t"
    "s_add_u32 s72, s72, 4096\n\t"
    "v_mfma_f32_16x16x32_bf16 v[228:231], a[52:55], a[68:71], v[228:231]\n\t"
    "ds_read_b64_tr_b16 a[50:51], v10 offset:23456\n\t"
    "s_add_u32 m0, s74, 86016\n\t"
    "v_mfma_f32_16x16x32_bf16 v[232:235], a[52:55], a[72:75], v[232:235]\n\t"
    "v_mfma_f32_16x16x32_bf16 v[236:239], a[52:55], a[76:79], v[236:239]\n\t"
    "s_waitcnt vmcnt(30) lgkmcnt(15)\n\t"
    "v_mfma_f32_16x16x32_bf16 v[16:19], a[0:3], a[80:83], v[16:19]\n\t"
    "s_waitcnt vmcnt(23)\n\t"
    "s_barrier\n\t"
    "ds_read_b64_tr_b16 a[52:53], v10 offset:21696\n\t"
    "v_mfma_f32_16x16x32_bf16 v[20:23], a[0:3], a[84:87], v[20:23]\n\t"
    "ds_read_b64_tr_b16 a[54:55], v10 offset:23488\n\t"
    "v_mfma_f32_16x16x32_bf16 v[24:27], a[0:3], a[88:91], v[24:27]\n\t"
    "buffer_load_dwordx4 v3, s[56:59], s73 offen offset:0 lds\n\t"
    "v_mfma_f32_16x16x32_bf16 v[28:31], a[0:3], a[92:95], v[28:31]\n\t"
    "v_mfma_f32_16x16x32_bf16 v[32:35], a[4:7], a[80:83], v[32:35]\n\t"
    "ds_read_b64_tr_b16 a[0:1], v10 offset:28672\n\t"
    "v_mfma_f32_16x16x32_bf16 v[36:39], a[4:7], a[84:87], v[36:39]\n\t"
    "ds_read_b64_tr_b16 a[2:3], v10 offset:30464\n\t"
    "v_mfma_f32_16x16x32_bf16 v[40:43], a[4:7], a[88:91], v[40:43]\n\t"
    "v_mfma_f32_16x16x32_bf16 v[44:47], a[4:7], a[92:95], v[44:47]\n\t"
    "v_mfma_f32_16x16x32_bf16 v[48:51], a[8:11], a[80:83], v[48:51]\n\t"
    "ds_read_b64_tr_b16 a[4:5], v10 offset:28704\n\t"
    "v_mfma_f32_16x16x32_bf16 v[52:55], a[8:11], a[84:87], v[52:55]\n\t"
    "ds_read_b64_tr_b16 a[6:7], v10 offset:30496\n\t"
    "v_mfma_f32_16x16x32_bf16 v[56:59], a[8:11], a[88:91], v[56:59]\n\t"
    "buffer_load_dwordx4 v4, s[56:59], s73 offen offset:1024 lds\n\t"
    "v_mfma_f32_16x16x32_bf16 v[60:63], a[8:11], a[92:95], v[60:63]\n\t"
    "v_mfma_f32_16x16x32_bf16 v[64:67], a[12:15], a[80:83], v[64:67]\n\t"
    "ds_read_b64_tr_b16 a[8:9], v10 offset:28736\n\t"
    "v_mfma_f32_16x16x32_bf16 v[68:71], a[12:15], a[84:87], v[68:71]\n\t"
    "ds_read_b64_tr_b16 a[10:11], v10 offset:30528\n\t"
    "v_mfma_f32_16x16x32_bf16 v[72:75], a[12:15], a[88:91], v[72:75]\n\t"
    "v_mfma_f32_16x16x32_bf16 v[76:79], a[12:15], a[92:95], v[76:79]\n\t"
    "s_waitcnt lgkmcnt(15)\n\t"
    "v_mfma_f32_16x16x32_bf16 v[80:83], a[16:19], a[80:83], v[80:83]\n\t"
    "ds_read_b64_tr_b16 a[12:13], v10 offset:28768\n\t"
    "v_mfma_f32_16x16x32_bf16 v[84:87], a[16:19], a[84:87], v[84:87]\n\t"
    "ds_read_b64_tr_b16 a[14:15], v10 offset:30560\n\t"
    "v_mfma_f32_16x16x32_bf16 v[88:91], a[16:19], a[88:91], v[88:91]\n\t"
    "buffer_load_dwordx4 v5, s[56:59], s73 offen offset:2048 lds\n\t"
    "v_mfma_f32_16x16x32_bf16 v[92:95], a[16:19], a[92:95], v[92:95]\n\t"
    "v_mfma_f32_16x16x32_bf16 v[96:99], a[20:23], a[80:83], v[96:99]\n\t"
    "ds_read_b64_tr_b16 a[16:17], v10 offset:28800\n\t"
    "v_mfma_f32_16x16x32_bf16 v[100:103], a[20:23], a[84:87], v[100:103]\n\t"
    "ds_read_b64_tr_b16 a[18:19], v10 offset:30592\n\t"
    "v_mfma_f32_16x16x32_bf16 v[104:107], a[20:23], a[88:91], v[104:107]\n\t"
    "v_mfma_f32_16x16x32_bf16 v[108:111], a[20:23], a[92:95], v[108:111]\n\t"
    "v_mfma_f32_16x16x32_bf16 v[112:115], a[24:27], a[80:83], v[112:115]\n\t"
    "ds_read_b64_tr_b16 a[20:21], v10 offset:28832\n\t"
    "v_mfma_f32_16x16x32_bf16 v[116:119], a[24:27], a[84:87], v[116:119]\n\t"
    "ds_read_b64_tr_b16 a[22:23], v10 offset:30624\n\t"
    "v_mfma_f32_16x16x32_bf16 v[120:123], a[24:27], a[88:91], v[120:123]\n\t"
    "buffer_load_dwordx4 v6, s[56:59], s73 offen offset:3072 lds\n\t"
    "v_mfma_f32_16x16x32_bf16 v[124:127], a[24:27], a[92:95], v[124:127]\n\t"
    "v_mfma_f32_16x16x32_bf16 v[128:131], a[28:31], a[80:83], v[128:131]\n\t"
    "ds_read_b64_tr_b16 a[24:25], v10 offset:28864\n\t"
    "s_add_u32 m0, s74, 90112\n\t"
    "v_mfma_f32_16x16x32_bf16 v[132:135], a[28:31], a[84:87], v[132:135]\n\t"
    "ds_read_b64_tr_b16 a[26:27], v10 offset:30656\n\t"
    "v_mfma_f32_16x16x32_bf16 v[136:139], a[28:31], a[88:91], v[136:139]\n\t"
    "buffer_load_dwordx4 v7, s[56:59], s73 offen offset:0 lds\n\t"
    "v_mfma_f32_16x16x32_bf16 v[140:143], a[28:31], a[92:95], v[140:143]\n\t"
    "s_waitcnt lgkmcnt(15)\n\t"
    "v_mfma_f32_16x16x32_bf16 v[144:147], a[32:35], a[80:83], v[144:147]\n\t"
    "ds_read_b64_tr_b16 a[28:29], v10 offset:43008\n\t"
    "v_mfma_f32_16x16x32_bf16 v[148:151], a[32:35], a[84:87], v[148:151]\n\t"
    "ds_read_b64_tr_b16 a[30:31], v10 offset:44800\n\t"
    "v_mfma_f32_16x16x32_bf16 v[152:155], a[32:35], a[88:91], v[152:155]\n\t"
    "v_mfma_f32_16x16x32_bf16 v[156:159], a[32:35], a[92:95], v[156:159]\n\t"
    "v_mfma_f32_16x16x32_bf16 v[160:163], a[36:39], a[80:83], v[160:163]\n\t"
    "ds_read_b64_tr_b16 a[32:33], v10 offset:43040\n\t"
    "v_mfma_f32_16x16x32_bf16 v[164:167], a[36:39], a[84:87], v[164:167]\n\t"
    "ds_read_b64_tr_b16 a[34:35], v10 offset:44832\n\t"
    "v_mfma_f32_16x16x32_bf16 v[168:171], a[36:39], a[88:91], v[168:171]\n\t"
    "buffer_load_dwordx4 v8, s[56:59], s73 offen offset:1024 lds\n\t"
    "v_mfma_f32_16x16x32_bf16 v[172:175], a[36:39], a[92:95], v[172:175]\n\t"
    "v_mfma_f32_16x16x32_bf16 v[176:179], a[40:43], a[80:83], v[176:179]\n\t"
    "ds_read_b64_tr_b16 a[36:37], v10 offset:43072\n\t"
    "v_mfma_f32_16x16x32_bf16 v[180:183], a[40:43], a[84:87], v[180:183]\n\t"
    "ds_read_b64_tr_b16 a[38:39], v10 offset:44864\n\t"
    "v_mfma_f32_16x16x32_bf16 v[184:187], a[40:43], a[88:91], v[184:187]\n\t"
    "v_mfma_f32_16x16x32_bf16 v[188:191], a[40:43], a[92:95], v[188:191]\n\t"
    "v_mfma_f32_16x16x32_bf16 v[192:195], a[44:47], a[80:83], v[192:195]\n\t"
    "ds_read_b64_tr_b16 a[40:41], v10 offset:43104\n\t"
    "v_mfma_f32_16x16x32_bf16 v[196:199], a[44:47], a[84:87], v[196:199]\n\t"
    "ds_read_b64_tr_b16 a[42:43], v10 offset:44896\n\t"
    "v_mfma_f32_16x16x32_bf16 v[200:203], a[44:47], a[88:91], v[200:203]\n\t"
    "buffer_load_dwordx4 v9, s[56:59], s73 offen offset:2048 lds\n\t"
    "v_mfma_f32_16x16x32_bf16 v[204:207], a[44:47], a[92:95], v[204:207]\n\t"
    "s_waitcnt lgkmcnt(15)\n\t"
    "s_add_u32 s73, s73, 458752\n\t"
    "v_mfma_f32_16x16x32_bf16 v[208:211], a[48:51], a[80:83], v[208:211]\n\t"
    "ds_read_b64_tr_b16 a[44:45], v10 offset:43136\n\t"
    "v_mfma_f32_16x16x32_bf16 v[212:215], a[48:51], a[84:87], v[212:215]\n\t"
    "ds_read_b64_tr_b16 a[46:47], v10 offset:44928\n\t"
    "v_mfma_f32_16x16x32_bf16 v[216:219], a[48:51], a[88:91], v[216:219]\n\t"
    "v_mfma_f32_16x16x32_bf16 v[220:223], a[48:51], a[92:95], v[220:223]\n\t"
    "v_mfma_f32_16x16x32_bf16 v[224:227], a[52:55], a[80:83], v[224:227]\n\t"
    "ds_read_b64_tr_b16 a[48:49], v10 offset:43168\n\t"
    "v_mfma_f32_16x16x32_bf16 v[228:231], a[52:55], a[84:87], v[228:231]\n\t"
    "ds_read_b64_tr_b16 a[50:51], v10 offset:44960\n\t"
    "v_mfma_f32_16x16x32_bf16 v[232:235], a[52:55], a[88:91], v[232:235]\n\t"
    "buffer_load_dwordx4 a[160:163], v1, s[52:55], s72 offen offset:0 sc1\n\t"
    "v_mfma_f32_16x16x32_bf16 v[236:239], a[52:55], a[92:95], v[236:239]\n\t"
    "s_waitcnt vmcnt(27)\n\t"
    "s_sub_u32 s79, s79, 1\n\t"
    "s_cmp_lg_u32 s79, 0\n\t"
    "s_cbranch_scc1 Lm_gu_0_0\n\t"
    "v_mfma_f32_16x16x32_bf16 v[16:19], a[0:3], a[96:99], v[16:19]\n\t"
    "ds_read_b64_tr_b16 a[52:53], v10 offset:43200\n\t"
    "v_mfma_f32_16x16x32_bf16 v[20:23], a[0:3], a[100:103], v[20:23]\n\t"
    "ds_read_b64_tr_b16 a[54:55], v10 offset:44992\n\t"
    "v_mfma_f32_16x16x32_bf16 v[24:27], a[0:3], a[104:107], v[24:27]\n\t"
    "v_mfma_f32_16x16x32_bf16 v[28:31], a[0:3], a[108:111], v[28:31]\n\t"
    "buffer_load_dwordx4 a[164:167], v1, s[52:55], s72 offen offset:256 sc1\n\t"
    "v_mfma_f32_16x16x32_bf16 v[32:35], a[4:7], a[96:99], v[32:35]\n\t"
    "ds_read_b64_tr_b16 a[0:1], v10 offset:35840\n\t"
    "v_mfma_f32_16x16x32_bf16 v[36:39], a[4:7], a[100:103], v[36:39]\n\t"
    "ds_read_b64_tr_b16 a[2:3], v10 offset:37632\n\t"
    "v_mfma_f32_16x16x32_bf16 v[40:43], a[4:7], a[104:107], v[40:43]\n\t"
    "v_mfma_f32_16x16x32_bf16 v[44:47], a[4:7], a[108:111], v[44:47]\n\t"
    "s_waitcnt lgkmcnt(15)\n\t"
    "v_mfma_f32_16x16x32_bf16 v[48:51], a[8:11], a[96:99], v[48:51]\n\t"
    "ds_read_b64_tr_b16 a[4:5], v10 offset:35872\n\t"
    "v_mfma_f32_16x16x32_bf16 v[52:55], a[8:11], a[100:103], v[52:55]\n\t"
    "ds_read_b64_tr_b16 a[6:7], v10 offset:37664\n\t"
    "v_mfma_f32_16x16x32_bf16 v[56:59], a[8:11], a[104:107], v[56:59]\n\t"
    "v_mfma_f32_16x16x32_bf16 v[60:63], a[8:11], a[108:111], v[60:63]\n\t"
    "buffer_load_dwordx4 a[168:171], v2, s[52:55], s72 offen offset:0 sc1\n\t"
    "v_mfma_f32_16x16x32_bf16 v[64:67], a[12:15], a[96:99], v[64:67]\n\t"
    "ds_read_b64_tr_b16 a[8:9], v10 offset:35904\n\t"
    "v_mfma_f32_16x16x32_bf16 v[68:71], a[12:15], a[100:103], v[68:71]\n\t"
    "ds_read_b64_tr_b16 a[10:11], v10 offset:37696\n\t"
    "v_mfma_f32_16x16x32_bf16 v[72:75], a[12:15], a[104:107], v[72:75]\n\t"
    "v_mfma_f32_16x16x32_bf16 v[76:79], a[12:15], a[108:111], v[76:79]\n\t"
    "v_mfma_f32_16x16x32_bf16 v[80:83], a[16:19], a[96:99], v[80:83]\n\t"
    "ds_read_b64_tr_b16 a[12:13], v10 offset:35936\n\t"
    "v_mfma_f32_16x16x32_bf16 v[84:87], a[16:19], a[100:103], v[84:87]\n\t"
    "ds_read_b64_tr_b16 a[14:15], v10 offset:37728\n\t"
    "v_mfma_f32_16x16x32_bf16 v[88:91], a[16:19], a[104:107], v[88:91]\n\t"
    "buffer_load_dwordx4 a[172:175], v2, s[52:55], s72 offen offset:256 sc1\n\t"
    "v_mfma_f32_16x16x32_bf16 v[92:95], a[16:19], a[108:111], v[92:95]\n\t"
    "v_mfma_f32_16x16x32_bf16 v[96:99], a[20:23], a[96:99], v[96:99]\n\t"
    "ds_read_b64_tr_b16 a[16:17], v10 offset:35968\n\t"
    "v_mfma_f32_16x16x32_bf16 v[100:103], a[20:23], a[100:103], v[100:103]\n\t"
    "ds_read_b64_tr_b16 a[18:19], v10 offset:37760\n\t"
    "v_mfma_f32_16x16x32_bf16 v[104:107], a[20:23], a[104:107], v[104:107]\n\t"
    "v_mfma_f32_16x16x32_bf16 v[108:111], a[20:23], a[108:111], v[108:111]\n\t"
    "s_waitcnt lgkmcnt(15)\n\t"
    "v_mfma_f32_16x16x32_bf16 v[112:115], a[24:27], a[96:99], v[112:115]\n\t"
    "ds_read_b64_tr_b16 a[20:21], v10 offset:36000\n\t"
    "v_mfma_f32_16x16x32_bf16 v[116:119], a[24:27], a[100:103], v[116:119]\n\t"
    "ds_read_b64_tr_b16 a[22:23], v10 offset:37792\n\t"
    "v_mfma_f32_16x16x32_bf16 v[120:123], a[24:27], a[104:107], v[120:123]\n\t"
    "buffer_load_dwordx4 a[176:179], v1, s[52:55], s72 offen offset:2048 sc1\n\t"
    "v_mfma_f32_16x16x32_bf16 v[124:127], a[24:27], a[108:111], v[124:127]\n\t"
    "v_mfma_f32_16x16x32_bf16 v[128:131], a[28:31], a[96:99], v[128:131]\n\t"
    "ds_read_b64_tr_b16 a[24:25], v10 offset:36032\n\t"
    "v_mfma_f32_16x16x32_bf16 v[132:135], a[28:31], a[100:103], v[132:135]\n\t"
    "ds_read_b64_tr_b16 a[26:27], v10 offset:37824\n\t"
    "v_mfma_f32_16x16x32_bf16 v[136:139], a[28:31], a[104:107], v[136:139]\n\t"
    "v_mfma_f32_16x16x32_bf16 v[140:143], a[28:31], a[108:111], v[140:143]\n\t"
    "v_mfma_f32_16x16x32_bf16 v[144:147], a[32:35], a[96:99], v[144:147]\n\t"
    "ds_read_b64_tr_b16 a[28:29], v10 offset:50176\n\t"
    "v_mfma_f32_16x16x32_bf16 v[148:151], a[32:35], a[100:103], v[148:151]\n\t"
    "ds_read_b64_tr_b16 a[30:31], v10 offset:51968\n\t"
    "v_mfma_f32_16x16x32_bf16 v[152:155], a[32:35], a[104:107], v[152:155]\n\t"
    "buffer_load_dwordx4 a[180:183], v1, s[52:55], s72 offen offset:2304 sc1\n\t"
    "v_mfma_f32_16x16x32_bf16 v[156:159], a[32:35], a[108:111], v[156:159]\n\t"
    "v_mfma_f32_16x16x32_bf16 v[160:163], a[36:39], a[96:99], v[160:163]\n\t"
    "ds_read_b64_tr_b16 a[32:33], v10 offset:50208\n\t"
    "v_mfma_f32_16x16x32_bf16 v[164:167], a[36:39], a[100:103], v[164:167]\n\t"
    "ds_read_b64_tr_b16 a[34:35], v10 offset:52000\n\t"
    "v_mfma_f32_16x16x32_bf16 v[168:171], a[36:39], a[104:107], v[168:171]\n\t"
    "v_mfma_f32_16x16x32_bf16 v[172:175], a[36:39], a[108:111], v[172:175]\n\t"
    "s_waitcnt lgkmcnt(15)\n\t"
    "v_mfma_f32_16x16x32_bf16 v[176:179], a[40:43], a[96:99], v[176:179]\n\t"
    "ds_read_b64_tr_b16 a[36:37], v10 offset:50240\n\t"
    "v_mfma_f32_16x16x32_bf16 v[180:183], a[40:43], a[100:103], v[180:183]\n\t"
    "ds_read_b64_tr_b16 a[38:39], v10 offset:52032\n\t"
    "v_mfma_f32_16x16x32_bf16 v[184:187], a[40:43], a[104:107], v[184:187]\n\t"
    "buffer_load_dwordx4 a[184:187], v2, s[52:55], s72 offen offset:2048 sc1\n\t"
    "v_mfma_f32_16x16x32_bf16 v[188:191], a[40:43], a[108:111], v[188:191]\n\t"
    "v_mfma_f32_16x16x32_bf16 v[192:195], a[44:47], a[96:99], v[192:195]\n\t"
    "ds_read_b64_tr_b16 a[40:41], v10 offset:50272\n\t"
    "v_mfma_f32_16x16x32_bf16 v[196:199], a[44:47], a[100:103], v[196:199]\n\t"
    "ds_read_b64_tr_b16 a[42:43], v10 offset:52064\n\t"
    "v_mfma_f32_16x16x32_bf16 v[200:203], a[44:47], a[104:107], v[200:203]\n\t"
    "v_mfma_f32_16x16x32_bf16 v[204:207], a[44:47], a[108:111], v[204:207]\n\t"
    "v_mfma_f32_16x16x32_bf16 v[208:211], a[48:51], a[96:99], v[208:211]\n\t"
    "ds_read_b64_tr_b16 a[44:45], v10 offset:50304\n\t"
    "v_mfma_f32_16x16x32_bf16 v[212:215], a[48:51], a[100:103], v[212:215]\n\t"
    "ds_read_b64_tr_b16 a[46:47], v10 offset:52096\n\t"
    "v_mfma_f32_16x16x32_bf16 v[216:219], a[48:51], a[104:107], v[216:219]\n\t"
    "buffer_load_dwordx4 a[188:191], v2, s[52:55], s72 offen offset:2304 sc1\n\t"
    "v_mfma_f32_16x16x32_bf16 v[220:223], a[48:51], a[108:111], v[220:223]\n\t"
    "v_mfma_f32_16x16x32_bf16 v[224:227], a[52:55], a[96:99], v[224:227]\n\t"
    "ds_read_b64_tr_b16 a[48:49], v10 offset:50336\n\t"
    "s_add_u32 s72, s72, 4096\n\t"
    "v_mfma_f32_16x16x32_bf16 v[228:231], a[52:55], a[100:103], v[228:231]\n\t"
    "ds_read_b64_tr_b16 a[50:51], v10 offset:52128\n\t"
    "v_mfma_f32_16x16x32_bf16 v[232:235], a[52:55], a[104:107], v[232:235]\n\t"
    "v_mfma_f32_16x16x32_bf16 v[236:239], a[52:55], a[108:111], v[236:239]\n\t"
    "s_waitcnt vmcnt(30) lgkmcnt(15)\n\t"
    "v_mfma_f32_16x16x32_bf16 v[16:19], a[0:3], a[112:115], v[16:19]\n\t"
    "s_waitcnt vmcnt(23)\n\t"
    "s_barrier\n\t"
    "ds_read_b64_tr_b16 a[52:53], v10 offset:50368\n\t"
    "v_mfma_f32_16x16x32_bf16 v[20:23], a[0:3], a[116:119], v[20:23]\n\t"
    "ds_read_b64_tr_b16 a[54:55], v10 offset:52160\n\t"
    "v_mfma_f32_16x16x32_bf16 v[24:27], a[0:3], a[120:123], v[24:27]\n\t"
    "v_mfma_f32_16x16x32_bf16 v[28:31], a[0:3], a[124:127], v[28:31]\n\t"
    "v_mfma_f32_16x16x32_bf16 v[32:35], a[4:7], a[112:115], v[32:35]\n\t"
    "ds_read_b64_tr_b16 a[0:1], v10 offset:57344\n\t"
    "v_mfma_f32_16x16x32_bf16 v[36:39], a[4:7], a[116:119], v[36:39]\n\t"
    "ds_read_b64_tr_b16 a[2:3], v10 offset:59136\n\t"
    "v_mfma_f32_16x16x32_bf16 v[40:43], a[4:7], a[120:123], v[40:43]\n\t"
    "v_mfma_f32_16x16x32_bf16 v[44:47], a[4:7], a[124:127], v[44:47]\n\t"
    "v_mfma_f32_16x16x32_bf16 v[48:51], a[8:11], a[112:115], v[48:51]\n\t"
    "ds_read_b64_tr_b16 a[4:5], v10 offset:57376\n\t"
    "v_mfma_f32_16x16x32_bf16 v[52:55], a[8:11], a[116:119], v[52:55]\n\t"
    "ds_read_b64_tr_b16 a[6:7], v10 offset:59168\n\t"
    "v_mfma_f32_16x16x32_bf16 v[56:59], a[8:11], a[120:123], v[56:59]\n\t"
    "v_mfma_f32_16x16x32_bf16 v[60:63], a[8:11], a[124:127], v[60:63]\n\t"
    "v_mfma_f32_16x16x32_bf16 v[64:67], a[12:15], a[112:115], v[64:67]\n\t"
    "ds_read_b64_tr_b16 a[8:9], v10 offset:57408\n\t"
    "v_mfma_f32_16x16x32_bf16 v[68:71], a[12:15], a[116:119], v[68:71]\n\t"
    "ds_read_b64_tr_b16 a[10:11], v10 offset:59200\n\t"
    "v_mfma_f32_16x16x32_bf16 v[72:75], a[12:15], a[120:123], v[72:75]\n\t"
    "v_mfma_f32_16x16x32_bf16 v[76:79], a[12:15], a[124:127], v[76:79]\n\t"
    "s_waitcnt lgkmcnt(15)\n\t"
    "v_mfma_f32_16x16x32_bf16 v[80:83], a[16:19], a[112:115], v[80:83]\n\t"
    "ds_read_b64_tr_b16 a[12:13], v10 offset:57440\n\t"
    "v_mfma_f32_16x16x32_bf16 v[84:87], a[16:19], a[116:119], v[84:87]\n\t"
    "ds_read_b64_tr_b16 a[14:15], v10 offset:59232\n\t"
    "v_mfma_f32_16x16x32_bf16 v[88:91], a[16:19], a[120:123], v[88:91]\n\t"
    "v_mfma_f32_16x16x32_bf16 v[92:95], a[16:19], a[124:127], v[92:95]\n\t"
    "v_mfma_f32_16x16x32_bf16 v[96:99], a[20:23], a[112:115], v[96:99]\n\t"
    "ds_read_b64_tr_b16 a[16:17], v10 offset:57472\n\t"
    "v_mfma_f32_16x16x32_bf16 v[100:103], a[20:23], a[116:119], v[100:103]\n\t"
    "ds_read_b64_tr_b16 a[18:19], v10 offset:59264\n\t"
    "v_mfma_f32_16x16x32_bf16 v[104:107], a[20:23], a[120:123], v[104:107]\n\t"
    "v_mfma_f32_16x16x32_bf16 v[108:111], a[20:23], a[124:127], v[108:111]\n\t"
    "v_mfma_f32_16x16x32_bf16 v[112:115], a[24:27], a[112:115], v[112:115]\n\t"
    "ds_read_b64_tr_b16 a[20:21], v10 offset:57504\n\t"
    "v_mfma_f32_16x16x32_bf16 v[116:119], a[24:27], a[116:119], v[116:119]\n\t"
    "ds_read_b64_tr_b16 a[22:23], v10 offset:59296\n\t"
    "v_mfma_f32_16x16x32_bf16 v[120:123], a[24:27], a[120:123], v[120:123]\n\t"
    "v_mfma_f32_16x16x32_bf16 v[124:127], a[24:27], a[124:127], v[124:127]\n\t"
    "v_mfma_f32_16x16x32_bf16 v[128:131], a[28:31], a[112:115], v[128:131]\n\t"
    "ds_read_b64_tr_b16 a[24:25], v10 offset:57536\n\t"
    "v_mfma_f32_16x16x32_bf16 v[132:135], a[28:31], a[116:119], v[132:135]\n\t"
    "ds_read_b64_tr_b16 a[26:27], v10 offset:59328\n\t"
    "v_mfma_f32_16x16x32_bf16 v[136:139], a[28:31], a[120:123], v[136:139]\n\t"
    "v_mfma_f32_16x16x32_bf16 v[140:143], a[28:31], a[124:127], v[140:143]\n\t"
    "s_waitcnt lgkmcnt(15)\n\t"
    "v_mfma_f32_16x16x32_bf16 v[144:147], a[32:35], a[112:115], v[144:147]\n\t"
    "ds_read_b64_tr_b16 a[28:29], v11 offset:6144\n\t"
    "v_mfma_f32_16x16x32_bf16 v[148:151], a[32:35], a[116:119], v[148:151]\n\t"
    "ds_read_b64_tr_b16 a[30:31], v11 offset:7936\n\t"
    "v_mfma_f32_16x16x32_bf16 v[152:155], a[32:35], a[120:123], v[152:155]\n\t"
    "v_mfma_f32_16x16x32_bf16 v[156:159], a[32:35], a[124:127], v[156:159]\n\t"
    "v_mfma_f32_16x16x32_bf16 v[160:163], a[36:39], a[112:115], v[160:163]\n\t"
    "ds_read_b64_tr_b16 a[32:33], v11 offset:6176\n\t"
    "v_mfma_f32_16x16x32_bf16 v[164:167], a[36:39], a[116:119], v[164:167]\n\t"
    "ds_read_b64_tr_b16 a[34:35], v11 offset:7968\n\t"
    "v_mfma_f32_16x16x32_bf16 v[168:171], a[36:39], a[120:123], v[168:171]\n\t"
    "v_mfma_f32_16x16x32_bf16 v[172:175], a[36:39], a[124:127], v[172:175]\n\t"
    "v_mfma_f32_16x16x32_bf16 v[176:179], a[40:43], a[112:115], v[176:179]\n\t"
    "ds_read_b64_tr_b16 a[36:37], v11 offset:6208\n\t"
    "v_mfma_f32_16x16x32_bf16 v[180:183], a[40:43], a[116:119], v[180:183]\n\t"
    "ds_read_b64_tr_b16 a[38:39], v11 offset:8000\n\t"
    "v_mfma_f32_16x16x32_bf16 v[184:187], a[40:43], a[120:123], v[184:187]\n\t"
    "v_mfma_f32_16x16x32_bf16 v[188:191], a[40:43], a[124:127], v[188:191]\n\t"
    "v_mfma_f32_16x16x32_bf16 v[192:195], a[44:47], a[112:115], v[192:195]\n\t"
    "ds_read_b64_tr_b16 a[40:41], v11 offset:6240\n\t"
    "v_mfma_f32_16x16x32_bf16 v[196:199], a[44:47], a[116:119], v[196:199]\n\t"
    "ds_read_b64_tr_b16 a[42:43], v11 offset:8032\n\t"
    "v_mfma_f32_16x16x32_bf16 v[200:203], a[44:47], a[120:123], v[200:203]\n\t"
    "v_mfma_f32_16x16x32_bf16 v[204:207], a[44:47], a[124:127], v[204:207]\n\t"
    "s_waitcnt lgkmcnt(15)\n\t"
    "v_mfma_f32_16x16x32_bf16 v[208:211], a[48:51], a[112:115], v[208:211]\n\t"
    "ds_read_b64_tr_b16 a[44:45], v11 offset:6272\n\t"
    "v_mfma_f32_16x16x32_bf16 v[212:215], a[48:51], a[116:119], v[212:215]\n\t"
    "ds_read_b64_tr_b16 a[46:47], v11 offset:8064\n\t"
    "v_mfma_f32_16x16x32_bf16 v[216:219], a[48:51], a[120:123], v[216:219]\n\t"
    "v_mfma_f32_16x16x32_bf16 v[220:223], a[48:51], a[124:127], v[220:223]\n\t"
    "v_mfma_f32_16x16x32_bf16 v[224:227], a[52:55], a[112:115], v[224:227]\n\t"
    "ds_read_b64_tr_b16 a[48:49], v11 offset:6304\n\t"
    "v_mfma_f32_16x16x32_bf16 v[228:231], a[52:55], a[116:119], v[228:231]\n\t"
    "ds_read_b64_tr_b16 a[50:51], v11 offset:8096\n\t"
    "v_mfma_f32_16x16x32_bf16 v[232:235], a[52:55], a[120:123], v[232:235]\n\t"
    "v_mfma_f32_16x16x32_bf16 v[236:239], a[52:55], a[124:127], v[236:239]\n\t"
    "s_waitcnt vmcnt(19)\n\t"
    "v_mfma_f32_16x16x32_bf16 v[16:19], a[0:3], a[128:131], v[16:19]\n\t"
    "ds_read_b64_tr_b16 a[52:53], v11 offset:6336\n\t"
    "v_mfma_f32_16x16x32_bf16 v[20:23], a[0:3], a[132:135], v[20:23]\n\t"
    "ds_read_b64_tr_b16 a[54:55], v11 offset:8128\n\t"
    "v_mfma_f32_16x16x32_bf16 v[24:27], a[0:3], a[136:139], v[24:27]\n\t"
    "v_mfma_f32_16x16x32_bf16 v[28:31], a[0:3], a[140:143], v[28:31]\n\t"
    "v_mfma_f32_16x16x32_bf16 v[32:35], a[4:7], a[128:131], v[32:35]\n\t"
    "ds_read_b64_tr_b16 a[0:1], v10 offset:64512\n\t"
    "v_mfma_f32_16x16x32_bf16 v[36:39], a[4:7], a[132:135], v[36:39]\n\t"
    "ds_read_b64_tr_b16 a[2:3], v11 offset:768\n\t"
    "v_mfma_f32_16x16x32_bf16 v[40:43], a[4:7], a[136:139], v[40:43]\n\t"
    "v_mfma_f32_16x16x32_bf16 v[44:47], a[4:7], a[140:143], v[44:47]\n\t"
    "s_waitcnt lgkmcnt(15)\n\t"
    "v_mfma_f32_16x16x32_bf16 v[48:51], a[8:11], a[128:131], v[48:51]\n\t"
    "ds_read_b64_tr_b16 a[4:5], v10 offset:64544\n\t"
    "v_mfma_f32_16x16x32_bf16 v[52:55], a[8:11], a[132:135], v[52:55]\n\t"
    "ds_read_b64_tr_b16 a[6:7], v11 offset:800\n\t"
    "v_mfma_f32_16x16x32_bf16 v[56:59], a[8:11], a[136:139], v[56:59]\n\t"
    "v_mfma_f32_16x16x32_bf16 v[60:63], a[8:11], a[140:143], v[60:63]\n\t"
    "v_mfma_f32_16x16x32_bf16 v[64:67], a[12:15], a[128:131], v[64:67]\n\t"
    "ds_read_b64_tr_b16 a[8:9], v10 offset:64576\n\t"
    "v_mfma_f32_16x16x32_bf16 v[68:71], a[12:15], a[132:135], v[68:71]\n\t"
    "ds_read_b64_tr_b16 a[10:11], v11 offset:832\n\t"
    "v_mfma_f32_16x16x32_bf16 v[72:75], a[12:15], a[136:139], v[72:75]\n\t"
    "v_mfma_f32_16x16x32_bf16 v[76:79], a[12:15], a[140:143], v[76:79]\n\t"
    "v_mfma_f32_16x16x32_bf16 v[80:83], a[16:19], a[128:131], v[80:83]\n\t"
    "ds_read_b64_tr_b16 a[12:13], v10 offset:64608\n\t"
    "v_mfma_f32_16x16x32_bf16 v[84:87], a[16:19], a[132:135], v[84:87]\n\t"
    "ds_read_b64_tr_b16 a[14:15], v11 offset:864\n\t"
    "v_mfma_f32_16x16x32_bf16 v[88:91], a[16:19], a[136:139], v[88:91]\n\t"
    "v_mfma_f32_16x16x32_bf16 v[92:95], a[16:19], a[140:143], v[92:95]\n\t"
    "v_mfma_f32_16x16x32_bf16 v[96:99], a[20:23], a[128:131], v[96:99]\n\t"
    "ds_read_b64_tr_b16 a[16:17], v10 offset:64640\n\t"
    "v_mfma_f32_16x16x32_bf16 v[100:103], a[20:23], a[132:135], v[100:103]\n\t"
    "ds_read_b64_tr_b16 a[18:19], v11 offset:896\n\t"
    "v_mfma_f32_16x16x32_bf16 v[104:107], a[20:23], a[136:139], v[104:107]\n\t"
    "v_mfma_f32_16x16x32_bf16 v[108:111], a[20:23], a[140:143], v[108:111]\n\t"
    "s_waitcnt lgkmcnt(15)\n\t"
    "v_mfma_f32_16x16x32_bf16 v[112:115], a[24:27], a[128:131], v[112:115]\n\t"
    "ds_read_b64_tr_b16 a[20:21], v10 offset:64672\n\t"
    "v_mfma_f32_16x16x32_bf16 v[116:119], a[24:27], a[132:135], v[116:119]\n\t"
    "ds_read_b64_tr_b16 a[22:23], v11 offset:928\n\t"
    "v_mfma_f32_16x16x32_bf16 v[120:123], a[24:27], a[136:139], v[120:123]\n\t"
    "v_mfma_f32_16x16x32_bf16 v[124:127], a[24:27], a[140:143], v[124:127]\n\t"
    "v_mfma_f32_16x16x32_bf16 v[128:131], a[28:31], a[128:131], v[128:131]\n\t"
    "ds_read_b64_tr_b16 a[24:25], v10 offset:64704\n\t"
    "v_mfma_f32_16x16x32_bf16 v[132:135], a[28:31], a[132:135], v[132:135]\n\t"
    "ds_read_b64_tr_b16 a[26:27], v11 offset:960\n\t"
    "v_mfma_f32_16x16x32_bf16 v[136:139], a[28:31], a[136:139], v[136:139]\n\t"
    "v_mfma_f32_16x16x32_bf16 v[140:143], a[28:31], a[140:143], v[140:143]\n\t"
    "v_mfma_f32_16x16x32_bf16 v[144:147], a[32:35], a[128:131], v[144:147]\n\t"
    "ds_read_b64_tr_b16 a[28:29], v11 offset:13312\n\t"
    "v_mfma_f32_16x16x32_bf16 v[148:151], a[32:35], a[132:135], v[148:151]\n\t"
    "ds_read_b64_tr_b16 a[30:31], v11 offset:15104\n\t"
    "v_mfma_f32_16x16x32_bf16 v[152:155], a[32:35], a[136:139], v[152:155]\n\t"
    "v_mfma_f32_16x16x32_bf16 v[156:159], a[32:35], a[140:143], v[156:159]\n\t"
    "v_mfma_f32_16x16x32_bf16 v[160:163], a[36:39], a[128:131], v[160:163]\n\t"
    "ds_read_b64_tr_b16 a[32:33], v11 offset:13344\n\t"
    "v_mfma_f32_16x16x32_bf16 v[164:167], a[36:39], a[132:135], v[164:167]\n\t"
    "ds_read_b64_tr_b16 a[34:35], v11 offset:15136\n\t"
    "v_mfma_f32_16x16x32_bf16 v[168:171], a[36:39], a[136:139], v[168:171]\n\t"
    "v_mfma_f32_16x16x32_bf16 v[172:175], a[36:39], a[140:143], v[172:175]\n\t"
    "s_waitcnt lgkmcnt(15)\n\t"
    "v_mfma_f32_16x16x32_bf16 v[176:179], a[40:43], a[128:131], v[176:179]\n\t"
    "ds_read_b64_tr_b16 a[36:37], v11 offset:13376\n\t"
    "v_mfma_f32_16x16x32_bf16 v[180:183], a[40:43], a[132:135], v[180:183]\n\t"
    "ds_read_b64_tr_b16 a[38:39], v11 offset:15168\n\t"
    "v_mfma_f32_16x16x32_bf16 v[184:187], a[40:43], a[136:139], v[184:187]\n\t"
    "v_mfma_f32_16x16x32_bf16 v[188:191], a[40:43], a[140:143], v[188:191]\n\t"
    "v_mfma_f32_16x16x32_bf16 v[192:195], a[44:47], a[128:131], v[192:195]\n\t"
    "ds_read_b64_tr_b16 a[40:41], v11 offset:13408\n\t"
    "v_mfma_f32_16x16x32_bf16 v[196:199], a[44:47], a[132:135], v[196:199]\n\t"
    "ds_read_b64_tr_b16 a[42:43], v11 offset:15200\n\t"
    "v_mfma_f32_16x16x32_bf16 v[200:203], a[44:47], a[136:139], v[200:203]\n\t"
    "v_mfma_f32_16x16x32_bf16 v[204:207], a[44:47], a[140:143], v[204:207]\n\t"
    "v_mfma_f32_16x16x32_bf16 v[208:211], a[48:51], a[128:131], v[208:211]\n\t"
    "ds_read_b64_tr_b16 a[44:45], v11 offset:13440\n\t"
    "v_mfma_f32_16x16x32_bf16 v[212:215], a[48:51], a[132:135], v[212:215]\n\t"
    "ds_read_b64_tr_b16 a[46:47], v11 offset:15232\n\t"
    "v_mfma_f32_16x16x32_bf16 v[216:219], a[48:51], a[136:139], v[216:219]\n\t"
    "v_mfma_f32_16x16x32_bf16 v[220:223], a[48:51], a[140:143], v[220:223]\n\t"
    "v_mfma_f32_16x16x32_bf16 v[224:227], a[52:55], a[128:131], v[224:227]\n\t"
    "ds_read_b64_tr_b16 a[48:49], v11 offset:13472\n\t"
    "v_mfma_f32_16x16x32_bf16 v[228:231], a[52:55], a[132:135], v[228:231]\n\t"
    "ds_read_b64_tr_b16 a[50:51], v11 offset:15264\n\t"
    "v_mfma_f32_16x16x32_bf16 v[232:235], a[52:55], a[136:139], v[232:235]\n\t"
    "v_mfma_f32_16x16x32_bf16 v[236:239], a[52:55], a[140:143], v[236:239]\n\t"
    "s_waitcnt vmcnt(15) lgkmcnt(15)\n\t"
    "v_mfma_f32_16x16x32_bf16 v[16:19], a[0:3], a[144:147], v[16:19]\n\t"
    "s_waitcnt vmcnt(8)\n\t"
    "s_barrier\n\t"
    "ds_read_b64_tr_b16 a[52:53], v11 offset:13504\n\t"
    "v_mfma_f32_16x16x32_bf16 v[20:23], a[0:3], a[148:151], v[20:23]\n\t"
    "ds_read_b64_tr_b16 a[54:55], v11 offset:15296\n\t"
    "v_mfma_f32_16x16x32_bf16 v[24:27], a[0:3], a[152:155], v[24:27]\n\t"
    "v_mfma_f32_16x16x32_bf16 v[28:31], a[0:3], a[156:159], v[28:31]\n\t"
    "v_mfma_f32_16x16x32_bf16 v[32:35], a[4:7], a[144:147], v[32:35]\n\t"
    "ds_read_b64_tr_b16 a[0:1], v11 offset:20480\n\t"
    "v_mfma_f32_16x16x32_bf16 v[36:39], a[4:7], a[148:151], v[36:39]\n\t"
    "ds_read_b64_tr_b16 a[2:3], v11 offset:22272\n\t"
    "v_mfma_f32_16x16x32_bf16 v[40:43], a[4:7], a[152:155], v[40:43]\n\t"
    "v_mfma_f32_16x16x32_bf16 v[44:47], a[4:7], a[156:159], v[44:47]\n\t"
    "v_mfma_f32_16x16x32_bf16 v[48:51], a[8:11], a[144:147], v[48:51]\n\t"
    "ds_read_b64_tr_b16 a[4:5], v11 offset:20512\n\t"
    "v_mfma_f32_16x16x32_bf16 v[52:55], a[8:11], a[148:151], v[52:55]\n\t"
    "ds_read_b64_tr_b16 a[6:7], v11 offset:22304\n\t"
    "v_mfma_f32_16x16x32_bf16 v[56:59], a[8:11], a[152:155], v[56:59]\n\t"
    "v_mfma_f32_16x16x32_bf16 v[60:63], a[8:11], a[156:159], v[60:63]\n\t"
    "v_mfma_f32_16x16x32_bf16 v[64:67], a[12:15], a[144:147], v[64:67]\n\t"
    "ds_read_b64_tr_b16 a[8:9], v11 offset:20544\n\t"
    "v_mfma_f32_16x16x32_bf16 v[68:71], a[12:15], a[148:151], v[68:71]\n\t"
    "ds_read_b64_tr_b16 a[10:11], v11 offset:22336\n\t"
    "v_mfma_f32_16x16x32_bf16 v[72:75], a[12:15], a[152:155], v[72:75]\n\t"
    "v_mfma_f32_16x16x32_bf16 v[76:79], a[12:15], a[156:159], v[76:79]\n\t"
    "s_waitcnt lgkmcnt(15)\n\t"
    "v_mfma_f32_16x16x32_bf16 v[80:83], a[16:19], a[144:147], v[80:83]\n\t"
    "ds_read_b64_tr_b16 a[12:13], v11 offset:20576\n\t"
    "v_mfma_f32_16x16x32_bf16 v[84:87], a[16:19], a[148:151], v[84:87]\n\t"
    "ds_read_b64_tr_b16 a[14:15], v11 offset:22368\n\t"
    "v_mfma_f32_16x16x32_bf16 v[88:91], a[16:19], a[152:155], v[88:91]\n\t"
    "v_mfma_f32_16x16x32_bf16 v[92:95], a[16:19], a[156:159], v[92:95]\n\t"
    "v_mfma_f32_16x16x32_bf16 v[96:99], a[20:23], a[144:147], v[96:99]\n\t"
    "ds_read_b64_tr_b16 a[16:17], v11 offset:20608\n\t"
    "v_mfma_f32_16x16x32_bf16 v[100:103], a[20:23], a[148:151], v[100:103]\n\t"
    "ds_read_b64_tr_b16 a[18:19], v11 offset:22400\n\t"
    "v_mfma_f32_16x16x32_bf16 v[104:107], a[20:23], a[152:155], v[104:107]\n\t"
    "v_mfma_f32_16x16x32_bf16 v[108:111], a[20:23], a[156:159], v[108:111]\n\t"
    "v_mfma_f32_16x16x32_bf16 v[112:115], a[24:27], a[144:147], v[112:115]\n\t"
    "ds_read_b64_tr_b16 a[20:21], v11 offset:20640\n\t"
    "v_mfma_f32_16x16x32_bf16 v[116:119], a[24:27], a[148:151], v[116:119]\n\t"
    "ds_read_b64_tr_b16 a[22:23], v11 offset:22432\n\t"
    "v_mfma_f32_16x16x32_bf16 v[120:123], a[24:27], a[152:155], v[120:123]\n\t"
    "v_mfma_f32_16x16x32_bf16 v[124:127], a[24:27], a[156:159], v[124:127]\n\t"
    "v_mfma_f32_16x16x32_bf16 v[128:131], a[28:31], a[144:147], v[128:131]\n\t"
    "ds_read_b64_tr_b16 a[24:25], v11 offset:20672\n\t"
    "v_mfma_f32_16x16x32_bf16 v[132:135], a[28:31], a[148:151], v[132:135]\n\t"
    "ds_read_b64_tr_b16 a[26:27], v11 offset:22464\n\t"
    "v_mfma_f32_16x16x32_bf16 v[136:139], a[28:31], a[152:155], v[136:139]\n\t"
    "v_mfma_f32_16x16x32_bf16 v[140:143], a[28:31], a[156:159], v[140:143]\n\t"
    "s_waitcnt lgkmcnt(15)\n\t"
    "v_mfma_f32_16x16x32_bf16 v[144:147], a[32:35], a[144:147], v[144:147]\n\t"
    "ds_read_b64_tr_b16 a[28:29], v11 offset:34816\n\t"
    "v_mfma_f32_16x16x32_bf16 v[148:151], a[32:35], a[148:151], v[148:151]\n\t"
    "ds_read_b64_tr_b16 a[30:31], v11 offset:36608\n\t"
    "v_mfma_f32_16x16x32_bf16 v[152:155], a[32:35], a[152:155], v[152:155]\n\t"
    "v_mfma_f32_16x16x32_bf16 v[156:159], a[32:35], a[156:159], v[156:159]\n\t"
    "v_mfma_f32_16x16x32_bf16 v[160:163], a[36:39], a[144:147], v[160:163]\n\t"
    "ds_read_b64_tr_b16 a[32:33], v11 offset:34848\n\t"
    "v_mfma_f32_16x16x32_bf16 v[164:167], a[36:39], a[148:151], v[164:167]\n\t"
    "ds_read_b64_tr_b16 a[34:35], v11 offset:36640\n\t"
    "v_mfma_f32_16x16x32_bf16 v[168:171], a[36:39], a[152:155], v[168:171]\n\t"
    "v_mfma_f32_16x16x32_bf16 v[172:175], a[36:39], a[156:159], v[172:175]\n\t"
    "v_mfma_f32_16x16x32_bf16 v[176:179], a[40:43], a[144:147], v[176:179]\n\t"
    "ds_read_b64_tr_b16 a[36:37], v11 offset:34880\n\t"
    "v_mfma_f32_16x16x32_bf16 v[180:183], a[40:43], a[148:151], v[180:183]\n\t"
    "ds_read_b64_tr_b16 a[38:39], v11 offset:36672\n\t"
    "v_mfma_f32_16x16x32_bf16 v[184:187], a[40:43], a[152:155], v[184:187]\n\t"
    "v_mfma_f32_16x16x32_bf16 v[188:191], a[40:43], a[156:159], v[188:191]\n\t"
    "v_mfma_f32_16x16x32_bf16 v[192:195], a[44:47], a[144:147], v[192:195]\n\t"
    "ds_read_b64_tr_b16 a[40:41], v11 offset:34912\n\t"
    "v_mfma_f32_16x16x32_bf16 v[196:199], a[44:47], a[148:151], v[196:199]\n\t"
    "ds_read_b64_tr_b16 a[42:43], v11 offset:36704\n\t"
    "v_mfma_f32_16x16x32_bf16 v[200:203], a[44:47], a[152:155], v[200:203]\n\t"
    "v_mfma_f32_16x16x32_bf16 v[204:207], a[44:47], a[156:159], v[204:207]\n\t"
    "s_waitcnt lgkmcnt(15)\n\t"
    "v_mfma_f32_16x16x32_bf16 v[208:211], a[48:51], a[144:147], v[208:211]\n\t"
    "ds_read_b64_tr_b16 a[44:45], v11 offset:34944\n\t"
    "v_mfma_f32_16x16x32_bf16 v[212:215], a[48:51], a[148:151], v[212:215]\n\t"
    "ds_read_b64_tr_b16 a[46:47], v11 offset:36736\n\t"
    "v_mfma_f32_16x16x32_bf16 v[216:219], a[48:51], a[152:155], v[216:219]\n\t"
    "v_mfma_f32_16x16x32_bf16 v[220:223], a[48:51], a[156:159], v[220:223]\n\t"
    "v_mfma_f32_16x16x32_bf16 v[224:227], a[52:55], a[144:147], v[224:227]\n\t"
    "ds_read_b64_tr_b16 a[48:49], v11 offset:34976\n\t"
    "v_mfma_f32_16x16x32_bf16 v[228:231], a[52:55], a[148:151], v[228:231]\n\t"
    "ds_read_b64_tr_b16 a[50:51], v11 offset:36768\n\t"
    "v_mfma_f32_16x16x32_bf16 v[232:235], a[52:55], a[152:155], v[232:235]\n\t"
    "v_mfma_f32_16x16x32_bf16 v[236:239], a[52:55], a[156:159], v[236:239]\n\t"
    "s_waitcnt vmcnt(4)\n\t"
    "v_mfma_f32_16x16x32_bf16 v[16:19], a[0:3], a[160:163], v[16:19]\n\t"
    "ds_read_b64_tr_b16 a[52:53], v11 offset:35008\n\t"
    "v_mfma_f32_16x16x32_bf16 v[20:23], a[0:3], a[164:167], v[20:23]\n\t"
    "ds_read_b64_tr_b16 a[54:55], v11 offset:36800\n\t"
    "v_mfma_f32_16x16x32_bf16 v[24:27], a[0:3], a[168:171], v[24:27]\n\t"
    "v_mfma_f32_16x16x32_bf16 v[28:31], a[0:3], a[172:175], v[28:31]\n\t"
    "v_mfma_f32_16x16x32_bf16 v[32:35], a[4:7], a[160:163], v[32:35]\n\t"
    "ds_read_b64_tr_b16 a[0:1], v11 offset:27648\n\t"
    "v_mfma_f32_16x16x32_bf16 v[36:39], a[4:7], a[164:167], v[36:39]\n\t"
    "ds_read_b64_tr_b16 a[2:3], v11 offset:29440\n\t"
    "v_mfma_f32_16x16x32_bf16 v[40:43], a[4:7], a[168:171], v[40:43]\n\t"
    "v_mfma_f32_16x16x32_bf16 v[44:47], a[4:7], a[172:175], v[44:47]\n\t"
    "s_waitcnt lgkmcnt(15)\n\t"
    "v_mfma_f32_16x16x32_bf16 v[48:51], a[8:11], a[160:163], v[48:51]\n\t"
    "ds_read_b64_tr_b16 a[4:5], v11 offset:27680\n\t"
    "v_mfma_f32_16x16x32_bf16 v[52:55], a[8:11], a[164:167], v[52:55]\n\t"
    "ds_read_b64_tr_b16 a[6:7], v11 offset:29472\n\t"
    "v_mfma_f32_16x16x32_bf16 v[56:59], a[8:11], a[168:171], v[56:59]\n\t"
    "v_mfma_f32_16x16x32_bf16 v[60:63], a[8:11], a[172:175], v[60:63]\n\t"
    "v_mfma_f32_16x16x32_bf16 v[64:67], a[12:15], a[160:163], v[64:67]\n\t"
    "ds_read_b64_tr_b16 a[8:9], v11 offset:27712\n\t"
    "v_mfma_f32_16x16x32_bf16 v[68:71], a[12:15], a[164:167], v[68:71]\n\t"
    "ds_read_b64_tr_b16 a[10:11], v11 offset:29504\n\t"
    "v_mfma_f32_16x16x32_bf16 v[72:75], a[12:15], a[168:171], v[72:75]\n\t"
    "v_mfma_f32_16x16x32_bf16 v[76:79], a[12:15], a[172:175], v[76:79]\n\t"
    "v_mfma_f32_16x16x32_bf16 v[80:83], a[16:19], a[160:163], v[80:83]\n\t"
    "ds_read_b64_tr_b16 a[12:13], v11 offset:27744\n\t"
    "v_mfma_f32_16x16x32_bf16 v[84:87], a[16:19], a[164:167], v[84:87]\n\t"
    "ds_read_b64_tr_b16 a[14:15], v11 offset:29536\n\t"
    "v_mfma_f32_16x16x32_bf16 v[88:91], a[16:19], a[168:171], v[88:91]\n\t"
    "v_mfma_f32_16x16x32_bf16 v[92:95], a[16:19], a[172:175], v[92:95]\n\t"
    "v_mfma_f32_16x16x32_bf16 v[96:99], a[20:23], a[160:163], v[96:99]\n\t"
    "ds_read_b64_tr_b16 a[16:17], v11 offset:27776\n\t"
    "v_mfma_f32_16x16x32_bf16 v[100:103], a[20:23], a[164:167], v[100:103]\n\t"
    "ds_read_b64_tr_b16 a[18:19], v11 offset:29568\n\t"
    "v_mfma_f32_16x16x32_bf16 v[104:107], a[20:23], a[168:171], v[104:107]\n\t"
    "v_mfma_f32_16x16x32_bf16 v[108:111], a[20:23], a[172:175], v[108:111]\n\t"
    "s_waitcnt lgkmcnt(15)\n\t"
    "v_mfma_f32_16x16x32_bf16 v[112:115], a[24:27], a[160:163], v[112:115]\n\t"
    "ds_read_b64_tr_b16 a[20:21], v11 offset:27808\n\t"
    "v_mfma_f32_16x16x32_bf16 v[116:119], a[24:27], a[164:167], v[116:119]\n\t"
    "ds_read_b64_tr_b16 a[22:23], v11 offset:29600\n\t"
    "v_mfma_f32_16x16x32_bf16 v[120:123], a[24:27], a[168:171], v[120:123]\n\t"
    "v_mfma_f32_16x16x32_bf16 v[124:127], a[24:27], a[172:175], v[124:127]\n\t"
    "v_mfma_f32_16x16x32_bf16 v[128:131], a[28:31], a[160:163], v[128:131]\n\t"
    "ds_read_b64_tr_b16 a[24:25], v11 offset:27840\n\t"
    "v_mfma_f32_16x16x32_bf16 v[132:135], a[28:31], a[164:167], v[132:135]\n\t"
    "ds_read_b64_tr_b16 a[26:27], v11 offset:29632\n\t"
    "v_mfma_f32_16x16x32_bf16 v[136:139], a[28:31], a[168:171], v[136:139]\n\t"
    "v_mfma_f32_16x16x32_bf16 v[140:143], a[28:31], a[172:175], v[140:143]\n\t"
    "v_mfma_f32_16x16x32_bf16 v[144:147], a[32:35], a[160:163], v[144:147]\n\t"
    "ds_read_b64_tr_b16 a[28:29], v11 offset:41984\n\t"
    "v_mfma_f32_16x16x32_bf16 v[148:151], a[32:35], a[164:167], v[148:151]\n\t"
    "ds_read_b64_tr_b16 a[30:31], v11 offset:43776\n\t"
    "v_mfma_f32_16x16x32_bf16 v[152:155], a[32:35], a[168:171], v[152:155]\n\t"
    "v_mfma_f32_16x16x32_bf16 v[156:159], a[32:35], a[172:175], v[156:159]\n\t"
    "v_mfma_f32_16x16x32_bf16 v[160:163], a[36:39], a[160:163], v[160:163]\n\t"
    "ds_read_b64_tr_b16 a[32:33], v11 offset:42016\n\t"
    "v_mfma_f32_16x16x32_bf16 v[164:167], a[36:39], a[164:167], v[164:167]\n\t"
    "ds_read_b64_tr_b16 a[34:35], v11 offset:43808\n\t"
    "v_mfma_f32_16x16x32_bf16 v[168:171], a[36:39], a[168:171], v[168:171]\n\t"
    "v_mfma_f32_16x16x32_bf16 v[172:175], a[36:39], a[172:175], v[172:175]\n\t"
    "s_waitcnt lgkmcnt(15)\n\t"
    "v_mfma_f32_16x16x32_bf16 v[176:179], a[40:43], a[160:163], v[176:179]\n\t"
    "ds_read_b64_tr_b16 a[36:37], v11 offset:42048\n\t"
    "v_mfma_f32_16x16x32_bf16 v[180:183], a[40:43], a[164:167], v[180:183]\n\t"
    "ds_read_b64_tr_b16 a[38:39], v11 offset:43840\n\t"
    "v_mfma_f32_16x16x32_bf16 v[184:187], a[40:43], a[168:171], v[184:187]\n\t"
    "v_mfma_f32_16x16x32_bf16 v[188:191], a[40:43], a[172:175], v[188:191]\n\t"
    "v_mfma_f32_16x16x32_bf16 v[192:195], a[44:47], a[160:163], v[192:195]\n\t"
    "ds_read_b64_tr_b16 a[40:41], v11 offset:42080\n\t"
    "v_mfma_f32_16x16x32_bf16 v[196:199], a[44:47], a[164:167], v[196:199]\n\t"
    "ds_read_b64_tr_b16 a[42:43], v11 offset:43872\n\t"
    "v_mfma_f32_16x16x32_bf16 v[200:203], a[44:47], a[168:171], v[200:203]\n\t"
    "v_mfma_f32_16x16x32_bf16 v[204:207], a[44:47], a[172:175], v[204:207]\n\t"
    "v_mfma_f32_16x16x32_bf16 v[208:211], a[48:51], a[160:163], v[208:211]\n\t"
    "ds_read_b64_tr_b16 a[44:45], v11 offset:42112\n\t"
    "v_mfma_f32_16x16x32_bf16 v[212:215], a[48:51], a[164:167], v[212:215]\n\t"
    "ds_read_b64_tr_b16 a[46:47], v11 offset:43904\n\t"
    "v_mfma_f32_16x16x32_bf16 v[216:219], a[48:51], a[168:171], v[216:219]\n\t"
    "v_mfma_f32_16x16x32_bf16 v[220:223], a[48:51], a[172:175], v[220:223]\n\t"
    "v_mfma_f32_16x16x32_bf16 v[224:227], a[52:55], a[160:163], v[224:227]\n\t"
    "ds_read_b64_tr_b16 a[48:49], v11 offset:42144\n\t"
    "v_mfma_f32_16x16x32_bf16 v[228:231], a[52:55], a[164:167], v[228:231]\n\t"
    "ds_read_b64_tr_b16 a[50:51], v11 offset:43936\n\t"
    "v_mfma_f32_16x16x32_bf16 v[232:235], a[52:55], a[168:171], v[232:235]\n\t"
    "v_mfma_f32_16x16x32_bf16 v[236:239], a[52:55], a[172:175], v[236:239]\n\t"
    "s_waitcnt vmcnt(0) lgkmcnt(15)\n\t"
    "v_mfma_f32_16x16x32_bf16 v[16:19], a[0:3], a[176:179], v[16:19]\n\t"
    "ds_read_b64_tr_b16 a[52:53], v11 offset:42176\n\t"
    "v_mfma_f32_16x16x32_bf16 v[20:23], a[0:3], a[180:183], v[20:23]\n\t"
    "ds_read_b64_tr_b16 a[54:55], v11 offset:43968\n\t"
    "v_mfma_f32_16x16x32_bf16 v[24:27], a[0:3], a[184:187], v[24:27]\n\t"
    "v_mfma_f32_16x16x32_bf16 v[28:31], a[0:3], a[188:191], v[28:31]\n\t"
    "v_mfma_f32_16x16x32_bf16 v[32:35], a[4:7], a[176:179], v[32:35]\n\t"
    "v_mfma_f32_16x16x32_bf16 v[36:39], a[4:7], a[180:183], v[36:39]\n\t"
    "v_mfma_f32_16x16x32_bf16 v[40:43], a[4:7], a[184:187], v[40:43]\n\t"
    "v_mfma_f32_16x16x32_bf16 v[44:47], a[4:7], a[188:191], v[44:47]\n\t"
    "v_mfma_f32_16x16x32_bf16 v[48:51], a[8:11], a[176:179], v[48:51]\n\t"
    "v_mfma_f32_16x16x32_bf16 v[52:55], a[8:11], a[180:183], v[52:55]\n\t"
    "v_mfma_f32_16x16x32_bf16 v[56:59], a[8:11], a[184:187], v[56:59]\n\t"
    "v_mfma_f32_16x16x32_bf16 v[60:63], a[8:11], a[188:191], v[60:63]\n\t"
    "v_mfma_f32_16x16x32_bf16 v[64:67], a[12:15], a[176:179], v[64:67]\n\t"
    "v_mfma_f32_16x16x32_bf16 v[68:71], a[12:15], a[180:183], v[68:71]\n\t"
    "v_mfma_f32_16x16x32_bf16 v[72:75], a[12:15], a[184:187], v[72:75]\n\t"
    "v_mfma_f32_16x16x32_bf16 v[76:79], a[12:15], a[188:191], v[76:79]\n\t"
    "s_waitcnt lgkmcnt(15)\n\t"
    "v_mfma_f32_16x16x32_bf16 v[80:83], a[16:19], a[176:179], v[80:83]\n\t"
    "v_mfma_f32_16x16x32_bf16 v[84:87], a[16:19], a[180:183], v[84:87]\n\t"
    "v_mfma_f32_16x16x32_bf16 v[88:91], a[16:19], a[184:187], v[88:91]\n\t"
    "v_mfma_f32_16x16x32_bf16 v[92:95], a[16:19], a[188:191], v[92:95]\n\t"
    "v_mfma_f32_16x16x32_bf16 v[96:99], a[20:23], a[176:179], v[96:99]\n\t"
    "v_mfma_f32_16x16x32_bf16 v[100:103], a[20:23], a[180:183], v[100:103]\n\t"
    "v_mfma_f32_16x16x32_bf16 v[104:107], a[20:23], a[184:187], v[104:107]\n\t"
    "v_mfma_f32_16x16x32_bf16 v[108:111], a[20:23], a[188:191], v[108:111]\n\t"
    "s_waitcnt lgkmcnt(12)\n\t"
    "v_mfma_f32_16x16x32_bf16 v[112:115], a[24:27], a[176:179], v[112:115]\n\t"
    "v_mfma_f32_16x16x32_bf16 v[116:119], a[24:27], a[180:183], v[116:119]\n\t"
    "v_mfma_f32_16x16x32_bf16 v[120:123], a[24:27], a[184:187], v[120:123]\n\t"
    "v_mfma_f32_16x16x32_bf16 v[124:127], a[24:27], a[188:191], v[124:127]\n\t"
    "v_mfma_f32_16x16x32_bf16 v[128:131], a[28:31], a[176:179], v[128:131]\n\t"
    "v_mfma_f32_16x16x32_bf16 v[132:135], a[28:31], a[180:183], v[132:135]\n\t"
    "v_mfma_f32_16x16x32_bf16 v[136:139], a[28:31], a[184:187], v[136:139]\n\t"
    "v_mfma_f32_16x16x32_bf16 v[140:143], a[28:31], a[188:191], v[140:143]\n\t"
    "s_waitcnt lgkmcnt(8)\n\t"
    "v_mfma_f32_16x16x32_bf16 v[144:147], a[32:35], a[176:179], v[144:147]\n\t"
    "v_mfma_f32_16x16x32_bf16 v[148:151], a[32:35], a[180:183], v[148:151]\n\t"
    "v_mfma_f32_16x16x32_bf16 v[152:155], a[32:35], a[184:187], v[152:155]\n\t"
    "v_mfma_f32_16x16x32_bf16 v[156:159], a[32:35], a[188:191], v[156:159]\n\t"
    "v_mfma_f32_16x16x32_bf16 v[160:163], a[36:39], a[176:179], v[160:163]\n\t"
    "v_mfma_f32_16x16x32_bf16 v[164:167], a[36:39], a[180:183], v[164:167]\n\t"
    "v_mfma_f32_16x16x32_bf16 v[168:171], a[36:39], a[184:187], v[168:171]\n\t"
    "v_mfma_f32_16x16x32_bf16 v[172:175], a[36:39], a[188:191], v[172:175]\n\t"
    "s_waitcnt lgkmcnt(4)\n\t"
    "v_mfma_f32_16x16x32_bf16 v[176:179], a[40:43], a[176:179], v[176:179]\n\t"
    "v_mfma_f32_16x16x32_bf16 v[180:183], a[40:43], a[180:183], v[180:183]\n\t"
    "v_mfma_f32_16x16x32_bf16 v[184:187], a[40:43], a[184:187], v[184:187]\n\t"
    "v_mfma_f32_16x16x32_bf16 v[188:191], a[40:43], a[188:191], v[188:191]\n\t"
    "v_mfma_f32_16x16x32_bf16 v[192:195], a[44:47], a[176:179], v[192:195]\n\t"
    "v_mfma_f32_16x16x32_bf16 v[196:199], a[44:47], a[180:183], v[196:199]\n\t"
    "v_mfma_f32_16x16x32_bf16 v[200:203], a[44:47], a[184:187], v[200:203]\n\t"
    "v_mfma_f32_16x16x32_bf16 v[204:207], a[44:47], a[188:191], v[204:207]\n\t"
    "s_waitcnt lgkmcnt(0)\n\t"
    "v_mfma_f32_16x16x32_bf16 v[208:211], a[48:51], a[176:179], v[208:211]\n\t"
    "v_mfma_f32_16x16x32_bf16 v[212:215], a[48:51], a[180:183], v[212:215]\n\t"
    "v_mfma_f32_16x16x32_bf16 v[216:219], a[48:51], a[184:187], v[216:219]\n\t"
    "v_mfma_f32_16x16x32_bf16 v[220:223], a[48:51], a[188:191], v[220:223]\n\t"
    "v_mfma_f32_16x16x32_bf16 v[224:227], a[52:55], a[176:179], v[224:227]\n\t"
    "v_mfma_f32_16x16x32_bf16 v[228:231], a[52:55], a[180:183], v[228:231]\n\t"
    "v_mfma_f32_16x16x32_bf16 v[232:235], a[52:55], a[184:187], v[232:235]\n\t"
    "v_mfma_f32_16x16x32_bf16 v[236:239], a[52:55], a[188:191], v[236:239]\n\t"
    "CODE_GU_EPI:\n\t"
    "v_mov_b32 v2, 0xbdd2d3e8\n\t"
    "v_mov_b32 v3, v2\n\t"
    "v_mov_b32 v4, 0xc0135761\n\t"
    "v_mov_b32 v5, v4\n\t"
    "v_mov_b32 v6, 1.0\n\t"
    "v_mov_b32 v7, 1.0\n\t"
    "v_pk_mul_f32 v[8:9], v[16:17], v[16:17]\n\t"
    "v_pk_mul_f32 v[10:11], v[18:19], v[18:19]\n\t"
    "v_pk_fma_f32 v[8:9], v[8:9], v[2:3], v[4:5]\n\t"
    "v_pk_fma_f32 v[10:11], v[10:11], v[2:3], v[4:5]\n\t"
    "v_pk_mul_f32 v[8:9], v[16:17], v[8:9]\n\t"
    "v_pk_mul_f32 v[10:11], v[18:19], v[10:11]\n\t"
    "v_exp_f32 v8, v8\n\t"
    "v_exp_f32 v10, v10\n\t"
    "v_exp_f32 v9, v9\n\t"
    "v_exp_f32 v11, v11\n\t"
    "v_pk_mul_f32 v[128:129], v[16:17], v[128:129]\n\t"
    "v_pk_mul_f32 v[130:131], v[18:19], v[130:131]\n\t"
    "v_pk_add_f32 v[8:9], v[8:9], v[6:7]\n\t"
    "v_pk_add_f32 v[10:11], v[10:11], v[6:7]\n\t"
    "v_rcp_f32 v8, v8\n\t"
    "v_rcp_f32 v10, v10\n\t"
    "v_rcp_f32 v9, v9\n\t"
    "v_rcp_f32 v11, v11\n\t"
    "v_pk_mul_f32 v[128:129], v[128:129], v[8:9]\n\t"
    "v_pk_mul_f32 v[130:131], v[130:131], v[10:11]\n\t"
    "v_cvt_pk_bf16_f32 v244, v128, v129\n\t"
    "v_cvt_pk_bf16_f32 v245, v130, v131\n\t"
    "v_pk_mul_f32 v[8:9], v[32:33], v[32:33]\n\t"
    "v_pk_mul_f32 v[10:11], v[34:35], v[34:35]\n\t"
    "v_pk_fma_f32 v[8:9], v[8:9], v[2:3], v[4:5]\n\t"
    "v_pk_fma_f32 v[10:11], v[10:11], v[2:3], v[4:5]\n\t"
    "v_pk_mul_f32 v[8:9], v[32:33], v[8:9]\n\t"
    "v_pk_mul_f32 v[10:11], v[34:35], v[10:11]\n\t"
    "v_exp_f32 v8, v8\n\t"
    "v_exp_f32 v10, v10\n\t"
    "v_exp_f32 v9, v9\n\t"
    "v_exp_f32 v11, v11\n\t"
    "v_pk_mul_f32 v[144:145], v[32:33], v[144:145]\n\t"
    "v_pk_mul_f32 v[146:147], v[34:35], v[146:147]\n\t"
    "v_pk_add_f32 v[8:9], v[8:9], v[6:7]\n\t"
    "v_pk_add_f32 v[10:11], v[10:11], v[6:7]\n\t"
    "v_rcp_f32 v8, v8\n\t"
    "v_rcp_f32 v10, v10\n\t"
    "v_rcp_f32 v9, v9\n\t"
    "v_rcp_f32 v11, v11\n\t"
    "v_pk_mul_f32 v[144:145], v[144:145], v[8:9]\n\t"
    "v_pk_mul_f32 v[146:147], v[146:147], v[10:11]\n\t"
    "v_cvt_pk_bf16_f32 v246, v144, v145\n\t"
    "v_cvt_pk_bf16_f32 v247, v146, v147\n\t"
    "s_nop 0\n\t"
    "v_permlane16_swap_b32 v244, v246\n\t"
    "v_permlane16_swap_b32 v245, v247\n\t"
    "buffer_store_dwordx4 v[244:247], v240, s[60:63], s88 offen offset:0 sc1\n\t"
    "v_pk_mul_f32 v[8:9], v[20:21], v[20:21]\n\t"
    "v_pk_mul_f32 v[10:11], v[22:23], v[22:23]\n\t"
    "v_pk_fma_f32 v[8:9], v[8:9], v[2:3], v[4:5]\n\t"
    "v_pk_fma_f32 v[10:11], v[10:11], v[2:3], v[4:5]\n\t"
    "v_pk_mul_f32 v[8:9], v[20:21], v[8:9]\n\t"
    "v_pk_mul_f32 v[10:11], v[22:23], v[10:11]\n\t"
    "v_exp_f32 v8, v8\n\t"
    "v_exp_f32 v10, v10\n\t"
    "v_exp_f32 v9, v9\n\t"
    "v_exp_f32 v11, v11\n\t"
    "v_pk_mul_f32 v[132:133], v[20:21], v[132:133]\n\t"
    "v_pk_mul_f32 v[134:135], v[22:23], v[134:135]\n\t"
    "v_pk_add_f32 v[8:9], v[8:9], v[6:7]\n\t"
    "v_pk_add_f32 v[10:11], v[10:11], v[6:7]\n\t"
    "v_rcp_f32 v8, v8\n\t"
    "v_rcp_f32 v10, v10\n\t"
    "v_rcp_f32 v9, v9\n\t"
    "v_rcp_f32 v11, v11\n\t"
    "v_pk_mul_f32 v[132:133], v[132:133], v[8:9]\n\t"
    "v_pk_mul_f32 v[134:135], v[134:135], v[10:11]\n\t"
    "v_cvt_pk_bf16_f32 v248, v132, v133\n\t"
    "v_cvt_pk_bf16_f32 v249, v134, v135\n\t"
    "v_pk_mul_f32 v[8:9], v[36:37], v[36:37]\n\t"
    "v_pk_mul_f32 v[10:11], v[38:39], v[38:39]\n\t"
    "v_pk_fma_f32 v[8:9], v[8:9], v[2:3], v[4:5]\n\t"
    "v_pk_fma_f32 v[10:11], v[10:11], v[2:3], v[4:5]\n\t"
    "v_pk_mul_f32 v[8:9], v[36:37], v[8:9]\n\t"
    "v_pk_mul_f32 v[10:11], v[38:39], v[10:11]\n\t"
    "v_exp_f32 v8, v8\n\t"
    "v_exp_f32 v10, v10\n\t"
    "v_exp_f32 v9, v9\n\t"
    "v_exp_f32 v11, v11\n\t"
    "v_pk_mul_f32 v[148:149], v[36:37], v[148:149]\n\t"
    "v_pk_mul_f32 v[150:151], v[38:39], v[150:151]\n\t"
    "v_pk_add_f32 v[8:9], v[8:9], v[6:7]\n\t"
    "v_pk_add_f32 v[10:11], v[10:11], v[6:7]\n\t"
    "v_rcp_f32 v8, v8\n\t"
    "v_rcp_f32 v10, v10\n\t"
    "v_rcp_f32 v9, v9\n\t"
    "v_rcp_f32 v11, v11\n\t"
    "v_pk_mul_f32 v[148:149], v[148:149], v[8:9]\n\t"
    "v_pk_mul_f32 v[150:151], v[150:151], v[10:11]\n\t"
    "v_cvt_pk_bf16_f32 v250, v148, v149\n\t"
    "v_cvt_pk_bf16_f32 v251, v150, v151\n\t"
    "s_nop 0\n\t"
    "v_permlane16_swap_b32 v248, v250\n\t"
    "v_permlane16_swap_b32 v249, v251\n\t"
    "buffer_store_dwordx4 v[248:251], v240, s[60:63], s88 offen offset:256 sc1\n\t"
    "v_pk_mul_f32 v[8:9], v[24:25], v[24:25]\n\t"
    "v_pk_mul_f32 v[10:11], v[26:27], v[26:27]\n\t"
    "v_pk_fma_f32 v[8:9], v[8:9], v[2:3], v[4:5]\n\t"
    "v_pk_fma_f32 v[10:11], v[10:11], v[2:3], v[4:5]\n\t"
    "v_pk_mul_f32 v[8:9], v[24:25], v[8:9]\n\t"
    "v_pk_mul_f32 v[10:11], v[26:27], v[10:11]\n\t"
    "v_exp_f32 v8, v8\n\t"
    "v_exp_f32 v10, v10\n\t"
    "v_exp_f32 v9, v9\n\t"
    "v_exp_f32 v11, v11\n\t"
    "v_pk_mul_f32 v[136:137], v[24:25], v[136:137]\n\t"
    "v_pk_mul_f32 v[138:139], v[26:27], v[138:139]\n\t"
    "v_pk_add_f32 v[8:9], v[8:9], v[6:7]\n\t"
    "v_pk_add_f32 v[10:11], v[10:11], v[6:7]\n\t"
    "v_rcp_f32 v8, v8\n\t"
    "v_rcp_f32 v10, v10\n\t"
    "v_rcp_f32 v9, v9\n\t"
    "v_rcp_f32 v11, v11\n\t"
    "v_pk_mul_f32 v[136:137], v[136:137], v[8:9]\n\t"
    "v_pk_mul_f32 v[138:139], v[138:139], v[10:11]\n\t"
    "v_cvt_pk_bf16_f32 v244, v136, v137\n\t"
    "v_cvt_pk_bf16_f32 v245, v138, v139\n\t"
    "v_pk_mul_f32 v[8:9], v[40:41], v[40:41]\n\t"
    "v_pk_mul_f32 v[10:11], v[42:43], v[42:43]\n\t"
    "v_pk_fma_f32 v[8:9], v[8:9], v[2:3], v[4:5]\n\t"
    "v_pk_fma_f32 v[10:11], v[10:11], v[2:3], v[4:5]\n\t"
    "v_pk_mul_f32 v[8:9], v[40:41], v[8:9]\n\t"
    "v_pk_mul_f32 v[10:11], v[42:43], v[10:11]\n\t"
    "v_exp_f32 v8, v8\n\t"
    "v_exp_f32 v10, v10\n\t"
    "v_exp_f32 v9, v9\n\t"
    "v_exp_f32 v11, v11\n\t"
    "v_pk_mul_f32 v[152:153], v[40:41], v[152:153]\n\t"
    "v_pk_mul_f32 v[154:155], v[42:43], v[154:155]\n\t"
    "v_pk_add_f32 v[8:9], v[8:9], v[6:7]\n\t"
    "v_pk_add_f32 v[10:11], v[10:11], v[6:7]\n\t"
    "v_rcp_f32 v8, v8\n\t"
    "v_rcp_f32 v10, v10\n\t"
    "v_rcp_f32 v9, v9\n\t"
    "v_rcp_f32 v11, v11\n\t"
    "v_pk_mul_f32 v[152:153], v[152:153], v[8:9]\n\t"
    "v_pk_mul_f32 v[154:155], v[154:155], v[10:11]\n\t"
    "v_cvt_pk_bf16_f32 v246, v152, v153\n\t"
    "v_cvt_pk_bf16_f32 v247, v154, v155\n\t"
    "s_nop 0\n\t"
    "v_permlane16_swap_b32 v244, v246\n\t"
    "v_permlane16_swap_b32 v245, v247\n\t"
    "buffer_store_dwordx4 v[244:247], v240, s[60:63], s89 offen offset:0 sc1\n\t"
    "v_pk_mul_f32 v[8:9], v[28:29], v[28:29]\n\t"
    "v_pk_mul_f32 v[10:11], v[30:31], v[30:31]\n\t"
    "v_pk_fma_f32 v[8:9], v[8:9], v[2:3], v[4:5]\n\t"
    "v_pk_fma_f32 v[10:11], v[10:11], v[2:3], v[4:5]\n\t"
    "v_pk_mul_f32 v[8:9], v[28:29], v[8:9]\n\t"
    "v_pk_mul_f32 v[10:11], v[30:31], v[10:11]\n\t"
    "v_exp_f32 v8, v8\n\t"
    "v_exp_f32 v10, v10\n\t"
    "v_exp_f32 v9, v9\n\t"
    "v_exp_f32 v11, v11\n\t"
    "v_pk_mul_f32 v[140:141], v[28:29], v[140:141]\n\t"
    "v_pk_mul_f32 v[142:143], v[30:31], v[142:143]\n\t"
    "v_pk_add_f32 v[8:9], v[8:9], v[6:7]\n\t"
    "v_pk_add_f32 v[10:11], v[10:11], v[6:7]\n\t"
    "v_rcp_f32 v8, v8\n\t"
    "v_rcp_f32 v10, v10\n\t"
    "v_rcp_f32 v9, v9\n\t"
    "v_rcp_f32 v11, v11\n\t"
    "v_pk_mul_f32 v[140:141], v[140:141], v[8:9]\n\t"
    "v_pk_mul_f32 v[142:143], v[142:143], v[10:11]\n\t"
    "v_cvt_pk_bf16_f32 v248, v140, v141\n\t"
    "v_cvt_pk_bf16_f32 v249, v142, v143\n\t"
    "v_pk_mul_f32 v[8:9], v[44:45], v[44:45]\n\t"
    "v_pk_mul_f32 v[10:11], v[46:47], v[46:47]\n\t"
    "v_pk_fma_f32 v[8:9], v[8:9], v[2:3], v[4:5]\n\t"
    "v_pk_fma_f32 v[10:11], v[10:11], v[2:3], v[4:5]\n\t"
    "v_pk_mul_f32 v[8:9], v[44:45], v[8:9]\n\t"
    "v_pk_mul_f32 v[10:11], v[46:47], v[10:11]\n\t"
    "v_exp_f32 v8, v8\n\t"
    "v_exp_f32 v10, v10\n\t"
    "v_exp_f32 v9, v9\n\t"
    "v_exp_f32 v11, v11\n\t"
    "v_pk_mul_f32 v[156:157], v[44:45], v[156:157]\n\t"
    "v_pk_mul_f32 v[158:159], v[46:47], v[158:159]\n\t"
    "v_pk_add_f32 v[8:9], v[8:9], v[6:7]\n\t"
    "v_pk_add_f32 v[10:11], v[10:11], v[6:7]\n\t"
    "v_rcp_f32 v8, v8\n\t"
    "v_rcp_f32 v10, v10\n\t"
    "v_rcp_f32 v9, v9\n\t"
    "v_rcp_f32 v11, v11\n\t"
    "v_pk_mul_f32 v[156:157], v[156:157], v[8:9]\n\t"
    "v_pk_mul_f32 v[158:159], v[158:159], v[10:11]\n\t"
    "v_cvt_pk_bf16_f32 v250, v156, v157\n\t"
    "v_cvt_pk_bf16_f32 v251, v158, v159\n\t"
    "s_nop 0\n\t"
    "v_permlane16_swap_b32 v248, v250\n\t"
    "v_permlane16_swap_b32 v249, v251\n\t"
    "buffer_store_dwordx4 v[248:251], v240, s[60:63], s89 offen offset:256 sc1\n\t"
    "s_add_u32 s88, s88, 32768\n\t"
    "s_add_u32 s89, s89, 32768\n\t"
    "v_pk_mul_f32 v[8:9], v[48:49], v[48:49]\n\t"
    "v_pk_mul_f32 v[10:11], v[50:51], v[50:51]\n\t"
    "v_pk_fma_f32 v[8:9], v[8:9], v[2:3], v[4:5]\n\t"
    "v_pk_fma_f32 v[10:11], v[10:11], v[2:3], v[4:5]\n\t"
    "v_pk_mul_f32 v[8:9], v[48:49], v[8:9]\n\t"
    "v_pk_mul_f32 v[10:11], v[50:51], v[10:11]\n\t"
    "v_exp_f32 v8, v8\n\t"
    "v_exp_f32 v10, v10\n\t"
    "v_exp_f32 v9, v9\n\t"
    "v_exp_f32 v11, v11\n\t"
    "v_pk_mul_f32 v[160:161], v[48:49], v[160:161]\n\t"
    "v_pk_mul_f32 v[162:163], v[50:51], v[162:163]\n\t"
    "v_pk_add_f32 v[8:9], v[8:9], v[6:7]\n\t"
    "v_pk_add_f32 v[10:11], v[10:11], v[6:7]\n\t"
    "v_rcp_f32 v8, v8\n\t"
    "v_rcp_f32 v10, v10\n\t"
    "v_rcp_f32 v9, v9\n\t"
    "v_rcp_f32 v11, v11\n\t"
    "v_pk_mul_f32 v[160:161], v[160:161], v[8:9]\n\t"
    "v_pk_mul_f32 v[162:163], v[162:163], v[10:11]\n\t"
    "v_cvt_pk_bf16_f32 v244, v160, v161\n\t"
    "v_cvt_pk_bf16_f32 v245, v162, v163\n\t"
    "v_pk_mul_f32 v[8:9], v[64:65], v[64:65]\n\t"
    "v_pk_mul_f32 v[10:11], v[66:67], v[66:67]\n\t"
    "v_pk_fma_f32 v[8:9], v[8:9], v[2:3], v[4:5]\n\t"
    "v_pk_fma_f32 v[10:11], v[10:11], v[2:3], v[4:5]\n\t"
    "v_pk_mul_f32 v[8:9], v[64:65], v[8:9]\n\t"
    "v_pk_mul_f32 v[10:11], v[66:67], v[10:11]\n\t"
    "v_exp_f32 v8, v8\n\t"
    "v_exp_f32 v10, v10\n\t"
    "v_exp_f32 v9, v9\n\t"
    "v_exp_f32 v11, v11\n\t"
    "v_pk_mul_f32 v[176:177], v[64:65], v[176:177]\n\t"
    "v_pk_mul_f32 v[178:179], v[66:67], v[178:179]\n\t"
    "v_pk_add_f32 v[8:9], v[8:9], v[6:7]\n\t"
    "v_pk_add_f32 v[10:11], v[10:11], v[6:7]\n\t"
    "v_rcp_f32 v8, v8\n\t"
    "v_rcp_f32 v10, v10\n\t"
    "v_rcp_f32 v9, v9\n\t"
    "v_rcp_f32 v11, v11\n\t"
    "v_pk_mul_f32 v[176:177], v[176:177], v[8:9]\n\t"
    "v_pk_mul_f32 v[178:179], v[178:179], v[10:11]\n\t"
    "v_cvt_pk_bf16_f32 v246, v176, v177\n\t"
    "v_cvt_pk_bf16_f32 v247, v178, v179\n\t"
    "s_nop 0\n\t"
    "v_permlane16_swap_b32 v244, v246\n\t"
    "v_permlane16_swap_b32 v245, v247\n\t"
    "buffer_store_dwordx4 v[244:247], v240, s[60:63], s88 offen offset:0 sc1\n\t"
    "v_pk_mul_f32 v[8:9], v[52:53], v[52:53]\n\t"
    "v_pk_mul_f32 v[10:11], v[54:55], v[54:55]\n\t"
    "v_pk_fma_f32 v[8:9], v[8:9], v[2:3], v[4:5]\n\t"
    "v_pk_fma_f32 v[10:11], v[10:11], v[2:3], v[4:5]\n\t"
    "v_pk_mul_f32 v[8:9], v[52:53], v[8:9]\n\t"
    "v_pk_mul_f32 v[10:11], v[54:55], v[10:11]\n\t"
    "v_exp_f32 v8, v8\n\t"
    "v_exp_f32 v10, v10\n\t"
    "v_exp_f32 v9, v9\n\t"
    "v_exp_f32 v11, v11\n\t"
    "v_pk_mul_f32 v[164:165], v[52:53], v[164:165]\n\t"
    "v_pk_mul_f32 v[166:167], v[54:55], v[166:167]\n\t"
    "v_pk_add_f32 v[8:9], v[8:9], v[6:7]\n\t"
    "v_pk_add_f32 v[10:11], v[10:11], v[6:7]\n\t"
    "v_rcp_f32 v8, v8\n\t"
    "v_rcp_f32 v10, v10\n\t"
    "v_rcp_f32 v9, v9\n\t"
    "v_rcp_f32 v11, v11\n\t"
    "v_pk_mul_f32 v[164:165], v[164:165], v[8:9]\n\t"
    "v_pk_mul_f32 v[166:167], v[166:167], v[10:11]\n\t"
    "v_cvt_pk_bf16_f32 v248, v164, v165\n\t"
    "v_cvt_pk_bf16_f32 v249, v166, v167\n\t"
    "v_pk_mul_f32 v[8:9], v[68:69], v[68:69]\n\t"
    "v_pk_mul_f32 v[10:11], v[70:71], v[70:71]\n\t"
    "v_pk_fma_f32 v[8:9], v[8:9], v[2:3], v[4:5]\n\t"
    "v_pk_fma_f32 v[10:11], v[10:11], v[2:3], v[4:5]\n\t"
    "v_pk_mul_f32 v[8:9], v[68:69], v[8:9]\n\t"
    "v_pk_mul_f32 v[10:11], v[70:71], v[10:11]\n\t"
    "v_exp_f32 v8, v8\n\t"
    "v_exp_f32 v10, v10\n\t"
    "v_exp_f32 v9, v9\n\t"
    "v_exp_f32 v11, v11\n\t"
    "v_pk_mul_f32 v[180:181], v[68:69], v[180:181]\n\t"
    "v_pk_mul_f32 v[182:183], v[70:71], v[182:183]\n\t"
    "v_pk_add_f32 v[8:9], v[8:9], v[6:7]\n\t"
    "v_pk_add_f32 v[10:11], v[10:11], v[6:7]\n\t"
    "v_rcp_f32 v8, v8\n\t"
    "v_rcp_f32 v10, v10\n\t"
    "v_rcp_f32 v9, v9\n\t"
    "v_rcp_f32 v11, v11\n\t"
    "v_pk_mul_f32 v[180:181], v[180:181], v[8:9]\n\t"
    "v_pk_mul_f32 v[182:183], v[182:183], v[10:11]\n\t"
    "v_cvt_pk_bf16_f32 v250, v180, v181\n\t"
    "v_cvt_pk_bf16_f32 v251, v182, v183\n\t"
    "s_nop 0\n\t"
    "v_permlane16_swap_b32 v248, v250\n\t"
    "v_permlane16_swap_b32 v249, v251\n\t"
    "buffer_store_dwordx4 v[248:251], v240, s[60:63], s88 offen offset:256 sc1\n\t"
    "s_waitcnt vmcnt(2)\n\t"
    "s_barrier\n\t"
    "s_add_u32 s80, s91, 0\n\t"
    "s_lshl_b32 s80, s80, 4\n\t"
    "v_mov_b32 v12, s80\n\t"
    "v_mov_b32 v13, %4\n\t"
    "s_cmp_eq_u32 s34, 0\n\t"
    "s_cselect_b32 s80, 1, 0\n\t"
    "s_mov_b32 exec_hi, 0\n\t"
    "s_mov_b32 exec_lo, s80\n\t"
    "global_store_dword v12, v13, s[44:45] sc1\n\t"
    "s_mov_b64 exec, -1\n\t"
    "v_pk_mul_f32 v[8:9], v[56:57], v[56:57]\n\t"
    "v_pk_mul_f32 v[10:11], v[58:59], v[58:59]\n\t"
    "v_pk_fma_f32 v[8:9], v[8:9], v[2:3], v[4:5]\n\t"
    "v_pk_fma_f32 v[10:11], v[10:11], v[2:3], v[4:5]\n\t"
    "v_pk_mul_f32 v[8:9], v[56:57], v[8:9]\n\t"
    "v_pk_mul_f32 v[10:11], v[58:59], v[10:11]\n\t"
    "v_exp_f32 v8, v8\n\t"
    "v_exp_f32 v10, v10\n\t"
    "v_exp_f32 v9, v9\n\t"
    "v_exp_f32 v11, v11\n\t"
    "v_pk_mul_f32 v[168:169], v[56:57], v[168:169]\n\t"
    "v_pk_mul_f32 v[170:171], v[58:59], v[170:171]\n\t"
    "v_pk_add_f32 v[8:9], v[8:9], v[6:7]\n\t"
    "v_pk_add_f32 v[10:11], v[10:11], v[6:7]\n\t"
    "v_rcp_f32 v8, v8\n\t"
    "v_rcp_f32 v10, v10\n\t"
    "v_rcp_f32 v9, v9\n\t"
    "v_rcp_f32 v11, v11\n\t"
    "v_pk_mul_f32 v[168:169], v[168:169], v[8:9]\n\t"
    "v_pk_mul_f32 v[170:171], v[170:171], v[10:11]\n\t"
    "v_cvt_pk_bf16_f32 v244, v168, v169\n\t"
    "v_cvt_pk_bf16_f32 v245, v170, v171\n\t"
    "v_pk_mul_f32 v[8:9], v[72:73], v[72:73]\n\t"
    "v_pk_mul_f32 v[10:11], v[74:75], v[74:75]\n\t"
    "v_pk_fma_f32 v[8:9], v[8:9], v[2:3], v[4:5]\n\t"
    "v_pk_fma_f32 v[10:11], v[10:11], v[2:3], v[4:5]\n\t"
    "v_pk_mul_f32 v[8:9], v[72:73], v[8:9]\n\t"
    "v_pk_mul_f32 v[10:11], v[74:75], v[10:11]\n\t"
    "v_exp_f32 v8, v8\n\t"
    "v_exp_f32 v10, v10\n\t"
    "v_exp_f32 v9, v9\n\t"
    "v_exp_f32 v11, v11\n\t"
    "v_pk_mul_f32 v[184:185], v[72:73], v[184:185]\n\t"
    "v_pk_mul_f32 v[186:187], v[74:75], v[186:187]\n\t"
    "v_pk_add_f32 v[8:9], v[8:9], v[6:7]\n\t"
    "v_pk_add_f32 v[10:11], v[10:11], v[6:7]\n\t"
    "v_rcp_f32 v8, v8\n\t"
    "v_rcp_f32 v10, v10\n\t"
    "v_rcp_f32 v9, v9\n\t"
    "v_rcp_f32 v11, v11\n\t"
    "v_pk_mul_f32 v[184:185], v[184:185], v[8:9]\n\t"
    "v_pk_mul_f32 v[186:187], v[186:187], v[10:11]\n\t"
    "v_cvt_pk_bf16_f32 v246, v184, v185\n\t"
    "v_cvt_pk_bf16_f32 v247, v186, v187\n\t"
    "s_nop 0\n\t"
    "v_permlane16_swap_b32 v244, v246\n\t"
    "v_permlane16_swap_b32 v245, v247\n\t"
    "buffer_store_dwordx4 v[244:247], v240, s[60:63], s89 offen offset:0 sc1\n\t"
    "v_pk_mul_f32 v[8:9], v[60:61], v[60:61]\n\t"
    "v_pk_mul_f32 v[10:11], v[62:63], v[62:63]\n\t"
    "v_pk_fma_f32 v[8:9], v[8:9], v[2:3], v[4:5]\n\t"
    "v_pk_fma_f32 v[10:11], v[10:11], v[2:3], v[4:5]\n\t"
    "v_pk_mul_f32 v[8:9], v[60:61], v[8:9]\n\t"
    "v_pk_mul_f32 v[10:11], v[62:63], v[10:11]\n\t"
    "v_exp_f32 v8, v8\n\t"
    "v_exp_f32 v10, v10\n\t"
    "v_exp_f32 v9, v9\n\t"
    "v_exp_f32 v11, v11\n\t"
    "v_pk_mul_f32 v[172:173], v[60:61], v[172:173]\n\t"
    "v_pk_mul_f32 v[174:175], v[62:63], v[174:175]\n\t"
    "v_pk_add_f32 v[8:9], v[8:9], v[6:7]\n\t"
    "v_pk_add_f32 v[10:11], v[10:11], v[6:7]\n\t"
    "v_rcp_f32 v8, v8\n\t"
    "v_rcp_f32 v10, v10\n\t"
    "v_rcp_f32 v9, v9\n\t"
    "v_rcp_f32 v11, v11\n\t"
    "v_pk_mul_f32 v[172:173], v[172:173], v[8:9]\n\t"
    "v_pk_mul_f32 v[174:175], v[174:175], v[10:11]\n\t"
    "v_cvt_pk_bf16_f32 v248, v172, v173\n\t"
    "v_cvt_pk_bf16_f32 v249, v174, v175\n\t"
    "v_pk_mul_f32 v[8:9], v[76:77], v[76:77]\n\t"
    "v_pk_mul_f32 v[10:11], v[78:79], v[78:79]\n\t"
    "v_pk_fma_f32 v[8:9], v[8:9], v[2:3], v[4:5]\n\t"
    "v_pk_fma_f32 v[10:11], v[10:11], v[2:3], v[4:5]\n\t"
    "v_pk_mul_f32 v[8:9], v[76:77], v[8:9]\n\t"
    "v_pk_mul_f32 v[10:11], v[78:79], v[10:11]\n\t"
    "v_exp_f32 v8, v8\n\t"
    "v_exp_f32 v10, v10\n\t"
    "v_exp_f32 v9, v9\n\t"
    "v_exp_f32 v11, v11\n\t"
    "v_pk_mul_f32 v[188:189], v[76:77], v[188:189]\n\t"
    "v_pk_mul_f32 v[190:191], v[78:79], v[190:191]\n\t"
    "v_pk_add_f32 v[8:9], v[8:9], v[6:7]\n\t"
    "v_pk_add_f32 v[10:11], v[10:11], v[6:7]\n\t"
    "v_rcp_f32 v8, v8\n\t"
    "v_rcp_f32 v10, v10\n\t"
    "v_rcp_f32 v9, v9\n\t"
    "v_rcp_f32 v11, v11\n\t"
    "v_pk_mul_f32 v[188:189], v[188:189], v[8:9]\n\t"
    "v_pk_mul_f32 v[190:191], v[190:191], v[10:11]\n\t"
    "v_cvt_pk_bf16_f32 v250, v188, v189\n\t"
    "v_cvt_pk_bf16_f32 v251, v190, v191\n\t"
    "s_nop 0\n\t"
    "v_permlane16_swap_b32 v248, v250\n\t"
    "v_permlane16_swap_b32 v249, v251\n\t"
    "buffer_store_dwordx4 v[248:251], v240, s[60:63], s89 offen offset:256 sc1\n\t"
    "s_add_u32 s88, s88, 32768\n\t"
    "s_add_u32 s89, s89, 32768\n\t"
    "v_pk_mul_f32 v[8:9], v[80:81], v[80:81]\n\t"
    "v_pk_mul_f32 v[10:11], v[82:83], v[82:83]\n\t"
    "v_pk_fma_f32 v[8:9], v[8:9], v[2:3], v[4:5]\n\t"
    "v_pk_fma_f32 v[10:11], v[10:11], v[2:3], v[4:5]\n\t"
    "v_pk_mul_f32 v[8:9], v[80:81], v[8:9]\n\t"
    "v_pk_mul_f32 v[10:11], v[82:83], v[10:11]\n\t"
    "v_exp_f32 v8, v8\n\t"
    "v_exp_f32 v10, v10\n\t"
    "v_exp_f32 v9, v9\n\t"
    "v_exp_f32 v11, v11\n\t"
    "v_pk_mul_f32 v[192:193], v[80:81], v[192:193]\n\t"
    "v_pk_mul_f32 v[194:195], v[82:83], v[194:195]\n\t"
    "v_pk_add_f32 v[8:9], v[8:9], v[6:7]\n\t"
    "v_pk_add_f32 v[10:11], v[10:11], v[6:7]\n\t"
    "v_rcp_f32 v8, v8\n\t"
    "v_rcp_f32 v10, v10\n\t"
    "v_rcp_f32 v9, v9\n\t"
    "v_rcp_f32 v11, v11\n\t"
    "v_pk_mul_f32 v[192:193], v[192:193], v[8:9]\n\t"
    "v_pk_mul_f32 v[194:195], v[194:195], v[10:11]\n\t"
    "v_cvt_pk_bf16_f32 v244, v192, v193\n\t"
    "v_cvt_pk_bf16_f32 v245, v194, v195\n\t"
    "v_pk_mul_f32 v[8:9], v[96:97], v[96:97]\n\t"
    "v_pk_mul_f32 v[10:11], v[98:99], v[98:99]\n\t"
    "v_pk_fma_f32 v[8:9], v[8:9], v[2:3], v[4:5]\n\t"
    "v_pk_fma_f32 v[10:11], v[10:11], v[2:3], v[4:5]\n\t"
    "v_pk_mul_f32 v[8:9], v[96:97], v[8:9]\n\t"
    "v_pk_mul_f32 v[10:11], v[98:99], v[10:11]\n\t"
    "v_exp_f32 v8, v8\n\t"
    "v_exp_f32 v10, v10\n\t"
    "v_exp_f32 v9, v9\n\t"
    "v_exp_f32 v11, v11\n\t"
    "v_pk_mul_f32 v[208:209], v[96:97], v[208:209]\n\t"
    "v_pk_mul_f32 v[210:211], v[98:99], v[210:211]\n\t"
    "v_pk_add_f32 v[8:9], v[8:9], v[6:7]\n\t"
    "v_pk_add_f32 v[10:11], v[10:11], v[6:7]\n\t"
    "v_rcp_f32 v8, v8\n\t"
    "v_rcp_f32 v10, v10\n\t"
    "v_rcp_f32 v9, v9\n\t"
    "v_rcp_f32 v11, v11\n\t"
    "v_pk_mul_f32 v[208:209], v[208:209], v[8:9]\n\t"
    "v_pk_mul_f32 v[210:211], v[210:211], v[10:11]\n\t"
    "v_cvt_pk_bf16_f32 v246, v208, v209\n\t"
    "v_cvt_pk_bf16_f32 v247, v210, v211\n\t"
    "s_nop 0\n\t"
    "v_permlane16_swap_b32 v244, v246\n\t"
    "v_permlane16_swap_b32 v245, v247\n\t"
    "buffer_store_dwordx4 v[244:247], v240, s[60:63], s88 offen offset:0 sc1\n\t"
    "v_pk_mul_f32 v[8:9], v[84:85], v[84:85]\n\t"
    "v_pk_mul_f32 v[10:11], v[86:87], v[86:87]\n\t"
    "v_pk_fma_f32 v[8:9], v[8:9], v[2:3], v[4:5]\n\t"
    "v_pk_fma_f32 v[10:11], v[10:11], v[2:3], v[4:5]\n\t"
    "v_pk_mul_f32 v[8:9], v[84:85], v[8:9]\n\t"
    "v_pk_mul_f32 v[10:11], v[86:87], v[10:11]\n\t"
    "v_exp_f32 v8, v8\n\t"
    "v_exp_f32 v10, v10\n\t"
    "v_exp_f32 v9, v9\n\t"
    "v_exp_f32 v11, v11\n\t"
    "v_pk_mul_f32 v[196:197], v[84:85], v[196:197]\n\t"
    "v_pk_mul_f32 v[198:199], v[86:87], v[198:199]\n\t"
    "v_pk_add_f32 v[8:9], v[8:9], v[6:7]\n\t"
    "v_pk_add_f32 v[10:11], v[10:11], v[6:7]\n\t"
    "v_rcp_f32 v8, v8\n\t"
    "v_rcp_f32 v10, v10\n\t"
    "v_rcp_f32 v9, v9\n\t"
    "v_rcp_f32 v11, v11\n\t"
    "v_pk_mul_f32 v[196:197], v[196:197], v[8:9]\n\t"
    "v_pk_mul_f32 v[198:199], v[198:199], v[10:11]\n\t"
    "v_cvt_pk_bf16_f32 v248, v196, v197\n\t"
    "v_cvt_pk_bf16_f32 v249, v198, v199\n\t"
    "v_pk_mul_f32 v[8:9], v[100:101], v[100:101]\n\t"
    "v_pk_mul_f32 v[10:11], v[102:103], v[102:103]\n\t"
    "v_pk_fma_f32 v[8:9], v[8:9], v[2:3], v[4:5]\n\t"
    "v_pk_fma_f32 v[10:11], v[10:11], v[2:3], v[4:5]\n\t"
    "v_pk_mul_f32 v[8:9], v[100:101], v[8:9]\n\t"
    "v_pk_mul_f32 v[10:11], v[102:103], v[10:11]\n\t"
    "v_exp_f32 v8, v8\n\t"
    "v_exp_f32 v10, v10\n\t"
    "v_exp_f32 v9, v9\n\t"
    "v_exp_f32 v11, v11\n\t"
    "v_pk_mul_f32 v[212:213], v[100:101], v[212:213]\n\t"
    "v_pk_mul_f32 v[214:215], v[102:103], v[214:215]\n\t"
    "v_pk_add_f32 v[8:9], v[8:9], v[6:7]\n\t"
    "v_pk_add_f32 v[10:11], v[10:11], v[6:7]\n\t"
    "v_rcp_f32 v8, v8\n\t"
    "v_rcp_f32 v10, v10\n\t"
    "v_rcp_f32 v9, v9\n\t"
    "v_rcp_f32 v11, v11\n\t"
    "v_pk_mul_f32 v[212:213], v[212:213], v[8:9]\n\t"
    "v_pk_mul_f32 v[214:215], v[214:215], v[10:11]\n\t"
    "v_cvt_pk_bf16_f32 v250, v212, v213\n\t"
    "v_cvt_pk_bf16_f32 v251, v214, v215\n\t"
    "s_nop 0\n\t"
    "v_permlane16_swap_b32 v248, v250\n\t"
    "v_permlane16_swap_b32 v249, v251\n\t"
    "buffer_store_dwordx4 v[248:251], v240, s[60:63], s88 offen offset:256 sc1\n\t"
    "s_waitcnt vmcnt(2)\n\t"
    "s_barrier\n\t"
    "s_add_u32 s80, s91, 1024\n\t"
    "s_lshl_b32 s80, s80, 4\n\t"
    "v_mov_b32 v12, s80\n\t"
    "v_mov_b32 v13, %4\n\t"
    "s_cmp_eq_u32 s34, 0\n\t"
    "s_cselect_b32 s80, 1, 0\n\t"
    "s_mov_b32 exec_hi, 0\n\t"
    "s_mov_b32 exec_lo, s80\n\t"
    "global_store_dword v12, v13, s[44:45] sc1\n\t"
    "s_mov_b64 exec, -1\n\t"
    "v_pk_mul_f32 v[8:9], v[88:89], v[88:89]\n\t"
    "v_pk_mul_f32 v[10:11], v[90:91], v[90:91]\n\t"
    "v_pk_fma_f32 v[8:9], v[8:9], v[2:3], v[4:5]\n\t"
    "v_pk_fma_f32 v[10:11], v[10:11], v[2:3], v[4:5]\n\t"
    "v_pk_mul_f32 v[8:9], v[88:89], v[8:9]\n\t"
    "v_pk_mul_f32 v[10:11], v[90:91], v[10:11]\n\t"
    "v_exp_f32 v8, v8\n\t"
    "v_exp_f32 v10, v10\n\t"
    "v_exp_f32 v9, v9\n\t"
    "v_exp_f32 v11, v11\n\t"
    "v_pk_mul_f32 v[200:201], v[88:89], v[200:201]\n\t"
    "v_pk_mul_f32 v[202:203], v[90:91], v[202:203]\n\t"
    "v_pk_add_f32 v[8:9], v[8:9], v[6:7]\n\t"
    "v_pk_add_f32 v[10:11], v[10:11], v[6:7]\n\t"
    "v_rcp_f32 v8, v8\n\t"
    "v_rcp_f32 v10, v10\n\t"
    "v_rcp_f32 v9, v9\n\t"
    "v_rcp_f32 v11, v11\n\t"
    "v_pk_mul_f32 v[200:201], v[200:201], v[8:9]\n\t"
    "v_pk_mul_f32 v[202:203], v[202:203], v[10:11]\n\t"
    "v_cvt_pk_bf16_f32 v244, v200, v201\n\t"
    "v_cvt_pk_bf16_f32 v245, v202, v203\n\t"
    "v_pk_mul_f32 v[8:9], v[104:105], v[104:105]\n\t"
    "v_pk_mul_f32 v[10:11], v[106:107], v[106:107]\n\t"
    "v_pk_fma_f32 v[8:9], v[8:9], v[2:3], v[4:5]\n\t"
    "v_pk_fma_f32 v[10:11], v[10:11], v[2:3], v[4:5]\n\t"
    "v_pk_mul_f32 v[8:9], v[104:105], v[8:9]\n\t"
    "v_pk_mul_f32 v[10:11], v[106:107], v[10:11]\n\t"
    "v_exp_f32 v8, v8\n\t"
    "v_exp_f32 v10, v10\n\t"
    "v_exp_f32 v9, v9\n\t"
    "v_exp_f32 v11, v11\n\t"
    "v_pk_mul_f32 v[216:217], v[104:105], v[216:217]\n\t"
    "v_pk_mul_f32 v[218:219], v[106:107], v[218:219]\n\t"
    "v_pk_add_f32 v[8:9], v[8:9], v[6:7]\n\t"
    "v_pk_add_f32 v[10:11], v[10:11], v[6:7]\n\t"
    "v_rcp_f32 v8, v8\n\t"
    "v_rcp_f32 v10, v10\n\t"
    "v_rcp_f32 v9, v9\n\t"
    "v_rcp_f32 v11, v11\n\t"
    "v_pk_mul_f32 v[216:217], v[216:217], v[8:9]\n\t"
    "v_pk_mul_f32 v[218:219], v[218:219], v[10:11]\n\t"
    "v_cvt_pk_bf16_f32 v246, v216, v217\n\t"
    "v_cvt_pk_bf16_f32 v247, v218, v219\n\t"
    "s_nop 0\n\t"
    "v_permlane16_swap_b32 v244, v246\n\t"
    "v_permlane16_swap_b32 v245, v247\n\t"
    "buffer_store_dwordx4 v[244:247], v240, s[60:63], s89 offen offset:0 sc1\n\t"
    "v_pk_mul_f32 v[8:9], v[92:93], v[92:93]\n\t"
    "v_pk_mul_f32 v[10:11], v[94:95], v[94:95]\n\t"
    "v_pk_fma_f32 v[8:9], v[8:9], v[2:3], v[4:5]\n\t"
    "v_pk_fma_f32 v[10:11], v[10:11], v[2:3], v[4:5]\n\t"
    "v_pk_mul_f32 v[8:9], v[92:93], v[8:9]\n\t"
    "v_pk_mul_f32 v[10:11], v[94:95], v[10:11]\n\t"
    "v_exp_f32 v8, v8\n\t"
    "v_exp_f32 v10, v10\n\t"
    "v_exp_f32 v9, v9\n\t"
    "v_exp_f32 v11, v11\n\t"
    "v_pk_mul_f32 v[204:205], v[92:93], v[204:205]\n\t"
    "v_pk_mul_f32 v[206:207], v[94:95], v[206:207]\n\t"
    "v_pk_add_f32 v[8:9], v[8:9], v[6:7]\n\t"
    "v_pk_add_f32 v[10:11], v[10:11], v[6:7]\n\t"
    "v_rcp_f32 v8, v8\n\t"
    "v_rcp_f32 v10, v10\n\t"
    "v_rcp_f32 v9, v9\n\t"
    "v_rcp_f32 v11, v11\n\t"
    "v_pk_mul_f32 v[204:205], v[204:205], v[8:9]\n\t"
    "v_pk_mul_f32 v[206:207], v[206:207], v[10:11]\n\t"
    "v_cvt_pk_bf16_f32 v248, v204, v205\n\t"
    "v_cvt_pk_bf16_f32 v249, v206, v207\n\t"
    "v_pk_mul_f32 v[8:9], v[108:109], v[108:109]\n\t"
    "v_pk_mul_f32 v[10:11], v[110:111], v[110:111]\n\t"
    "v_pk_fma_f32 v[8:9], v[8:9], v[2:3], v[4:5]\n\t"
    "v_pk_fma_f32 v[10:11], v[10:11], v[2:3], v[4:5]\n\t"
    "v_pk_mul_f32 v[8:9], v[108:109], v[8:9]\n\t"
    "v_pk_mul_f32 v[10:11], v[110:111], v[10:11]\n\t"
    "v_exp_f32 v8, v8\n\t"
    "v_exp_f32 v10, v10\n\t"
    "v_exp_f32 v9, v9\n\t"
    "v_exp_f32 v11, v11\n\t"
    "v_pk_mul_f32 v[220:221], v[108:109], v[220:221]\n\t"
    "v_pk_mul_f32 v[222:223], v[110:111], v[222:223]\n\t"
    "v_pk_add_f32 v[8:9], v[8:9], v[6:7]\n\t"
    "v_pk_add_f32 v[10:11], v[10:11], v[6:7]\n\t"
    "v_rcp_f32 v8, v8\n\t"
    "v_rcp_f32 v10, v10\n\t"
    "v_rcp_f32 v9, v9\n\t"
    "v_rcp_f32 v11, v11\n\t"
    "v_pk_mul_f32 v[220:221], v[220:221], v[8:9]\n\t"
    "v_pk_mul_f32 v[222:223], v[222:223], v[10:11]\n\t"
    "v_cvt_pk_bf16_f32 v250, v220, v221\n\t"
    "v_cvt_pk_bf16_f32 v251, v222, v223\n\t"
    "s_nop 0\n\t"
    "v_permlane16_swap_b32 v248, v250\n\t"
    "v_permlane16_swap_b32 v249, v251\n\t"
    "buffer_store_dwordx4 v[248:251], v240, s[60:63], s89 offen offset:256 sc1\n\t"
    "s_add_u32 s88, s88, 32768\n\t"
    "s_add_u32 s89, s89, 32768\n\t"
    "s_sub_u32 s88, s88, s90\n\t"
    "s_sub_u32 s89, s89, s90\n\t"
    "v_pk_mul_f32 v[8:9], v[112:113], v[112:113]\n\t"
    "v_pk_mul_f32 v[10:11], v[114:115], v[114:115]\n\t"
    "v_pk_fma_f32 v[8:9], v[8:9], v[2:3], v[4:5]\n\t"
    "v_pk_fma_f32 v[10:11], v[10:11], v[2:3], v[4:5]\n\t"
    "v_pk_mul_f32 v[8:9], v[112:113], v[8:9]\n\t"
    "v_pk_mul_f32 v[10:11], v[114:115], v[10:11]\n\t"
    "v_exp_f32 v8, v8\n\t"
    "v_exp_f32 v10, v10\n\t"
    "v_exp_f32 v9, v9\n\t"
    "v_exp_f32 v11, v11\n\t"
    "v_pk_mul_f32 v[224:225], v[112:113], v[224:225]\n\t"
    "v_pk_mul_f32 v[226:227], v[114:115], v[226:227]\n\t"
    "v_pk_add_f32 v[8:9], v[8:9], v[6:7]\n\t"
    "v_pk_add_f32 v[10:11], v[10:11], v[6:7]\n\t"
    "v_rcp_f32 v8, v8\n\t"
    "v_rcp_f32 v10, v10\n\t"
    "v_rcp_f32 v9, v9\n\t"
    "v_rcp_f32 v11, v11\n\t"
    "v_pk_mul_f32 v[224:225], v[224:225], v[8:9]\n\t"
    "v_pk_mul_f32 v[226:227], v[226:227], v[10:11]\n\t"
    "v_cvt_pk_bf16_f32 v244, v224, v225\n\t"
    "v_cvt_pk_bf16_f32 v245, v226, v227\n\t"
    "buffer_store_dwordx2 v[244:245], v242, s[60:63], s88 offen offset:0 sc1\n\t"
    "v_pk_mul_f32 v[8:9], v[116:117], v[116:117]\n\t"
    "v_pk_mul_f32 v[10:11], v[118:119], v[118:119]\n\t"
    "v_pk_fma_f32 v[8:9], v[8:9], v[2:3], v[4:5]\n\t"
    "v_pk_fma_f32 v[10:11], v[10:11], v[2:3], v[4:5]\n\t"
    "v_pk_mul_f32 v[8:9], v[116:117], v[8:9]\n\t"
    "v_pk_mul_f32 v[10:11], v[118:119], v[10:11]\n\t"
    "v_exp_f32 v8, v8\n\t"
    "v_exp_f32 v10, v10\n\t"
    "v_exp_f32 v9, v9\n\t"
    "v_exp_f32 v11, v11\n\t"
    "v_pk_mul_f32 v[228:229], v[116:117], v[228:229]\n\t"
    "v_pk_mul_f32 v[230:231], v[118:119], v[230:231]\n\t"
    "v_pk_add_f32 v[8:9], v[8:9], v[6:7]\n\t"
    "v_pk_add_f32 v[10:11], v[10:11], v[6:7]\n\t"
    "v_rcp_f32 v8, v8\n\t"
    "v_rcp_f32 v10, v10\n\t"
    "v_rcp_f32 v9, v9\n\t"
    "v_rcp_f32 v11, v11\n\t"
    "v_pk_mul_f32 v[228:229], v[228:229], v[8:9]\n\t"
    "v_pk_mul_f32 v[230:231], v[230:231], v[10:11]\n\t"
    "v_cvt_pk_bf16_f32 v248, v228, v229\n\t"
    "v_cvt_pk_bf16_f32 v249, v230, v231\n\t"
    "buffer_store_dwordx2 v[248:249], v242, s[60:63], s88 offen offset:256 sc1\n\t"
    "v_pk_mul_f32 v[8:9], v[120:121], v[120:121]\n\t"
    "v_pk_mul_f32 v[10:11], v[122:123], v[122:123]\n\t"
    "v_pk_fma_f32 v[8:9], v[8:9], v[2:3], v[4:5]\n\t"
    "v_pk_fma_f32 v[10:11], v[10:11], v[2:3], v[4:5]\n\t"
    "v_pk_mul_f32 v[8:9], v[120:121], v[8:9]\n\t"
    "v_pk_mul_f32 v[10:11], v[122:123], v[10:11]\n\t"
    "v_exp_f32 v8, v8\n\t"
    "v_exp_f32 v10, v10\n\t"
    "v_exp_f32 v9, v9\n\t"
    "v_exp_f32 v11, v11\n\t"
    "v_pk_mul_f32 v[232:233], v[120:121], v[232:233]\n\t"
    "v_pk_mul_f32 v[234:235], v[122:123], v[234:235]\n\t"
    "v_pk_add_f32 v[8:9], v[8:9], v[6:7]\n\t"
    "v_pk_add_f32 v[10:11], v[10:11], v[6:7]\n\t"
    "v_rcp_f32 v8, v8\n\t"
    "v_rcp_f32 v10, v10\n\t"
    "v_rcp_f32 v9, v9\n\t"
    "v_rcp_f32 v11, v11\n\t"
    "v_pk_mul_f32 v[232:233], v[232:233], v[8:9]\n\t"
    "v_pk_mul_f32 v[234:235], v[234:235], v[10:11]\n\t"
    "v_cvt_pk_bf16_f32 v244, v232, v233\n\t"
    "v_cvt_pk_bf16_f32 v245, v234, v235\n\t"
    "buffer_store_dwordx2 v[244:245], v242, s[60:63], s89 offen offset:0 sc1\n\t"
    "v_pk_mul_f32 v[8:9], v[124:125], v[124:125]\n\t"
    "v_pk_mul_f32 v[10:11], v[126:127], v[126:127]\n\t"
    "v_pk_fma_f32 v[8:9], v[8:9], v[2:3], v[4:5]\n\t"
    "v_pk_fma_f32 v[10:11], v[10:11], v[2:3], v[4:5]\n\t"
    "v_pk_mul_f32 v[8:9], v[124:125], v[8:9]\n\t"
    "v_pk_mul_f32 v[10:11], v[126:127], v[10:11]\n\t"
    "v_exp_f32 v8, v8\n\t"
    "v_exp_f32 v10, v10\n\t"
    "v_exp_f32 v9, v9\n\t"
    "v_exp_f32 v11, v11\n\t"
    "v_pk_mul_f32 v[236:237], v[124:125], v[236:237]\n\t"
    "v_pk_mul_f32 v[238:239], v[126:127], v[238:239]\n\t"
    "v_pk_add_f32 v[8:9], v[8:9], v[6:7]\n\t"
    "v_pk_add_f32 v[10:11], v[10:11], v[6:7]\n\t"
    "v_rcp_f32 v8, v8\n\t"
    "v_rcp_f32 v10, v10\n\t"
    "v_rcp_f32 v9, v9\n\t"
    "v_rcp_f32 v11, v11\n\t"
    "v_pk_mul_f32 v[236:237], v[236:237], v[8:9]\n\t"
    "v_pk_mul_f32 v[238:239], v[238:239], v[10:11]\n\t"
    "v_cvt_pk_bf16_f32 v248, v236, v237\n\t"
    "v_cvt_pk_bf16_f32 v249, v238, v239\n\t"
    "buffer_store_dwordx2 v[248:249], v242, s[60:63], s89 offen offset:256 sc1\n\t"
    "s_waitcnt vmcnt(4)\n\t"
    "s_barrier\n\t"
    "s_add_u32 s80, s91, 2048\n\t"
    "s_lshl_b32 s80, s80, 4\n\t"
    "v_mov_b32 v12, s80\n\t"
    "v_mov_b32 v13, %4\n\t"
    "s_cmp_eq_u32 s34, 0\n\t"
    "s_cselect_b32 s80, 1, 0\n\t"
    "s_mov_b32 exec_hi, 0\n\t"
    "s_mov_b32 exec_lo, s80\n\t"
    "global_store_dword v12, v13, s[44:45] sc1\n\t"
    "s_mov_b64 exec, -1\n\t"
    :  : "s"(karg), "s"(wid), "s"(bid), "s"(ldsb), "s"(tag)
    : "s33", "v0", "v1", "v2", "v3", "v4", "v5", "v6", "v7", "v8", "v9", "v10", "v11", "v12", "v13", "v14", "v15", "v16", "v17", "v18", "v19", "v20", "v21", "v22", "v23", "v24", "v25", "v26", "v27", "v28", "v29", "v30", "v31", "v32", "v33", "v34", "v35", "v36", "v37", "v38", "v39", "v40", "v41", "v42", "v43", "v44", "v45", "v46", "v47", "v48", "v49", "v50", "v51", "v52", "v53", "v54", "v55", "v56", "v57", "v58", "v59", "v60", "v61", "v62", "v63", "v64", "v65", "v66", "v67", "v68", "v69", "v70", "v71", "v72", "v73", "v74", "v75", "v76", "v77", "v78", "v79", "v80", "v81", "v82", "v83", "v84", "v85", "v86", "v87", "v88", "v89", "v90", "v91", "v92", "v93", "v94", "v95", "v96", "v97", "v98", "v99", "v100", "v101", "v102", "v103", "v104", "v105", "v106", "v107", "v108", "v109", "v110", "v111", "v112", "v113", "v114", "v115", "v116", "v117", "v118", "v119", "v120", "v121", "v122", "v123", "v124", "v125", "v126", "v127", "v128", "v129", "v130", "v131", "v132", "v133", "v134", "v135", "v136", "v137", "v138", "v139", "v140", "v141", "v142", "v143", "v144", "v145", "v146", "v147", "v148", "v149", "v150", "v151", "v152", "v153", "v154", "v155", "v156", "v157", "v158", "v159", "v160", "v161", "v162", "v163", "v164", "v165", "v166", "v167", "v168", "v169", "v170", "v171", "v172", "v173", "v174", "v175", "v176", "v177", "v178", "v179", "v180", "v181", "v182", "v183", "v184", "v185", "v186", "v187", "v188", "v189", "v190", "v191", "v192", "v193", "v194", "v195", "v196", "v197", "v198", "v199", "v200", "v201", "v202", "v203", "v204", "v205", "v206", "v207", "v208", "v209", "v210", "v211", "v212", "v213", "v214", "v215", "v216", "v217", "v218", "v219", "v220", "v221", "v222", "v223", "v224", "v225", "v226", "v227", "v228", "v229", "v230", "v231", "v232", "v233", "v234", "v235", "v236", "v237", "v238", "v239", "v240", "v241", "v242", "v243", "v244", "v245", "v246", "v247", "v248", "v249", "v250", "v251", "v252", "v253", "v254", "v255", "a0", "a1", "a2", "a3", "a4", "a5", "a6", "a7", "a8", "a9", "a10", "a11", "a12", "a13", "a14", "a15", "a16", "a17", "a18", "a19", "a20", "a21", "a22", "a23", "a24", "a25", "a26", "a27", "a28", "a29", "a30", "a31", "a32", "a33", "a34", "a35", "a36", "a37", "a38", "a39", "a40", "a41", "a42", "a43", "a44", "a45", "a46", "a47", "a48", "a49", "a50", "a51", "a52", "a53", "a54", "a55", "a56", "a57", "a58", "a59", "a60", "a61", "a62", "a63", "a64", "a65", "a66", "a67", "a68", "a69", "a70", "a71", "a72", "a73", "a74", "a75", "a76", "a77", "a78", "a79", "a80", "a81", "a82", "a83", "a84", "a85", "a86", "a87", "a88", "a89", "a90", "a91", "a92", "a93", "a94", "a95", "a96", "a97", "a98", "a99", "a100", "a101", "a102", "a103", "a104", "a105", "a106", "a107", "a108", "a109", "a110", "a111", "a112", "a113", "a114", "a115", "a116", "a117", "a118", "a119", "a120", "a121", "a122", "a123", "a124", "a125", "a126", "a127", "a128", "a129", "a130", "a131", "a132", "a133", "a134", "a135", "a136", "a137", "a138", "a139", "a140", "a141", "a142", "a143", "a144", "a145", "a146", "a147", "a148", "a149", "a150", "a151", "a152", "a153", "a154", "a155", "a156", "a157", "a158", "a159", "a160", "a161", "a162", "a163", "a164", "a165", "a166", "a167", "a168", "a169", "a170", "a171", "a172", "a173", "a174", "a175", "a176", "a177", "a178", "a179", "a180", "a181", "a182", "a183", "a184", "a185", "a186", "a187", "a188", "a189", "a190", "a191", "s34", "s35", "s36", "s37", "s38", "s39", "s40", "s41", "s42", "s43", "s44", "s45", "s46", "s47", "s48", "s49", "s50", "s51", "s52", "s53", "s54", "s55", "s56", "s57", "s58", "s59", "s60", "s61", "s62", "s63", "s64", "s65", "s66", "s67", "s68", "s69", "s70", "s71", "s72", "s73", "s74", "s75", "s76", "s77", "s78", "s79", "s80", "s81", "s82", "s83", "s84", "s85", "s86", "s87", "s88", "s89", "s90", "s91", "s92", "s93", "s94", "s95", "s96", "s97", "s98", "s99", "vcc", "scc", "memory");

  asm volatile(
    "s_load_dwordx2 s[36:37], %0, 112\n\t"
    "s_load_dwordx2 s[38:39], %0, 64\n\t"
    "s_load_dwordx2 s[40:41], %0, 136\n\t"
    "s_load_dwordx2 s[42:43], %0, 120\n\t"
    "s_load_dwordx2 s[44:45], %0, 88\n\t"
    "s_load_dwordx2 s[46:47], %0, 128\n\t"
    "s_load_dwordx2 s[48:49], %0, 72\n\t"
    "s_mov_b32 s34, %1\n\t"
    "s_mov_b32 s35, %2\n\t"
    "s_mov_b32 s33, %3\n\t"
    "s_waitcnt lgkmcnt(0)\n\t"
    "v_mbcnt_lo_u32_b32 v0, -1, 0\n\t"
    "v_mbcnt_hi_u32_b32 v0, -1, v0\n\t"
    "s_and_b32 s80, s35, 7\n\t"
    "s_lshr_b32 s81, s35, 3\n\t"
    "s_lshr_b32 s77, s80, 2\n\t"
    "s_and_b32 s98, s80, 3\n\t"
    "s_lshl_b32 s98, s98, 2\n\t"
    "s_and_b32 s99, s81, 3\n\t"
    "s_add_u32 s75, s98, s99\n\t"
    "s_lshr_b32 s76, s81, 2\n\t"
    "s_mov_b32 s52, s36\n\t"
    "s_and_b32 s53, s37, 0xffff\n\t"
    "s_mov_b32 s54, 14680064\n\t"
    "s_mov_b32 s55, 0x20000\n\t"
    "s_sub_u32 s56, s38, 4096\n\t"
    "s_subb_u32 s57, s39, 0\n\t"
    "s_and_b32 s57, s57, 0xffff\n\t"
    "s_mov_b32 s58, 7344128\n\t"
    "s_mov_b32 s59, 0x20000\n\t"
    "s_lshl_b32 s80, s77, 4\n\t"
    "s_lshr_b32 s81, s34, 1\n\t"
    "s_add_u32 s80, s80, s81\n\t"
    "s_mul_i32 s80, s80, 112\n\t"
    "s_and_b32 s81, s34, 1\n\t"
    "s_lshl_b32 s81, s81, 4\n\t"
    "s_add_u32 s80, s80, s81\n\t"
    "s_lshl_b32 s73, s80, 11\n\t"
    "s_mov_b32 s87, 4291821568\n\t"
    "s_lshr_b32 s80, s34, 1\n\t"
    "s_sub_u32 s80, s34, s80\n\t"
    "s_sub_u32 s80, s80, 14\n\t"
    "s_mul_i32 s80, s80, 112\n\t"
    "s_add_u32 s80, s80, 32\n\t"
    "s_sub_u32 s80, s80, s81\n\t"
    "s_lshl_b32 s90, s80, 11\n\t"
    "s_lshl_b32 s80, s76, 8\n\t"
    "s_add_u32 s73, s73, s80\n\t"
    "s_lshl_b32 s80, s34, 12\n\t"
    "s_add_u32 s74, s33, s80\n\t"
    "s_lshl_b32 s80, s75, 2\n\t"
    "s_add_u32 s78, s80, s34\n\t"
    "s_mul_i32 s72, s78, 229376\n\t"
    "s_mul_i32 s80, s77, 114688\n\t"
    "s_add_u32 s72, s72, s80\n\t"
    "v_mov_b32 v3, 4096\n\t"
    "v_bfe_u32 v253, v0, 0, 2\n\t"
    "v_lshl_add_u32 v3, v253, 4, v3\n\t"
    "v_bfe_u32 v253, v0, 2, 2\n\t"
    "v_lshl_add_u32 v3, v253, 11, v3\n\t"
    "v_bfe_u32 v253, v0, 4, 1\n\t"
    "s_mov_b32 s98, 16416\n\t"
    "v_mad_i32_i24 v3, v253, s98, v3\n\t"
    "v_bfe_u32 v253, v0, 5, 1\n\t"
    "v_lshl_add_u32 v3, v253, 6, v3\n\t"
    "v_bfe_u32 v253, v0, 1, 1\n\t"
    "v_bfe_u32 v254, v0, 4, 1\n\t"
    "v_and_b32 v253, v253, v254\n\t"
    "s_mov_b32 s98, 4294967232\n\t"
    "v_mad_i32_i24 v3, v253, s98, v3\n\t"
    "v_add_u32 v4, 4294966400, v3\n\t"
    "v_add_u32 v5, 6144, v3\n\t"
    "v_add_u32 v6, 5248, v3\n\t"
    "s_lshl_b32 s80, s77, 7\n\t"
    "v_add_u32 v3, s80, v3\n\t"
    "v_subrev_u32 v4, s80, v4\n\t"
    "v_add_u32 v5, s80, v5\n\t"
    "v_subrev_u32 v6, s80, v6\n\t"
    "v_mov_b32 v1, 0\n\t"
    "v_bfe_u32 v253, v0, 0, 4\n\t"
    "v_lshl_add_u32 v1, v253, 4, v1\n\t"
    "v_bfe_u32 v253, v0, 4, 2\n\t"
    "v_lshl_add_u32 v1, v253, 9, v1\n\t"
    "s_barrier\n\t"
    "s_add_u32 m0, s74, 0\n\t"
    "s_nop 0\n\t"
    "buffer_load_dwordx4 v3, s[56:59], s73 offen offset:0 lds\n\t"
    "buffer_load_dwordx4 v4, s[56:59], s73 offen offset:1024 lds\n\t"
    "buffer_load_dwordx4 v5, s[56:59], s73 offen offset:2048 lds\n\t"
    "buffer_load_dwordx4 v6, s[56:59], s73 offen offset:3072 lds\n\t"
    "s_add_u32 s73, s73, 458752\n\t"
    "s_add_u32 m0, s74, 16384\n\t"
    "s_nop 0\n\t"
    "buffer_load_dwordx4 v3, s[56:59], s73 offen offset:0 lds\n\t"
    "buffer_load_dwordx4 v4, s[56:59], s73 offen offset:1024 lds\n\t"
    "buffer_load_dwordx4 v5, s[56:59], s73 offen offset:2048 lds\n\t"
    "buffer_load_dwordx4 v6, s[56:59], s73 offen offset:3072 lds\n\t"
    "s_add_u32 s73, s73, 458752\n\t"
    "s_add_u32 m0, s74, 32768\n\t"
    "s_nop 0\n\t"
    "buffer_load_dwordx4 v3, s[56:59], s73 offen offset:0 lds\n\t"
    "buffer_load_dwordx4 v4, s[56:59], s73 offen offset:1024 lds\n\t"
    "buffer_load_dwordx4 v5, s[56:59], s73 offen offset:2048 lds\n\t"
    "buffer_load_dwordx4 v6, s[56:59], s73 offen offset:3072 lds\n\t"
    "s_add_u32 s73, s73, 458752\n\t"
    "v_mov_b32 v10, 0\n\t"
    "v_bfe_u32 v253, v0, 0, 2\n\t"
    "v_lshl_add_u32 v10, v253, 3, v10\n\t"
    "v_bfe_u32 v253, v0, 2, 2\n\t"
    "v_lshl_add_u32 v10, v253, 6, v10\n\t"
    "v_bfe_u32 v253, v0, 4, 1\n\t"
    "s_mov_b32 s98, 288\n\t"
    "v_mad_i32_i24 v10, v253, s98, v10\n\t"
    "v_bfe_u32 v253, v0, 5, 1\n\t"
    "v_lshl_add_u32 v10, v253, 12, v10\n\t"
    "v_add_u32 v10, s33, v10\n\t"
    "v_mov_b32 v11, 32\n\t"
    "v_bfe_u32 v253, v0, 0, 2\n\t"
    "v_lshl_add_u32 v11, v253, 3, v11\n\t"
    "v_bfe_u32 v253, v0, 2, 2\n\t"
    "v_lshl_add_u32 v11, v253, 6, v11\n\t"
    "v_bfe_u32 v253, v0, 4, 1\n\t"
    "s_mov_b32 s98, 224\n\t"
    "v_mad_i32_i24 v11, v253, s98, v11\n\t"
    "v_bfe_u32 v253, v0, 5, 1\n\t"
    "v_lshl_add_u32 v11, v253, 12, v11\n\t"
    "v_add_u32 v11, s33, v11\n\t"
    "s_mov_b32 s60, s40\n\t"
    "s_and_b32 s61, s41, 0xffff\n\t"
    "s_mov_b32 s62, 4194304\n\t"
    "s_mov_b32 s63, 0x20000\n\t"
    "s_mov_b32 s64, s42\n\t"
    "s_and_b32 s65, s43, 0xffff\n\t"
    "s_mov_b32 s66, 4194304\n\t"
    "s_mov_b32 s67, 0x20000\n\t"
    "s_mov_b32 s68, s46\n\t"
    "s_and_b32 s69, s47, 0xffff\n\t"
    "s_mov_b32 s70, 8388608\n\t"
    "s_mov_b32 s71, 0x20000\n\t"
    "s_lshl_b32 s80, s75, 3\n\t"
    "s_add_u32 s80, s80, s76\n\t"
    "s_lshl_b32 s81, s80, 1\n\t"
    "s_add_u32 s81, s81, s77\n\t"
    "s_lshl_b32 s81, s81, 14\n\t"
    "s_lshl_b32 s80, s34, 12\n\t"
    "s_add_u32 s88, s81, s80\n\t"
    "s_xor_b32 s89, s88, 16384\n\t"
    "v_lshlrev_b32 v240, 4, v0\n\t"
    "v_mov_b32 v241, 0\n\t"
    "v_bfe_u32 v253, v0, 0, 4\n\t"
    "v_lshl_add_u32 v241, v253, 11, v241\n\t"
    "v_bfe_u32 v253, v0, 4, 1\n\t"
    "v_lshl_add_u32 v241, v253, 5, v241\n\t"
    "v_bfe_u32 v253, v0, 5, 1\n\t"
    "v_lshl_add_u32 v241, v253, 4, v241\n\t"
    "s_lshl_b32 s80, s78, 16\n\t"
    "s_lshl_b32 s81, s76, 8\n\t"
    "s_add_u32 s80, s80, s81\n\t"
    "s_lshl_b32 s81, s77, 7\n\t"
    "s_add_u32 s91, s80, s81\n\t"
    "v_mov_b32 v242, 0\n\t"
    "v_bfe_u32 v253, v0, 4, 1\n\t"
    "v_lshl_add_u32 v242, v253, 6, v242\n\t"
    "v_bfe_u32 v253, v0, 5, 1\n\t"
    "v_lshl_add_u32 v242, v253, 5, v242\n\t"
    "s_lshl_b32 s80, s76, 9\n\t"
    "s_lshl_b32 s81, s77, 8\n\t"
    "s_add_u32 s80, s80, s81\n\t"
    "v_add_u32 v242, s80, v242\n\t"
    "v_add_u32 v242, 45056, v242\n\t"
    "v_add_u32 v239, 16384, v242\n\t"
    "global_load_dwordx4 v[160:163], v242, s[44:45] offset:0 sc1\n\t"
    "global_load_dwordx4 v[164:167], v242, s[44:45] offset:16 sc1\n\t"
    "global_load_dwordx4 v[80:83], v239, s[44:45] offset:0 sc1\n\t"
    "global_load_dwordx4 v[84:87], v239, s[44:45] offset:16 sc1\n\t"
    "global_load_dwordx4 v[168:171], v242, s[44:45] offset:128 sc1\n\t"
    "global_load_dwordx4 v[172:175], v242, s[44:45] offset:144 sc1\n\t"
    "global_load_dwordx4 v[88:91], v239, s[44:45] offset:128 sc1\n\t"
    "global_load_dwordx4 v[92:95], v239, s[44:45] offset:144 sc1\n\t"
    "s_and_b32 s80, s35, 7\n\t"
    "s_lshr_b32 s81, s35, 3\n\t"
    "s_lshl_b32 s80, s80, 2\n\t"
    "s_lshr_b32 s98, s81, 3\n\t"
    "s_add_u32 s80, s80, s98\n\t"
    "s_and_b32 s81, s81, 7\n\t"
    "s_lshl_b32 s81, s81, 5\n\t"
    "s_add_u32 s98, s80, s81\n\t"
    "s_waitcnt vmcnt(20)\n\t"
    "s_barrier\n\t"
    "s_cmp_lg_u32 s34, 0\n\t"
    "s_cbranch_scc1 LPB_m_down\n\t"
    "s_lshl_b32 s86, s98, 2\n\t"
    "s_add_u32 s86, s86, 19456\n\t"
    "s_lshl_b32 s86, s86, 4\n\t"
    "v_mov_b32 v244, s86\n\t"
    "v_mov_b32 v245, %4\n\t"
    "s_mov_b64 exec, 1\n\t"
    "global_store_dword v244, v245, s[48:49] sc1\n\t"
    "s_mov_b64 exec, -1\n\t"
    "LPB_m_down:\n\t"
    "s_lshr_b32 s98, s75, 1\n\t"
    "s_lshl_b32 s98, s98, 5\n\t"
    "s_lshl_b32 s99, s77, 4\n\t"
    "s_add_u32 s98, s98, s99\n\t"
    "s_lshl_b32 s99, s75, 4\n\t"
    "v_mbcnt_lo_u32_b32 v250, -1, 0\n\t"
    "v_mbcnt_hi_u32_b32 v250, -1, v250\n\t"
    "v_and_b32 v250, 15, v250\n\t"
    "v_lshlrev_b32 v250, 2, v250\n\t"
    "s_lshl_b32 s85, s98, 2\n\t"
    "s_add_u32 s85, s85, 16384\n\t"
    "v_add_u32 v250, s85, v250\n\t"
    "v_lshlrev_b32 v250, 4, v250\n\t"
    "s_xor_b32 s85, s35, 4\n\t"
    "s_cmp_lg_u32 s34, 0\n\t"
    "s_cbranch_scc1 LPW_m_down\n\t"
    "v_mbcnt_lo_u32_b32 v245, -1, 0\n\t"
    "v_mbcnt_hi_u32_b32 v245, -1, v245\n\t"
    "s_lshl_b32 s86, s98, 2\n\t"
    "s_add_u32 s86, s86, 16384\n\t"
    "v_mov_b32 v244, v245\n\t"
    "v_min_u32 v244, 15, v244\n\t"
    "v_lshlrev_b32 v244, 2, v244\n\t"
    "v_add_u32 v246, s86, v244\n\t"
    "s_lshl_b32 s86, s99, 2\n\t"
    "s_add_u32 s86, s86, 9216\n\t"
    "v_cmp_le_u32 vcc, 16, v245\n\t"
    "s_nop 0\n\t"
    "s_and_b64 exec, exec, vcc\n\t"
    "v_subrev_u32 v244, 16, v245\n\t"
    "v_min_u32 v244, 15, v244\n\t"
    "v_lshlrev_b32 v244, 2, v244\n\t"
    "v_add_u32 v246, s86, v244\n\t"
    "s_lshl_b32 s86, s85, 2\n\t"
    "s_add_u32 s86, s86, 9216\n\t"
    "v_cmp_le_u32 vcc, 32, v245\n\t"
    "s_nop 0\n\t"
    "s_and_b64 exec, exec, vcc\n\t"
    "v_subrev_u32 v244, 32, v245\n\t"
    "v_min_u32 v244, 0, v244\n\t"
    "v_lshlrev_b32 v244, 2, v244\n\t"
    "v_add_u32 v246, s86, v244\n\t"
    "s_mov_b64 exec, -1\n\t"
    "v_lshlrev_b32 v246, 4, v246\n\t"
    "s_mov_b32 s50, 0x400000\n\t"
    "LPP_m_down:\n\t"
    "global_load_dword v245, v246, s[48:49] sc1\n\t"
    "s_waitcnt vmcnt(0)\n\t"
    "v_cmp_ne_u32 vcc, %4, v245\n\t"
    "s_sub_u32 s50, s50, 1\n\t"
    "s_cmp_eq_u32 s50, 0\n\t"
    "s_cbranch_scc1 LPD_m_down\n\t"
    "s_cmp_eq_u64 vcc, 0\n\t"
    "s_cbranch_scc1 LPD_m_down\n\t"
    "s_sleep 1\n\t"
    "s_branch LPP_m_down\n\t"
    "LPD_m_down:\n\t"
    "LPW_m_down:\n\t"
    "s_barrier\n\t"
    "s_add_u32 s80, s91, 0\n\t"
    "buffer_load_dwordx4 v[144:147], v241, s[64:67], s80 offen offset:0 sc1\n\t"
    "buffer_load_dwordx4 v[148:151], v241, s[64:67], s80 offen offset:64 sc1\n\t"
    "s_add_u32 s80, s91, 32768\n\t"
    "buffer_load_dwordx4 v[152:155], v241, s[64:67], s80 offen offset:0 sc1\n\t"
    "buffer_load_dwordx4 v[156:159], v241, s[64:67], s80 offen offset:64 sc1\n\t"
    "buffer_load_dwordx4 a[64:67], v1, s[52:55], s72 offen offset:0 sc1\n\t"
    "buffer_load_dwordx4 a[68:71], v1, s[52:55], s72 offen offset:256 sc1\n\t"
    "buffer_load_dwordx4 a[72:75], v1, s[52:55], s72 offen offset:2048 sc1\n\t"
    "buffer_load_dwordx4 a[76:79], v1, s[52:55], s72 offen offset:2304 sc1\n\t"
    "s_add_u32 s72, s72, 4096\n\t"
    "s_waitcnt vmcnt(24)\n\t"
    "s_barrier\n\t"
    "ds_read_b64_tr_b16 a[0:1], v10 offset:0\n\t"
    "ds_read_b64_tr_b16 a[2:3], v10 offset:2048\n\t"
    "ds_read_b64_tr_b16 a[4:5], v11 offset:0\n\t"
    "ds_read_b64_tr_b16 a[6:7], v11 offset:2048\n\t"
    "ds_read_b64_tr_b16 a[8:9], v10 offset:512\n\t"
    "ds_read_b64_tr_b16 a[10:11], v10 offset:2560\n\t"
    "ds_read_b64_tr_b16 a[12:13], v11 offset:512\n\t"
    "ds_read_b64_tr_b16 a[14:15], v11 offset:2560\n\t"
    "ds_read_b64_tr_b16 a[16:17], v10 offset:1024\n\t"
    "ds_read_b64_tr_b16 a[18:19], v10 offset:3072\n\t"
    "ds_read_b64_tr_b16 a[20:21], v11 offset:1024\n\t"
    "ds_read_b64_tr_b16 a[22:23], v11 offset:3072\n\t"
    "ds_read_b64_tr_b16 a[24:25], v10 offset:1536\n\t"
    "ds_read_b64_tr_b16 a[26:27], v10 offset:3584\n\t"
    "ds_read_b64_tr_b16 a[28:29], v11 offset:1536\n\t"
    "ds_read_b64_tr_b16 a[30:31], v11 offset:3584\n\t"
    "s_mov_b32 s51, 4\n\t"
    "s_lshr_b32 s98, s51, 3\n\t"
    "s_lshl_b32 s98, s98, 14\n\t"
    "v_add_u32 v248, s98, v250\n\t"
    "global_load_dword v249, v248, s[48:49] sc1\n\t"
    "s_add_u32 s51, s51, 4\n\t"
    "s_waitcnt vmcnt(4) lgkmcnt(14)\n\t"
    "v_mfma_f32_16x16x32_bf16 v[16:19], a[0:3], a[64:67], 0\n\t"
    "buffer_load_dwordx4 a[80:83], v1, s[52:55], s72 offen offset:0 sc1\n\t"
    "s_waitcnt vmcnt(4)\n\t"
    "v_mfma_f32_16x16x32_bf16 v[20:23], a[0:3], a[68:71], 0\n\t"
    "buffer_load_dwordx4 a[84:87], v1, s[52:55], s72 offen offset:256 sc1\n\t"
    "s_waitcnt lgkmcnt(12)\n\t"
    "v_mfma_f32_16x16x32_bf16 v[24:27], a[4:7], a[64:67], 0\n\t"
    "v_mfma_f32_16x16x32_bf16 v[28:31], a[4:7], a[68:71], 0\n\t"
    "s_waitcnt lgkmcnt(8)\n\t"
    "buffer_load_dwordx4 a[88:91], v1, s[52:55], s72 offen offset:2048 sc1\n\t"
    "v_mfma_f32_16x16x32_bf16 v[32:35], a[8:11], a[64:67], 0\n\t"
    "ds_read_b64_tr_b16 a[0:1], v10 offset:8192\n\t"
    "ds_read_b64_tr_b16 a[2:3], v10 offset:10240\n\t"
    "buffer_load_dwordx4 a[92:95], v1, s[52:55], s72 offen offset:2304 sc1\n\t"
    "v_mfma_f32_16x16x32_bf16 v[36:39], a[8:11], a[68:71], 0\n\t"
    "ds_read_b64_tr_b16 a[4:5], v11 offset:8192\n\t"
    "ds_read_b64_tr_b16 a[6:7], v11 offset:10240\n\t"
    "v_mfma_f32_16x16x32_bf16 v[40:43], a[12:15], a[64:67], 0\n\t"
    "s_add_u32 s72, s72, 4096\n\t"
    "v_mfma_f32_16x16x32_bf16 v[44:47], a[12:15], a[68:71], 0\n\t"
    "s_waitcnt lgkmcnt(8)\n\t"
    "buffer_load_dwordx4 a[96:99], v1, s[52:55], s72 offen offset:0 sc1\n\t"
    "v_mfma_f32_16x16x32_bf16 v[48:51], a[16:19], a[64:67], 0\n\t"
    "ds_read_b64_tr_b16 a[8:9], v10 offset:8704\n\t"
    "ds_read_b64_tr_b16 a[10:11], v10 offset:10752\n\t"
    "v_mfma_f32_16x16x32_bf16 v[52:55], a[16:19], a[68:71], 0\n\t"
    "ds_read_b64_tr_b16 a[12:13], v11 offset:8704\n\t"
    "ds_read_b64_tr_b16 a[14:15], v11 offset:10752\n\t"
    "buffer_load_dwordx4 a[100:103], v1, s[52:55], s72 offen offset:256 sc1\n\t"
    "v_mfma_f32_16x16x32_bf16 v[56:59], a[20:23], a[64:67], 0\n\t"
    "buffer_load_dwordx4 a[104:107], v1, s[52:55], s72 offen offset:2048 sc1\n\t"
    "v_mfma_f32_16x16x32_bf16 v[60:63], a[20:23], a[68:71], 0\n\t"
    "s_waitcnt lgkmcnt(8)\n\t"
    "v_mfma_f32_16x16x32_bf16 v[64:67], a[24:27], a[64:67], 0\n\t"
    "ds_read_b64_tr_b16 a[16:17], v10 offset:9216\n\t"
    "ds_read_b64_tr_b16 a[18:19], v10 offset:11264\n\t"
    "buffer_load_dwordx4 a[108:111], v1, s[52:55], s72 offen offset:2304 sc1\n\t"
    "v_mfma_f32_16x16x32_bf16 v[68:71], a[24:27], a[68:71], 0\n\t"
    "ds_read_b64_tr_b16 a[20:21], v11 offset:9216\n\t"
    "ds_read_b64_tr_b16 a[22:23], v11 offset:11264\n\t"
    "s_add_u32 s72, s72, 4096\n\t"
    "v_mfma_f32_16x16x32_bf16 v[72:75], a[28:31], a[64:67], 0\n\t"
    "v_mfma_f32_16x16x32_bf16 v[76:79], a[28:31], a[68:71], 0\n\t"
    "s_waitcnt vmcnt(9) lgkmcnt(8)\n\t"
    "s_add_u32 m0, s74, 49152\n\t"
    "v_mfma_f32_16x16x32_bf16 v[16:19], a[0:3], a[72:75], v[16:19]\n\t"
    "s_barrier\n\t"
    "ds_read_b64_tr_b16 a[24:25], v10 offset:9728\n\t"
    "ds_read_b64_tr_b16 a[26:27], v10 offset:11776\n\t"
    "v_mfma_f32_16x16x32_bf16 v[20:23], a[0:3], a[76:79], v[20:23]\n\t"
    "ds_read_b64_tr_b16 a[28:29], v11 offset:9728\n\t"
    "ds_read_b64_tr_b16 a[30:31], v11 offset:11776\n\t"
    "v_mfma_f32_16x16x32_bf16 v[24:27], a[4:7], a[72:75], v[24:27]\n\t"
    "buffer_load_dwordx4 v3, s[56:59], s73 offen offset:0 lds\n\t"
    "v_mfma_f32_16x16x32_bf16 v[28:31], a[4:7], a[76:79], v[28:31]\n\t"
    "s_waitcnt lgkmcnt(8)\n\t"
    "v_mfma_f32_16x16x32_bf16 v[32:35], a[8:11], a[72:75], v[32:35]\n\t"
    "ds_read_b64_tr_b16 a[0:1], v10 offset:16384\n\t"
    "ds_read_b64_tr_b16 a[2:3], v10 offset:18432\n\t"
    "v_mfma_f32_16x16x32_bf16 v[36:39], a[8:11], a[76:79], v[36:39]\n\t"
    "ds_read_b64_tr_b16 a[4:5], v11 offset:16384\n\t"
    "ds_read_b64_tr_b16 a[6:7], v11 offset:18432\n\t"
    "v_mfma_f32_16x16x32_bf16 v[40:43], a[12:15], a[72:75], v[40:43]\n\t"
    "buffer_load_dwordx4 v4, s[56:59], s73 offen offset:1024 lds\n\t"
    "v_mfma_f32_16x16x32_bf16 v[44:47], a[12:15], a[76:79], v[44:47]\n\t"
    "s_waitcnt lgkmcnt(8)\n\t"
    "v_mfma_f32_16x16x32_bf16 v[48:51], a[16:19], a[72:75], v[48:51]\n\t"
    "ds_read_b64_tr_b16 a[8:9], v10 offset:16896\n\t"
    "ds_read_b64_tr_b16 a[10:11], v10 offset:18944\n\t"
    "v_mfma_f32_16x16x32_bf16 v[52:55], a[16:19], a[76:79], v[52:55]\n\t"
    "ds_read_b64_tr_b16 a[12:13], v11 offset:16896\n\t"
    "ds_read_b64_tr_b16 a[14:15], v11 offset:18944\n\t"
    "v_mfma_f32_16x16x32_bf16 v[56:59], a[20:23], a[72:75], v[56:59]\n\t"
    "buffer_load_dwordx4 v5, s[56:59], s73 offen offset:2048 lds\n\t"
    "v_mfma_f32_16x16x32_bf16 v[60:63], a[20:23], a[76:79], v[60:63]\n\t"
    "s_waitcnt lgkmcnt(8)\n\t"
    "v_mfma_f32_16x16x32_bf16 v[64:67], a[24:27], a[72:75], v[64:67]\n\t"
    "ds_read_b64_tr_b16 a[16:17], v10 offset:17408\n\t"
    "ds_read_b64_tr_b16 a[18:19], v10 offset:19456\n\t"
    "v_mfma_f32_16x16x32_bf16 v[68:71], a[24:27], a[76:79], v[68:71]\n\t"
    "ds_read_b64_tr_b16 a[20:21], v11 offset:17408\n\t"
    "ds_read_b64_tr_b16 a[22:23], v11 offset:19456\n\t"
    "v_mfma_f32_16x16x32_bf16 v[72:75], a[28:31], a[72:75], v[72:75]\n\t"
    "buffer_load_dwordx4 v6, s[56:59], s73 offen offset:3072 lds\n\t"
    "v_mfma_f32_16x16x32_bf16 v[76:79], a[28:31], a[76:79], v[76:79]\n\t"
    "s_waitcnt vmcnt(10) lgkmcnt(8)\n\t"
    "s_add_u32 s73, s73, 458752\n\t"
    "v_cmp_ne_u32 vcc, %4, v249\n\t"
    "s_cbranch_vccz 2f\n\t"
    "s_mov_b32 s86, 0x400000\n\t"
    "1:\n\t"
    "global_load_dword v249, v248, s[48:49] sc1\n\t"
    "s_waitcnt vmcnt(0)\n\t"
    "v_cmp_ne_u32 vcc, %4, v249\n\t"
    "s_sub_u32 s86, s86, 1\n\t"
    "s_cmp_eq_u32 s86, 0\n\t"
    "s_cbranch_scc1 2f\n\t"
    "s_cbranch_vccz 2f\n\t"
    "s_sleep 1\n\t"
    "s_branch 1b\n\t"
    "2:\n\t"
    "v_mfma_f32_16x16x32_bf16 v[16:19], a[0:3], a[80:83], v[16:19]\n\t"
    "ds_read_b64_tr_b16 a[24:25], v10 offset:17920\n\t"
    "ds_read_b64_tr_b16 a[26:27], v10 offset:19968\n\t"
    "v_mfma_f32_16x16x32_bf16 v[20:23], a[0:3], a[84:87], v[20:23]\n\t"
    "ds_read_b64_tr_b16 a[28:29], v11 offset:17920\n\t"
    "ds_read_b64_tr_b16 a[30:31], v11 offset:19968\n\t"
    "v_mfma_f32_16x16x32_bf16 v[24:27], a[4:7], a[80:83], v[24:27]\n\t"
    "buffer_load_dwordx4 a[112:115], v1, s[52:55], s72 offen offset:0 sc1\n\t"
    "v_mfma_f32_16x16x32_bf16 v[28:31], a[4:7], a[84:87], v[28:31]\n\t"
    "s_waitcnt lgkmcnt(8)\n\t"
    "v_mfma_f32_16x16x32_bf16 v[32:35], a[8:11], a[80:83], v[32:35]\n\t"
    "ds_read_b64_tr_b16 a[0:1], v10 offset:24576\n\t"
    "ds_read_b64_tr_b16 a[2:3], v10 offset:26624\n\t"
    "v_mfma_f32_16x16x32_bf16 v[36:39], a[8:11], a[84:87], v[36:39]\n\t"
    "ds_read_b64_tr_b16 a[4:5], v11 offset:24576\n\t"
    "ds_read_b64_tr_b16 a[6:7], v11 offset:26624\n\t"
    "v_mfma_f32_16x16x32_bf16 v[40:43], a[12:15], a[80:83], v[40:43]\n\t"
    "buffer_load_dwordx4 a[116:119], v1, s[52:55], s72 offen offset:256 sc1\n\t"
    "v_mfma_f32_16x16x32_bf16 v[44:47], a[12:15], a[84:87], v[44:47]\n\t"
    "s_waitcnt lgkmcnt(8)\n\t"
    "v_mfma_f32_16x16x32_bf16 v[48:51], a[16:19], a[80:83], v[48:51]\n\t"
    "ds_read_b64_tr_b16 a[8:9], v10 offset:25088\n\t"
    "ds_read_b64_tr_b16 a[10:11], v10 offset:27136\n\t"
    "v_mfma_f32_16x16x32_bf16 v[52:55], a[16:19], a[84:87], v[52:55]\n\t"
    "ds_read_b64_tr_b16 a[12:13], v11 offset:25088\n\t"
    "ds_read_b64_tr_b16 a[14:15], v11 offset:27136\n\t"
    "v_mfma_f32_16x16x32_bf16 v[56:59], a[20:23], a[80:83], v[56:59]\n\t"
    "buffer_load_dwordx4 a[120:123], v1, s[52:55], s72 offen offset:2048 sc1\n\t"
    "v_mfma_f32_16x16x32_bf16 v[60:63], a[20:23], a[84:87], v[60:63]\n\t"
    "s_waitcnt lgkmcnt(8)\n\t"
    "v_mfma_f32_16x16x32_bf16 v[64:67], a[24:27], a[80:83], v[64:67]\n\t"
    "ds_read_b64_tr_b16 a[16:17], v10 offset:25600\n\t"
    "ds_read_b64_tr_b16 a[18:19], v10 offset:27648\n\t"
    "v_mfma_f32_16x16x32_bf16 v[68:71], a[24:27], a[84:87], v[68:71]\n\t"
    "ds_read_b64_tr_b16 a[20:21], v11 offset:25600\n\t"
    "ds_read_b64_tr_b16 a[22:23], v11 offset:27648\n\t"
    "v_mfma_f32_16x16x32_bf16 v[72:75], a[28:31], a[80:83], v[72:75]\n\t"
    "buffer_load_dwordx4 a[124:127], v1, s[52:55], s72 offen offset:2304 sc1\n\t"
    "v_mfma_f32_16x16x32_bf16 v[76:79], a[28:31], a[84:87], v[76:79]\n\t"
    "s_waitcnt vmcnt(12) lgkmcnt(8)\n\t"
    "s_add_u32 s72, s72, 4096\n\t"
    "v_mfma_f32_16x16x32_bf16 v[16:19], a[0:3], a[88:91], v[16:19]\n\t"
    "s_barrier\n\t"
    "ds_read_b64_tr_b16 a[24:25], v10 offset:26112\n\t"
    "ds_read_b64_tr_b16 a[26:27], v10 offset:28160\n\t"
    "v_mfma_f32_16x16x32_bf16 v[20:23], a[0:3], a[92:95], v[20:23]\n\t"
    "ds_read_b64_tr_b16 a[28:29], v11 offset:26112\n\t"
    "ds_read_b64_tr_b16 a[30:31], v11 offset:28160\n\t"
    "v_mfma_f32_16x16x32_bf16 v[24:27], a[4:7], a[88:91], v[24:27]\n\t"
    "s_add_u32 m0, s74, 0\n\t"
    "v_mfma_f32_16x16x32_bf16 v[28:31], a[4:7], a[92:95], v[28:31]\n\t"
    "s_waitcnt lgkmcnt(8)\n\t"
    "v_mfma_f32_16x16x32_bf16 v[32:35], a[8:11], a[88:91], v[32:35]\n\t"
    "ds_read_b64_tr_b16 a[0:1], v10 offset:32768\n\t"
    "ds_read_b64_tr_b16 a[2:3], v10 offset:34816\n\t"
    "v_mfma_f32_16x16x32_bf16 v[36:39], a[8:11], a[92:95], v[36:39]\n\t"
    "ds_read_b64_tr_b16 a[4:5], v11 offset:32768\n\t"
    "ds_read_b64_tr_b16 a[6:7], v11 offset:34816\n\t"
    "v_mfma_f32_16x16x32_bf16 v[40:43], a[12:15], a[88:91], v[40:43]\n\t"
    "buffer_load_dwordx4 v3, s[56:59], s73 offen offset:0 lds\n\t"
    "v_mfma_f32_16x16x32_bf16 v[44:47], a[12:15], a[92:95], v[44:47]\n\t"
    "s_waitcnt lgkmcnt(8)\n\t"
    "v_mfma_f32_16x16x32_bf16 v[48:51], a[16:19], a[88:91], v[48:51]\n\t"
    "ds_read_b64_tr_b16 a[8:9], v10 offset:33280\n\t"
    "ds_read_b64_tr_b16 a[10:11], v10 offset:35328\n\t"
    "v_mfma_f32_16x16x32_bf16 v[52:55], a[16:19], a[92:95], v[52:55]\n\t"
    "ds_read_b64_tr_b16 a[12:13], v11 offset:33280\n\t"
    "ds_read_b64_tr_b16 a[14:15], v11 offset:35328\n\t"
    "v_mfma_f32_16x16x32_bf16 v[56:59], a[20:23], a[88:91], v[56:59]\n\t"
    "buffer_load_dwordx4 v4, s[56:59], s73 offen offset:1024 lds\n\t"
    "v_mfma_f32_16x16x32_bf16 v[60:63], a[20:23], a[92:95], v[60:63]\n\t"
    "s_waitcnt lgkmcnt(8)\n\t"
    "v_mfma_f32_16x16x32_bf16 v[64:67], a[24:27], a[88:91], v[64:67]\n\t"
    "ds_read_b64_tr_b16 a[16:17], v10 offset:33792\n\t"
    "ds_read_b64_tr_b16 a[18:19], v10 offset:35840\n\t"
    "v_mfma_f32_16x16x32_bf16 v[68:71], a[24:27], a[92:95], v[68:71]\n\t"
    "ds_read_b64_tr_b16 a[20:21], v11 offset:33792\n\t"
    "ds_read_b64_tr_b16 a[22:23], v11 offset:35840\n\t"
    "v_mfma_f32_16x16x32_bf16 v[72:75], a[28:31], a[88:91], v[72:75]\n\t"
    "buffer_load_dwordx4 v5, s[56:59], s73 offen offset:2048 lds\n\t"
    "v_mfma_f32_16x16x32_bf16 v[76:79], a[28:31], a[92:95], v[76:79]\n\t"
    "s_waitcnt vmcnt(13) lgkmcnt(8)\n\t"
    "s_lshr_b32 s98, s51, 3\n\t"
    "s_lshl_b32 s98, s98, 14\n\t"
    "v_add_u32 v248, s98, v250\n\t"
    "global_load_dword v249, v248, s[48:49] sc1\n\t"
    "s_add_u32 s51, s51, 4\n\t"
    "v_mfma_f32_16x16x32_bf16 v[16:19], a[0:3], a[96:99], v[16:19]\n\t"
    "ds_read_b64_tr_b16 a[24:25], v10 offset:34304\n\t"
    "ds_read_b64_tr_b16 a[26:27], v10 offset:36352\n\t"
    "v_mfma_f32_16x16x32_bf16 v[20:23], a[0:3], a[100:103], v[20:23]\n\t"
    "ds_read_b64_tr_b16 a[28:29], v11 offset:34304\n\t"
    "ds_read_b64_tr_b16 a[30:31], v11 offset:36352\n\t"
    "v_mfma_f32_16x16x32_bf16 v[24:27], a[4:7], a[96:99], v[24:27]\n\t"
    "buffer_load_dwordx4 v6, s[56:59], s73 offen offset:3072 lds\n\t"
    "v_mfma_f32_16x16x32_bf16 v[28:31], a[4:7], a[100:103], v[28:31]\n\t"
    "s_waitcnt lgkmcnt(8)\n\t"
    "s_add_u32 s73, s73, 458752\n\t"
    "v_mfma_f32_16x16x32_bf16 v[32:35], a[8:11], a[96:99], v[32:35]\n\t"
    "ds_read_b64_tr_b16 a[0:1], v10 offset:40960\n\t"
    "ds_read_b64_tr_b16 a[2:3], v10 offset:43008\n\t"
    "v_mfma_f32_16x16x32_bf16 v[36:39], a[8:11], a[100:103], v[36:39]\n\t"
    "ds_read_b64_tr_b16 a[4:5], v11 offset:40960\n\t"
    "ds_read_b64_tr_b16 a[6:7], v11 offset:43008\n\t"
    "v_mfma_f32_16x16x32_bf16 v[40:43], a[12:15], a[96:99], v[40:43]\n\t"
    "buffer_load_dwordx4 a[64:67], v1, s[52:55], s72 offen offset:0 sc1\n\t"
    "v_mfma_f32_16x16x32_bf16 v[44:47], a[12:15], a[100:103], v[44:47]\n\t"
    "s_waitcnt lgkmcnt(8)\n\t"
    "v_mfma_f32_16x16x32_bf16 v[48:51], a[16:19], a[96:99], v[48:51]\n\t"
    "ds_read_b64_tr_b16 a[8:9], v10 offset:41472\n\t"
    "ds_read_b64_tr_b16 a[10:11], v10 offset:43520\n\t"
    "v_mfma_f32_16x16x32_bf16 v[52:55], a[16:19], a[100:103], v[52:55]\n\t"
    "ds_read_b64_tr_b16 a[12:13], v11 offset:41472\n\t"
    "ds_read_b64_tr_b16 a[14:15], v11 offset:43520\n\t"
    "v_mfma_f32_16x16x32_bf16 v[56:59], a[20:23], a[96:99], v[56:59]\n\t"
    "buffer_load_dwordx4 a[68:71], v1, s[52:55], s72 offen offset:256 sc1\n\t"
    "v_mfma_f32_16x16x32_bf16 v[60:63], a[20:23], a[100:103], v[60:63]\n\t"
    "s_waitcnt lgkmcnt(8)\n\t"
    "v_mfma_f32_16x16x32_bf16 v[64:67], a[24:27], a[96:99], v[64:67]\n\t"
    "ds_read_b64_tr_b16 a[16:17], v10 offset:41984\n\t"
    "ds_read_b64_tr_b16 a[18:19], v10 offset:44032\n\t"
    "v_mfma_f32_16x16x32_bf16 v[68:71], a[24:27], a[100:103], v[68:71]\n\t"
    "ds_read_b64_tr_b16 a[20:21], v11 offset:41984\n\t"
    "ds_read_b64_tr_b16 a[22:23], v11 offset:44032\n\t"
    "v_mfma_f32_16x16x32_bf16 v[72:75], a[28:31], a[96:99], v[72:75]\n\t"
    "buffer_load_dwordx4 a[72:75], v1, s[52:55], s72 offen offset:2048 sc1\n\t"
    "v_mfma_f32_16x16x32_bf16 v[76:79], a[28:31], a[100:103], v[76:79]\n\t"
    "s_waitcnt vmcnt(16) lgkmcnt(8)\n\t"
    "s_add_u32 m0, s74, 16384\n\t"
    "v_mfma_f32_16x16x32_bf16 v[16:19], a[0:3], a[104:107], v[16:19]\n\t"
    "s_waitcnt vmcnt(12)\n\t"
    "s_barrier\n\t"
    "ds_read_b64_tr_b16 a[24:25], v10 offset:42496\n\t"
    "ds_read_b64_tr_b16 a[26:27], v10 offset:44544\n\t"
    "v_mfma_f32_16x16x32_bf16 v[20:23], a[0:3], a[108:111], v[20:23]\n\t"
    "ds_read_b64_tr_b16 a[28:29], v11 offset:42496\n\t"
    "ds_read_b64_tr_b16 a[30:31], v11 offset:44544\n\t"
    "v_mfma_f32_16x16x32_bf16 v[24:27], a[4:7], a[104:107], v[24:27]\n\t"
    "buffer_load_dwordx4 a[76:79], v1, s[52:55], s72 offen offset:2304 sc1\n\t"
    "v_mfma_f32_16x16x32_bf16 v[28:31], a[4:7], a[108:111], v[28:31]\n\t"
    "s_waitcnt lgkmcnt(8)\n\t"
    "s_add_u32 s72, s72, 4096\n\t"
    "v_mfma_f32_16x16x32_bf16 v[32:35], a[8:11], a[104:107], v[32:35]\n\t"
    "ds_read_b64_tr_b16 a[0:1], v10 offset:49152\n\t"
    "ds_read_b64_tr_b16 a[2:3], v10 offset:51200\n\t"
    "v_mfma_f32_16x16x32_bf16 v[36:39], a[8:11], a[108:111], v[36:39]\n\t"
    "ds_read_b64_tr_b16 a[4:5], v11 offset:49152\n\t"
    "ds_read_b64_tr_b16 a[6:7], v11 offset:51200\n\t"
    "v_mfma_f32_16x16x32_bf16 v[40:43], a[12:15], a[104:107], v[40:43]\n\t"
    "buffer_load_dwordx4 v3, s[56:59], s73 offen offset:0 lds\n\t"
    "v_mfma_f32_16x16x32_bf16 v[44:47], a[12:15], a[108:111], v[44:47]\n\t"
    "s_waitcnt lgkmcnt(8)\n\t"
    "v_mfma_f32_16x16x32_bf16 v[48:51], a[16:19], a[104:107], v[48:51]\n\t"
    "ds_read_b64_tr_b16 a[8:9], v10 offset:49664\n\t"
    "ds_read_b64_tr_b16 a[10:11], v10 offset:51712\n\t"
    "v_mfma_f32_16x16x32_bf16 v[52:55], a[16:19], a[108:111], v[52:55]\n\t"
    "ds_read_b64_tr_b16 a[12:13], v11 offset:49664\n\t"
    "ds_read_b64_tr_b16 a[14:15], v11 offset:51712\n\t"
    "v_mfma_f32_16x16x32_bf16 v[56:59], a[20:23], a[104:107], v[56:59]\n\t"
    "buffer_load_dwordx4 v4, s[56:59], s73 offen offset:1024 lds\n\t"
    "v_mfma_f32_16x16x32_bf16 v[60:63], a[20:23], a[108:111], v[60:63]\n\t"
    "s_waitcnt lgkmcnt(8)\n\t"
    "v_mfma_f32_16x16x32_bf16 v[64:67], a[24:27], a[104:107], v[64:67]\n\t"
    "ds_read_b64_tr_b16 a[16:17], v10 offset:50176\n\t"
    "ds_read_b64_tr_b16 a[18:19], v10 offset:52224\n\t"
    "v_mfma_f32_16x16x32_bf16 v[68:71], a[24:27], a[108:111], v[68:71]\n\t"
    "ds_read_b64_tr_b16 a[20:21], v11 offset:50176\n\t"
    "ds_read_b64_tr_b16 a[22:23], v11 offset:52224\n\t"
    "v_mfma_f32_16x16x32_bf16 v[72:75], a[28:31], a[104:107], v[72:75]\n\t"
    "buffer_load_dwordx4 v5, s[56:59], s73 offen offset:2048 lds\n\t"
    "v_mfma_f32_16x16x32_bf16 v[76:79], a[28:31], a[108:111], v[76:79]\n\t"
    "s_waitcnt vmcnt(14) lgkmcnt(8)\n\t"
    "v_mfma_f32_16x16x32_bf16 v[16:19], a[0:3], a[112:115], v[16:19]\n\t"
    "ds_read_b64_tr_b16 a[24:25], v10 offset:50688\n\t"
    "ds_read_b64_tr_b16 a[26:27], v10 offset:52736\n\t"
    "v_mfma_f32_16x16x32_bf16 v[20:23], a[0:3], a[116:119], v[20:23]\n\t"
    "ds_read_b64_tr_b16 a[28:29], v11 offset:50688\n\t"
    "ds_read_b64_tr_b16 a[30:31], v11 offset:52736\n\t"
    "v_mfma_f32_16x16x32_bf16 v[24:27], a[4:7], a[112:115], v[24:27]\n\t"
    "buffer_load_dwordx4 v6, s[56:59], s73 offen offset:3072 lds\n\t"
    "v_mfma_f32_16x16x32_bf16 v[28:31], a[4:7], a[116:119], v[28:31]\n\t"
    "s_waitcnt lgkmcnt(8)\n\t"
    "s_add_u32 s73, s73, 458752\n\t"
    "v_mfma_f32_16x16x32_bf16 v[32:35], a[8:11], a[112:115], v[32:35]\n\t"
    "ds_read_b64_tr_b16 a[0:1], v10 offset:57344\n\t"
    "ds_read_b64_tr_b16 a[2:3], v10 offset:59392\n\t"
    "v_mfma_f32_16x16x32_bf16 v[36:39], a[8:11], a[116:119], v[36:39]\n\t"
    "ds_read_b64_tr_b16 a[4:5], v11 offset:57344\n\t"
    "ds_read_b64_tr_b16 a[6:7], v11 offset:59392\n\t"
    "v_mfma_f32_16x16x32_bf16 v[40:43], a[12:15], a[112:115], v[40:43]\n\t"
    "buffer_load_dwordx4 a[80:83], v1, s[52:55], s72 offen offset:0 sc1\n\t"
    "v_mfma_f32_16x16x32_bf16 v[44:47], a[12:15], a[116:119], v[44:47]\n\t"
    "s_waitcnt lgkmcnt(8)\n\t"
    "v_mfma_f32_16x16x32_bf16 v[48:51], a[16:19], a[112:115], v[48:51]\n\t"
    "ds_read_b64_tr_b16 a[8:9], v10 offset:57856\n\t"
    "ds_read_b64_tr_b16 a[10:11], v10 offset:59904\n\t"
    "v_mfma_f32_16x16x32_bf16 v[52:55], a[16:19], a[116:119], v[52:55]\n\t"
    "ds_read_b64_tr_b16 a[12:13], v11 offset:57856\n\t"
    "ds_read_b64_tr_b16 a[14:15], v11 offset:59904\n\t"
    "v_mfma_f32_16x16x32_bf16 v[56:59], a[20:23], a[112:115], v[56:59]\n\t"
    "buffer_load_dwordx4 a[84:87], v1, s[52:55], s72 offen offset:256 sc1\n\t"
    "v_mfma_f32_16x16x32_bf16 v[60:63], a[20:23], a[116:119], v[60:63]\n\t"
    "s_waitcnt lgkmcnt(8)\n\t"
    "v_mfma_f32_16x16x32_bf16 v[64:67], a[24:27], a[112:115], v[64:67]\n\t"
    "ds_read_b64_tr_b16 a[16:17], v10 offset:58368\n\t"
    "ds_read_b64_tr_b16 a[18:19], v10 offset:60416\n\t"
    "v_mfma_f32_16x16x32_bf16 v[68:71], a[24:27], a[116:119], v[68:71]\n\t"
    "ds_read_b64_tr_b16 a[20:21], v11 offset:58368\n\t"
    "ds_read_b64_tr_b16 a[22:23], v11 offset:60416\n\t"
    "v_mfma_f32_16x16x32_bf16 v[72:75], a[28:31], a[112:115], v[72:75]\n\t"
    "buffer_load_dwordx4 a[88:91], v1, s[52:55], s72 offen offset:2048 sc1\n\t"
    "v_mfma_f32_16x16x32_bf16 v[76:79], a[28:31], a[116:119], v[76:79]\n\t"
    "s_waitcnt vmcnt(16) lgkmcnt(8)\n\t"
    "s_add_u32 m0, s74, 32768\n\t"
    "v_mfma_f32_16x16x32_bf16 v[16:19], a[0:3], a[120:123], v[16:19]\n\t"
    "s_waitcnt vmcnt(11)\n\t"
    "s_barrier\n\t"
    "ds_read_b64_tr_b16 a[24:25], v10 offset:58880\n\t"
    "ds_read_b64_tr_b16 a[26:27], v10 offset:60928\n\t"
    "v_mfma_f32_16x16x32_bf16 v[20:23], a[0:3], a[124:127], v[20:23]\n\t"
    "ds_read_b64_tr_b16 a[28:29], v11 offset:58880\n\t"
    "ds_read_b64_tr_b16 a[30:31], v11 offset:60928\n\t"
    "v_mfma_f32_16x16x32_bf16 v[24:27], a[4:7], a[120:123], v[24:27]\n\t"
    "buffer_load_dwordx4 a[92:95], v1, s[52:55], s72 offen offset:2304 sc1\n\t"
    "v_mfma_f32_16x16x32_bf16 v[28:31], a[4:7], a[124:127], v[28:31]\n\t"
    "s_waitcnt lgkmcnt(8)\n\t"
    "s_add_u32 s72, s72, 4096\n\t"
    "v_mfma_f32_16x16x32_bf16 v[32:35], a[8:11], a[120:123], v[32:35]\n\t"
    "ds_read_b64_tr_b16 a[0:1], v10 offset:0\n\t"
    "ds_read_b64_tr_b16 a[2:3], v10 offset:2048\n\t"
    "v_mfma_f32_16x16x32_bf16 v[36:39], a[8:11], a[124:127], v[36:39]\n\t"
    "ds_read_b64_tr_b16 a[4:5], v11 offset:0\n\t"
    "ds_read_b64_tr_b16 a[6:7], v11 offset:2048\n\t"
    "v_mfma_f32_16x16x32_bf16 v[40:43], a[12:15], a[120:123], v[40:43]\n\t"
    "buffer_load_dwordx4 v3, s[56:59], s73 offen offset:0 lds\n\t"
    "v_mfma_f32_16x16x32_bf16 v[44:47], a[12:15], a[124:127], v[44:47]\n\t"
    "s_waitcnt lgkmcnt(8)\n\t"
    "v_mfma_f32_16x16x32_bf16 v[48:51], a[16:19], a[120:123], v[48:51]\n\t"
    "ds_read_b64_tr_b16 a[8:9], v10 offset:512\n\t"
    "ds_read_b64_tr_b16 a[10:11], v10 offset:2560\n\t"
    "v_mfma_f32_16x16x32_bf16 v[52:55], a[16:19], a[124:127], v[52:55]\n\t"
    "ds_read_b64_tr_b16 a[12:13], v11 offset:512\n\t"
    "ds_read_b64_tr_b16 a[14:15], v11 offset:2560\n\t"
    "v_mfma_f32_16x16x32_bf16 v[56:59], a[20:23], a[120:123], v[56:59]\n\t"
    "buffer_load_dwordx4 v4, s[56:59], s73 offen offset:1024 lds\n\t"
    "v_mfma_f32_16x16x32_bf16 v[60:63], a[20:23], a[124:127], v[60:63]\n\t"
    "s_waitcnt lgkmcnt(8)\n\t"
    "v_mfma_f32_16x16x32_bf16 v[64:67], a[24:27], a[120:123], v[64:67]\n\t"
    "ds_read_b64_tr_b16 a[16:17], v10 offset:1024\n\t"
    "ds_read_b64_tr_b16 a[18:19], v10 offset:3072\n\t"
    "v_mfma_f32_16x16x32_bf16 v[68:71], a[24:27], a[124:127], v[68:71]\n\t"
    "ds_read_b64_tr_b16 a[20:21], v11 offset:1024\n\t"
    "ds_read_b64_tr_b16 a[22:23], v11 offset:3072\n\t"
    "v_mfma_f32_16x16x32_bf16 v[72:75], a[28:31], a[120:123], v[72:75]\n\t"
    "buffer_load_dwordx4 v5, s[56:59], s73 offen offset:2048 lds\n\t"
    "v_mfma_f32_16x16x32_bf16 v[76:79], a[28:31], a[124:127], v[76:79]\n\t"
    "s_waitcnt vmcnt(13) lgkmcnt(8)\n\t"
    "v_mfma_f32_16x16x32_bf16 v[16:19], a[0:3], a[64:67], v[16:19]\n\t"
    "ds_read_b64_tr_b16 a[24:25], v10 offset:1536\n\t"
    "ds_read_b64_tr_b16 a[26:27], v10 offset:3584\n\t"
    "v_mfma_f32_16x16x32_bf16 v[20:23], a[0:3], a[68:71], v[20:23]\n\t"
    "ds_read_b64_tr_b16 a[28:29], v11 offset:1536\n\t"
    "ds_read_b64_tr_b16 a[30:31], v11 offset:3584\n\t"
    "v_mfma_f32_16x16x32_bf16 v[24:27], a[4:7], a[64:67], v[24:27]\n\t"
    "buffer_load_dwordx4 v6, s[56:59], s73 offen offset:3072 lds\n\t"
    "v_mfma_f32_16x16x32_bf16 v[28:31], a[4:7], a[68:71], v[28:31]\n\t"
    "s_waitcnt lgkmcnt(8)\n\t"
    "s_add_u32 s73, s73, 458752\n\t"
    "v_mfma_f32_16x16x32_bf16 v[32:35], a[8:11], a[64:67], v[32:35]\n\t"
    "ds_read_b64_tr_b16 a[0:1], v10 offset:8192\n\t"
    "ds_read_b64_tr_b16 a[2:3], v10 offset:10240\n\t"
    "v_mfma_f32_16x16x32_bf16 v[36:39], a[8:11], a[68:71], v[36:39]\n\t"
    "ds_read_b64_tr_b16 a[4:5], v11 offset:8192\n\t"
    "ds_read_b64_tr_b16 a[6:7], v11 offset:10240\n\t"
    "v_mfma_f32_16x16x32_bf16 v[40:43], a[12:15], a[64:67], v[40:43]\n\t"
    "buffer_load_dwordx4 a[96:99], v1, s[52:55], s72 offen offset:0 sc1\n\t"
    "v_mfma_f32_16x16x32_bf16 v[44:47], a[12:15], a[68:71], v[44:47]\n\t"
    "s_waitcnt lgkmcnt(8)\n\t"
    "v_mfma_f32_16x16x32_bf16 v[48:51], a[16:19], a[64:67], v[48:51]\n\t"
    "ds_read_b64_tr_b16 a[8:9], v10 offset:8704\n\t"
    "ds_read_b64_tr_b16 a[10:11], v10 offset:10752\n\t"
    "v_mfma_f32_16x16x32_bf16 v[52:55], a[16:19], a[68:71], v[52:55]\n\t"
    "ds_read_b64_tr_b16 a[12:13], v11 offset:8704\n\t"
    "ds_read_b64_tr_b16 a[14:15], v11 offset:10752\n\t"
    "v_mfma_f32_16x16x32_bf16 v[56:59], a[20:23], a[64:67], v[56:59]\n\t"
    "buffer_load_dwordx4 a[100:103], v1, s[52:55], s72 offen offset:256 sc1\n\t"
    "v_mfma_f32_16x16x32_bf16 v[60:63], a[20:23], a[68:71], v[60:63]\n\t"
    "s_waitcnt lgkmcnt(8)\n\t"
    "v_mfma_f32_16x16x32_bf16 v[64:67], a[24:27], a[64:67], v[64:67]\n\t"
    "ds_read_b64_tr_b16 a[16:17], v10 offset:9216\n\t"
    "ds_read_b64_tr_b16 a[18:19], v10 offset:11264\n\t"
    "v_mfma_f32_16x16x32_bf16 v[68:71], a[24:27], a[68:71], v[68:71]\n\t"
    "ds_read_b64_tr_b16 a[20:21], v11 offset:9216\n\t"
    "ds_read_b64_tr_b16 a[22:23], v11 offset:11264\n\t"
    "v_mfma_f32_16x16x32_bf16 v[72:75], a[28:31], a[64:67], v[72:75]\n\t"
    "buffer_load_dwordx4 a[104:107], v1, s[52:55], s72 offen offset:2048 sc1\n\t"
    "v_mfma_f32_16x16x32_bf16 v[76:79], a[28:31], a[68:71], v[76:79]\n\t"
    "s_waitcnt vmcnt(15) lgkmcnt(8)\n\t"
    "s_add_u32 m0, s74, 49152\n\t"
    "v_mfma_f32_16x16x32_bf16 v[16:19], a[0:3], a[72:75], v[16:19]\n\t"
    "s_waitcnt vmcnt(11)\n\t"
    "s_barrier\n\t"
    "ds_read_b64_tr_b16 a[24:25], v10 offset:9728\n\t"
    "ds_read_b64_tr_b16 a[26:27], v10 offset:11776\n\t"
    "v_mfma_f32_16x16x32_bf16 v[20:23], a[0:3], a[76:79], v[20:23]\n\t"
    "ds_read_b64_tr_b16 a[28:29], v11 offset:9728\n\t"
    "ds_read_b64_tr_b16 a[30:31], v11 offset:11776\n\t"
    "v_mfma_f32_16x16x32_bf16 v[24:27], a[4:7], a[72:75], v[24:27]\n\t"
    "buffer_load_dwordx4 a[108:111], v1, s[52:55], s72 offen offset:2304 sc1\n\t"
    "v_mfma_f32_16x16x32_bf16 v[28:31], a[4:7], a[76:79], v[28:31]\n\t"
    "s_waitcnt lgkmcnt(8)\n\t"
    "s_add_u32 s72, s72, 4096\n\t"
    "v_mfma_f32_16x16x32_bf16 v[32:35], a[8:11], a[72:75], v[32:35]\n\t"
    "ds_read_b64_tr_b16 a[0:1], v10 offset:16384\n\t"
    "ds_read_b64_tr_b16 a[2:3], v10 offset:18432\n\t"
    "v_mfma_f32_16x16x32_bf16 v[36:39], a[8:11], a[76:79], v[36:39]\n\t"
    "ds_read_b64_tr_b16 a[4:5], v11 offset:16384\n\t"
    "ds_read_b64_tr_b16 a[6:7], v11 offset:18432\n\t"
    "v_mfma_f32_16x16x32_bf16 v[40:43], a[12:15], a[72:75], v[40:43]\n\t"
    "buffer_load_dwordx4 v3, s[56:59], s73 offen offset:0 lds\n\t"
    "v_mfma_f32_16x16x32_bf16 v[44:47], a[12:15], a[76:79], v[44:47]\n\t"
    "s_waitcnt lgkmcnt(8)\n\t"
    "v_mfma_f32_16x16x32_bf16 v[48:51], a[16:19], a[72:75], v[48:51]\n\t"
    "ds_read_b64_tr_b16 a[8:9], v10 offset:16896\n\t"
    "ds_read_b64_tr_b16 a[10:11], v10 offset:18944\n\t"
    "v_mfma_f32_16x16x32_bf16 v[52:55], a[16:19], a[76:79], v[52:55]\n\t"
    "ds_read_b64_tr_b16 a[12:13], v11 offset:16896\n\t"
    "ds_read_b64_tr_b16 a[14:15], v11 offset:18944\n\t"
    "v_mfma_f32_16x16x32_bf16 v[56:59], a[20:23], a[72:75], v[56:59]\n\t"
    "buffer_load_dwordx4 v4, s[56:59], s73 offen offset:1024 lds\n\t"
    "v_mfma_f32_16x16x32_bf16 v[60:63], a[20:23], a[76:79], v[60:63]\n\t"
    "s_waitcnt lgkmcnt(8)\n\t"
    "v_mfma_f32_16x16x32_bf16 v[64:67], a[24:27], a[72:75], v[64:67]\n\t"
    "ds_read_b64_tr_b16 a[16:17], v10 offset:17408\n\t"
    "ds_read_b64_tr_b16 a[18:19], v10 offset:19456\n\t"
    "v_mfma_f32_16x16x32_bf16 v[68:71], a[24:27], a[76:79], v[68:71]\n\t"
    "ds_read_b64_tr_b16 a[20:21], v11 offset:17408\n\t"
    "ds_read_b64_tr_b16 a[22:23], v11 offset:19456\n\t"
    "v_mfma_f32_16x16x32_bf16 v[72:75], a[28:31], a[72:75], v[72:75]\n\t"
    "buffer_load_dwordx4 v5, s[56:59], s73 offen offset:2048 lds\n\t"
    "v_mfma_f32_16x16x32_bf16 v[76:79], a[28:31], a[76:79], v[76:79]\n\t"
    "s_waitcnt vmcnt(13) lgkmcnt(8)\n\t"
    "s_mov_b32 s79, 4\n\t"
    ".p2align 6\n\t"
    "Lm_down_0_0:\n\t"
    "v_cmp_ne_u32 vcc, %4, v249\n\t"
    "s_cbranch_vccz 2f\n\t"
    "s_mov_b32 s86, 0x400000\n\t"
    "1:\n\t"
    "global_load_dword v249, v248, s[48:49] sc1\n\t"
    "s_waitcnt vmcnt(0)\n\t"
    "v_cmp_ne_u32 vcc, %4, v249\n\t"
    "s_sub_u32 s86, s86, 1\n\t"
    "s_cmp_eq_u32 s86, 0\n\t"
    "s_cbranch_scc1 2f\n\t"
    "s_cbranch_vccz 2f\n\t"
    "s_sleep 1\n\t"
    "s_branch 1b\n\t"
    "2:\n\t"
    "v_mfma_f32_16x16x32_bf16 v[16:19], a[0:3], a[80:83], v[16:19]\n\t"
    "ds_read_b64_tr_b16 a[24:25], v10 offset:17920\n\t"
    "ds_read_b64_tr_b16 a[26:27], v10 offset:19968\n\t"
    "v_mfma_f32_16x16x32_bf16 v[20:23], a[0:3], a[84:87], v[20:23]\n\t"
    "ds_read_b64_tr_b16 a[28:29], v11 offset:17920\n\t"
    "ds_read_b64_tr_b16 a[30:31], v11 offset:19968\n\t"
    "v_mfma_f32_16x16x32_bf16 v[24:27], a[4:7], a[80:83], v[24:27]\n\t"
    "buffer_load_dwordx4 v6, s[56:59], s73 offen offset:3072 lds\n\t"
    "v_mfma_f32_16x16x32_bf16 v[28:31], a[4:7], a[84:87], v[28:31]\n\t"
    "s_waitcnt lgkmcnt(8)\n\t"
    "s_add_u32 s73, s73, s87\n\t"
    "v_mfma_f32_16x16x32_bf16 v[32:35], a[8:11], a[80:83], v[32:35]\n\t"
    "ds_read_b64_tr_b16 a[0:1], v10 offset:24576\n\t"
    "ds_read_b64_tr_b16 a[2:3], v10 offset:26624\n\t"
    "v_mfma_f32_16x16x32_bf16 v[36:39], a[8:11], a[84:87], v[36:39]\n\t"
    "ds_read_b64_tr_b16 a[4:5], v11 offset:24576\n\t"
    "ds_read_b64_tr_b16 a[6:7], v11 offset:26624\n\t"
    "v_mfma_f32_16x16x32_bf16 v[40:43], a[12:15], a[80:83], v[40:43]\n\t"
    "buffer_load_dwordx4 a[112:115], v1, s[52:55], s72 offen offset:0 sc1\n\t"
    "v_mfma_f32_16x16x32_bf16 v[44:47], a[12:15], a[84:87], v[44:47]\n\t"
    "s_waitcnt lgkmcnt(8)\n\t"
    "s_xor_b32 s87, s87, 4292280320\n\ts_cmp_eq_u32 s51, 24\n\ts_cselect_b32 s87, s90, s87\n\t"
    "v_mfma_f32_16x16x32_bf16 v[48:51], a[16:19], a[80:83], v[48:51]\n\t"
    "ds_read_b64_tr_b16 a[8:9], v10 offset:25088\n\t"
    "ds_read_b64_tr_b16 a[10:11], v10 offset:27136\n\t"
    "v_mfma_f32_16x16x32_bf16 v[52:55], a[16:19], a[84:87], v[52:55]\n\t"
    "ds_read_b64_tr_b16 a[12:13], v11 offset:25088\n\t"
    "ds_read_b64_tr_b16 a[14:15], v11 offset:27136\n\t"
    "v_mfma_f32_16x16x32_bf16 v[56:59], a[20:23], a[80:83], v[56:59]\n\t"
    "buffer_load_dwordx4 a[116:119], v1, s[52:55], s72 offen offset:256 sc1\n\t"
    "v_mfma_f32_16x16x32_bf16 v[60:63], a[20:23], a[84:87], v[60:63]\n\t"
    "s_waitcnt lgkmcnt(8)\n\t"
    "v_mfma_f32_16x16x32_bf16 v[64:67], a[24:27], a[80:83], v[64:67]\n\t"
    "ds_read_b64_tr_b16 a[16:17], v10 offset:25600\n\t"
    "ds_read_b64_tr_b16 a[18:19], v10 offset:27648\n\t"
    "v_mfma_f32_16x16x32_bf16 v[68:71], a[24:27], a[84:87], v[68:71]\n\t"
    "ds_read_b64_tr_b16 a[20:21], v11 offset:25600\n\t"
    "ds_read_b64_tr_b16 a[22:23], v11 offset:27648\n\t"
    "v_mfma_f32_16x16x32_bf16 v[72:75], a[28:31], a[80:83], v[72:75]\n\t"
    "buffer_load_dwordx4 a[120:123], v1, s[52:55], s72 offen offset:2048 sc1\n\t"
    "v_mfma_f32_16x16x32_bf16 v[76:79], a[28:31], a[84:87], v[76:79]\n\t"
    "s_waitcnt vmcnt(15) lgkmcnt(8)\n\t"
    "s_add_u32 m0, s74, 0\n\t"
    "v_mfma_f32_16x16x32_bf16 v[16:19], a[0:3], a[88:91], v[16:19]\n\t"
    "s_waitcnt vmcnt(11)\n\t"
    "s_barrier\n\t"
    "ds_read_b64_tr_b16 a[24:25], v10 offset:26112\n\t"
    "ds_read_b64_tr_b16 a[26:27], v10 offset:28160\n\t"
    "v_mfma_f32_16x16x32_bf16 v[20:23], a[0:3], a[92:95], v[20:23]\n\t"
    "ds_read_b64_tr_b16 a[28:29], v11 offset:26112\n\t"
    "ds_read_b64_tr_b16 a[30:31], v11 offset:28160\n\t"
    "v_mfma_f32_16x16x32_bf16 v[24:27], a[4:7], a[88:91], v[24:27]\n\t"
    "buffer_load_dwordx4 a[124:127], v1, s[52:55], s72 offen offset:2304 sc1\n\t"
    "v_mfma_f32_16x16x32_bf16 v[28:31], a[4:7], a[92:95], v[28:31]\n\t"
    "s_waitcnt lgkmcnt(8)\n\t"
    "s_add_u32 s72, s72, 4096\n\t"
    "v_mfma_f32_16x16x32_bf16 v[32:35], a[8:11], a[88:91], v[32:35]\n\t"
    "ds_read_b64_tr_b16 a[0:1], v10 offset:32768\n\t"
    "ds_read_b64_tr_b16 a[2:3], v10 offset:34816\n\t"
    "v_mfma_f32_16x16x32_bf16 v[36:39], a[8:11], a[92:95], v[36:39]\n\t"
    "ds_read_b64_tr_b16 a[4:5], v11 offset:32768\n\t"
    "ds_read_b64_tr_b16 a[6:7], v11 offset:34816\n\t"
    "v_mfma_f32_16x16x32_bf16 v[40:43], a[12:15], a[88:91], v[40:43]\n\t"
    "buffer_load_dwordx4 v3, s[56:59], s73 offen offset:0 lds\n\t"
    "v_mfma_f32_16x16x32_bf16 v[44:47], a[12:15], a[92:95], v[44:47]\n\t"
    "s_waitcnt lgkmcnt(8)\n\t"
    "v_mfma_f32_16x16x32_bf16 v[48:51], a[16:19], a[88:91], v[48:51]\n\t"
    "ds_read_b64_tr_b16 a[8:9], v10 offset:33280\n\t"
    "ds_read_b64_tr_b16 a[10:11], v10 offset:35328\n\t"
    "v_mfma_f32_16x16x32_bf16 v[52:55], a[16:19], a[92:95], v[52:55]\n\t"
    "ds_read_b64_tr_b16 a[12:13], v11 offset:33280\n\t"
    "ds_read_b64_tr_b16 a[14:15], v11 offset:35328\n\t"
    "v_mfma_f32_16x16x32_bf16 v[56:59], a[20:23], a[88:91], v[56:59]\n\t"
    "buffer_load_dwordx4 v4, s[56:59], s73 offen offset:1024 lds\n\t"
    "v_mfma_f32_16x16x32_bf16 v[60:63], a[20:23], a[92:95], v[60:63]\n\t"
    "s_waitcnt lgkmcnt(8)\n\t"
    "v_mfma_f32_16x16x32_bf16 v[64:67], a[24:27], a[88:91], v[64:67]\n\t"
    "ds_read_b64_tr_b16 a[16:17], v10 offset:33792\n\t"
    "ds_read_b64_tr_b16 a[18:19], v10 offset:35840\n\t"
    "v_mfma_f32_16x16x32_bf16 v[68:71], a[24:27], a[92:95], v[68:71]\n\t"
    "ds_read_b64_tr_b16 a[20:21], v11 offset:33792\n\t"
    "ds_read_b64_tr_b16 a[22:23], v11 offset:35840\n\t"
    "v_mfma_f32_16x16x32_bf16 v[72:75], a[28:31], a[88:91], v[72:75]\n\t"
    "buffer_load_dwordx4 v5, s[56:59], s73 offen offset:2048 lds\n\t"
    "v_mfma_f32_16x16x32_bf16 v[76:79], a[28:31], a[92:95], v[76:79]\n\t"
    "s_waitcnt vmcnt(13) lgkmcnt(8)\n\t"
    "s_lshr_b32 s98, s51, 3\n\t"
    "s_lshl_b32 s98, s98, 14\n\t"
    "v_add_u32 v248, s98, v250\n\t"
    "global_load_dword v249, v248, s[48:49] sc1\n\t"
    "s_add_u32 s51, s51, 4\n\t"
    "v_mfma_f32_16x16x32_bf16 v[16:19], a[0:3], a[96:99], v[16:19]\n\t"
    "ds_read_b64_tr_b16 a[24:25], v10 offset:34304\n\t"
    "ds_read_b64_tr_b16 a[26:27], v10 offset:36352\n\t"
    "v_mfma_f32_16x16x32_bf16 v[20:23], a[0:3], a[100:103], v[20:23]\n\t"
    "ds_read_b64_tr_b16 a[28:29], v11 offset:34304\n\t"
    "ds_read_b64_tr_b16 a[30:31], v11 offset:36352\n\t"
    "v_mfma_f32_16x16x32_bf16 v[24:27], a[4:7], a[96:99], v[24:27]\n\t"
    "buffer_load_dwordx4 v6, s[56:59], s73 offen offset:3072 lds\n\t"
    "v_mfma_f32_16x16x32_bf16 v[28:31], a[4:7], a[100:103], v[28:31]\n\t"
    "s_waitcnt lgkmcnt(8)\n\t"
    "s_add_u32 s73, s73, 458752\n\t"
    "v_mfma_f32_16x16x32_bf16 v[32:35], a[8:11], a[96:99], v[32:35]\n\t"
    "ds_read_b64_tr_b16 a[0:1], v10 offset:40960\n\t"
    "ds_read_b64_tr_b16 a[2:3], v10 offset:43008\n\t"
    "v_mfma_f32_16x16x32_bf16 v[36:39], a[8:11], a[100:103], v[36:39]\n\t"
    "ds_read_b64_tr_b16 a[4:5], v11 offset:40960\n\t"
    "ds_read_b64_tr_b16 a[6:7], v11 offset:43008\n\t"
    "v_mfma_f32_16x16x32_bf16 v[40:43], a[12:15], a[96:99], v[40:43]\n\t"
    "buffer_load_dwordx4 a[64:67], v1, s[52:55], s72 offen offset:0 sc1\n\t"
    "v_mfma_f32_16x16x32_bf16 v[44:47], a[12:15], a[100:103], v[44:47]\n\t"
    "s_waitcnt lgkmcnt(8)\n\t"
    "v_mfma_f32_16x16x32_bf16 v[48:51], a[16:19], a[96:99], v[48:51]\n\t"
    "ds_read_b64_tr_b16 a[8:9], v10 offset:41472\n\t"
    "ds_read_b64_tr_b16 a[10:11], v10 offset:43520\n\t"
    "v_mfma_f32_16x16x32_bf16 v[52:55], a[16:19], a[100:103], v[52:55]\n\t"
    "ds_read_b64_tr_b16 a[12:13], v11 offset:41472\n\t"
    "ds_read_b64_tr_b16 a[14:15], v11 offset:43520\n\t"
    "v_mfma_f32_16x16x32_bf16 v[56:59], a[20:23], a[96:99], v[56:59]\n\t"
    "buffer_load_dwordx4 a[68:71], v1, s[52:55], s72 offen offset:256 sc1\n\t"
    "v_mfma_f32_16x16x32_bf16 v[60:63], a[20:23], a[100:103], v[60:63]\n\t"
    "s_waitcnt lgkmcnt(8)\n\t"
    "v_mfma_f32_16x16x32_bf16 v[64:67], a[24:27], a[96:99], v[64:67]\n\t"
    "ds_read_b64_tr_b16 a[16:17], v10 offset:41984\n\t"
    "ds_read_b64_tr_b16 a[18:19], v10 offset:44032\n\t"
    "v_mfma_f32_16x16x32_bf16 v[68:71], a[24:27], a[100:103], v[68:71]\n\t"
    "ds_read_b64_tr_b16 a[20:21], v11 offset:41984\n\t"
    "ds_read_b64_tr_b16 a[22:23], v11 offset:44032\n\t"
    "v_mfma_f32_16x16x32_bf16 v[72:75], a[28:31], a[96:99], v[72:75]\n\t"
    "buffer_load_dwordx4 a[72:75], v1, s[52:55], s72 offen offset:2048 sc1\n\t"
    "v_mfma_f32_16x16x32_bf16 v[76:79], a[28:31], a[100:103], v[76:79]\n\t"
    "s_waitcnt vmcnt(16) lgkmcnt(8)\n\t"
    "s_add_u32 m0, s74, 16384\n\t"
    "v_mfma_f32_16x16x32_bf16 v[16:19], a[0:3], a[104:107], v[16:19]\n\t"
    "s_waitcnt vmcnt(12)\n\t"
    "s_barrier\n\t"
    "ds_read_b64_tr_b16 a[24:25], v10 offset:42496\n\t"
    "ds_read_b64_tr_b16 a[26:27], v10 offset:44544\n\t"
    "v_mfma_f32_16x16x32_bf16 v[20:23], a[0:3], a[108:111], v[20:23]\n\t"
    "ds_read_b64_tr_b16 a[28:29], v11 offset:42496\n\t"
    "ds_read_b64_tr_b16 a[30:31], v11 offset:44544\n\t"
    "v_mfma_f32_16x16x32_bf16 v[24:27], a[4:7], a[104:107], v[24:27]\n\t"
    "buffer_load_dwordx4 a[76:79], v1, s[52:55], s72 offen offset:2304 sc1\n\t"
    "v_mfma_f32_16x16x32_bf16 v[28:31], a[4:7], a[108:111], v[28:31]\n\t"
    "s_waitcnt lgkmcnt(8)\n\t"
    "s_add_u32 s72, s72, 4096\n\t"
    "v_mfma_f32_16x16x32_bf16 v[32:35], a[8:11], a[104:107], v[32:35]\n\t"
    "ds_read_b64_tr_b16 a[0:1], v10 offset:49152\n\t"
    "ds_read_b64_tr_b16 a[2:3], v10 offset:51200\n\t"
    "v_mfma_f32_16x16x32_bf16 v[36:39], a[8:11], a[108:111], v[36:39]\n\t"
    "ds_read_b64_tr_b16 a[4:5], v11 offset:49152\n\t"
    "ds_read_b64_tr_b16 a[6:7], v11 offset:51200\n\t"
    "v_mfma_f32_16x16x32_bf16 v[40:43], a[12:15], a[104:107], v[40:43]\n\t"
    "buffer_load_dwordx4 v3, s[56:59], s73 offen offset:0 lds\n\t"
    "v_mfma_f32_16x16x32_bf16 v[44:47], a[12:15], a[108:111], v[44:47]\n\t"
    "s_waitcnt lgkmcnt(8)\n\t"
    "v_mfma_f32_16x16x32_bf16 v[48:51], a[16:19], a[104:107], v[48:51]\n\t"
    "ds_read_b64_tr_b16 a[8:9], v10 offset:49664\n\t"
    "ds_read_b64_tr_b16 a[10:11], v10 offset:51712\n\t"
    "v_mfma_f32_16x16x32_bf16 v[52:55], a[16:19], a[108:111], v[52:55]\n\t"
    "ds_read_b64_tr_b16 a[12:13], v11 offset:49664\n\t"
    "ds_read_b64_tr_b16 a[14:15], v11 offset:51712\n\t"
    "v_mfma_f32_16x16x32_bf16 v[56:59], a[20:23], a[104:107], v[56:59]\n\t"
    "buffer_load_dwordx4 v4, s[56:59], s73 offen offset:1024 lds\n\t"
    "v_mfma_f32_16x16x32_bf16 v[60:63], a[20:23], a[108:111], v[60:63]\n\t"
    "s_waitcnt lgkmcnt(8)\n\t"
    "v_mfma_f32_16x16x32_bf16 v[64:67], a[24:27], a[104:107], v[64:67]\n\t"
    "ds_read_b64_tr_b16 a[16:17], v10 offset:50176\n\t"
    "ds_read_b64_tr_b16 a[18:19], v10 offset:52224\n\t"
    "v_mfma_f32_16x16x32_bf16 v[68:71], a[24:27], a[108:111], v[68:71]\n\t"
    "ds_read_b64_tr_b16 a[20:21], v11 offset:50176\n\t"
    "ds_read_b64_tr_b16 a[22:23], v11 offset:52224\n\t"
    "v_mfma_f32_16x16x32_bf16 v[72:75], a[28:31], a[104:107], v[72:75]\n\t"
    "buffer_load_dwordx4 v5, s[56:59], s73 offen offset:2048 lds\n\t"
    "v_mfma_f32_16x16x32_bf16 v[76:79], a[28:31], a[108:111], v[76:79]\n\t"
    "s_waitcnt vmcnt(14) lgkmcnt(8)\n\t"
    "v_mfma_f32_16x16x32_bf16 v[16:19], a[0:3], a[112:115], v[16:19]\n\t"
    "ds_read_b64_tr_b16 a[24:25], v10 offset:50688\n\t"
    "ds_read_b64_tr_b16 a[26:27], v10 offset:52736\n\t"
    "v_mfma_f32_16x16x32_bf16 v[20:23], a[0:3], a[116:119], v[20:23]\n\t"
    "ds_read_b64_tr_b16 a[28:29], v11 offset:50688\n\t"
    "ds_read_b64_tr_b16 a[30:31], v11 offset:52736\n\t"
    "v_mfma_f32_16x16x32_bf16 v[24:27], a[4:7], a[112:115], v[24:27]\n\t"
    "buffer_load_dwordx4 v6, s[56:59], s73 offen offset:3072 lds\n\t"
    "v_mfma_f32_16x16x32_bf16 v[28:31], a[4:7], a[116:119], v[28:31]\n\t"
    "s_waitcnt lgkmcnt(8)\n\t"
    "s_add_u32 s73, s73, 458752\n\t"
    "v_mfma_f32_16x16x32_bf16 v[32:35], a[8:11], a[112:115], v[32:35]\n\t"
    "ds_read_b64_tr_b16 a[0:1], v10 offset:57344\n\t"
    "ds_read_b64_tr_b16 a[2:3], v10 offset:59392\n\t"
    "v_mfma_f32_16x16x32_bf16 v[36:39], a[8:11], a[116:119], v[36:39]\n\t"
    "ds_read_b64_tr_b16 a[4:5], v11 offset:57344\n\t"
    "ds_read_b64_tr_b16 a[6:7], v11 offset:59392\n\t"
    "v_mfma_f32_16x16x32_bf16 v[40:43], a[12:15], a[112:115], v[40:43]\n\t"
    "buffer_load_dwordx4 a[80:83], v1, s[52:55], s72 offen offset:0 sc1\n\t"
    "v_mfma_f32_16x16x32_bf16 v[44:47], a[12:15], a[116:119], v[44:47]\n\t"
    "s_waitcnt lgkmcnt(8)\n\t"
    "v_mfma_f32_16x16x32_bf16 v[48:51], a[16:19], a[112:115], v[48:51]\n\t"
    "ds_read_b64_tr_b16 a[8:9], v10 offset:57856\n\t"
    "ds_read_b64_tr_b16 a[10:11], v10 offset:59904\n\t"
    "v_mfma_f32_16x16x32_bf16 v[52:55], a[16:19], a[116:119], v[52:55]\n\t"
    "ds_read_b64_tr_b16 a[12:13], v11 offset:57856\n\t"
    "ds_read_b64_tr_b16 a[14:15], v11 offset:59904\n\t"
    "v_mfma_f32_16x16x32_bf16 v[56:59], a[20:23], a[112:115], v[56:59]\n\t"
    "buffer_load_dwordx4 a[84:87], v1, s[52:55], s72 offen offset:256 sc1\n\t"
    "v_mfma_f32_16x16x32_bf16 v[60:63], a[20:23], a[116:119], v[60:63]\n\t"
    "s_waitcnt lgkmcnt(8)\n\t"
    "v_mfma_f32_16x16x32_bf16 v[64:67], a[24:27], a[112:115], v[64:67]\n\t"
    "ds_read_b64_tr_b16 a[16:17], v10 offset:58368\n\t"
    "ds_read_b64_tr_b16 a[18:19], v10 offset:60416\n\t"
    "v_mfma_f32_16x16x32_bf16 v[68:71], a[24:27], a[116:119], v[68:71]\n\t"
    "ds_read_b64_tr_b16 a[20:21], v11 offset:58368\n\t"
    "ds_read_b64_tr_b16 a[22:23], v11 offset:60416\n\t"
    "v_mfma_f32_16x16x32_bf16 v[72:75], a[28:31], a[112:115], v[72:75]\n\t"
    "buffer_load_dwordx4 a[88:91], v1, s[52:55], s72 offen offset:2048 sc1\n\t"
    "v_mfma_f32_16x16x32_bf16 v[76:79], a[28:31], a[116:119], v[76:79]\n\t"
    "s_waitcnt vmcnt(16) lgkmcnt(8)\n\t"
    "s_add_u32 m0, s74, 32768\n\t"
    "v_mfma_f32_16x16x32_bf16 v[16:19], a[0:3], a[120:123], v[16:19]\n\t"
    "s_waitcnt vmcnt(11)\n\t"
    "s_barrier\n\t"
    "ds_read_b64_tr_b16 a[24:25], v10 offset:58880\n\t"
    "ds_read_b64_tr_b16 a[26:27], v10 offset:60928\n\t"
    "v_mfma_f32_16x16x32_bf16 v[20:23], a[0:3], a[124:127], v[20:23]\n\t"
    "ds_read_b64_tr_b16 a[28:29], v11 offset:58880\n\t"
    "ds_read_b64_tr_b16 a[30:31], v11 offset:60928\n\t"
    "v_mfma_f32_16x16x32_bf16 v[24:27], a[4:7], a[120:123], v[24:27]\n\t"
    "buffer_load_dwordx4 a[92:95], v1, s[52:55], s72 offen offset:2304 sc1\n\t"
    "v_mfma_f32_16x16x32_bf16 v[28:31], a[4:7], a[124:127], v[28:31]\n\t"
    "s_waitcnt lgkmcnt(8)\n\t"
    "s_add_u32 s72, s72, 4096\n\t"
    "v_mfma_f32_16x16x32_bf16 v[32:35], a[8:11], a[120:123], v[32:35]\n\t"
    "ds_read_b64_tr_b16 a[0:1], v10 offset:0\n\t"
    "ds_read_b64_tr_b16 a[2:3], v10 offset:2048\n\t"
    "v_mfma_f32_16x16x32_bf16 v[36:39], a[8:11], a[124:127], v[36:39]\n\t"
    "ds_read_b64_tr_b16 a[4:5], v11 offset:0\n\t"
    "ds_read_b64_tr_b16 a[6:7], v11 offset:2048\n\t"
    "v_mfma_f32_16x16x32_bf16 v[40:43], a[12:15], a[120:123], v[40:43]\n\t"
    "buffer_load_dwordx4 v3, s[56:59], s73 offen offset:0 lds\n\t"
    "v_mfma_f32_16x16x32_bf16 v[44:47], a[12:15], a[124:127], v[44:47]\n\t"
    "s_waitcnt lgkmcnt(8)\n\t"
    "v_mfma_f32_16x16x32_bf16 v[48:51], a[16:19], a[120:123], v[48:51]\n\t"
    "ds_read_b64_tr_b16 a[8:9], v10 offset:512\n\t"
    "ds_read_b64_tr_b16 a[10:11], v10 offset:2560\n\t"
    "v_mfma_f32_16x16x32_bf16 v[52:55], a[16:19], a[124:127], v[52:55]\n\t"
    "ds_read_b64_tr_b16 a[12:13], v11 offset:512\n\t"
    "ds_read_b64_tr_b16 a[14:15], v11 offset:2560\n\t"
    "v_mfma_f32_16x16x32_bf16 v[56:59], a[20:23], a[120:123], v[56:59]\n\t"
    "buffer_load_dwordx4 v4, s[56:59], s73 offen offset:1024 lds\n\t"
    "v_mfma_f32_16x16x32_bf16 v[60:63], a[20:23], a[124:127], v[60:63]\n\t"
    "s_waitcnt lgkmcnt(8)\n\t"
    "v_mfma_f32_16x16x32_bf16 v[64:67], a[24:27], a[120:123], v[64:67]\n\t"
    "ds_read_b64_tr_b16 a[16:17], v10 offset:1024\n\t"
    "ds_read_b64_tr_b16 a[18:19], v10 offset:3072\n\t"
    "v_mfma_f32_16x16x32_bf16 v[68:71], a[24:27], a[124:127], v[68:71]\n\t"
    "ds_read_b64_tr_b16 a[20:21], v11 offset:1024\n\t"
    "ds_read_b64_tr_b16 a[22:23], v11 offset:3072\n\t"
    "v_mfma_f32_16x16x32_bf16 v[72:75], a[28:31], a[120:123], v[72:75]\n\t"
    "buffer_load_dwordx4 v5, s[56:59], s73 offen offset:2048 lds\n\t"
    "v_mfma_f32_16x16x32_bf16 v[76:79], a[28:31], a[124:127], v[76:79]\n\t"
    "s_waitcnt vmcnt(13) lgkmcnt(8)\n\t"
    "v_mfma_f32_16x16x32_bf16 v[16:19], a[0:3], a[64:67], v[16:19]\n\t"
    "ds_read_b64_tr_b16 a[24:25], v10 offset:1536\n\t"
    "ds_read_b64_tr_b16 a[26:27], v10 offset:3584\n\t"
    "v_mfma_f32_16x16x32_bf16 v[20:23], a[0:3], a[68:71], v[20:23]\n\t"
    "ds_read_b64_tr_b16 a[28:29], v11 offset:1536\n\t"
    "ds_read_b64_tr_b16 a[30:31], v11 offset:3584\n\t"
    "v_mfma_f32_16x16x32_bf16 v[24:27], a[4:7], a[64:67], v[24:27]\n\t"
    "buffer_load_dwordx4 v6, s[56:59], s73 offen offset:3072 lds\n\t"
    "v_mfma_f32_16x16x32_bf16 v[28:31], a[4:7], a[68:71], v[28:31]\n\t"
    "s_waitcnt lgkmcnt(8)\n\t"
    "s_add_u32 s73, s73, 458752\n\t"
    "v_mfma_f32_16x16x32_bf16 v[32:35], a[8:11], a[64:67], v[32:35]\n\t"
    "ds_read_b64_tr_b16 a[0:1], v10 offset:8192\n\t"
    "ds_read_b64_tr_b16 a[2:3], v10 offset:10240\n\t"
    "v_mfma_f32_16x16x32_bf16 v[36:39], a[8:11], a[68:71], v[36:39]\n\t"
    "ds_read_b64_tr_b16 a[4:5], v11 offset:8192\n\t"
    "ds_read_b64_tr_b16 a[6:7], v11 offset:10240\n\t"
    "v_mfma_f32_16x16x32_bf16 v[40:43], a[12:15], a[64:67], v[40:43]\n\t"
    "buffer_load_dwordx4 a[96:99], v1, s[52:55], s72 offen offset:0 sc1\n\t"
    "v_mfma_f32_16x16x32_bf16 v[44:47], a[12:15], a[68:71], v[44:47]\n\t"
    "s_waitcnt lgkmcnt(8)\n\t"
    "v_mfma_f32_16x16x32_bf16 v[48:51], a[16:19], a[64:67], v[48:51]\n\t"
    "ds_read_b64_tr_b16 a[8:9], v10 offset:8704\n\t"
    "ds_read_b64_tr_b16 a[10:11], v10 offset:10752\n\t"
    "v_mfma_f32_16x16x32_bf16 v[52:55], a[16:19], a[68:71], v[52:55]\n\t"
    "ds_read_b64_tr_b16 a[12:13], v11 offset:8704\n\t"
    "ds_read_b64_tr_b16 a[14:15], v11 offset:10752\n\t"
    "v_mfma_f32_16x16x32_bf16 v[56:59], a[20:23], a[64:67], v[56:59]\n\t"
    "buffer_load_dwordx4 a[100:103], v1, s[52:55], s72 offen offset:256 sc1\n\t"
    "v_mfma_f32_16x16x32_bf16 v[60:63], a[20:23], a[68:71], v[60:63]\n\t"
    "s_waitcnt lgkmcnt(8)\n\t"
    "v_mfma_f32_16x16x32_bf16 v[64:67], a[24:27], a[64:67], v[64:67]\n\t"
    "ds_read_b64_tr_b16 a[16:17], v10 offset:9216\n\t"
    "ds_read_b64_tr_b16 a[18:19], v10 offset:11264\n\t"
    "v_mfma_f32_16x16x32_bf16 v[68:71], a[24:27], a[68:71], v[68:71]\n\t"
    "ds_read_b64_tr_b16 a[20:21], v11 offset:9216\n\t"
    "ds_read_b64_tr_b16 a[22:23], v11 offset:11264\n\t"
    "v_mfma_f32_16x16x32_bf16 v[72:75], a[28:31], a[64:67], v[72:75]\n\t"
    "buffer_load_dwordx4 a[104:107], v1, s[52:55], s72 offen offset:2048 sc1\n\t"
    "v_mfma_f32_16x16x32_bf16 v[76:79], a[28:31], a[68:71], v[76:79]\n\t"
    "s_waitcnt vmcnt(15) lgkmcnt(8)\n\t"
    "s_add_u32 m0, s74, 49152\n\t"
    "v_mfma_f32_16x16x32_bf16 v[16:19], a[0:3], a[72:75], v[16:19]\n\t"
    "s_waitcnt vmcnt(11)\n\t"
    "s_barrier\n\t"
    "ds_read_b64_tr_b16 a[24:25], v10 offset:9728\n\t"
    "ds_read_b64_tr_b16 a[26:27], v10 offset:11776\n\t"
    "v_mfma_f32_16x16x32_bf16 v[20:23], a[0:3], a[76:79], v[20:23]\n\t"
    "ds_read_b64_tr_b16 a[28:29], v11 offset:9728\n\t"
    "ds_read_b64_tr_b16 a[30:31], v11 offset:11776\n\t"
    "v_mfma_f32_16x16x32_bf16 v[24:27], a[4:7], a[72:75], v[24:27]\n\t"
    "buffer_load_dwordx4 a[108:111], v1, s[52:55], s72 offen offset:2304 sc1\n\t"
    "v_mfma_f32_16x16x32_bf16 v[28:31], a[4:7], a[76:79], v[28:31]\n\t"
    "s_waitcnt lgkmcnt(8)\n\t"
    "s_add_u32 s72, s72, 4096\n\t"
    "v_mfma_f32_16x16x32_bf16 v[32:35], a[8:11], a[72:75], v[32:35]\n\t"
    "ds_read_b64_tr_b16 a[0:1], v10 offset:16384\n\t"
    "ds_read_b64_tr_b16 a[2:3], v10 offset:18432\n\t"
    "v_mfma_f32_16x16x32_bf16 v[36:39], a[8:11], a[76:79], v[36:39]\n\t"
    "ds_read_b64_tr_b16 a[4:5], v11 offset:16384\n\t"
    "ds_read_b64_tr_b16 a[6:7], v11 offset:18432\n\t"
    "v_mfma_f32_16x16x32_bf16 v[40:43], a[12:15], a[72:75], v[40:43]\n\t"
    "buffer_load_dwordx4 v3, s[56:59], s73 offen offset:0 lds\n\t"
    "v_mfma_f32_16x16x32_bf16 v[44:47], a[12:15], a[76:79], v[44:47]\n\t"
    "s_waitcnt lgkmcnt(8)\n\t"
    "v_mfma_f32_16x16x32_bf16 v[48:51], a[16:19], a[72:75], v[48:51]\n\t"
    "ds_read_b64_tr_b16 a[8:9], v10 offset:16896\n\t"
    "ds_read_b64_tr_b16 a[10:11], v10 offset:18944\n\t"
    "v_mfma_f32_16x16x32_bf16 v[52:55], a[16:19], a[76:79], v[52:55]\n\t"
    "ds_read_b64_tr_b16 a[12:13], v11 offset:16896\n\t"
    "ds_read_b64_tr_b16 a[14:15], v11 offset:18944\n\t"
    "v_mfma_f32_16x16x32_bf16 v[56:59], a[20:23], a[72:75], v[56:59]\n\t"
    "buffer_load_dwordx4 v4, s[56:59], s73 offen offset:1024 lds\n\t"
    "v_mfma_f32_16x16x32_bf16 v[60:63], a[20:23], a[76:79], v[60:63]\n\t"
    "s_waitcnt lgkmcnt(8)\n\t"
    "v_mfma_f32_16x16x32_bf16 v[64:67], a[24:27], a[72:75], v[64:67]\n\t"
    "ds_read_b64_tr_b16 a[16:17], v10 offset:17408\n\t"
    "ds_read_b64_tr_b16 a[18:19], v10 offset:19456\n\t"
    "v_mfma_f32_16x16x32_bf16 v[68:71], a[24:27], a[76:79], v[68:71]\n\t"
    "ds_read_b64_tr_b16 a[20:21], v11 offset:17408\n\t"
    "ds_read_b64_tr_b16 a[22:23], v11 offset:19456\n\t"
    "v_mfma_f32_16x16x32_bf16 v[72:75], a[28:31], a[72:75], v[72:75]\n\t"
    "buffer_load_dwordx4 v5, s[56:59], s73 offen offset:2048 lds\n\t"
    "v_mfma_f32_16x16x32_bf16 v[76:79], a[28:31], a[76:79], v[76:79]\n\t"
    "s_waitcnt vmcnt(13) lgkmcnt(8)\n\t"
    "s_sub_u32 s79, s79, 1\n\t"
    "s_cmp_lg_u32 s79, 0\n\t"
    "s_cbranch_scc1 Lm_down_0_0\n\t"
    "v_cmp_ne_u32 vcc, %4, v249\n\t"
    "s_cbranch_vccz 2f\n\t"
    "s_mov_b32 s86, 0x400000\n\t"
    "1:\n\t"
    "global_load_dword v249, v248, s[48:49] sc1\n\t"
    "s_waitcnt vmcnt(0)\n\t"
    "v_cmp_ne_u32 vcc, %4, v249\n\t"
    "s_sub_u32 s86, s86, 1\n\t"
    "s_cmp_eq_u32 s86, 0\n\t"
    "s_cbranch_scc1 2f\n\t"
    "s_cbranch_vccz 2f\n\t"
    "s_sleep 1\n\t"
    "s_branch 1b\n\t"
    "2:\n\t"
    "v_mfma_f32_16x16x32_bf16 v[16:19], a[0:3], a[80:83], v[16:19]\n\t"
    "ds_read_b64_tr_b16 a[24:25], v10 offset:17920\n\t"
    "ds_read_b64_tr_b16 a[26:27], v10 offset:19968\n\t"
    "v_mfma_f32_16x16x32_bf16 v[20:23], a[0:3], a[84:87], v[20:23]\n\t"
    "ds_read_b64_tr_b16 a[28:29], v11 offset:17920\n\t"
    "ds_read_b64_tr_b16 a[30:31], v11 offset:19968\n\t"
    "v_mfma_f32_16x16x32_bf16 v[24:27], a[4:7], a[80:83], v[24:27]\n\t"
    "buffer_load_dwordx4 v6, s[56:59], s73 offen offset:3072 lds\n\t"
    "v_mfma_f32_16x16x32_bf16 v[28:31], a[4:7], a[84:87], v[28:31]\n\t"
    "s_waitcnt lgkmcnt(8)\n\t"
    "s_add_u32 s73, s73, s87\n\t"
    "v_mfma_f32_16x16x32_bf16 v[32:35], a[8:11], a[80:83], v[32:35]\n\t"
    "ds_read_b64_tr_b16 a[0:1], v10 offset:24576\n\t"
    "ds_read_b64_tr_b16 a[2:3], v10 offset:26624\n\t"
    "v_mfma_f32_16x16x32_bf16 v[36:39], a[8:11], a[84:87], v[36:39]\n\t"
    "ds_read_b64_tr_b16 a[4:5], v11 offset:24576\n\t"
    "ds_read_b64_tr_b16 a[6:7], v11 offset:26624\n\t"
    "v_mfma_f32_16x16x32_bf16 v[40:43], a[12:15], a[80:83], v[40:43]\n\t"
    "buffer_load_dwordx4 a[112:115], v1, s[52:55], s72 offen offset:0 sc1\n\t"
    "v_mfma_f32_16x16x32_bf16 v[44:47], a[12:15], a[84:87], v[44:47]\n\t"
    "s_waitcnt lgkmcnt(8)\n\t"
    "s_xor_b32 s87, s87, 4292280320\n\ts_cmp_eq_u32 s51, 24\n\ts_cselect_b32 s87, s90, s87\n\t"
    "v_mfma_f32_16x16x32_bf16 v[48:51], a[16:19], a[80:83], v[48:51]\n\t"
    "ds_read_b64_tr_b16 a[8:9], v10 offset:25088\n\t"
    "ds_read_b64_tr_b16 a[10:11], v10 offset:27136\n\t"
    "v_mfma_f32_16x16x32_bf16 v[52:55], a[16:19], a[84:87], v[52:55]\n\t"
    "ds_read_b64_tr_b16 a[12:13], v11 offset:25088\n\t"
    "ds_read_b64_tr_b16 a[14:15], v11 offset:27136\n\t"
    "v_mfma_f32_16x16x32_bf16 v[56:59], a[20:23], a[80:83], v[56:59]\n\t"
    "buffer_load_dwordx4 a[116:119], v1, s[52:55], s72 offen offset:256 sc1\n\t"
    "v_mfma_f32_16x16x32_bf16 v[60:63], a[20:23], a[84:87], v[60:63]\n\t"
    "s_waitcnt lgkmcnt(8)\n\t"
    "v_mfma_f32_16x16x32_bf16 v[64:67], a[24:27], a[80:83], v[64:67]\n\t"
    "ds_read_b64_tr_b16 a[16:17], v10 offset:25600\n\t"
    "ds_read_b64_tr_b16 a[18:19], v10 offset:27648\n\t"
    "v_mfma_f32_16x16x32_bf16 v[68:71], a[24:27], a[84:87], v[68:71]\n\t"
    "ds_read_b64_tr_b16 a[20:21], v11 offset:25600\n\t"
    "ds_read_b64_tr_b16 a[22:23], v11 offset:27648\n\t"
    "v_mfma_f32_16x16x32_bf16 v[72:75], a[28:31], a[80:83], v[72:75]\n\t"
    "buffer_load_dwordx4 a[120:123], v1, s[52:55], s72 offen offset:2048 sc1\n\t"
    "v_mfma_f32_16x16x32_bf16 v[76:79], a[28:31], a[84:87], v[76:79]\n\t"
    "s_waitcnt vmcnt(15) lgkmcnt(8)\n\t"
    "s_add_u32 m0, s74, 0\n\t"
    "v_mfma_f32_16x16x32_bf16 v[16:19], a[0:3], a[88:91], v[16:19]\n\t"
    "s_waitcnt vmcnt(11)\n\t"
    "s_barrier\n\t"
    "ds_read_b64_tr_b16 a[24:25], v10 offset:26112\n\t"
    "ds_read_b64_tr_b16 a[26:27], v10 offset:28160\n\t"
    "v_mfma_f32_16x16x32_bf16 v[20:23], a[0:3], a[92:95], v[20:23]\n\t"
    "ds_read_b64_tr_b16 a[28:29], v11 offset:26112\n\t"
    "ds_read_b64_tr_b16 a[30:31], v11 offset:28160\n\t"
    "v_mfma_f32_16x16x32_bf16 v[24:27], a[4:7], a[88:91], v[24:27]\n\t"
    "buffer_load_dwordx4 a[124:127], v1, s[52:55], s72 offen offset:2304 sc1\n\t"
    "v_mfma_f32_16x16x32_bf16 v[28:31], a[4:7], a[92:95], v[28:31]\n\t"
    "s_waitcnt lgkmcnt(8)\n\t"
    "s_add_u32 s72, s72, 4096\n\t"
    "v_mfma_f32_16x16x32_bf16 v[32:35], a[8:11], a[88:91], v[32:35]\n\t"
    "ds_read_b64_tr_b16 a[0:1], v10 offset:32768\n\t"
    "ds_read_b64_tr_b16 a[2:3], v10 offset:34816\n\t"
    "v_mfma_f32_16x16x32_bf16 v[36:39], a[8:11], a[92:95], v[36:39]\n\t"
    "ds_read_b64_tr_b16 a[4:5], v11 offset:32768\n\t"
    "ds_read_b64_tr_b16 a[6:7], v11 offset:34816\n\t"
    "v_mfma_f32_16x16x32_bf16 v[40:43], a[12:15], a[88:91], v[40:43]\n\t"
    "buffer_load_dwordx4 v3, s[56:59], s73 offen offset:0 lds\n\t"
    "v_mfma_f32_16x16x32_bf16 v[44:47], a[12:15], a[92:95], v[44:47]\n\t"
    "s_waitcnt lgkmcnt(8)\n\t"
    "v_mfma_f32_16x16x32_bf16 v[48:51], a[16:19], a[88:91], v[48:51]\n\t"
    "ds_read_b64_tr_b16 a[8:9], v10 offset:33280\n\t"
    "ds_read_b64_tr_b16 a[10:11], v10 offset:35328\n\t"
    "v_mfma_f32_16x16x32_bf16 v[52:55], a[16:19], a[92:95], v[52:55]\n\t"
    "ds_read_b64_tr_b16 a[12:13], v11 offset:33280\n\t"
    "ds_read_b64_tr_b16 a[14:15], v11 offset:35328\n\t"
    "v_mfma_f32_16x16x32_bf16 v[56:59], a[20:23], a[88:91], v[56:59]\n\t"
    "buffer_load_dwordx4 v4, s[56:59], s73 offen offset:1024 lds\n\t"
    "v_mfma_f32_16x16x32_bf16 v[60:63], a[20:23], a[92:95], v[60:63]\n\t"
    "s_waitcnt lgkmcnt(8)\n\t"
    "v_mfma_f32_16x16x32_bf16 v[64:67], a[24:27], a[88:91], v[64:67]\n\t"
    "ds_read_b64_tr_b16 a[16:17], v10 offset:33792\n\t"
    "ds_read_b64_tr_b16 a[18:19], v10 offset:35840\n\t"
    "v_mfma_f32_16x16x32_bf16 v[68:71], a[24:27], a[92:95], v[68:71]\n\t"
    "ds_read_b64_tr_b16 a[20:21], v11 offset:33792\n\t"
    "ds_read_b64_tr_b16 a[22:23], v11 offset:35840\n\t"
    "v_mfma_f32_16x16x32_bf16 v[72:75], a[28:31], a[88:91], v[72:75]\n\t"
    "buffer_load_dwordx4 v5, s[56:59], s73 offen offset:2048 lds\n\t"
    "v_mfma_f32_16x16x32_bf16 v[76:79], a[28:31], a[92:95], v[76:79]\n\t"
    "s_waitcnt vmcnt(13) lgkmcnt(8)\n\t"
    "s_lshr_b32 s98, s51, 3\n\t"
    "s_lshl_b32 s98, s98, 14\n\t"
    "v_add_u32 v248, s98, v250\n\t"
    "global_load_dword v249, v248, s[48:49] sc1\n\t"
    "s_add_u32 s51, s51, 4\n\t"
    "v_mfma_f32_16x16x32_bf16 v[16:19], a[0:3], a[96:99], v[16:19]\n\t"
    "ds_read_b64_tr_b16 a[24:25], v10 offset:34304\n\t"
    "ds_read_b64_tr_b16 a[26:27], v10 offset:36352\n\t"
    "v_mfma_f32_16x16x32_bf16 v[20:23], a[0:3], a[100:103], v[20:23]\n\t"
    "ds_read_b64_tr_b16 a[28:29], v11 offset:34304\n\t"
    "ds_read_b64_tr_b16 a[30:31], v11 offset:36352\n\t"
    "v_mfma_f32_16x16x32_bf16 v[24:27], a[4:7], a[96:99], v[24:27]\n\t"
    "buffer_load_dwordx4 v6, s[56:59], s73 offen offset:3072 lds\n\t"
    "v_mfma_f32_16x16x32_bf16 v[28:31], a[4:7], a[100:103], v[28:31]\n\t"
    "s_waitcnt lgkmcnt(8)\n\t"
    "s_add_u32 s73, s73, 917504\n\t"
    "v_mfma_f32_16x16x32_bf16 v[32:35], a[8:11], a[96:99], v[32:35]\n\t"
    "ds_read_b64_tr_b16 a[0:1], v10 offset:40960\n\t"
    "ds_read_b64_tr_b16 a[2:3], v10 offset:43008\n\t"
    "v_mfma_f32_16x16x32_bf16 v[36:39], a[8:11], a[100:103], v[36:39]\n\t"
    "ds_read_b64_tr_b16 a[4:5], v11 offset:40960\n\t"
    "ds_read_b64_tr_b16 a[6:7], v11 offset:43008\n\t"
    "v_mfma_f32_16x16x32_bf16 v[40:43], a[12:15], a[96:99], v[40:43]\n\t"
    "buffer_load_dwordx4 a[64:67], v1, s[52:55], s72 offen offset:0 sc1\n\t"
    "v_mfma_f32_16x16x32_bf16 v[44:47], a[12:15], a[100:103], v[44:47]\n\t"
    "s_waitcnt lgkmcnt(8)\n\t"
    "v_mfma_f32_16x16x32_bf16 v[48:51], a[16:19], a[96:99], v[48:51]\n\t"
    "ds_read_b64_tr_b16 a[8:9], v10 offset:41472\n\t"
    "ds_read_b64_tr_b16 a[10:11], v10 offset:43520\n\t"
    "v_mfma_f32_16x16x32_bf16 v[52:55], a[16:19], a[100:103], v[52:55]\n\t"
    "ds_read_b64_tr_b16 a[12:13], v11 offset:41472\n\t"
    "ds_read_b64_tr_b16 a[14:15], v11 offset:43520\n\t"
    "v_mfma_f32_16x16x32_bf16 v[56:59], a[20:23], a[96:99], v[56:59]\n\t"
    "buffer_load_dwordx4 a[68:71], v1, s[52:55], s72 offen offset:256 sc1\n\t"
    "v_mfma_f32_16x16x32_bf16 v[60:63], a[20:23], a[100:103], v[60:63]\n\t"
    "s_waitcnt lgkmcnt(8)\n\t"
    "v_mfma_f32_16x16x32_bf16 v[64:67], a[24:27], a[96:99], v[64:67]\n\t"
    "ds_read_b64_tr_b16 a[16:17], v10 offset:41984\n\t"
    "ds_read_b64_tr_b16 a[18:19], v10 offset:44032\n\t"
    "v_mfma_f32_16x16x32_bf16 v[68:71], a[24:27], a[100:103], v[68:71]\n\t"
    "ds_read_b64_tr_b16 a[20:21], v11 offset:41984\n\t"
    "ds_read_b64_tr_b16 a[22:23], v11 offset:44032\n\t"
    "v_mfma_f32_16x16x32_bf16 v[72:75], a[28:31], a[96:99], v[72:75]\n\t"
    "buffer_load_dwordx4 a[72:75], v1, s[52:55], s72 offen offset:2048 sc1\n\t"
    "v_mfma_f32_16x16x32_bf16 v[76:79], a[28:31], a[100:103], v[76:79]\n\t"
    "s_waitcnt vmcnt(16) lgkmcnt(8)\n\t"
    "s_add_u32 m0, s74, 16384\n\t"
    "v_mfma_f32_16x16x32_bf16 v[16:19], a[0:3], a[104:107], v[16:19]\n\t"
    "s_waitcnt vmcnt(12)\n\t"
    "s_barrier\n\t"
    "ds_read_b64_tr_b16 a[24:25], v10 offset:42496\n\t"
    "ds_read_b64_tr_b16 a[26:27], v10 offset:44544\n\t"
    "v_mfma_f32_16x16x32_bf16 v[20:23], a[0:3], a[108:111], v[20:23]\n\t"
    "ds_read_b64_tr_b16 a[28:29], v11 offset:42496\n\t"
    "ds_read_b64_tr_b16 a[30:31], v11 offset:44544\n\t"
    "v_mfma_f32_16x16x32_bf16 v[24:27], a[4:7], a[104:107], v[24:27]\n\t"
    "buffer_load_dwordx4 a[76:79], v1, s[52:55], s72 offen offset:2304 sc1\n\t"
    "v_mfma_f32_16x16x32_bf16 v[28:31], a[4:7], a[108:111], v[28:31]\n\t"
    "s_waitcnt lgkmcnt(8)\n\t"
    "s_add_u32 s72, s72, 4096\n\t"
    "v_mfma_f32_16x16x32_bf16 v[32:35], a[8:11], a[104:107], v[32:35]\n\t"
    "ds_read_b64_tr_b16 a[0:1], v10 offset:49152\n\t"
    "ds_read_b64_tr_b16 a[2:3], v10 offset:51200\n\t"
    "v_mfma_f32_16x16x32_bf16 v[36:39], a[8:11], a[108:111], v[36:39]\n\t"
    "ds_read_b64_tr_b16 a[4:5], v11 offset:49152\n\t"
    "ds_read_b64_tr_b16 a[6:7], v11 offset:51200\n\t"
    "v_mfma_f32_16x16x32_bf16 v[40:43], a[12:15], a[104:107], v[40:43]\n\t"
    "buffer_load_dwordx4 v3, s[56:59], s73 offen offset:0 lds\n\t"
    "v_mfma_f32_16x16x32_bf16 v[44:47], a[12:15], a[108:111], v[44:47]\n\t"
    "s_waitcnt lgkmcnt(8)\n\t"
    "v_mfma_f32_16x16x32_bf16 v[48:51], a[16:19], a[104:107], v[48:51]\n\t"
    "ds_read_b64_tr_b16 a[8:9], v10 offset:49664\n\t"
    "ds_read_b64_tr_b16 a[10:11], v10 offset:51712\n\t"
    "v_mfma_f32_16x16x32_bf16 v[52:55], a[16:19], a[108:111], v[52:55]\n\t"
    "ds_read_b64_tr_b16 a[12:13], v11 offset:49664\n\t"
    "ds_read_b64_tr_b16 a[14:15], v11 offset:51712\n\t"
    "v_mfma_f32_16x16x32_bf16 v[56:59], a[20:23], a[104:107], v[56:59]\n\t"
    "buffer_load_dwordx4 v4, s[56:59], s73 offen offset:1024 lds\n\t"
    "v_mfma_f32_16x16x32_bf16 v[60:63], a[20:23], a[108:111], v[60:63]\n\t"
    "s_waitcnt lgkmcnt(8)\n\t"
    "v_mfma_f32_16x16x32_bf16 v[64:67], a[24:27], a[104:107], v[64:67]\n\t"
    "ds_read_b64_tr_b16 a[16:17], v10 offset:50176\n\t"
    "ds_read_b64_tr_b16 a[18:19], v10 offset:52224\n\t"
    "v_mfma_f32_16x16x32_bf16 v[68:71], a[24:27], a[108:111], v[68:71]\n\t"
    "ds_read_b64_tr_b16 a[20:21], v11 offset:50176\n\t"
    "ds_read_b64_tr_b16 a[22:23], v11 offset:52224\n\t"
    "v_mfma_f32_16x16x32_bf16 v[72:75], a[28:31], a[104:107], v[72:75]\n\t"
    "buffer_load_dwordx4 v5, s[56:59], s73 offen offset:2048 lds\n\t"
    "v_mfma_f32_16x16x32_bf16 v[76:79], a[28:31], a[108:111], v[76:79]\n\t"
    "s_waitcnt vmcnt(14) lgkmcnt(8)\n\t"
    "v_mfma_f32_16x16x32_bf16 v[16:19], a[0:3], a[112:115], v[16:19]\n\t"
    "ds_read_b64_tr_b16 a[24:25], v10 offset:50688\n\t"
    "ds_read_b64_tr_b16 a[26:27], v10 offset:52736\n\t"
    "v_mfma_f32_16x16x32_bf16 v[20:23], a[0:3], a[116:119], v[20:23]\n\t"
    "ds_read_b64_tr_b16 a[28:29], v11 offset:50688\n\t"
    "ds_read_b64_tr_b16 a[30:31], v11 offset:52736\n\t"
    "v_mfma_f32_16x16x32_bf16 v[24:27], a[4:7], a[112:115], v[24:27]\n\t"
    "buffer_load_dwordx4 v6, s[56:59], s73 offen offset:3072 lds\n\t"
    "v_mfma_f32_16x16x32_bf16 v[28:31], a[4:7], a[116:119], v[28:31]\n\t"
    "s_waitcnt lgkmcnt(8)\n\t"
    "s_add_u32 s73, s73, 917504\n\t"
    "v_mfma_f32_16x16x32_bf16 v[32:35], a[8:11], a[112:115], v[32:35]\n\t"
    "ds_read_b64_tr_b16 a[0:1], v10 offset:57344\n\t"
    "ds_read_b64_tr_b16 a[2:3], v10 offset:59392\n\t"
    "v_mfma_f32_16x16x32_bf16 v[36:39], a[8:11], a[116:119], v[36:39]\n\t"
    "ds_read_b64_tr_b16 a[4:5], v11 offset:57344\n\t"
    "ds_read_b64_tr_b16 a[6:7], v11 offset:59392\n\t"
    "v_mfma_f32_16x16x32_bf16 v[40:43], a[12:15], a[112:115], v[40:43]\n\t"
    "buffer_load_dwordx4 a[80:83], v1, s[52:55], s72 offen offset:0 sc1\n\t"
    "v_mfma_f32_16x16x32_bf16 v[44:47], a[12:15], a[116:119], v[44:47]\n\t"
    "s_waitcnt lgkmcnt(8)\n\t"
    "v_mfma_f32_16x16x32_bf16 v[48:51], a[16:19], a[112:115], v[48:51]\n\t"
    "ds_read_b64_tr_b16 a[8:9], v10 offset:57856\n\t"
    "ds_read_b64_tr_b16 a[10:11], v10 offset:59904\n\t"
    "v_mfma_f32_16x16x32_bf16 v[52:55], a[16:19], a[116:119], v[52:55]\n\t"
    "ds_read_b64_tr_b16 a[12:13], v11 offset:57856\n\t"
    "ds_read_b64_tr_b16 a[14:15], v11 offset:59904\n\t"
    "v_mfma_f32_16x16x32_bf16 v[56:59], a[20:23], a[112:115], v[56:59]\n\t"
    "buffer_load_dwordx4 a[84:87], v1, s[52:55], s72 offen offset:256 sc1\n\t"
    "v_mfma_f32_16x16x32_bf16 v[60:63], a[20:23], a[116:119], v[60:63]\n\t"
    "s_waitcnt lgkmcnt(8)\n\t"
    "v_mfma_f32_16x16x32_bf16 v[64:67], a[24:27], a[112:115], v[64:67]\n\t"
    "ds_read_b64_tr_b16 a[16:17], v10 offset:58368\n\t"
    "ds_read_b64_tr_b16 a[18:19], v10 offset:60416\n\t"
    "v_mfma_f32_16x16x32_bf16 v[68:71], a[24:27], a[116:119], v[68:71]\n\t"
    "ds_read_b64_tr_b16 a[20:21], v11 offset:58368\n\t"
    "ds_read_b64_tr_b16 a[22:23], v11 offset:60416\n\t"
    "v_mfma_f32_16x16x32_bf16 v[72:75], a[28:31], a[112:115], v[72:75]\n\t"
    "buffer_load_dwordx4 a[88:91], v1, s[52:55], s72 offen offset:2048 sc1\n\t"
    "v_mfma_f32_16x16x32_bf16 v[76:79], a[28:31], a[116:119], v[76:79]\n\t"
    "s_waitcnt vmcnt(16) lgkmcnt(8)\n\t"
    "s_add_u32 m0, s74, 32768\n\t"
    "v_mfma_f32_16x16x32_bf16 v[16:19], a[0:3], a[120:123], v[16:19]\n\t"
    "s_waitcnt vmcnt(11)\n\t"
    "s_barrier\n\t"
    "ds_read_b64_tr_b16 a[24:25], v10 offset:58880\n\t"
    "ds_read_b64_tr_b16 a[26:27], v10 offset:60928\n\t"
    "v_mfma_f32_16x16x32_bf16 v[20:23], a[0:3], a[124:127], v[20:23]\n\t"
    "ds_read_b64_tr_b16 a[28:29], v11 offset:58880\n\t"
    "ds_read_b64_tr_b16 a[30:31], v11 offset:60928\n\t"
    "v_mfma_f32_16x16x32_bf16 v[24:27], a[4:7], a[120:123], v[24:27]\n\t"
    "buffer_load_dwordx4 a[92:95], v1, s[52:55], s72 offen offset:2304 sc1\n\t"
    "v_mfma_f32_16x16x32_bf16 v[28:31], a[4:7], a[124:127], v[28:31]\n\t"
    "s_waitcnt lgkmcnt(8)\n\t"
    "s_add_u32 s72, s72, 4096\n\t"
    "v_mfma_f32_16x16x32_bf16 v[32:35], a[8:11], a[120:123], v[32:35]\n\t"
    "ds_read_b64_tr_b16 a[0:1], v10 offset:0\n\t"
    "ds_read_b64_tr_b16 a[2:3], v10 offset:2048\n\t"
    "v_mfma_f32_16x16x32_bf16 v[36:39], a[8:11], a[124:127], v[36:39]\n\t"
    "ds_read_b64_tr_b16 a[4:5], v11 offset:0\n\t"
    "ds_read_b64_tr_b16 a[6:7], v11 offset:2048\n\t"
    "v_mfma_f32_16x16x32_bf16 v[40:43], a[12:15], a[120:123], v[40:43]\n\t"
    "buffer_load_dwordx4 v3, s[56:59], s73 offen offset:0 lds\n\t"
    "v_mfma_f32_16x16x32_bf16 v[44:47], a[12:15], a[124:127], v[44:47]\n\t"
    "s_waitcnt lgkmcnt(8)\n\t"
    "v_mfma_f32_16x16x32_bf16 v[48:51], a[16:19], a[120:123], v[48:51]\n\t"
    "ds_read_b64_tr_b16 a[8:9], v10 offset:512\n\t"
    "ds_read_b64_tr_b16 a[10:11], v10 offset:2560\n\t"
    "v_mfma_f32_16x16x32_bf16 v[52:55], a[16:19], a[124:127], v[52:55]\n\t"
    "ds_read_b64_tr_b16 a[12:13], v11 offset:512\n\t"
    "ds_read_b64_tr_b16 a[14:15], v11 offset:2560\n\t"
    "v_mfma_f32_16x16x32_bf16 v[56:59], a[20:23], a[120:123], v[56:59]\n\t"
    "buffer_load_dwordx4 v4, s[56:59], s73 offen offset:1024 lds\n\t"
    "v_mfma_f32_16x16x32_bf16 v[60:63], a[20:23], a[124:127], v[60:63]\n\t"
    "s_waitcnt lgkmcnt(8)\n\t"
    "v_mfma_f32_16x16x32_bf16 v[64:67], a[24:27], a[120:123], v[64:67]\n\t"
    "ds_read_b64_tr_b16 a[16:17], v10 offset:1024\n\t"
    "ds_read_b64_tr_b16 a[18:19], v10 offset:3072\n\t"
    "v_mfma_f32_16x16x32_bf16 v[68:71], a[24:27], a[124:127], v[68:71]\n\t"
    "ds_read_b64_tr_b16 a[20:21], v11 offset:1024\n\t"
    "ds_read_b64_tr_b16 a[22:23], v11 offset:3072\n\t"
    "v_mfma_f32_16x16x32_bf16 v[72:75], a[28:31], a[120:123], v[72:75]\n\t"
    "buffer_load_dwordx4 v5, s[56:59], s73 offen offset:2048 lds\n\t"
    "v_mfma_f32_16x16x32_bf16 v[76:79], a[28:31], a[124:127], v[76:79]\n\t"
    "s_waitcnt vmcnt(13) lgkmcnt(8)\n\t"
    "v_mfma_f32_16x16x32_bf16 v[16:19], a[0:3], a[64:67], v[16:19]\n\t"
    "ds_read_b64_tr_b16 a[24:25], v10 offset:1536\n\t"
    "ds_read_b64_tr_b16 a[26:27], v10 offset:3584\n\t"
    "v_mfma_f32_16x16x32_bf16 v[20:23], a[0:3], a[68:71], v[20:23]\n\t"
    "ds_read_b64_tr_b16 a[28:29], v11 offset:1536\n\t"
    "ds_read_b64_tr_b16 a[30:31], v11 offset:3584\n\t"
    "v_mfma_f32_16x16x32_bf16 v[24:27], a[4:7], a[64:67], v[24:27]\n\t"
    "buffer_load_dwordx4 v6, s[56:59], s73 offen offset:3072 lds\n\t"
    "v_mfma_f32_16x16x32_bf16 v[28:31], a[4:7], a[68:71], v[28:31]\n\t"
    "s_waitcnt lgkmcnt(8)\n\t"
    "s_add_u32 s73, s73, 917504\n\t"
    "v_mfma_f32_16x16x32_bf16 v[32:35], a[8:11], a[64:67], v[32:35]\n\t"
    "ds_read_b64_tr_b16 a[0:1], v10 offset:8192\n\t"
    "ds_read_b64_tr_b16 a[2:3], v10 offset:10240\n\t"
    "v_mfma_f32_16x16x32_bf16 v[36:39], a[8:11], a[68:71], v[36:39]\n\t"
    "ds_read_b64_tr_b16 a[4:5], v11 offset:8192\n\t"
    "ds_read_b64_tr_b16 a[6:7], v11 offset:10240\n\t"
    "v_mfma_f32_16x16x32_bf16 v[40:43], a[12:15], a[64:67], v[40:43]\n\t"
    "buffer_load_dwordx4 a[96:99], v1, s[52:55], s72 offen offset:0 sc1\n\t"
    "v_mfma_f32_16x16x32_bf16 v[44:47], a[12:15], a[68:71], v[44:47]\n\t"
    "s_waitcnt lgkmcnt(8)\n\t"
    "v_mfma_f32_16x16x32_bf16 v[48:51], a[16:19], a[64:67], v[48:51]\n\t"
    "ds_read_b64_tr_b16 a[8:9], v10 offset:8704\n\t"
    "ds_read_b64_tr_b16 a[10:11], v10 offset:10752\n\t"
    "v_mfma_f32_16x16x32_bf16 v[52:55], a[16:19], a[68:71], v[52:55]\n\t"
    "ds_read_b64_tr_b16 a[12:13], v11 offset:8704\n\t"
    "ds_read_b64_tr_b16 a[14:15], v11 offset:10752\n\t"
    "v_mfma_f32_16x16x32_bf16 v[56:59], a[20:23], a[64:67], v[56:59]\n\t"
    "buffer_load_dwordx4 a[100:103], v1, s[52:55], s72 offen offset:256 sc1\n\t"
    "v_mfma_f32_16x16x32_bf16 v[60:63], a[20:23], a[68:71], v[60:63]\n\t"
    "s_waitcnt lgkmcnt(8)\n\t"
    "v_mfma_f32_16x16x32_bf16 v[64:67], a[24:27], a[64:67], v[64:67]\n\t"
    "ds_read_b64_tr_b16 a[16:17], v10 offset:9216\n\t"
    "ds_read_b64_tr_b16 a[18:19], v10 offset:11264\n\t"
    "v_mfma_f32_16x16x32_bf16 v[68:71], a[24:27], a[68:71], v[68:71]\n\t"
    "ds_read_b64_tr_b16 a[20:21], v11 offset:9216\n\t"
    "ds_read_b64_tr_b16 a[22:23], v11 offset:11264\n\t"
    "v_mfma_f32_16x16x32_bf16 v[72:75], a[28:31], a[64:67], v[72:75]\n\t"
    "buffer_load_dwordx4 a[104:107], v1, s[52:55], s72 offen offset:2048 sc1\n\t"
    "v_mfma_f32_16x16x32_bf16 v[76:79], a[28:31], a[68:71], v[76:79]\n\t"
    "s_waitcnt vmcnt(15) lgkmcnt(8)\n\t"
    "s_add_u32 m0, s74, 49152\n\t"
    "v_mfma_f32_16x16x32_bf16 v[16:19], a[0:3], a[72:75], v[16:19]\n\t"
    "s_waitcnt vmcnt(11)\n\t"
    "s_barrier\n\t"
    "ds_read_b64_tr_b16 a[24:25], v10 offset:9728\n\t"
    "ds_read_b64_tr_b16 a[26:27], v10 offset:11776\n\t"
    "v_mfma_f32_16x16x32_bf16 v[20:23], a[0:3], a[76:79], v[20:23]\n\t"
    "ds_read_b64_tr_b16 a[28:29], v11 offset:9728\n\t"
    "ds_read_b64_tr_b16 a[30:31], v11 offset:11776\n\t"
    "v_mfma_f32_16x16x32_bf16 v[24:27], a[4:7], a[72:75], v[24:27]\n\t"
    "buffer_load_dwordx4 a[108:111], v1, s[52:55], s72 offen offset:2304 sc1\n\t"
    "v_mfma_f32_16x16x32_bf16 v[28:31], a[4:7], a[76:79], v[28:31]\n\t"
    "s_waitcnt lgkmcnt(8)\n\t"
    "s_add_u32 s72, s72, 4096\n\t"
    "v_mfma_f32_16x16x32_bf16 v[32:35], a[8:11], a[72:75], v[32:35]\n\t"
    "ds_read_b64_tr_b16 a[0:1], v10 offset:16384\n\t"
    "ds_read_b64_tr_b16 a[2:3], v10 offset:18432\n\t"
    "v_mfma_f32_16x16x32_bf16 v[36:39], a[8:11], a[76:79], v[36:39]\n\t"
    "ds_read_b64_tr_b16 a[4:5], v11 offset:16384\n\t"
    "ds_read_b64_tr_b16 a[6:7], v11 offset:18432\n\t"
    "v_mfma_f32_16x16x32_bf16 v[40:43], a[12:15], a[72:75], v[40:43]\n\t"
    "buffer_load_dwordx4 v3, s[56:59], s73 offen offset:0 lds\n\t"
    "v_mfma_f32_16x16x32_bf16 v[44:47], a[12:15], a[76:79], v[44:47]\n\t"
    "s_waitcnt lgkmcnt(8)\n\t"
    "v_mfma_f32_16x16x32_bf16 v[48:51], a[16:19], a[72:75], v[48:51]\n\t"
    "ds_read_b64_tr_b16 a[8:9], v10 offset:16896\n\t"
    "ds_read_b64_tr_b16 a[10:11], v10 offset:18944\n\t"
    "v_mfma_f32_16x16x32_bf16 v[52:55], a[16:19], a[76:79], v[52:55]\n\t"
    "ds_read_b64_tr_b16 a[12:13], v11 offset:16896\n\t"
    "ds_read_b64_tr_b16 a[14:15], v11 offset:18944\n\t"
    "v_mfma_f32_16x16x32_bf16 v[56:59], a[20:23], a[72:75], v[56:59]\n\t"
    "buffer_load_dwordx4 v4, s[56:59], s73 offen offset:1024 lds\n\t"
    "v_mfma_f32_16x16x32_bf16 v[60:63], a[20:23], a[76:79], v[60:63]\n\t"
    "s_waitcnt lgkmcnt(8)\n\t"
    "v_mfma_f32_16x16x32_bf16 v[64:67], a[24:27], a[72:75], v[64:67]\n\t"
    "ds_read_b64_tr_b16 a[16:17], v10 offset:17408\n\t"
    "ds_read_b64_tr_b16 a[18:19], v10 offset:19456\n\t"
    "v_mfma_f32_16x16x32_bf16 v[68:71], a[24:27], a[76:79], v[68:71]\n\t"
    "ds_read_b64_tr_b16 a[20:21], v11 offset:17408\n\t"
    "ds_read_b64_tr_b16 a[22:23], v11 offset:19456\n\t"
    "v_mfma_f32_16x16x32_bf16 v[72:75], a[28:31], a[72:75], v[72:75]\n\t"
    "buffer_load_dwordx4 v5, s[56:59], s73 offen offset:2048 lds\n\t"
    "v_mfma_f32_16x16x32_bf16 v[76:79], a[28:31], a[76:79], v[76:79]\n\t"
    "s_waitcnt vmcnt(13) lgkmcnt(8)\n\t"
    "v_mfma_f32_16x16x32_bf16 v[16:19], a[0:3], a[80:83], v[16:19]\n\t"
    "ds_read_b64_tr_b16 a[24:25], v10 offset:17920\n\t"
    "ds_read_b64_tr_b16 a[26:27], v10 offset:19968\n\t"
    "v_mfma_f32_16x16x32_bf16 v[20:23], a[0:3], a[84:87], v[20:23]\n\t"
    "ds_read_b64_tr_b16 a[28:29], v11 offset:17920\n\t"
    "ds_read_b64_tr_b16 a[30:31], v11 offset:19968\n\t"
    "v_mfma_f32_16x16x32_bf16 v[24:27], a[4:7], a[80:83], v[24:27]\n\t"
    "buffer_load_dwordx4 v6, s[56:59], s73 offen offset:3072 lds\n\t"
    "v_mfma_f32_16x16x32_bf16 v[28:31], a[4:7], a[84:87], v[28:31]\n\t"
    "s_waitcnt lgkmcnt(8)\n\t"
    "s_add_u32 s73, s73, 917504\n\t"
    "v_mfma_f32_16x16x32_bf16 v[32:35], a[8:11], a[80:83], v[32:35]\n\t"
    "ds_read_b64_tr_b16 a[0:1], v10 offset:24576\n\t"
    "ds_read_b64_tr_b16 a[2:3], v10 offset:26624\n\t"
    "v_mfma_f32_16x16x32_bf16 v[36:39], a[8:11], a[84:87], v[36:39]\n\t"
    "ds_read_b64_tr_b16 a[4:5], v11 offset:24576\n\t"
    "ds_read_b64_tr_b16 a[6:7], v11 offset:26624\n\t"
    "v_mfma_f32_16x16x32_bf16 v[40:43], a[12:15], a[80:83], v[40:43]\n\t"
    "buffer_load_dwordx4 a[112:115], v1, s[52:55], s72 offen offset:0 sc1\n\t"
    "v_mfma_f32_16x16x32_bf16 v[44:47], a[12:15], a[84:87], v[44:47]\n\t"
    "s_waitcnt lgkmcnt(8)\n\t"
    "v_mfma_f32_16x16x32_bf16 v[48:51], a[16:19], a[80:83], v[48:51]\n\t"
    "ds_read_b64_tr_b16 a[8:9], v10 offset:25088\n\t"
    "ds_read_b64_tr_b16 a[10:11], v10 offset:27136\n\t"
    "v_mfma_f32_16x16x32_bf16 v[52:55], a[16:19], a[84:87], v[52:55]\n\t"
    "ds_read_b64_tr_b16 a[12:13], v11 offset:25088\n\t"
    "ds_read_b64_tr_b16 a[14:15], v11 offset:27136\n\t"
    "v_mfma_f32_16x16x32_bf16 v[56:59], a[20:23], a[80:83], v[56:59]\n\t"
    "buffer_load_dwordx4 a[116:119], v1, s[52:55], s72 offen offset:256 sc1\n\t"
    "v_mfma_f32_16x16x32_bf16 v[60:63], a[20:23], a[84:87], v[60:63]\n\t"
    "s_waitcnt lgkmcnt(8)\n\t"
    "v_mfma_f32_16x16x32_bf16 v[64:67], a[24:27], a[80:83], v[64:67]\n\t"
    "ds_read_b64_tr_b16 a[16:17], v10 offset:25600\n\t"
    "ds_read_b64_tr_b16 a[18:19], v10 offset:27648\n\t"
    "v_mfma_f32_16x16x32_bf16 v[68:71], a[24:27], a[84:87], v[68:71]\n\t"
    "ds_read_b64_tr_b16 a[20:21], v11 offset:25600\n\t"
    "ds_read_b64_tr_b16 a[22:23], v11 offset:27648\n\t"
    "v_mfma_f32_16x16x32_bf16 v[72:75], a[28:31], a[80:83], v[72:75]\n\t"
    "buffer_load_dwordx4 a[120:123], v1, s[52:55], s72 offen offset:2048 sc1\n\t"
    "v_mfma_f32_16x16x32_bf16 v[76:79], a[28:31], a[84:87], v[76:79]\n\t"
    "s_waitcnt vmcnt(15) lgkmcnt(8)\n\t"
    "v_mfma_f32_16x16x32_bf16 v[16:19], a[0:3], a[88:91], v[16:19]\n\t"
    "s_waitcnt vmcnt(11)\n\t"
    "s_barrier\n\t"
    "ds_read_b64_tr_b16 a[24:25], v10 offset:26112\n\t"
    "ds_read_b64_tr_b16 a[26:27], v10 offset:28160\n\t"
    "v_mfma_f32_16x16x32_bf16 v[20:23], a[0:3], a[92:95], v[20:23]\n\t"
    "ds_read_b64_tr_b16 a[28:29], v11 offset:26112\n\t"
    "ds_read_b64_tr_b16 a[30:31], v11 offset:28160\n\t"
    "v_mfma_f32_16x16x32_bf16 v[24:27], a[4:7], a[88:91], v[24:27]\n\t"
    "buffer_load_dwordx4 a[124:127], v1, s[52:55], s72 offen offset:2304 sc1\n\t"
    "v_mfma_f32_16x16x32_bf16 v[28:31], a[4:7], a[92:95], v[28:31]\n\t"
    "s_waitcnt lgkmcnt(8)\n\t"
    "s_add_u32 s72, s72, 4096\n\t"
    "v_mfma_f32_16x16x32_bf16 v[32:35], a[8:11], a[88:91], v[32:35]\n\t"
    "ds_read_b64_tr_b16 a[0:1], v10 offset:32768\n\t"
    "ds_read_b64_tr_b16 a[2:3], v10 offset:34816\n\t"
    "v_mfma_f32_16x16x32_bf16 v[36:39], a[8:11], a[92:95], v[36:39]\n\t"
    "ds_read_b64_tr_b16 a[4:5], v11 offset:32768\n\t"
    "ds_read_b64_tr_b16 a[6:7], v11 offset:34816\n\t"
    "v_mfma_f32_16x16x32_bf16 v[40:43], a[12:15], a[88:91], v[40:43]\n\t"
    "v_mfma_f32_16x16x32_bf16 v[44:47], a[12:15], a[92:95], v[44:47]\n\t"
    "s_waitcnt lgkmcnt(8)\n\t"
    "v_mfma_f32_16x16x32_bf16 v[48:51], a[16:19], a[88:91], v[48:51]\n\t"
    "ds_read_b64_tr_b16 a[8:9], v10 offset:33280\n\t"
    "ds_read_b64_tr_b16 a[10:11], v10 offset:35328\n\t"
    "v_mfma_f32_16x16x32_bf16 v[52:55], a[16:19], a[92:95], v[52:55]\n\t"
    "ds_read_b64_tr_b16 a[12:13], v11 offset:33280\n\t"
    "ds_read_b64_tr_b16 a[14:15], v11 offset:35328\n\t"
    "v_mfma_f32_16x16x32_bf16 v[56:59], a[20:23], a[88:91], v[56:59]\n\t"
    "v_mfma_f32_16x16x32_bf16 v[60:63], a[20:23], a[92:95], v[60:63]\n\t"
    "s_waitcnt lgkmcnt(8)\n\t"
    "v_mfma_f32_16x16x32_bf16 v[64:67], a[24:27], a[88:91], v[64:67]\n\t"
    "ds_read_b64_tr_b16 a[16:17], v10 offset:33792\n\t"
    "ds_read_b64_tr_b16 a[18:19], v10 offset:35840\n\t"
    "v_mfma_f32_16x16x32_bf16 v[68:71], a[24:27], a[92:95], v[68:71]\n\t"
    "ds_read_b64_tr_b16 a[20:21], v11 offset:33792\n\t"
    "ds_read_b64_tr_b16 a[22:23], v11 offset:35840\n\t"
    "v_mfma_f32_16x16x32_bf16 v[72:75], a[28:31], a[88:91], v[72:75]\n\t"
    "v_mfma_f32_16x16x32_bf16 v[76:79], a[28:31], a[92:95], v[76:79]\n\t"
    "s_waitcnt vmcnt(10) lgkmcnt(8)\n\t"
    "v_mfma_f32_16x16x32_bf16 v[16:19], a[0:3], a[96:99], v[16:19]\n\t"
    "ds_read_b64_tr_b16 a[24:25], v10 offset:34304\n\t"
    "ds_read_b64_tr_b16 a[26:27], v10 offset:36352\n\t"
    "v_mfma_f32_16x16x32_bf16 v[20:23], a[0:3], a[100:103], v[20:23]\n\t"
    "ds_read_b64_tr_b16 a[28:29], v11 offset:34304\n\t"
    "ds_read_b64_tr_b16 a[30:31], v11 offset:36352\n\t"
    "v_mfma_f32_16x16x32_bf16 v[24:27], a[4:7], a[96:99], v[24:27]\n\t"
    "v_mfma_f32_16x16x32_bf16 v[28:31], a[4:7], a[100:103], v[28:31]\n\t"
    "s_waitcnt lgkmcnt(8)\n\t"
    "v_mfma_f32_16x16x32_bf16 v[32:35], a[8:11], a[96:99], v[32:35]\n\t"
    "ds_read_b64_tr_b16 a[0:1], v10 offset:40960\n\t"
    "ds_read_b64_tr_b16 a[2:3], v10 offset:43008\n\t"
    "v_mfma_f32_16x16x32_bf16 v[36:39], a[8:11], a[100:103], v[36:39]\n\t"
    "ds_read_b64_tr_b16 a[4:5], v11 offset:40960\n\t"
    "ds_read_b64_tr_b16 a[6:7], v11 offset:43008\n\t"
    "v_mfma_f32_16x16x32_bf16 v[40:43], a[12:15], a[96:99], v[40:43]\n\t"
    "v_mfma_f32_16x16x32_bf16 v[44:47], a[12:15], a[100:103], v[44:47]\n\t"
    "s_waitcnt lgkmcnt(8)\n\t"
    "v_mfma_f32_16x16x32_bf16 v[48:51], a[16:19], a[96:99], v[48:51]\n\t"
    "ds_read_b64_tr_b16 a[8:9], v10 offset:41472\n\t"
    "ds_read_b64_tr_b16 a[10:11], v10 offset:43520\n\t"
    "v_mfma_f32_16x16x32_bf16 v[52:55], a[16:19], a[100:103], v[52:55]\n\t"
    "ds_read_b64_tr_b16 a[12:13], v11 offset:41472\n\t"
    "ds_read_b64_tr_b16 a[14:15], v11 offset:43520\n\t"
    "v_mfma_f32_16x16x32_bf16 v[56:59], a[20:23], a[96:99], v[56:59]\n\t"
    "v_mfma_f32_16x16x32_bf16 v[60:63], a[20:23], a[100:103], v[60:63]\n\t"
    "s_waitcnt lgkmcnt(8)\n\t"
    "v_mfma_f32_16x16x32_bf16 v[64:67], a[24:27], a[96:99], v[64:67]\n\t"
    "ds_read_b64_tr_b16 a[16:17], v10 offset:41984\n\t"
    "ds_read_b64_tr_b16 a[18:19], v10 offset:44032\n\t"
    "v_mfma_f32_16x16x32_bf16 v[68:71], a[24:27], a[100:103], v[68:71]\n\t"
    "ds_read_b64_tr_b16 a[20:21], v11 offset:41984\n\t"
    "ds_read_b64_tr_b16 a[22:23], v11 offset:44032\n\t"
    "v_mfma_f32_16x16x32_bf16 v[72:75], a[28:31], a[96:99], v[72:75]\n\t"
    "v_mfma_f32_16x16x32_bf16 v[76:79], a[28:31], a[100:103], v[76:79]\n\t"
    "s_waitcnt vmcnt(8) lgkmcnt(8)\n\t"
    "v_mfma_f32_16x16x32_bf16 v[16:19], a[0:3], a[104:107], v[16:19]\n\t"
    "s_waitcnt vmcnt(4)\n\t"
    "s_barrier\n\t"
    "ds_read_b64_tr_b16 a[24:25], v10 offset:42496\n\t"
    "ds_read_b64_tr_b16 a[26:27], v10 offset:44544\n\t"
    "v_mfma_f32_16x16x32_bf16 v[20:23], a[0:3], a[108:111], v[20:23]\n\t"
    "ds_read_b64_tr_b16 a[28:29], v11 offset:42496\n\t"
    "ds_read_b64_tr_b16 a[30:31], v11 offset:44544\n\t"
    "v_mfma_f32_16x16x32_bf16 v[24:27], a[4:7], a[104:107], v[24:27]\n\t"
    "v_mfma_f32_16x16x32_bf16 v[28:31], a[4:7], a[108:111], v[28:31]\n\t"
    "s_waitcnt lgkmcnt(8)\n\t"
    "v_mfma_f32_16x16x32_bf16 v[32:35], a[8:11], a[104:107], v[32:35]\n\t"
    "ds_read_b64_tr_b16 a[0:1], v10 offset:49152\n\t"
    "ds_read_b64_tr_b16 a[2:3], v10 offset:51200\n\t"
    "v_mfma_f32_16x16x32_bf16 v[36:39], a[8:11], a[108:111], v[36:39]\n\t"
    "ds_read_b64_tr_b16 a[4:5], v11 offset:49152\n\t"
    "ds_read_b64_tr_b16 a[6:7], v11 offset:51200\n\t"
    "v_mfma_f32_16x16x32_bf16 v[40:43], a[12:15], a[104:107], v[40:43]\n\t"
    "v_mfma_f32_16x16x32_bf16 v[44:47], a[12:15], a[108:111], v[44:47]\n\t"
    "s_waitcnt lgkmcnt(8)\n\t"
    "v_mfma_f32_16x16x32_bf16 v[48:51], a[16:19], a[104:107], v[48:51]\n\t"
    "ds_read_b64_tr_b16 a[8:9], v10 offset:49664\n\t"
    "ds_read_b64_tr_b16 a[10:11], v10 offset:51712\n\t"
    "v_mfma_f32_16x16x32_bf16 v[52:55], a[16:19], a[108:111], v[52:55]\n\t"
    "ds_read_b64_tr_b16 a[12:13], v11 offset:49664\n\t"
    "ds_read_b64_tr_b16 a[14:15], v11 offset:51712\n\t"
    "v_mfma_f32_16x16x32_bf16 v[56:59], a[20:23], a[104:107], v[56:59]\n\t"
    "v_mfma_f32_16x16x32_bf16 v[60:63], a[20:23], a[108:111], v[60:63]\n\t"
    "s_waitcnt lgkmcnt(8)\n\t"
    "v_mfma_f32_16x16x32_bf16 v[64:67], a[24:27], a[104:107], v[64:67]\n\t"
    "ds_read_b64_tr_b16 a[16:17], v10 offset:50176\n\t"
    "ds_read_b64_tr_b16 a[18:19], v10 offset:52224\n\t"
    "v_mfma_f32_16x16x32_bf16 v[68:71], a[24:27], a[108:111], v[68:71]\n\t"
    "ds_read_b64_tr_b16 a[20:21], v11 offset:50176\n\t"
    "ds_read_b64_tr_b16 a[22:23], v11 offset:52224\n\t"
    "v_mfma_f32_16x16x32_bf16 v[72:75], a[28:31], a[104:107], v[72:75]\n\t"
    "v_mfma_f32_16x16x32_bf16 v[76:79], a[28:31], a[108:111], v[76:79]\n\t"
    "s_waitcnt vmcnt(2) lgkmcnt(8)\n\t"
    "v_mfma_f32_16x16x32_bf16 v[16:19], a[0:3], a[112:115], v[16:19]\n\t"
    "ds_read_b64_tr_b16 a[24:25], v10 offset:50688\n\t"
    "ds_read_b64_tr_b16 a[26:27], v10 offset:52736\n\t"
    "v_mfma_f32_16x16x32_bf16 v[20:23], a[0:3], a[116:119], v[20:23]\n\t"
    "ds_read_b64_tr_b16 a[28:29], v11 offset:50688\n\t"
    "ds_read_b64_tr_b16 a[30:31], v11 offset:52736\n\t"
    "v_mfma_f32_16x16x32_bf16 v[24:27], a[4:7], a[112:115], v[24:27]\n\t"
    "v_mfma_f32_16x16x32_bf16 v[28:31], a[4:7], a[116:119], v[28:31]\n\t"
    "s_waitcnt lgkmcnt(8)\n\t"
    "v_mfma_f32_16x16x32_bf16 v[32:35], a[8:11], a[112:115], v[32:35]\n\t"
    "ds_read_b64_tr_b16 a[0:1], v10 offset:57344\n\t"
    "ds_read_b64_tr_b16 a[2:3], v10 offset:59392\n\t"
    "v_mfma_f32_16x16x32_bf16 v[36:39], a[8:11], a[116:119], v[36:39]\n\t"
    "ds_read_b64_tr_b16 a[4:5], v11 offset:57344\n\t"
    "ds_read_b64_tr_b16 a[6:7], v11 offset:59392\n\t"
    "v_mfma_f32_16x16x32_bf16 v[40:43], a[12:15], a[112:115], v[40:43]\n\t"
    "v_mfma_f32_16x16x32_bf16 v[44:47], a[12:15], a[116:119], v[44:47]\n\t"
    "s_waitcnt lgkmcnt(8)\n\t"
    "v_mfma_f32_16x16x32_bf16 v[48:51], a[16:19], a[112:115], v[48:51]\n\t"
    "ds_read_b64_tr_b16 a[8:9], v10 offset:57856\n\t"
    "ds_read_b64_tr_b16 a[10:11], v10 offset:59904\n\t"
    "v_mfma_f32_16x16x32_bf16 v[52:55], a[16:19], a[116:119], v[52:55]\n\t"
    "ds_read_b64_tr_b16 a[12:13], v11 offset:57856\n\t"
    "ds_read_b64_tr_b16 a[14:15], v11 offset:59904\n\t"
    "v_mfma_f32_16x16x32_bf16 v[56:59], a[20:23], a[112:115], v[56:59]\n\t"
    "v_mfma_f32_16x16x32_bf16 v[60:63], a[20:23], a[116:119], v[60:63]\n\t"
    "s_waitcnt lgkmcnt(8)\n\t"
    "v_mfma_f32_16x16x32_bf16 v[64:67], a[24:27], a[112:115], v[64:67]\n\t"
    "ds_read_b64_tr_b16 a[16:17], v10 offset:58368\n\t"
    "ds_read_b64_tr_b16 a[18:19], v10 offset:60416\n\t"
    "v_mfma_f32_16x16x32_bf16 v[68:71], a[24:27], a[116:119], v[68:71]\n\t"
    "ds_read_b64_tr_b16 a[20:21], v11 offset:58368\n\t"
    "ds_read_b64_tr_b16 a[22:23], v11 offset:60416\n\t"
    "v_mfma_f32_16x16x32_bf16 v[72:75], a[28:31], a[112:115], v[72:75]\n\t"
    "v_mfma_f32_16x16x32_bf16 v[76:79], a[28:31], a[116:119], v[76:79]\n\t"
    "s_waitcnt vmcnt(0) lgkmcnt(8)\n\t"
    "v_mfma_f32_16x16x32_bf16 v[16:19], a[0:3], a[120:123], v[16:19]\n\t"
    "ds_read_b64_tr_b16 a[24:25], v10 offset:58880\n\t"
    "ds_read_b64_tr_b16 a[26:27], v10 offset:60928\n\t"
    "v_mfma_f32_16x16x32_bf16 v[20:23], a[0:3], a[124:127], v[20:23]\n\t"
    "ds_read_b64_tr_b16 a[28:29], v11 offset:58880\n\t"
    "ds_read_b64_tr_b16 a[30:31], v11 offset:60928\n\t"
    "v_mfma_f32_16x16x32_bf16 v[24:27], a[4:7], a[120:123], v[24:27]\n\t"
    "v_mfma_f32_16x16x32_bf16 v[28:31], a[4:7], a[124:127], v[28:31]\n\t"
    "s_waitcnt lgkmcnt(8)\n\t"
    "v_mfma_f32_16x16x32_bf16 v[32:35], a[8:11], a[120:123], v[32:35]\n\t"
    "v_mfma_f32_16x16x32_bf16 v[36:39], a[8:11], a[124:127], v[36:39]\n\t"
    "v_mfma_f32_16x16x32_bf16 v[40:43], a[12:15], a[120:123], v[40:43]\n\t"
    "v_mfma_f32_16x16x32_bf16 v[44:47], a[12:15], a[124:127], v[44:47]\n\t"
    "s_waitcnt lgkmcnt(4)\n\t"
    "v_mfma_f32_16x16x32_bf16 v[48:51], a[16:19], a[120:123], v[48:51]\n\t"
    "v_mfma_f32_16x16x32_bf16 v[52:55], a[16:19], a[124:127], v[52:55]\n\t"
    "v_mfma_f32_16x16x32_bf16 v[56:59], a[20:23], a[120:123], v[56:59]\n\t"
    "v_mfma_f32_16x16x32_bf16 v[60:63], a[20:23], a[124:127], v[60:63]\n\t"
    "s_waitcnt lgkmcnt(0)\n\t"
    "v_mfma_f32_16x16x32_bf16 v[64:67], a[24:27], a[120:123], v[64:67]\n\t"
    "v_mfma_f32_16x16x32_bf16 v[68:71], a[24:27], a[124:127], v[68:71]\n\t"
    "v_mfma_f32_16x16x32_bf16 v[72:75], a[28:31], a[120:123], v[72:75]\n\t"
    "v_mfma_f32_16x16x32_bf16 v[76:79], a[28:31], a[124:127], v[76:79]\n\t"
    "v_pk_add_f32 v[160:161], v[160:161], v[80:81]\n\t"
    "v_pk_add_f32 v[162:163], v[162:163], v[82:83]\n\t"
    "v_pk_add_f32 v[164:165], v[164:165], v[84:85]\n\t"
    "v_pk_add_f32 v[166:167], v[166:167], v[86:87]\n\t"
    "v_pk_add_f32 v[168:169], v[168:169], v[88:89]\n\t"
    "v_pk_add_f32 v[170:171], v[170:171], v[90:91]\n\t"
    "v_pk_add_f32 v[172:173], v[172:173], v[92:93]\n\t"
    "v_pk_add_f32 v[174:175], v[174:175], v[94:95]\n\t"
    "v_cvt_pk_bf16_f32 v176, v48, v49\n\t"
    "v_cvt_pk_bf16_f32 v177, v50, v51\n\t"
    "v_cvt_pk_bf16_f32 v178, v56, v57\n\t"
    "v_cvt_pk_bf16_f32 v179, v58, v59\n\t"
    "s_nop 0\n\t"
    "v_permlane16_swap_b32 v176, v178\n\t"
    "v_permlane16_swap_b32 v177, v179\n\t"
    "buffer_store_dwordx4 v[176:179], v240, s[68:71], s88 offen offset:0 sc1\n\t"
    "v_cvt_pk_bf16_f32 v180, v64, v65\n\t"
    "v_cvt_pk_bf16_f32 v181, v66, v67\n\t"
    "v_cvt_pk_bf16_f32 v182, v72, v73\n\t"
    "v_cvt_pk_bf16_f32 v183, v74, v75\n\t"
    "s_nop 0\n\t"
    "v_permlane16_swap_b32 v180, v182\n\t"
    "v_permlane16_swap_b32 v181, v183\n\t"
    "buffer_store_dwordx4 v[180:183], v240, s[68:71], s88 offen offset:1024 sc1\n\t"
    "v_cvt_pk_bf16_f32 v184, v52, v53\n\t"
    "v_cvt_pk_bf16_f32 v185, v54, v55\n\t"
    "v_cvt_pk_bf16_f32 v186, v60, v61\n\t"
    "v_cvt_pk_bf16_f32 v187, v62, v63\n\t"
    "s_nop 0\n\t"
    "v_permlane16_swap_b32 v184, v186\n\t"
    "v_permlane16_swap_b32 v185, v187\n\t"
    "buffer_store_dwordx4 v[184:187], v240, s[68:71], s88 offen offset:2048 sc1\n\t"
    "v_cvt_pk_bf16_f32 v188, v68, v69\n\t"
    "v_cvt_pk_bf16_f32 v189, v70, v71\n\t"
    "v_cvt_pk_bf16_f32 v190, v76, v77\n\t"
    "v_cvt_pk_bf16_f32 v191, v78, v79\n\t"
    "s_nop 0\n\t"
    "v_permlane16_swap_b32 v188, v190\n\t"
    "v_permlane16_swap_b32 v189, v191\n\t"
    "buffer_store_dwordx4 v[188:191], v240, s[68:71], s88 offen offset:3072 sc1\n\t"
    "v_permlane16_swap_b32 v16, v24\n\t"
    "v_permlane16_swap_b32 v17, v25\n\t"
    "v_permlane16_swap_b32 v18, v26\n\t"
    "v_permlane16_swap_b32 v19, v27\n\t"
    "v_permlane16_swap_b32 v32, v40\n\t"
    "v_permlane16_swap_b32 v33, v41\n\t"
    "v_permlane16_swap_b32 v34, v42\n\t"
    "v_permlane16_swap_b32 v35, v43\n\t"
    "v_permlane16_swap_b32 v20, v28\n\t"
    "v_permlane16_swap_b32 v21, v29\n\t"
    "v_permlane16_swap_b32 v22, v30\n\t"
    "v_permlane16_swap_b32 v23, v31\n\t"
    "v_permlane16_swap_b32 v36, v44\n\t"
    "v_permlane16_swap_b32 v37, v45\n\t"
    "v_permlane16_swap_b32 v38, v46\n\t"
    "v_permlane16_swap_b32 v39, v47\n\t"
    "s_mov_b32 s81, 0x10000\n\t"
    "LS_m_down:\n\t"
    "buffer_load_dwordx4 v[192:195], v240, s[68:71], s89 offen offset:0 sc1\n\t"
    "buffer_load_dwordx4 v[196:199], v240, s[68:71], s89 offen offset:1024 sc1\n\t"
    "buffer_load_dwordx4 v[200:203], v240, s[68:71], s89 offen offset:2048 sc1\n\t"
    "buffer_load_dwordx4 v[204:207], v240, s[68:71], s89 offen offset:3072 sc1\n\t"
    "s_waitcnt vmcnt(0)\n\t"
    "v_max3_u32 v2, v192, v193, v194\n\t"
    "v_max3_u32 v2, v195, v196, v2\n\t"
    "v_max3_u32 v2, v197, v198, v2\n\t"
    "v_max3_u32 v2, v199, v200, v2\n\t"
    "v_max3_u32 v2, v201, v202, v2\n\t"
    "v_max3_u32 v2, v203, v204, v2\n\t"
    "v_max3_u32 v2, v205, v206, v2\n\t"
    "v_max_u32 v2, v207, v2\n\t"
    "v_cmp_eq_u32 vcc, -1, v2\n\t"
    "s_sub_u32 s81, s81, 1\n\t"
    "s_cmp_eq_u32 s81, 0\n\t"
    "s_cbranch_scc1 LT_m_down\n\t"
    "s_cbranch_vccz LT_m_down\n\t"
    "s_sleep 1\n\t"
    "s_branch LS_m_down\n\t"
    "LT_m_down:\n\t"
    "v_lshlrev_b32 v208, 16, v192\n\t"
    "v_and_b32 v209, 0xffff0000, v192\n\t"
    "v_lshlrev_b32 v210, 16, v193\n\t"
    "v_and_b32 v211, 0xffff0000, v193\n\t"
    "v_lshlrev_b32 v212, 16, v194\n\t"
    "v_and_b32 v213, 0xffff0000, v194\n\t"
    "v_lshlrev_b32 v214, 16, v195\n\t"
    "v_and_b32 v215, 0xffff0000, v195\n\t"
    "v_pk_add_f32 v[16:17], v[16:17], v[208:209]\n\t"
    "v_pk_add_f32 v[18:19], v[18:19], v[210:211]\n\t"
    "v_pk_add_f32 v[24:25], v[24:25], v[212:213]\n\t"
    "v_pk_add_f32 v[26:27], v[26:27], v[214:215]\n\t"
    "v_lshlrev_b32 v208, 16, v144\n\t"
    "v_and_b32 v209, 0xffff0000, v144\n\t"
    "v_lshlrev_b32 v210, 16, v145\n\t"
    "v_and_b32 v211, 0xffff0000, v145\n\t"
    "v_lshlrev_b32 v212, 16, v146\n\t"
    "v_and_b32 v213, 0xffff0000, v146\n\t"
    "v_lshlrev_b32 v214, 16, v147\n\t"
    "v_and_b32 v215, 0xffff0000, v147\n\t"
    "v_pk_fma_f32 v[16:17], v[16:17], v[160:161], v[208:209]\n\t"
    "v_pk_fma_f32 v[18:19], v[18:19], v[162:163], v[210:211]\n\t"
    "v_pk_fma_f32 v[24:25], v[24:25], v[164:165], v[212:213]\n\t"
    "v_pk_fma_f32 v[26:27], v[26:27], v[166:167], v[214:215]\n\t"
    "v_cvt_pk_bf16_f32 v224, v16, v17\n\t"
    "v_cvt_pk_bf16_f32 v225, v18, v19\n\t"
    "v_cvt_pk_bf16_f32 v226, v24, v25\n\t"
    "v_cvt_pk_bf16_f32 v227, v26, v27\n\t"
    "s_add_u32 s80, s91, 0\n\t"
    "buffer_store_dwordx4 v[224:227], v241, s[60:63], s80 offen offset:0 sc1\n\t"
    "v_lshlrev_b32 v216, 16, v196\n\t"
    "v_and_b32 v217, 0xffff0000, v196\n\t"
    "v_lshlrev_b32 v218, 16, v197\n\t"
    "v_and_b32 v219, 0xffff0000, v197\n\t"
    "v_lshlrev_b32 v220, 16, v198\n\t"
    "v_and_b32 v221, 0xffff0000, v198\n\t"
    "v_lshlrev_b32 v222, 16, v199\n\t"
    "v_and_b32 v223, 0xffff0000, v199\n\t"
    "v_pk_add_f32 v[32:33], v[32:33], v[216:217]\n\t"
    "v_pk_add_f32 v[34:35], v[34:35], v[218:219]\n\t"
    "v_pk_add_f32 v[40:41], v[40:41], v[220:221]\n\t"
    "v_pk_add_f32 v[42:43], v[42:43], v[222:223]\n\t"
    "v_lshlrev_b32 v216, 16, v148\n\t"
    "v_and_b32 v217, 0xffff0000, v148\n\t"
    "v_lshlrev_b32 v218, 16, v149\n\t"
    "v_and_b32 v219, 0xffff0000, v149\n\t"
    "v_lshlrev_b32 v220, 16, v150\n\t"
    "v_and_b32 v221, 0xffff0000, v150\n\t"
    "v_lshlrev_b32 v222, 16, v151\n\t"
    "v_and_b32 v223, 0xffff0000, v151\n\t"
    "v_pk_fma_f32 v[32:33], v[32:33], v[168:169], v[216:217]\n\t"
    "v_pk_fma_f32 v[34:35], v[34:35], v[170:171], v[218:219]\n\t"
    "v_pk_fma_f32 v[40:41], v[40:41], v[172:173], v[220:221]\n\t"
    "v_pk_fma_f32 v[42:43], v[42:43], v[174:175], v[222:223]\n\t"
    "v_cvt_pk_bf16_f32 v228, v32, v33\n\t"
    "v_cvt_pk_bf16_f32 v229, v34, v35\n\t"
    "v_cvt_pk_bf16_f32 v230, v40, v41\n\t"
    "v_cvt_pk_bf16_f32 v231, v42, v43\n\t"
    "s_add_u32 s80, s91, 0\n\t"
    "buffer_store_dwordx4 v[228:231], v241, s[60:63], s80 offen offset:64 sc1\n\t"
    "v_lshlrev_b32 v208, 16, v200\n\t"
    "v_and_b32 v209, 0xffff0000, v200\n\t"
    "v_lshlrev_b32 v210, 16, v201\n\t"
    "v_and_b32 v211, 0xffff0000, v201\n\t"
    "v_lshlrev_b32 v212, 16, v202\n\t"
    "v_and_b32 v213, 0xffff0000, v202\n\t"
    "v_lshlrev_b32 v214, 16, v203\n\t"
    "v_and_b32 v215, 0xffff0000, v203\n\t"
    "v_pk_add_f32 v[20:21], v[20:21], v[208:209]\n\t"
    "v_pk_add_f32 v[22:23], v[22:23], v[210:211]\n\t"
    "v_pk_add_f32 v[28:29], v[28:29], v[212:213]\n\t"
    "v_pk_add_f32 v[30:31], v[30:31], v[214:215]\n\t"
    "v_lshlrev_b32 v208, 16, v152\n\t"
    "v_and_b32 v209, 0xffff0000, v152\n\t"
    "v_lshlrev_b32 v210, 16, v153\n\t"
    "v_and_b32 v211, 0xffff0000, v153\n\t"
    "v_lshlrev_b32 v212, 16, v154\n\t"
    "v_and_b32 v213, 0xffff0000, v154\n\t"
    "v_lshlrev_b32 v214, 16, v155\n\t"
    "v_and_b32 v215, 0xffff0000, v155\n\t"
    "v_pk_fma_f32 v[20:21], v[20:21], v[160:161], v[208:209]\n\t"
    "v_pk_fma_f32 v[22:23], v[22:23], v[162:163], v[210:211]\n\t"
    "v_pk_fma_f32 v[28:29], v[28:29], v[164:165], v[212:213]\n\t"
    "v_pk_fma_f32 v[30:31], v[30:31], v[166:167], v[214:215]\n\t"
    "v_cvt_pk_bf16_f32 v232, v20, v21\n\t"
    "v_cvt_pk_bf16_f32 v233, v22, v23\n\t"
    "v_cvt_pk_bf16_f32 v234, v28, v29\n\t"
    "v_cvt_pk_bf16_f32 v235, v30, v31\n\t"
    "s_add_u32 s80, s91, 32768\n\t"
    "buffer_store_dwordx4 v[232:235], v241, s[60:63], s80 offen offset:0 sc1\n\t"
    "v_lshlrev_b32 v216, 16, v204\n\t"
    "v_and_b32 v217, 0xffff0000, v204\n\t"
    "v_lshlrev_b32 v218, 16, v205\n\t"
    "v_and_b32 v219, 0xffff0000, v205\n\t"
    "v_lshlrev_b32 v220, 16, v206\n\t"
    "v_and_b32 v221, 0xffff0000, v206\n\t"
    "v_lshlrev_b32 v222, 16, v207\n\t"
    "v_and_b32 v223, 0xffff0000, v207\n\t"
    "v_pk_add_f32 v[36:37], v[36:37], v[216:217]\n\t"
    "v_pk_add_f32 v[38:39], v[38:39], v[218:219]\n\t"
    "v_pk_add_f32 v[44:45], v[44:45], v[220:221]\n\t"
    "v_pk_add_f32 v[46:47], v[46:47], v[222:223]\n\t"
    "v_lshlrev_b32 v216, 16, v156\n\t"
    "v_and_b32 v217, 0xffff0000, v156\n\t"
    "v_lshlrev_b32 v218, 16, v157\n\t"
    "v_and_b32 v219, 0xffff0000, v157\n\t"
    "v_lshlrev_b32 v220, 16, v158\n\t"
    "v_and_b32 v221, 0xffff0000, v158\n\t"
    "v_lshlrev_b32 v222, 16, v159\n\t"
    "v_and_b32 v223, 0xffff0000, v159\n\t"
    "v_pk_fma_f32 v[36:37], v[36:37], v[168:169], v[216:217]\n\t"
    "v_pk_fma_f32 v[38:39], v[38:39], v[170:171], v[218:219]\n\t"
    "v_pk_fma_f32 v[44:45], v[44:45], v[172:173], v[220:221]\n\t"
    "v_pk_fma_f32 v[46:47], v[46:47], v[174:175], v[222:223]\n\t"
    "v_cvt_pk_bf16_f32 v236, v36, v37\n\t"
    "v_cvt_pk_bf16_f32 v237, v38, v39\n\t"
    "v_cvt_pk_bf16_f32 v238, v44, v45\n\t"
    "v_cvt_pk_bf16_f32 v239, v46, v47\n\t"
    "s_add_u32 s80, s91, 32768\n\t"
    "buffer_store_dwordx4 v[236:239], v241, s[60:63], s80 offen offset:64 sc1\n\t"
    :  : "s"(karg), "s"(wid), "s"(bid), "s"(ldsb), "s"(tag)
    : "s33", "v0", "v1", "v2", "v3", "v4", "v5", "v6", "v7", "v8", "v9", "v10", "v11", "v12", "v13", "v14", "v15", "v16", "v17", "v18", "v19", "v20", "v21", "v22", "v23", "v24", "v25", "v26", "v27", "v28", "v29", "v30", "v31", "v32", "v33", "v34", "v35", "v36", "v37", "v38", "v39", "v40", "v41", "v42", "v43", "v44", "v45", "v46", "v47", "v48", "v49", "v50", "v51", "v52", "v53", "v54", "v55", "v56", "v57", "v58", "v59", "v60", "v61", "v62", "v63", "v64", "v65", "v66", "v67", "v68", "v69", "v70", "v71", "v72", "v73", "v74", "v75", "v76", "v77", "v78", "v79", "v80", "v81", "v82", "v83", "v84", "v85", "v86", "v87", "v88", "v89", "v90", "v91", "v92", "v93", "v94", "v95", "v96", "v97", "v98", "v99", "v100", "v101", "v102", "v103", "v104", "v105", "v106", "v107", "v108", "v109", "v110", "v111", "v112", "v113", "v114", "v115", "v116", "v117", "v118", "v119", "v120", "v121", "v122", "v123", "v124", "v125", "v126", "v127", "v128", "v129", "v130", "v131", "v132", "v133", "v134", "v135", "v136", "v137", "v138", "v139", "v140", "v141", "v142", "v143", "v144", "v145", "v146", "v147", "v148", "v149", "v150", "v151", "v152", "v153", "v154", "v155", "v156", "v157", "v158", "v159", "v160", "v161", "v162", "v163", "v164", "v165", "v166", "v167", "v168", "v169", "v170", "v171", "v172", "v173", "v174", "v175", "v176", "v177", "v178", "v179", "v180", "v181", "v182", "v183", "v184", "v185", "v186", "v187", "v188", "v189", "v190", "v191", "v192", "v193", "v194", "v195", "v196", "v197", "v198", "v199", "v200", "v201", "v202", "v203", "v204", "v205", "v206", "v207", "v208", "v209", "v210", "v211", "v212", "v213", "v214", "v215", "v216", "v217", "v218", "v219", "v220", "v221", "v222", "v223", "v224", "v225", "v226", "v227", "v228", "v229", "v230", "v231", "v232", "v233", "v234", "v235", "v236", "v237", "v238", "v239", "v240", "v241", "v242", "v243", "v244", "v245", "v246", "v247", "v248", "v249", "v250", "v251", "v252", "v253", "v254", "v255", "a0", "a1", "a2", "a3", "a4", "a5", "a6", "a7", "a8", "a9", "a10", "a11", "a12", "a13", "a14", "a15", "a16", "a17", "a18", "a19", "a20", "a21", "a22", "a23", "a24", "a25", "a26", "a27", "a28", "a29", "a30", "a31", "a32", "a33", "a34", "a35", "a36", "a37", "a38", "a39", "a40", "a41", "a42", "a43", "a44", "a45", "a46", "a47", "a48", "a49", "a50", "a51", "a52", "a53", "a54", "a55", "a56", "a57", "a58", "a59", "a60", "a61", "a62", "a63", "a64", "a65", "a66", "a67", "a68", "a69", "a70", "a71", "a72", "a73", "a74", "a75", "a76", "a77", "a78", "a79", "a80", "a81", "a82", "a83", "a84", "a85", "a86", "a87", "a88", "a89", "a90", "a91", "a92", "a93", "a94", "a95", "a96", "a97", "a98", "a99", "a100", "a101", "a102", "a103", "a104", "a105", "a106", "a107", "a108", "a109", "a110", "a111", "a112", "a113", "a114", "a115", "a116", "a117", "a118", "a119", "a120", "a121", "a122", "a123", "a124", "a125", "a126", "a127", "s34", "s35", "s36", "s37", "s38", "s39", "s40", "s41", "s42", "s43", "s44", "s45", "s46", "s47", "s48", "s49", "s50", "s51", "s52", "s53", "s54", "s55", "s56", "s57", "s58", "s59", "s60", "s61", "s62", "s63", "s64", "s65", "s66", "s67", "s68", "s69", "s70", "s71", "s72", "s73", "s74", "s75", "s76", "s77", "s78", "s79", "s80", "s81", "s82", "s83", "s84", "s85", "s86", "s87", "s88", "s89", "s90", "s91", "s92", "s93", "s94", "s95", "s96", "s97", "s98", "s99", "vcc", "scc", "memory");

  asm volatile("MEGA_END:");

}

#define WS_MPART (64u<<10)
#define WS_FLAG (192u<<20)
#define WS_H   (1u<<20)
#define WS_O   (WS_H + 2048u*1024u*2u)
#define WS_X1  (WS_O + 2048u*4096u*2u)
#define WS_H2  (WS_X1 + 2048u*1024u*2u)
#define WS_ACT (WS_H2 + 2048u*1024u*2u)
#define WS_DBG (WS_ACT + 2048u*3584u*2u)
#define WS_PART (WS_DBG + (16u<<20))
#define WS_XCH (WS_PART + (16u<<20))
extern "C" void kernel_launch(void* const* d_in, const int* in_sizes, int n_in, void* d_out, int out_size, void* d_ws, size_t ws_size, hipStream_t stream) {
  const u16* x=(const u16*)d_in[0]; const u16* kc=(const u16*)d_in[1]; const u16* vc=(const u16*)d_in[2];
  const u16* wq=(const u16*)d_in[3]; const u16* wo=(const u16*)d_in[4];
  const u16* wg=(const u16*)d_in[6]; const u16* wu=(const u16*)d_in[7]; const u16* wd=(const u16*)d_in[8];
  const u16* c=(const u16*)d_in[9]; const u16* wm=(const u16*)d_in[10];
  char* ws=(char*)d_ws;
  u16* H=(u16*)(ws+WS_H); u16* O=(u16*)(ws+WS_O); u16* X1=(u16*)(ws+WS_X1); u16* H2=(u16*)(ws+WS_H2); u16* ACT=(u16*)(ws+WS_ACT);
  k_mega<<<256,256,0,stream>>>(H,(void*)wq,(void*)kc,(void*)vc,O,(void*)wo,(void*)wg,(void*)wu,(void*)wd,ws+WS_FLAG,(void*)x,ws+WS_MPART,ws+WS_PART,H2,ACT,X1,ws+WS_XCH,d_out,(void*)c,(void*)wm);
}
```
